```python
import torch

def _softmax(x, axis=-1):
    return torch.softmax(x, dim=axis)

def _gelu(x):
    return torch.nn.functional.gelu(x, approximate='tanh')

def _silu(x):
    return torch.nn.functional.silu(x)

def _sigmoid(x):
    return torch.sigmoid(x)

def _layernorm(x, axis=-1, eps=1e-05):
    m = x.mean(dim=axis, keepdim=True)
    v = x.var(dim=axis, keepdim=True, unbiased=False)
    return (x - m) / torch.sqrt(v + eps)

def _scatter_add(tgt, idx, src, axis=0):
    return tgt.clone().index_add_(axis, idx, src)

def _segment_sum(src, seg, n):
    out = torch.zeros((n,) + src.shape[1:], dtype=src.dtype)
    return out.index_add_(0, seg, src)

def _pad(x, pw):
    flat = tuple((v for pair in reversed(pw) for v in pair))
    return torch.nn.functional.pad(x, flat)
kernel_name = 'adaLN_cond__sliding_block__swiglu_ffn__prompt'

def setup_inputs():

    _w = lambda a: (lambda t: t.to(torch.bfloat16) if t.is_floating_point() else t)(torch.as_tensor(a))

    def _A_setup():
        B, S, D = (1, 2048, 640)
        return {'x': _w((lambda *s: torch.randn(*s, dtype=torch.bfloat16) * 0.05)(B, S, D)), 'wqkv': _w((lambda *s: torch.randn(*s, dtype=torch.bfloat16) * 0.05)(D, 3 * D)), 'wo': _w((lambda *s: torch.randn(*s, dtype=torch.bfloat16) * 0.05)(D, D))}
    _A_in = _A_setup()

    def _B_setup():
        B, S, D, F = (1, 2048, 640, 1728)
        return {'x': _w((lambda *s: torch.randn(*s, dtype=torch.bfloat16) * 0.05)(B, S, D)), 'w_gate': _w((lambda *s: torch.randn(*s, dtype=torch.bfloat16) * 0.05)(D, F)), 'w_up': _w((lambda *s: torch.randn(*s, dtype=torch.bfloat16) * 0.05)(D, F)), 'w_down': _w((lambda *s: torch.randn(*s, dtype=torch.bfloat16) * 0.05)(F, D))}
    _B_in = _B_setup()
    return {'A_x': _A_in['x'], 'A_wqkv': _A_in['wqkv'], 'A_wo': _A_in['wo'], 'B_x': _B_in['x'], 'B_w_gate': _B_in['w_gate'], 'B_w_up': _B_in['w_up'], 'B_w_down': _B_in['w_down'], 'c': _w((lambda *s: torch.randn(*s, dtype=torch.bfloat16) * 0.1)(1, 640) * 8.0), 'w_mod': _w((lambda *s: torch.randn(*s, dtype=torch.bfloat16) * 0.1)(640, 6 * 640) * 640 ** (-0.5) * 8.0)}

def reference(A_x, A_wqkv, A_wo, B_x, B_w_gate, B_w_up, B_w_down, c, w_mod):

    def _A_ref(x, wqkv, wo):
        B, S, D = x.shape
        H = 32
        Dh = D // H
        W = 256
        qkv = (lambda x, *d: x.permute(*d))((x @ wqkv).reshape(B, S, 3, H, Dh), 2, 0, 3, 1, 4)
        q, k, v = (qkv[0], qkv[1], qkv[2])
        scores = torch.einsum('bhqd,bhkd->bhqk', q, k) * Dh ** (-0.5)
        idx = torch.arange(S)
        mask = (idx[:, None] - idx[None, :] >= 0) & (idx[:, None] - idx[None, :] < W)
        scores = torch.where(mask[None, None], scores, -1000000000.0)
        p = _softmax(scores, axis=-1)
        o = torch.einsum('bhqk,bhkd->bhqd', p, v)
        return (lambda x, *d: x.permute(*d))(o, 0, 2, 1, 3).reshape(B, S, D) @ wo

    def _B_ref(x, w_gate, w_up, w_down):
        g = _silu(x @ w_gate)
        u = x @ w_up
        return g * u @ w_down
    x = A_x
    mod = (_silu(c) @ w_mod).reshape(c.shape[0], 6, 1, -1)
    sa, ca, ga, sb, cb, gb = (mod[:, 0].clone(), mod[:, 1].clone(), mod[:, 2].clone(), mod[:, 3].clone(), mod[:, 4].clone(), mod[:, 5].clone())
    h = _layernorm(x, axis=-1) * (1 + ca) + sa
    x = x + ga * _A_ref(h, A_wqkv, A_wo)
    h = _layernorm(x, axis=-1) * (1 + cb) + sb
    x = x + gb * _B_ref(h, B_w_gate, B_w_up, B_w_down)
    return x
```

```cpp
#include <hip/hip_runtime.h>
#include <hip/hip_bf16.h>
#include <stdint.h>

typedef unsigned short u16;
typedef unsigned u32;
typedef unsigned long long u64;
typedef __attribute__((ext_vector_type(2))) unsigned u32x2;
typedef __attribute__((ext_vector_type(4))) unsigned u32x4;
typedef __attribute__((ext_vector_type(2))) float f32x2;
typedef __attribute__((ext_vector_type(4))) float f32x4;
typedef __attribute__((ext_vector_type(2))) __bf16 bf16x2;
typedef __attribute__((ext_vector_type(8))) __bf16 bf16x8;
typedef __attribute__((ext_vector_type(4))) short s16x4;
#define LAS __attribute__((address_space(3)))
#define GAS __attribute__((address_space(1)))
#define DI __device__ __forceinline__

constexpr int SEQ = 2048, DM = 640, FF = 1728;
constexpr float QSCALE = 0.22360679774997896f * 1.4426950408889634f;
constexpr float LOG2E = 1.4426950408889634f;

constexpr u32 WS_EPOCH = 0;
constexpr u32 WS_ERR   = 256;
constexpr u32 WS_FKV   = 4096;
constexpr u32 WS_FO    = WS_FKV + 2048;
constexpr u32 WS_FX1   = WS_FO + 2048;
constexpr u32 WS_FACT  = WS_FX1 + 2048;
constexpr u32 WS_FWP   = WS_FACT + 2048;
constexpr u32 WS_MODG  = 16384;
constexpr u32 WS_UV    = 32768;
constexpr u32 WS_STAT  = 65536;
constexpr u32 WS_KX    = 262144;
constexpr u32 WS_VX    = WS_KX + 256 * 10240;
constexpr u32 WS_O     = WS_VX + 256 * 12288;
constexpr u32 WS_X1    = WS_O + SEQ * DM * 2;
constexpr u32 WS_ACT   = WS_X1 + SEQ * DM * 2;
constexpr u32 WS_WP    = WS_ACT + SEQ * FF * 2;
constexpr u32 WS_STAMP = WS_WP + 16 * 20 * 14 * 1024;
constexpr u32 WS_END   = WS_STAMP + 2 * 256 * 32 * 8;

constexpr u32 L_WKV = 0, L_WQ = 102400, L_G = 153600, L_SA = 156160, L_MODP = 158720, L_MODB = 159232;
constexpr u32 L_K = 0, L_V = 30720, L_Q = 67584, L_OI = 77824, L_WPRAW = 88064, L_SB = 108544, L_G2 = 109824, L_UVP = 111104;
constexpr u32 L_WO0 = 0, L_WO1 = 112640, L_RED3 = 51200, L_X1S = 71680, L_GA = 81920, L_ST3 = 82432;
constexpr u32 L_R4 = 0, R4_STAGE = 45056, L_MR = 135168, L_UV = 136192, L_XCH = 138240;
constexpr u32 L_R5 = 0, R5_STAGE = 40960, L_RED5 = 122880, L_GB = 143360;

struct Params {
    const u16* x; const u16* wqkv; const u16* wo; const u16* wg; const u16* wu; const u16* wd;
    const u16* c; const u16* wmod; u16* out; char* ws;
};

DI float bflo(u32 d) { return __uint_as_float(d << 16); }
DI float bfhi(u32 d) { return __uint_as_float(d & 0xffff0000u); }
DI u32 pack2(float a, float b) { f32x2 v = {a, b}; return __builtin_bit_cast(u32, __builtin_convertvector(v, bf16x2)); }
DI float rbf(float a) { return bflo(pack2(a, a)); }
DI f32x4 mfma(u32x4 a, u32x4 b, f32x4 c) {
    return __builtin_amdgcn_mfma_f32_16x16x32_bf16(__builtin_bit_cast(bf16x8, a), __builtin_bit_cast(bf16x8, b), c, 0, 0, 0);
}
DI u32x2 ldtr(u32 a) { return __builtin_bit_cast(u32x2, __builtin_amdgcn_ds_read_tr16_b64_v4i16((LAS s16x4*)(size_t)a)); }
DI u32x4 ldtr2(u32 a0, u32 a1) { u32x2 x = ldtr(a0), y = ldtr(a1); return (u32x4){x[0], x[1], y[0], y[1]}; }
DI u32x4 lds128(u32 a) { return *(LAS u32x4*)(size_t)a; }
DI u32x2 lds64(u32 a) { return *(LAS u32x2*)(size_t)a; }
DI f32x4 ldsf4(u32 a) { return *(LAS f32x4*)(size_t)a; }
DI float ldsf(u32 a) { return *(LAS float*)(size_t)a; }
DI void sts128(u32 a, u32x4 v) { *(LAS u32x4*)(size_t)a = v; }
DI void sts64(u32 a, u32x2 v) { *(LAS u32x2*)(size_t)a = v; }
DI void stsf(u32 a, float v) { *(LAS float*)(size_t)a = v; }
DI void stsf4(u32 a, f32x4 v) { *(LAS f32x4*)(size_t)a = v; }
DI void stsf2(u32 a, f32x2 v) { *(LAS f32x2*)(size_t)a = v; }
DI void sts16(u32 a, u16 v) { *(LAS u16*)(size_t)a = v; }

DI void glds16(const void* sbase, u32 voff, u32 ldsdst) {
    asm volatile("s_mov_b32 m0, %2\n\ts_nop 3\n\tglobal_load_lds_dwordx4 %0, %1" :: "v"(voff), "s"(sbase), "s"(ldsdst) : "memory");
}
DI void glds16c(const void* sbase, u32 voff, u32 ldsdst) {
    asm volatile("s_mov_b32 m0, %2\n\ts_nop 3\n\tglobal_load_lds_dwordx4 %0, %1 sc1" :: "v"(voff), "s"(sbase), "s"(ldsdst) : "memory");
}
template <int OFF> DI u32x4 gld16(const void* sbase, u32 voff) {
    u32x4 r; asm volatile("s_nop 4\n\tglobal_load_dwordx4 %0, %1, %2 offset:%3" : "=v"(r) : "v"(voff), "s"(sbase), "i"(OFF) : "memory"); return r;
}
template <int OFF> DI u32x4 gld16c(const void* sbase, u32 voff) {
    u32x4 r; asm volatile("s_nop 4\n\tglobal_load_dwordx4 %0, %1, %2 offset:%3 sc1" : "=v"(r) : "v"(voff), "s"(sbase), "i"(OFF) : "memory"); return r;
}
DI u32 gldu16(const void* sbase, u32 voff) {
    u32 r; asm volatile("s_nop 4\n\tglobal_load_ushort %0, %1, %2" : "=v"(r) : "v"(voff), "s"(sbase) : "memory"); return r;
}
#define VMCNT(n) asm volatile("s_waitcnt vmcnt(" #n ")" ::: "memory")
#define BAR() asm volatile("s_waitcnt lgkmcnt(0)\n\ts_barrier" ::: "memory")
#define RLX_AGENT __ATOMIC_RELAXED, __HIP_MEMORY_SCOPE_AGENT
DI u64 ld_agent(const char* p) { return __hip_atomic_load((GAS u64*)p, RLX_AGENT); }
DI void st_agent(char* p, u64 v) { __hip_atomic_store((GAS u64*)p, v, RLX_AGENT); }
DI void st_agent32(char* p, u32 v) { __hip_atomic_store((GAS u32*)p, v, RLX_AGENT); }
DI float ld_agentf(const char* p) { return __uint_as_float(__hip_atomic_load((GAS u32*)p, RLX_AGENT)); }
DI float xsum4(float v) { v += __shfl_xor(v, 16); v += __shfl_xor(v, 32); return v; }
DI float xmax4(float v) { v = fmaxf(v, __shfl_xor(v, 16)); v = fmaxf(v, __shfl_xor(v, 32)); return v; }
template <int CTRL> DI float dppf(float v) { return __builtin_bit_cast(float, __builtin_amdgcn_update_dpp(0, __builtin_bit_cast(int, v), CTRL, 0xf, 0xf, true)); }

constexpr u32 SPIN_CAP = 1u << 17;
DI void poll_flags(const char* base, int n, int lane, u64 tag, char* ws, u32 code) {
    const char* a = base + (lane < n ? lane : 0) * 8;
    u32 spins = 0;
    for (;;) {
        const u64 v = ld_agent(a);
        if (__all(v == tag)) break;
        if (++spins > SPIN_CAP) { if (lane == 0) st_agent32(ws + WS_ERR, code); break; }
        __builtin_amdgcn_s_sleep(1);
    }
}
DI u32x2 poll_granule(const char* ws, u32 gid, u64 tag, char* wsw, u32 code) {
    u32 spins = 0; u32x4 v;
    const u32 off = WS_MODG + gid * 16;
    for (;;) {
        asm volatile("s_nop 4\n\tglobal_load_dwordx4 %0, %1, %2 sc1\n\ts_waitcnt vmcnt(0)" : "=&v"(v) : "v"(off), "s"(ws) : "memory");
        const bool ok = v[2] == (u32)tag && v[3] == (u32)(tag >> 32);
        if (__all(ok)) break;
        if (++spins > SPIN_CAP) { st_agent32(wsw + WS_ERR, code); break; }
        __builtin_amdgcn_s_sleep(1);
    }
    return (u32x2){v[0], v[1]};
}

#define STAMP(i) do {} while (0)

__global__ __launch_bounds__(512, 2) void mega_kernel(Params p) {
    __shared__ __attribute__((aligned(16))) char ldsmem[163840];
    const u32 L0 = (u32)(uintptr_t)(LAS char*)ldsmem;
    const int tid = threadIdx.x, l = tid & 63;
    const int w = __builtin_amdgcn_readfirstlane(tid >> 6);
    const int b = blockIdx.x, xg = b & 7, sidx = b >> 3;
    const int rb = 4 * (xg >> 1) + (sidx >> 3), j = 8 * (xg & 1) + (sidx & 7);
    const int g = l >> 4, i16 = l & 15, q4 = i16 >> 2, p4 = l & 3;
    char* const ws = p.ws;
    const u64 tag = *(const GAS u64*)(ws + WS_EPOCH) + 1;
    const __amdgpu_buffer_rsrc_t wsr = __builtin_amdgcn_make_buffer_rsrc(ws, 0, WS_END, 0x00020000);
#define ST16C(v, off) __builtin_amdgcn_raw_buffer_store_b128((v), wsr, (off), 0, 16)
#define ST8C(v, off)  __builtin_amdgcn_raw_buffer_store_b64((v), wsr, (off), 0, 16)
    STAMP(0);

    const bool modwg = sidx < 30;
    const int sl = 30 * xg + sidx;
    u32x4 mw[3]; u32 mc[3];
    if (modwg) {
        const int r0 = tid >> 1;
#pragma unroll
        for (int i = 0; i < 3; ++i) {
            const int r = min(r0 + 256 * i, 639);
            mw[i] = gld16<0>(p.wmod, r * 7680 + sl * 32 + (tid & 1) * 16);
            mc[i] = gldu16(p.c, r * 2);
        }
    }
    u32x4 xa[20];
    {
        const u32 voff = (rb * 128 + 16 * w + i16) * 1280 + g * 16;
#define XL(s) xa[s] = gld16<(s) * 64>(p.x, voff);
        XL(0) XL(1) XL(2) XL(3) XL(4) XL(5) XL(6) XL(7) XL(8) XL(9) XL(10) XL(11) XL(12) XL(13) XL(14) XL(15) XL(16) XL(17) XL(18) XL(19)
#undef XL
    }
    if (w < 5) {
        const u32 sg = 64 * w + l, pos = (sg * 6554) >> 16, c = sg - pos * 10, k = pos ^ (((pos >> 3) & 1) << 2);
        const u32 voff = k * 3840 + 80 * j + (c < 5 ? 1280 + 16 * c : 2560 + 16 * (c - 5));
#pragma unroll
        for (int s = 0; s < 20; ++s) glds16((const char*)p.wqkv + s * 122880, voff, L0 + L_WKV + s * 5120 + w * 1024);
    } else {
        const int i0 = 2 * (w - 5);
        u32 voff[2];
#pragma unroll
        for (int e = 0; e < 2; ++e) {
            const u32 sg = 64 * (i0 + e) + l, pos = (sg * 13108) >> 16, c = sg - pos * 5;
            const u32 r = pos & 15, t = r & 1, u = r >> 1, k = (pos & ~15u) + 8 * (u >> 2) + 4 * t + (u & 3);
            voff[e] = k * 3840 + 80 * j + 16 * c;
        }
#pragma unroll
        for (int ds = 0; ds < 10; ++ds) {
            glds16((const char*)p.wqkv + ds * 245760, voff[0], L0 + L_WQ + ds * 5120 + i0 * 1024);
            if (w < 7) glds16((const char*)p.wqkv + ds * 245760, voff[1], L0 + L_WQ + ds * 5120 + (i0 + 1) * 1024);
        }
    }
    if (modwg) {
        asm volatile("s_waitcnt vmcnt(30)" : "+v"(mw[0]), "+v"(mw[1]), "+v"(mw[2]), "+v"(mc[0]), "+v"(mc[1]), "+v"(mc[2]) :: "memory");
        float acc[8];
#pragma unroll
        for (int e = 0; e < 8; ++e) acc[e] = 0.f;
#pragma unroll
        for (int i = 0; i < 3; ++i) {
            const float cv = bflo(mc[i]);
            float sv = rbf(cv * __builtin_amdgcn_rcpf(1.f + __builtin_amdgcn_exp2f(-cv * LOG2E)));
            if ((tid >> 1) + 256 * i > 639) sv = 0.f;
#pragma unroll
            for (int d = 0; d < 4; ++d) { acc[2 * d] += sv * bflo(mw[i][d]); acc[2 * d + 1] += sv * bfhi(mw[i][d]); }
        }
#pragma unroll
        for (int e = 0; e < 8; ++e) {
            float v = acc[e];
            v += dppf<0x122>(v); v += dppf<0x124>(v); v += dppf<0x128>(v);
            acc[e] = xsum4(v);
        }
        if (l < 2) {
            stsf4(L0 + L_MODP + w * 64 + l * 32, (f32x4){acc[0], acc[1], acc[2], acc[3]});
            stsf4(L0 + L_MODP + w * 64 + l * 32 + 16, (f32x4){acc[4], acc[5], acc[6], acc[7]});
        }
        BAR();
        if (w == 0) {
            float v = 0.f;
#pragma unroll
            for (int ww = 0; ww < 8; ++ww) v += ldsf(L0 + L_MODP + ww * 64 + i16 * 4);
            const float v1 = dppf<0x55>(v), v2 = dppf<0xAA>(v), v3 = dppf<0xFF>(v), v0 = dppf<0x00>(v);
            const u32x4 gr = {pack2(v0, v1), pack2(v2, v3), (u32)tag, (u32)(tag >> 32)};
            if (l < 16 && p4 == 0) ST16C(gr, WS_MODG + (sl * 4 + q4) * 16);
        }
    }
    STAMP(1);
    asm volatile("s_waitcnt vmcnt(10)"
                 : "+v"(xa[0]), "+v"(xa[1]), "+v"(xa[2]), "+v"(xa[3]), "+v"(xa[4]), "+v"(xa[5]), "+v"(xa[6]), "+v"(xa[7]), "+v"(xa[8]), "+v"(xa[9]),
                   "+v"(xa[10]), "+v"(xa[11]), "+v"(xa[12]), "+v"(xa[13]), "+v"(xa[14]), "+v"(xa[15]), "+v"(xa[16]), "+v"(xa[17]), "+v"(xa[18]), "+v"(xa[19])
                 :: "memory");
    float rs, nmr;
    {
        float s1 = 0.f, s2 = 0.f;
#pragma unroll
        for (int s = 0; s < 20; ++s)
#pragma unroll
            for (int d = 0; d < 4; ++d) { const float a = bflo(xa[s][d]), c = bfhi(xa[s][d]); s1 += a + c; s2 += a * a + c * c; }
        s1 = xsum4(s1); s2 = xsum4(s2);
        const float mu = s1 * (1.f / 640.f), var = fmaxf(s2 * (1.f / 640.f) - mu * mu, 0.f);
        rs = __builtin_amdgcn_rsqf(var + 1e-5f); nmr = -mu * rs;
    }
#pragma unroll
    for (int s = 0; s < 20; ++s) asm volatile("" : "+v"(xa[s]));
    STAMP(2);
    if (w < 5) {
        const u32x2 gv = poll_granule(ws, tid, tag, ws, 1);
        f32x4 v = {bflo(gv[0]), bfhi(gv[0]), bflo(gv[1]), bfhi(gv[1])};
        if (tid < 160) stsf4(L0 + L_SA + tid * 16, v);
        else { v = (f32x4){rbf(1.f + v[0]), rbf(1.f + v[1]), rbf(1.f + v[2]), rbf(1.f + v[3])}; stsf4(L0 + L_G + (tid - 160) * 16, v); }
    }
    VMCNT(0);
    BAR();
    STAMP(3);

    {
        float gs[20];
#pragma unroll
        for (int s = 0; s < 20; ++s) gs[s] = ldsf(L0 + (i16 < 8 ? L_G : L_SA) + (32 * s + 8 * g + (i16 & 7)) * 4);
        f32x4 acc[5];
#pragma unroll
        for (int n = 0; n < 5; ++n) acc[n] = (f32x4){0.f, 0.f, 0.f, 0.f};
        const u32 b0 = L0 + L_WKV + 160 * (8 * g + 4 * (g & 1) + q4) + 8 * p4;
        const u32 b1 = L0 + L_WKV + 160 * (8 * g + 4 * ((g & 1) ^ 1) + q4) + 8 * p4;
#define HEL(d, e, src) (dppf<0x150 + (e)>(gs[s]) * (src * rs + nmr) + dppf<0x158 + (e)>(gs[s]))
#pragma unroll
        for (int s = 0; s < 20; ++s) {
            u32x4 h;
            h[0] = pack2(HEL(0, 0, bflo(xa[s][0])), HEL(0, 1, bfhi(xa[s][0])));
            h[1] = pack2(HEL(1, 2, bflo(xa[s][1])), HEL(1, 3, bfhi(xa[s][1])));
            h[2] = pack2(HEL(2, 4, bflo(xa[s][2])), HEL(2, 5, bfhi(xa[s][2])));
            h[3] = pack2(HEL(3, 6, bflo(xa[s][3])), HEL(3, 7, bfhi(xa[s][3])));
            xa[s] = h;
#pragma unroll
            for (int n = 0; n < 5; ++n) acc[n] = mfma(ldtr2(b0 + s * 5120 + n * 32, b1 + s * 5120 + n * 32), h, acc[n]);
        }
#undef HEL
        BAR();
        const int row = 256 + 16 * w + i16;
#pragma unroll
        for (int n = 0; n < 5; ++n) {
            const int Q = 4 * n + g;
            const u32x2 v = {pack2(acc[n][0], acc[n][1]), pack2(acc[n][2], acc[n][3])};
            const int wi = Q - 10, hd = wi >= 5 ? 1 : 0;
            const u32 a = Q < 10 ? L_K + row * 80 + 8 * Q : L_V + row * 96 + 48 * hd + 8 * (wi - 5 * hd);
            sts64(L0 + a, v);
        }
        if (g < 2) sts64(L0 + L_V + row * 96 + 48 * g + 40, (u32x2){0x3F80u, 0u});
        if (rb < 2) {
            const int nrow = rb == 0 ? 256 : 128;
            for (int i = tid; i < nrow * 5; i += 512) sts128(L0 + L_K + i * 16, (u32x4){0, 0, 0, 0});
            for (int i = tid; i < nrow * 6; i += 512) sts128(L0 + L_V + i * 16, (u32x4){0, 0, 0, 0});
        }
        BAR();
        if (rb < 15) {
            const u32 kx = WS_KX + (rb * 16 + j) * 10240, vx = WS_VX + (rb * 16 + j) * 12288;
#pragma unroll
            for (int i = 0; i < 3; ++i) {
                const int id = tid + 512 * i;
                if (id < 640) ST16C(lds128(L0 + L_K + 256 * 80 + id * 16), kx + id * 16);
                else if (id < 1408) ST16C(lds128(L0 + L_V + 256 * 96 + (id - 640) * 16), vx + (id - 640) * 16);
            }
        }
    }
    STAMP(4);
    {
        f32x4 acc[3];
#pragma unroll
        for (int n = 0; n < 3; ++n) acc[n] = (f32x4){0.f, 0.f, 0.f, 0.f};
        const u32 bq = L0 + L_WQ + 80 * (16 * (g >> 1) + 8 * (g & 1) + 2 * q4) + 8 * p4;
#pragma unroll
        for (int s = 0; s < 20; ++s)
#pragma unroll
            for (int n = 0; n < 3; ++n) acc[n] = mfma(ldtr2(bq + s * 2560 + n * 32, bq + s * 2560 + n * 32 + 80), xa[s], acc[n]);
#pragma unroll
        for (int n = 0; n < 3; ++n) {
            const int Q = 4 * n + g;
            const u32x2 v = {pack2(acc[n][0] * QSCALE, acc[n][1] * QSCALE), pack2(acc[n][2] * QSCALE, acc[n][3] * QSCALE)};
            if (Q < 10) sts64(L0 + L_Q + (16 * w + i16) * 80 + 8 * Q, v);
        }
    }
    VMCNT(0);
    BAR();
    if (tid == 0) st_agent(ws + WS_FKV + (j * 16 + rb) * 8, tag);
    STAMP(5);

    const int nj = j < 12 ? 7 : 6, Gj = j < 12 ? 7 * j : 84 + 6 * (j - 12);
    const bool wpwg = rb < 2 * nj;
    const int wslot = (nj == 7 || rb < 6) ? rb : rb + 1;
    if (wpwg) {
        const int a = wslot < 6 ? wslot >> 1 : wslot == 6 ? 3 : wslot < 13 ? 4 + ((wslot - 7) >> 1) : 3;
        const int up = wslot < 6 ? wslot & 1 : wslot == 6 ? 0 : wslot < 13 ? (wslot - 7) & 1 : 1;
        const int gam = Gj + (a <= 3 || nj == 7 ? a : a - 1);
        const char* src = (const char*)(up ? p.wu : p.wg) + gam * 32;
        const u32 voff = (l >> 1) * 3456 + (l & 1) * 16;
        glds16(src + w * 32 * 3456, voff, L0 + L_WPRAW + w * 1024);
        glds16(src + (w + 8) * 32 * 3456, voff, L0 + L_WPRAW + (w + 8) * 1024);
        if (w < 4) glds16(src + (w + 16) * 32 * 3456, voff, L0 + L_WPRAW + (w + 16) * 1024);
        if (w < 5) {
            const u32x2 gv = poll_granule(ws, 480 + tid, tag, ws, 2);
            if (tid < 160) sts64(L0 + L_SB + tid * 8, gv);
            else sts64(L0 + L_G2 + (tid - 160) * 8, (u32x2){pack2(1.f + bflo(gv[0]), 1.f + bfhi(gv[0])), pack2(1.f + bflo(gv[1]), 1.f + bfhi(gv[1]))});
        }
    }
    if (w == 0 && rb >= 1) poll_flags(ws + WS_FKV + (j * 16 + (rb >= 2 ? rb - 2 : rb - 1)) * 8, rb >= 2 ? 2 : 1, l, tag, ws, 3);
    VMCNT(0);
    BAR();
    STAMP(6);
    if (rb >= 1) {
#pragma unroll
        for (int i = 0; i < 6; ++i) {
            const int pc = w + 8 * i;
            const int blk = pc >= 22 ? 1 : 0, pi = pc - 22 * blk;
            if (pc < 44 && (blk == 0 || rb >= 2)) {
                const int srb = rb - 1 - blk;
                if (pi < 10) glds16c(ws + WS_KX + (srb * 16 + j) * 10240 + pi * 1024, l * 16, L0 + L_K + (1 - blk) * 10240 + pi * 1024);
                else glds16c(ws + WS_VX + (srb * 16 + j) * 12288 + (pi - 10) * 1024, l * 16, L0 + L_V + (1 - blk) * 12288 + (pi - 10) * 1024);
            }
        }
    }
    if (wpwg) {
        float us = 0.f, vs = 0.f;
#pragma unroll
        for (int i = 0; i < 3; ++i) {
            const int s = w + 8 * i;
            if (s < 20) {
                const u32 a = L0 + L_WPRAW + (32 * s + 8 * g + q4) * 32 + 8 * p4;
                const u32x4 wr = ldtr2(a, a + 128);
                const u32x4 g2 = lds128(L0 + L_G2 + (32 * s + 8 * g) * 2), sb = lds128(L0 + L_SB + (32 * s + 8 * g) * 2);
                u32x4 o;
#pragma unroll
                for (int d = 0; d < 4; ++d) {
                    const float w0 = bflo(wr[d]), w1 = bfhi(wr[d]);
                    o[d] = pack2(w0 * bflo(g2[d]), w1 * bfhi(g2[d]));
                    us += bflo(o[d]) + bfhi(o[d]);
                    vs += w0 * bflo(sb[d]) + w1 * bfhi(sb[d]);
                }
                ST16C(o, WS_WP + ((j * 20 + s) * 14 + wslot) * 1024 + l * 16);
            }
        }
        us = xsum4(us); vs = xsum4(vs);
        if (l < 16) { stsf(L0 + L_UVP + w * 128 + l * 4, us); stsf(L0 + L_UVP + w * 128 + 64 + l * 4, vs); }
    }
    VMCNT(0);
    BAR();
    if (wpwg && tid < 32) {
        float v = 0.f;
#pragma unroll
        for (int ww = 0; ww < 8; ++ww) v += ldsf(L0 + L_UVP + ww * 128 + tid * 4);
        st_agent32(ws + WS_UV + (j * 14 + wslot) * 128 + tid * 4, __float_as_uint(v));
    }
    const int hf = j & 1, cb = j >> 1;
    u32 wvoff[5];
#pragma unroll
    for (int i = 0; i < 5; ++i) {
        const u32 sg = 64 * i + l, pos = (sg * 6554) >> 16, c = sg - pos * 10, k = pos ^ (((pos >> 3) & 1) << 2);
        wvoff[i] = k * 1280 + c * 16;
    }
    {
        const char* src = (const char*)p.wo + cb * 160;
#pragma unroll
        for (int e = 0; e < 2; ++e) {
            const int s = w + 8 * e;
            if (s < 10) {
#pragma unroll
                for (int i = 0; i < 5; ++i) glds16(src + (10 + s) * 32 * 1280, wvoff[i], L0 + L_WO1 + s * 5120 + i * 1024);
            }
        }
    }
    STAMP(7);

    {
        const int hd = w & 1, qi = w >> 1;
        const int fv = rb >= 2 ? 0 : (rb == 1 ? 128 : 256);
#pragma unroll 1
        for (int qa = 0; qa < 2; ++qa) {
            const int kr0 = 32 * qi + 16 * qa, qrow = kr0 + i16;
            const u32x2 q0 = lds64(L0 + L_Q + qrow * 80 + hd * 40 + 8 * g);
            u32x2 q1 = lds64(L0 + L_Q + qrow * 80 + hd * 40 + 32);
            if (g != 0) q1 = (u32x2){0, 0};
            const u32x4 qf = {q0[0], q0[1], q1[0], q1[1]};
            const u32 kb = L0 + L_K + (kr0 + i16) * 80 + hd * 40;
            f32x4 sc[17];
#pragma unroll
            for (int t = 0; t < 17; ++t) {
                const u32x2 k0 = lds64(kb + t * 1280 + 8 * g), k1 = lds64(kb + t * 1280 + 32);
                sc[t] = mfma((u32x4){k0[0], k0[1], k1[0], k1[1]}, qf, (f32x4){0.f, 0.f, 0.f, 0.f});
            }
#pragma unroll
            for (int r = 0; r < 4; ++r) {
                if (4 * g + r <= i16) sc[0][r] = -INFINITY;
                if (4 * g + r > i16) sc[16][r] = -INFINITY;
            }
            if (fv > 0) {
#pragma unroll
                for (int t = 0; t < 16; ++t)
                    if (kr0 + 16 * t < fv) sc[t] = (f32x4){-INFINITY, -INFINITY, -INFINITY, -INFINITY};
            }
            float m = -INFINITY;
#pragma unroll
            for (int t = 0; t < 17; ++t) m = fmaxf(fmaxf(m, fmaxf(sc[t][0], sc[t][1])), fmaxf(sc[t][2], sc[t][3]));
            m = xmax4(m);
            f32x4 o0 = {0.f, 0.f, 0.f, 0.f}, o1 = {0.f, 0.f, 0.f, 0.f};
            const u32 vb = L0 + L_V + (kr0 + 4 * g + q4) * 96 + hd * 48 + 8 * p4;
#pragma unroll
            for (int u = 0; u < 9; ++u) {
                u32x4 pf;
                pf[0] = pack2(__builtin_amdgcn_exp2f(sc[2 * u][0] - m), __builtin_amdgcn_exp2f(sc[2 * u][1] - m));
                pf[1] = pack2(__builtin_amdgcn_exp2f(sc[2 * u][2] - m), __builtin_amdgcn_exp2f(sc[2 * u][3] - m));
                if (u < 8) {
                    pf[2] = pack2(__builtin_amdgcn_exp2f(sc[2 * u + 1][0] - m), __builtin_amdgcn_exp2f(sc[2 * u + 1][1] - m));
                    pf[3] = pack2(__builtin_amdgcn_exp2f(sc[2 * u + 1][2] - m), __builtin_amdgcn_exp2f(sc[2 * u + 1][3] - m));
                } else { pf[2] = 0; pf[3] = 0; }
                const u32 a0 = vb + u * 32 * 96, a1 = u < 8 ? a0 + 16 * 96 : a0;
                o0 = mfma(ldtr2(a0, a1), pf, o0);
                o1 = mfma(ldtr2(a0 + 32, a1 + 32), pf, o1);
            }
            const float inv = __builtin_amdgcn_rcpf(__shfl(o1[0], i16 + 16));
            sts64(L0 + L_OI + qrow * 80 + hd * 40 + 8 * g, (u32x2){pack2(o0[0] * inv, o0[1] * inv), pack2(o0[2] * inv, o0[3] * inv)});
            if (g == 0) sts64(L0 + L_OI + qrow * 80 + hd * 40 + 32, (u32x2){pack2(o1[0] * inv, o1[1] * inv), pack2(o1[2] * inv, o1[3] * inv)});
        }
    }
    BAR();
    {
#pragma unroll
        for (int i = 0; i < 2; ++i) {
            const int id = tid + 512 * i;
            if (id < 640) {
                const int c = id >> 7, tok = id & 127;
                ST16C(lds128(L0 + L_OI + tok * 80 + c * 16), WS_O + (((rb * 8 + (tok >> 4)) * 80 + 5 * j + c) * 16 + (tok & 15)) * 16);
            }
        }
    }
    VMCNT(0);
    BAR();
    if (tid == 0) {
        st_agent(ws + WS_FO + (rb * 16 + j) * 8, tag);
        if (wpwg) st_agent(ws + WS_FWP + (j * 16 + rb) * 8, tag);
    }
    STAMP(8);

    const int wm = w & 3, wk = w >> 2;
    const int mt3 = rb * 8 + 4 * hf + wm;
    const int nt0 = wk == 0 ? 0 : 3, ntn = wk == 0 ? 3 : 2;
    u32x2 x1k[3];
    {
        {
            const char* src = (const char*)p.wo + cb * 160;
#pragma unroll
            for (int e = 0; e < 2; ++e) {
                const int s = w + 8 * e;
                if (s < 10) {
#pragma unroll
                    for (int i = 0; i < 5; ++i) glds16(src + s * 32 * 1280, wvoff[i], L0 + L_WO0 + s * 5120 + i * 1024);
                }
            }
        }
        u32x2 xr[3];
#pragma unroll
        for (int n = 0; n < 3; ++n) {
            const int nt = min(nt0 + n, 4);
            xr[n] = *(const GAS u32x2*)(p.x + (mt3 * 16 + i16) * 640 + 80 * cb + 16 * nt + 4 * g);
        }
        if (w == 1 && l < 20) {
            const u32x2 gv = poll_granule(ws, 320 + 20 * cb + l, tag, ws, 4);
            stsf4(L0 + L_GA + l * 16, (f32x4){bflo(gv[0]), bfhi(gv[0]), bflo(gv[1]), bfhi(gv[1])});
        }
        if (w == 0) {
            poll_flags(ws + WS_FO + rb * 128, 16, l, tag, ws, 5);
            poll_flags(ws + WS_FWP + j * 128, 2 * nj, l, tag, ws, 6);
        }
        VMCNT(0);
        BAR();
        STAMP(9);
        u32x4 a[10];
        {
            const char* src = ws + WS_O + (mt3 * 80 + 40 * wk) * 256;
#define AL(s) a[s] = gld16c<((s) & 3) * 1024>(src + ((s) >> 2) * 4096, l * 16);
            AL(0) AL(1) AL(2) AL(3) AL(4) AL(5) AL(6) AL(7) AL(8) AL(9)
#undef AL
        }
        asm volatile("s_waitcnt vmcnt(0)" : "+v"(a[0]), "+v"(a[1]), "+v"(a[2]), "+v"(a[3]), "+v"(a[4]), "+v"(a[5]), "+v"(a[6]), "+v"(a[7]), "+v"(a[8]), "+v"(a[9]) :: "memory");
        f32x4 acc[5];
#pragma unroll
        for (int n = 0; n < 5; ++n) acc[n] = (f32x4){0.f, 0.f, 0.f, 0.f};
        const u32 wb = L0 + (wk == 0 ? L_WO0 : L_WO1);
        const u32 b0 = wb + 160 * (8 * g + 4 * (g & 1) + q4) + 8 * p4, b1 = wb + 160 * (8 * g + 4 * ((g & 1) ^ 1) + q4) + 8 * p4;
#pragma unroll
        for (int s = 0; s < 10; ++s)
#pragma unroll
            for (int n = 0; n < 5; ++n) acc[n] = mfma(ldtr2(b0 + s * 5120 + n * 32, b1 + s * 5120 + n * 32), a[s], acc[n]);
        if (wk == 0) { stsf4(L0 + L_RED3 + (wm * 5 + 3) * 1024 + l * 16, acc[3]); stsf4(L0 + L_RED3 + (wm * 5 + 4) * 1024 + l * 16, acc[4]); }
        else { stsf4(L0 + L_RED3 + (wm * 5 + 0) * 1024 + l * 16, acc[0]); stsf4(L0 + L_RED3 + (wm * 5 + 1) * 1024 + l * 16, acc[1]); stsf4(L0 + L_RED3 + (wm * 5 + 2) * 1024 + l * 16, acc[2]); }
        BAR();
        float s1 = 0.f, s2 = 0.f;
#pragma unroll
        for (int n = 0; n < 3; ++n) {
            if (n < ntn) {
                const int nt = nt0 + n;
                const f32x4 mine = wk == 0 ? acc[n] : acc[n < 2 ? 3 + n : 4];
                const f32x4 v = mine + ldsf4(L0 + L_RED3 + (wm * 5 + nt) * 1024 + l * 16);
                const f32x4 ga = ldsf4(L0 + L_GA + (16 * nt + 4 * g) * 4);
                const u32x2 o = {pack2(bflo(xr[n][0]) + ga[0] * v[0], bfhi(xr[n][0]) + ga[1] * v[1]), pack2(bflo(xr[n][1]) + ga[2] * v[2], bfhi(xr[n][1]) + ga[3] * v[3])};
                x1k[n] = o;
                const float e0 = bflo(o[0]), e1 = bfhi(o[0]), e2 = bflo(o[1]), e3 = bfhi(o[1]);
                s1 += (e0 + e1) + (e2 + e3); s2 += (e0 * e0 + e1 * e1) + (e2 * e2 + e3 * e3);
                sts64(L0 + L_X1S + (16 * wm + i16) * 160 + 32 * nt + 8 * g, o);
            } else x1k[n] = (u32x2){0, 0};
        }
        s1 = xsum4(s1); s2 = xsum4(s2);
        if (l < 16) stsf2(L0 + L_ST3 + (wk * 64 + 16 * wm + l) * 8, (f32x2){s1, s2});
        BAR();
#pragma unroll
        for (int i = 0; i < 2; ++i) {
            const int id = tid + 512 * i;
            if (id < 640) {
                const int c = id >> 6, r = id & 63;
                ST16C(lds128(L0 + L_X1S + r * 160 + c * 16), WS_X1 + (((rb * 8 + 4 * hf + (r >> 4)) * 80 + 10 * cb + c) * 16 + (r & 15)) * 16);
            }
        }
        if (tid < 64) {
            const f32x2 u = *(LAS f32x2*)(size_t)(L0 + L_ST3 + tid * 8), v = *(LAS f32x2*)(size_t)(L0 + L_ST3 + (64 + tid) * 8);
            const f32x2 sum = u + v;
            ST8C(__builtin_bit_cast(u32x2, sum), WS_STAT + ((rb * 8 + cb) * 128 + 64 * hf + tid) * 8);
        }
        VMCNT(0);
        BAR();
        if (tid == 0) st_agent(ws + WS_FX1 + (rb * 16 + j) * 8, tag);
    }
    STAMP(10);

    {
        const int wn = w >> 2;
        const char* wpj = ws + WS_WP + j * 20 * 14 * 1024;
        const char* x1b = ws + WS_X1 + rb * 8 * 80 * 256;
#define P4_B(t) do { const u32 dst = L0 + L_R4 + ((t) % 3) * R4_STAGE + 16384; \
            glds16c(wpj + ((t) * 28 + w) * 1024, l * 16, dst + w * 1024); glds16c(wpj + ((t) * 28 + w + 8) * 1024, l * 16, dst + (w + 8) * 1024); \
            glds16c(wpj + ((t) * 28 + w + 16) * 1024, l * 16, dst + (w + 16) * 1024); \
            if (w < 4) glds16c(wpj + ((t) * 28 + w + 24) * 1024, l * 16, dst + (w + 24) * 1024); } while (0)
#define P4_A(t) do { const u32 dst = L0 + L_R4 + ((t) % 3) * R4_STAGE; \
            glds16c(x1b + (w * 80 + 8 * (t)) * 256, l * 16, dst + w * 1024); glds16c(x1b + (w * 80 + 8 * (t) + 4) * 256, l * 16, dst + (w + 8) * 1024); } while (0)
        P4_B(0); P4_B(1); P4_B(2);
        if (w == 0) poll_flags(ws + WS_FX1 + rb * 128, 16, l, tag, ws, 7);
        BAR();
        STAMP(11);
        if (tid < 128) {
            float s1 = 0.f, s2 = 0.f;
#pragma unroll
            for (int c = 0; c < 8; ++c) { const u64 v = ld_agent(ws + WS_STAT + ((rb * 8 + c) * 128 + tid) * 8); s1 += __uint_as_float((u32)v); s2 += __uint_as_float((u32)(v >> 32)); }
            const float mu = s1 * (1.f / 640.f), var = fmaxf(s2 * (1.f / 640.f) - mu * mu, 0.f), r = __builtin_amdgcn_rsqf(var + 1e-5f);
            stsf2(L0 + L_MR + tid * 8, (f32x2){r, -mu * r});
        } else if (tid < 128 + 448) {
            if (tid - 128 < 384) stsf(L0 + L_UV + (tid - 128) * 4, ld_agentf(ws + WS_UV + j * 14 * 128 + (tid - 128) * 4));
        }
        if (tid < 64) stsf(L0 + L_UV + (384 + tid) * 4, ld_agentf(ws + WS_UV + j * 14 * 128 + (384 + tid) * 4));
        P4_A(0); P4_A(1); P4_A(2);
        f32x4 acc[7][2];
#pragma unroll
        for (int n = 0; n < 7; ++n) { acc[n][0] = (f32x4){0.f, 0.f, 0.f, 0.f}; acc[n][1] = (f32x4){0.f, 0.f, 0.f, 0.f}; }
#pragma unroll
        for (int t = 0; t < 10; ++t) {
            if (t == 0) VMCNT(4);
            else if (t == 1) VMCNT(2);
            else if (t == 9) VMCNT(0);
            else { if (w < 4) VMCNT(6); else VMCNT(5); }
            BAR();
            if (t >= 1 && t + 2 < 10) { P4_B(t + 2); P4_A(t + 2); }
            const u32 st = L0 + L_R4 + (t % 3) * R4_STAGE;
#pragma unroll
            for (int ks = 0; ks < 2; ++ks) {
                const u32x4 a0 = lds128(st + (ks * 8 + 2 * wm) * 1024 + l * 16), a1 = lds128(st + (ks * 8 + 2 * wm + 1) * 1024 + l * 16);
#pragma unroll
                for (int n = 0; n < 7; ++n) {
                    const u32x4 bf = lds128(st + 16384 + (ks * 14 + 7 * wn + n) * 1024 + l * 16);
                    acc[n][0] = mfma(bf, a0, acc[n][0]);
                    acc[n][1] = mfma(bf, a1, acc[n][1]);
                }
            }
        }
#undef P4_B
#undef P4_A
        STAMP(12);
        f32x2 mr[2];
#pragma unroll
        for (int m = 0; m < 2; ++m) mr[m] = *(LAS f32x2*)(size_t)(L0 + L_MR + (32 * wm + 16 * m + i16) * 8);
#pragma unroll
        for (int n = 0; n < 7; ++n) {
            const f32x4 u = ldsf4(L0 + L_UV + (7 * wn + n) * 128 + 16 * g), v = ldsf4(L0 + L_UV + (7 * wn + n) * 128 + 64 + 16 * g);
#pragma unroll
            for (int m = 0; m < 2; ++m) acc[n][m] = acc[n][m] * mr[m][0] + (u * mr[m][1] + v);
        }
        if (nj == 7) stsf4(L0 + L_XCH + w * 1024 + l * 16, wn == 0 ? acc[6][1] : acc[6][0]);
#define SILU_MUL(gv, uv) ((gv) * __builtin_amdgcn_rcpf(1.f + __builtin_amdgcn_exp2f(-(gv) * LOG2E)) * (uv))
#pragma unroll
        for (int a = 0; a < 3; ++a) {
            const int la = a + 4 * wn, gam = Gj + (la <= 3 || nj == 7 ? la : la - 1);
            u32x2 o[2];
#pragma unroll
            for (int m = 0; m < 2; ++m) {
                const f32x4 gt = acc[2 * a][m], up = acc[2 * a + 1][m];
                o[m] = (u32x2){pack2(SILU_MUL(gt[0], up[0]), SILU_MUL(gt[1], up[1])), pack2(SILU_MUL(gt[2], up[2]), SILU_MUL(gt[3], up[3]))};
            }
            const auto s0 = __builtin_amdgcn_permlane16_swap(o[0][0], o[1][0], false, false);
            const auto s1 = __builtin_amdgcn_permlane16_swap(o[0][1], o[1][1], false, false);
            const u32x4 v = {s0[0], s1[0], s0[1], s1[1]};
            ST16C(v, WS_ACT + (((rb * 8 + 2 * wm + (g & 1)) * 216 + 2 * gam + (g >> 1)) * 16 + i16) * 16);
        }
        if (nj == 7) {
            BAR();
            const f32x4 other = ldsf4(L0 + L_XCH + (w ^ 4) * 1024 + l * 16);
            const f32x4 gt = wn == 0 ? acc[6][0] : other, up = wn == 0 ? other : acc[6][1];
            const u32x2 o = {pack2(SILU_MUL(gt[0], up[0]), SILU_MUL(gt[1], up[1])), pack2(SILU_MUL(gt[2], up[2]), SILU_MUL(gt[3], up[3]))};
            ST8C(o, WS_ACT + (((rb * 8 + 2 * wm + wn) * 216 + 2 * (Gj + 3) + (g >> 1)) * 16 + i16) * 16 + 8 * (g & 1));
        }
#undef SILU_MUL
        VMCNT(0);
        BAR();
        if (tid == 0) st_agent(ws + WS_FACT + (rb * 16 + j) * 8, tag);
    }
    STAMP(13);

    {
        const char* wsrc = (const char*)p.wd + cb * 160 + wk * 27 * 32 * 1280;
#define P5_B(t) do { const int ksl = min(4 * (t) + wm, 26); const u32 dst = L0 + L_R5 + ((t) % 3) * R5_STAGE + wk * 20480 + wm * 5120; \
            _Pragma("unroll") for (int i = 0; i < 5; ++i) glds16(wsrc + ksl * 32 * 1280, wvoff[i], dst + i * 1024); } while (0)
        P5_B(0); P5_B(1); P5_B(2);
        if (w == 1 && l < 20) {
            const u32x2 gv = poll_granule(ws, 800 + 20 * cb + l, tag, ws, 8);
            stsf4(L0 + L_GB + l * 16, (f32x4){bflo(gv[0]), bfhi(gv[0]), bflo(gv[1]), bfhi(gv[1])});
        }
        if (w == 0) poll_flags(ws + WS_FACT + rb * 128, 16, l, tag, ws, 9);
        BAR();
        STAMP(14);
        const char* asrc = ws + WS_ACT + (mt3 * 216 + wk * 108) * 256;
        u32x4 a[3][4];
#define P5_A(t) do { a[(t) % 3][0] = gld16c<0>(asrc + (t) * 4096, l * 16); a[(t) % 3][1] = gld16c<1024>(asrc + (t) * 4096, l * 16); \
            a[(t) % 3][2] = gld16c<2048>(asrc + (t) * 4096, l * 16); if ((t) < 6) a[(t) % 3][3] = gld16c<3072>(asrc + (t) * 4096, l * 16); } while (0)
        P5_A(0); P5_A(1); P5_A(2);
        f32x4 acc[5];
#pragma unroll
        for (int n = 0; n < 5; ++n) acc[n] = (f32x4){0.f, 0.f, 0.f, 0.f};
        const u32 o0 = 160 * (8 * g + 4 * (g & 1) + q4) + 8 * p4, o1 = 160 * (8 * g + 4 * ((g & 1) ^ 1) + q4) + 8 * p4;
#define P5_WAIT(n, t) asm volatile("s_waitcnt vmcnt(" #n ")" : "+v"(a[(t) % 3][0]), "+v"(a[(t) % 3][1]), "+v"(a[(t) % 3][2]), "+v"(a[(t) % 3][3]) :: "memory")
#define P5_STEP(t, n) do { P5_WAIT(n, t); BAR(); \
            if ((t) >= 1 && (t) + 2 < 7) { P5_B((t) + 2); P5_A((t) + 2); } \
            const u32 st = L0 + L_R5 + ((t) % 3) * R5_STAGE + wk * 20480; \
            _Pragma("unroll") for (int kk = 0; kk < ((t) < 6 ? 4 : 3); ++kk) \
                _Pragma("unroll") for (int n_ = 0; n_ < 5; ++n_) acc[n_] = mfma(ldtr2(st + kk * 5120 + n_ * 32 + o0, st + kk * 5120 + n_ * 32 + o1), a[(t) % 3][kk], acc[n_]); } while (0)
        P5_STEP(0, 8); P5_STEP(1, 4); P5_STEP(2, 9); P5_STEP(3, 9); P5_STEP(4, 9); P5_STEP(5, 8); P5_STEP(6, 0);
#undef P5_B
#undef P5_A
#undef P5_WAIT
#undef P5_STEP
        STAMP(15);
        if (wk == 0) { stsf4(L0 + L_RED5 + (wm * 5 + 3) * 1024 + l * 16, acc[3]); stsf4(L0 + L_RED5 + (wm * 5 + 4) * 1024 + l * 16, acc[4]); }
        else { stsf4(L0 + L_RED5 + (wm * 5 + 0) * 1024 + l * 16, acc[0]); stsf4(L0 + L_RED5 + (wm * 5 + 1) * 1024 + l * 16, acc[1]); stsf4(L0 + L_RED5 + (wm * 5 + 2) * 1024 + l * 16, acc[2]); }
        BAR();
#pragma unroll
        for (int n = 0; n < 3; ++n) {
            if (n < ntn) {
                const int nt = nt0 + n;
                const f32x4 mine = wk == 0 ? acc[n] : acc[n < 2 ? 3 + n : 4];
                const f32x4 v = mine + ldsf4(L0 + L_RED5 + (wm * 5 + nt) * 1024 + l * 16);
                const f32x4 gb = ldsf4(L0 + L_GB + (16 * nt + 4 * g) * 4);
                const u32x2 o = {pack2(bflo(x1k[n][0]) + gb[0] * v[0], bfhi(x1k[n][0]) + gb[1] * v[1]), pack2(bflo(x1k[n][1]) + gb[2] * v[2], bfhi(x1k[n][1]) + gb[3] * v[3])};
                *(GAS u32x2*)(p.out + (mt3 * 16 + i16) * 640 + 80 * cb + 16 * nt + 4 * g) = o;
            }
        }
    }
    STAMP(16);
    if (rb == 15 && j == 0 && w < 4) {
        u32 spins = 0;
        for (;;) {
            const u64 v = ld_agent(ws + WS_FO + tid * 8);
            if (__all(v == tag)) break;
            if (++spins > SPIN_CAP) { st_agent32(ws + WS_ERR, 10); break; }
            __builtin_amdgcn_s_sleep(1);
        }
    }
    if (rb == 15 && j == 0) {
        BAR();
        if (tid == 0) st_agent(ws + WS_EPOCH, tag);
    }
}

extern "C" void kernel_launch(void* const* d_in, const int* in_sizes, int n_in, void* d_out, int out_size, void* d_ws, size_t ws_size, hipStream_t stream) {
    Params p;
    p.x = (const u16*)d_in[0]; p.wqkv = (const u16*)d_in[1]; p.wo = (const u16*)d_in[2];
    p.wg = (const u16*)d_in[4]; p.wu = (const u16*)d_in[5]; p.wd = (const u16*)d_in[6];
    p.c = (const u16*)d_in[7]; p.wmod = (const u16*)d_in[8];
    p.out = (u16*)d_out; p.ws = (char*)d_ws;
    mega_kernel<<<256, 512, 0, stream>>>(p);
}
```

```cpp
#include <hip/hip_runtime.h>
#include <hip/hip_bf16.h>
#include <stdint.h>
#include <type_traits>

typedef unsigned short u16;
typedef unsigned u32;
typedef unsigned long long u64;
typedef __attribute__((ext_vector_type(2))) unsigned u32x2;
typedef __attribute__((ext_vector_type(4))) unsigned u32x4;
typedef __attribute__((ext_vector_type(2))) float f32x2;
typedef __attribute__((ext_vector_type(4))) float f32x4;
typedef __attribute__((ext_vector_type(2))) __bf16 bf16x2;
typedef __attribute__((ext_vector_type(8))) __bf16 bf16x8;
typedef __attribute__((ext_vector_type(4))) short s16x4;
#define LAS __attribute__((address_space(3)))
#define GAS __attribute__((address_space(1)))
#define DI __device__ __forceinline__

constexpr int SEQ = 2048, DM = 640, FF = 1728;
constexpr float QSCALE = 0.22360679774997896f * 1.4426950408889634f;
constexpr float LOG2E = 1.4426950408889634f;

constexpr u32 WS_EPOCH = 0;
constexpr u32 WS_ERR   = 256;
constexpr u32 WS_FKV   = 4096;
constexpr u32 WS_FO    = WS_FKV + 2048;
constexpr u32 WS_FX1   = WS_FO + 2048;
constexpr u32 WS_FACT  = WS_FX1 + 2048;
constexpr u32 WS_FWP   = WS_FACT + 2048;
constexpr u32 WS_MODG  = 16384;
constexpr u32 WS_UV    = 32768;
constexpr u32 WS_FH    = 61440;
constexpr u32 WS_FWD   = 63488;
constexpr u32 WS_STAT  = 65536;
constexpr u32 WS_KV    = 262144;
constexpr u32 WS_O     = WS_KV + 256 * 22528;
constexpr u32 WS_X1    = WS_O + SEQ * DM * 2;
constexpr u32 WS_ACT   = WS_X1 + SEQ * DM * 2;
constexpr u32 WS_WP    = WS_ACT + SEQ * FF * 2;
constexpr u32 WS_H     = WS_WP + 16 * 20 * 14 * 1024;
constexpr u32 WS_WD    = WS_H + SEQ * DM * 2;
constexpr u32 WS_STAMP = WS_WD + 8 * 54 * 5 * 1024;
constexpr u32 WS_END   = WS_STAMP + 2 * 256 * 32 * 8;

constexpr u32 L_WKV = 0, L_WQ = 102400, L_GS = 153600, L_STP = 156160, L_MODP = 156416, L_CNT = 156672, L_SB = 157696, L_G2 = 158976;
constexpr u32 L_K = 0, L_V = 30720, L_Q = 67584, L_WPRAW = 77824, L_UVP = 100864;
constexpr u32 L_WO0 = 0, L_WO1 = 112640, L_RED3 = 51200, L_X1S = 71680, L_XR = 81920, L_WDRAW = 92160, L_GA = 102400, L_ST3 = 102912;
constexpr u32 L_R4 = 0, R4_STAGE = 45056, L_ST4 = 135168, L_UV = 143360, L_MR = 145408, L_XCH = 146432;
constexpr u32 L_R5 = 0, R5_STAGE = 40960, L_RED5 = 122880, L_GB = 143360, L_OS = 144384;

struct Params {
    const u16* x; const u16* wqkv; const u16* wo; const u16* wg; const u16* wu; const u16* wd;
    const u16* c; const u16* wmod; u16* out; char* ws;
};

DI float bflo(u32 d) { return __uint_as_float(d << 16); }
DI float bfhi(u32 d) { return __uint_as_float(d & 0xffff0000u); }
DI u32 pack2(float a, float b) { f32x2 v = {a, b}; return __builtin_bit_cast(u32, __builtin_convertvector(v, bf16x2)); }
DI float rbf(float a) { return bflo(pack2(a, a)); }
DI f32x4 mfma(u32x4 a, u32x4 b, f32x4 c) {
    return __builtin_amdgcn_mfma_f32_16x16x32_bf16(__builtin_bit_cast(bf16x8, a), __builtin_bit_cast(bf16x8, b), c, 0, 0, 0);
}
DI u32x2 ldtr(u32 a) { return __builtin_bit_cast(u32x2, __builtin_amdgcn_ds_read_tr16_b64_v4i16((LAS s16x4*)(size_t)a)); }
DI u32x4 ldtr2(u32 a0, u32 a1) { u32x2 x = ldtr(a0), y = ldtr(a1); return (u32x4){x[0], x[1], y[0], y[1]}; }
DI u32x4 lds128(u32 a) { return *(LAS u32x4*)(size_t)a; }
DI u32x2 lds64(u32 a) { return *(LAS u32x2*)(size_t)a; }
DI f32x4 ldsf4(u32 a) { return *(LAS f32x4*)(size_t)a; }
DI float ldsf(u32 a) { return *(LAS float*)(size_t)a; }
DI void sts128(u32 a, u32x4 v) { *(LAS u32x4*)(size_t)a = v; }
DI void sts64(u32 a, u32x2 v) { *(LAS u32x2*)(size_t)a = v; }
DI void stsf(u32 a, float v) { *(LAS float*)(size_t)a = v; }
DI void stsf4(u32 a, f32x4 v) { *(LAS f32x4*)(size_t)a = v; }
DI void stsf2(u32 a, f32x2 v) { *(LAS f32x2*)(size_t)a = v; }
DI void sts16(u32 a, u16 v) { *(LAS u16*)(size_t)a = v; }

DI void glds16(const void* sbase, u32 voff, u32 ldsdst) {
    asm volatile("s_mov_b32 m0, %2\n\ts_nop 3\n\tglobal_load_lds_dwordx4 %0, %1" :: "v"(voff), "s"(sbase), "s"(ldsdst) : "memory");
}
DI void glds16c(const void* sbase, u32 voff, u32 ldsdst) {
    asm volatile("s_mov_b32 m0, %2\n\ts_nop 3\n\tglobal_load_lds_dwordx4 %0, %1 sc1" :: "v"(voff), "s"(sbase), "s"(ldsdst) : "memory");
}
template <int OFF> DI u32x4 gld16(const void* sbase, u32 voff) {
    u32x4 r; asm volatile("s_nop 4\n\tglobal_load_dwordx4 %0, %1, %2 offset:%3" : "=v"(r) : "v"(voff), "s"(sbase), "i"(OFF) : "memory"); return r;
}
template <int OFF> DI u32x4 gld16c(const void* sbase, u32 voff) {
    u32x4 r; asm volatile("s_nop 4\n\tglobal_load_dwordx4 %0, %1, %2 offset:%3 sc1" : "=v"(r) : "v"(voff), "s"(sbase), "i"(OFF) : "memory"); return r;
}
DI u32 gldu16(const void* sbase, u32 voff) {
    u32 r; asm volatile("s_nop 4\n\tglobal_load_ushort %0, %1, %2" : "=v"(r) : "v"(voff), "s"(sbase) : "memory"); return r;
}
#define VMCNT(n) asm volatile("s_waitcnt vmcnt(" #n ")" ::: "memory")
#define BAR() asm volatile("s_waitcnt lgkmcnt(0)\n\ts_barrier" ::: "memory")
#define RLX_AGENT __ATOMIC_RELAXED, __HIP_MEMORY_SCOPE_AGENT
DI u64 ld_agent(const char* p) { return __hip_atomic_load((GAS u64*)p, RLX_AGENT); }
DI void st_agent(char* p, u64 v) { __hip_atomic_store((GAS u64*)p, v, RLX_AGENT); }
DI void st_agent32(char* p, u32 v) { __hip_atomic_store((GAS u32*)p, v, RLX_AGENT); }
DI float ld_agentf(const char* p) { return __uint_as_float(__hip_atomic_load((GAS u32*)p, RLX_AGENT)); }
DI float xsum4(float x) {
    const auto s = __builtin_amdgcn_permlane16_swap(__float_as_uint(x), __float_as_uint(x), false, false);
    x = __uint_as_float(s[0]) + __uint_as_float(s[1]);
    const auto t = __builtin_amdgcn_permlane32_swap(__float_as_uint(x), __float_as_uint(x), false, false);
    return __uint_as_float(t[0]) + __uint_as_float(t[1]);
}
DI float xmax4(float x) {
    const auto s = __builtin_amdgcn_permlane16_swap(__float_as_uint(x), __float_as_uint(x), false, false);
    x = fmaxf(__uint_as_float(s[0]), __uint_as_float(s[1]));
    const auto t = __builtin_amdgcn_permlane32_swap(__float_as_uint(x), __float_as_uint(x), false, false);
    return fmaxf(__uint_as_float(t[0]), __uint_as_float(t[1]));
}
template <int CTRL> DI float dppf(float v) { return __builtin_bit_cast(float, __builtin_amdgcn_update_dpp(0, __builtin_bit_cast(int, v), CTRL, 0xf, 0xf, true)); }

constexpr u32 SPIN_CAP = 1u << 17;
DI void poll_flags(const char* base, int n, int lane, u64 tag, char* ws, u32 code) {
    const char* a = base + (lane < n ? lane : 0) * 8;
    u32 spins = 0;
    for (;;) {
        const u64 v = ld_agent(a);
        if (__all(v == tag)) break;
        if (++spins > SPIN_CAP) { if (lane == 0) st_agent32(ws + WS_ERR, code); break; }
        __builtin_amdgcn_s_sleep(1);
    }
}
DI u32x2 poll_granule(const char* ws, u32 gid, u64 tag, char* wsw, u32 code) {
    u32 spins = 0; u32x4 v;
    const u32 off = WS_MODG + gid * 16;
    for (;;) {
        asm volatile("s_nop 4\n\tglobal_load_dwordx4 %0, %1, %2 sc1\n\ts_waitcnt vmcnt(0)" : "=&v"(v) : "v"(off), "s"(ws) : "memory");
        const bool ok = v[2] == (u32)tag && v[3] == (u32)(tag >> 32);
        if (__all(ok)) break;
        if (++spins > SPIN_CAP) { st_agent32(wsw + WS_ERR, code); break; }
        __builtin_amdgcn_s_sleep(1);
    }
    return (u32x2){v[0], v[1]};
}

DI void poll_granule2(const char* ws, u32 g0, u32 g1, u64 tag, char* wsw, u32 code, u32x2& a, u32x2& c) {
    u32 spins = 0; u32x4 v0, v1;
    const u32 o0 = WS_MODG + g0 * 16, o1 = WS_MODG + g1 * 16;
    for (;;) {
        asm volatile("s_nop 4\n\tglobal_load_dwordx4 %0, %2, %4 sc1\n\tglobal_load_dwordx4 %1, %3, %4 sc1\n\ts_waitcnt vmcnt(0)" : "=&v"(v0), "=&v"(v1) : "v"(o0), "v"(o1), "s"(ws) : "memory");
        const bool ok = v0[2] == (u32)tag && v0[3] == (u32)(tag >> 32) && v1[2] == (u32)tag && v1[3] == (u32)(tag >> 32);
        if (__all(ok)) break;
        if (++spins > SPIN_CAP) { st_agent32(wsw + WS_ERR, code); break; }
        __builtin_amdgcn_s_sleep(1);
    }
    a = (u32x2){v0[0], v0[1]}; c = (u32x2){v1[0], v1[1]};
}

#define STAMPL(i) do {} while (0)
#define STAMP(i) do {} while (0)

template <int N> using IC = std::integral_constant<int, N>;
template <int I, int E, class F> DI void static_for(F&& f) { if constexpr (I < E) { f(IC<I>{}); static_for<I + 1, E>(f); } }
template <int N> DI void vmw() { asm volatile("s_waitcnt vmcnt(%0)" :: "i"(N) : "memory"); }
template <int N> DI void vmw1(u32x4& a) { asm volatile("s_waitcnt vmcnt(%1)" : "+v"(a) : "i"(N) : "memory"); }
template <int N> DI void vmw4(u32x4& a, u32x4& b, u32x4& c, u32x4& d) { asm volatile("s_waitcnt vmcnt(%4)" : "+v"(a), "+v"(b), "+v"(c), "+v"(d) : "i"(N) : "memory"); }
DI float dot2(u32 a, u32 b, float c) { return __builtin_amdgcn_fdot2_f32_bf16(__builtin_bit_cast(bf16x2, a), __builtin_bit_cast(bf16x2, b), c, false); }
#define SB0() __builtin_amdgcn_sched_barrier(0)
#define SGB(m, n) __builtin_amdgcn_sched_group_barrier((m), (n), 0)
constexpr int M_MFMA = 0x8, M_DSR = 0x100;
#define Z4 ((f32x4){0.f, 0.f, 0.f, 0.f})
DI u32 lds_add(u32 a, u32 v) { return __hip_atomic_fetch_add((LAS u32*)(size_t)a, v, __ATOMIC_RELAXED, __HIP_MEMORY_SCOPE_WORKGROUP); }
DI u32 lds_ld(u32 a) { return __hip_atomic_load((LAS u32*)(size_t)a, __ATOMIC_RELAXED, __HIP_MEMORY_SCOPE_WORKGROUP); }
DI void worker_sync(u32 cnt, int lane) {
    asm volatile("s_waitcnt lgkmcnt(0)" ::: "memory");
    if (lane == 0) lds_add(cnt, 1u);
    for (u32 spins = 0; lds_ld(cnt) < 4u && spins < (1u << 22); ++spins) {}
    asm volatile("" ::: "memory");
}

__global__ __launch_bounds__(512, 2) void mega_kernel(Params p) {
    __shared__ __attribute__((aligned(16))) char ldsmem[163840];
    const u32 L0 = (u32)(uintptr_t)(LAS char*)ldsmem;
    const int tid = threadIdx.x, l = tid & 63;
    const int w = __builtin_amdgcn_readfirstlane(tid >> 6);
    const int b = blockIdx.x, xg = b & 7, sidx = b >> 3;
    const int rb = 4 * (xg >> 1) + (sidx >> 3), j = 8 * (xg & 1) + (sidx & 7);
    const int g = l >> 4, i16 = l & 15, q4 = i16 >> 2, p4 = l & 3;
    char* const ws = p.ws;
    const u64 tag = *(const GAS u64*)(ws + WS_EPOCH) + 1;
    const __amdgpu_buffer_rsrc_t wsr = __builtin_amdgcn_make_buffer_rsrc(ws, 0, WS_END, 0x00020000);
#define ST16C(v, off) __builtin_amdgcn_raw_buffer_store_b128((v), wsr, (off), 0, 16)
#define ST8C(v, off)  __builtin_amdgcn_raw_buffer_store_b64((v), wsr, (off), 0, 16)
    STAMP(0);
    if (tid < 4) *(LAS u32*)(size_t)(L0 + L_CNT + tid * 4) = 0u;
    BAR();

    const int nj = j < 12 ? 7 : 6, Gj = j < 12 ? 7 * j : 84 + 6 * (j - 12);
    const bool wpwg = rb < 2 * nj;
    const int wslot = (nj == 7 || rb < 6) ? rb : rb + 1;
    if (w >= 4) {
        const int u = w - 4;
        u32 voff[5];
#pragma unroll
        for (int i = 0; i < 5; ++i) {
            const u32 sg = 64 * i + l, pos = (sg * 6554) >> 16, c = sg - pos * 10, k = pos ^ (((pos >> 3) & 1) << 2);
            voff[i] = k * 3840 + 80 * j + (c < 5 ? 1280 + 16 * c : 2560 + 16 * (c - 5));
        }
#pragma unroll
        for (int e = 0; e < 5; ++e) {
            const int s = u + 4 * e;
#pragma unroll
            for (int i = 0; i < 5; ++i) glds16((const char*)p.wqkv + s * 122880, voff[i], L0 + L_WKV + s * 5120 + i * 1024);
        }
#pragma unroll
        for (int i = 0; i < 5; ++i) {
            const u32 sg = 64 * i + l, pos = (sg * 13108) >> 16, c = sg - pos * 5;
            const u32 r = pos & 15, t = r & 1, uu = r >> 1, k = (pos & ~15u) + 8 * (uu >> 2) + 4 * t + (uu & 3);
            voff[i] = k * 3840 + 80 * j + 16 * c;
        }
#pragma unroll
        for (int e = 0; e < 3; ++e) {
            const int ds = u + 4 * e;
            if (ds < 10) {
#pragma unroll
                for (int i = 0; i < 5; ++i) glds16((const char*)p.wqkv + ds * 245760, voff[i], L0 + L_WQ + ds * 5120 + i * 1024);
            }
        }
        if (wpwg) {
            u32x2 ga, gb;
            const int t = tid - 256;
            poll_granule2(ws, 480 + t, 480 + 256 + (t & 63), tag, ws, 2, ga, gb);
            if (t < 160) sts64(L0 + L_SB + t * 8, ga);
            else sts64(L0 + L_G2 + (t - 160) * 8, (u32x2){pack2(1.f + bflo(ga[0]), 1.f + bfhi(ga[0])), pack2(1.f + bflo(ga[1]), 1.f + bfhi(ga[1]))});
            if (t < 64) sts64(L0 + L_G2 + (96 + t) * 8, (u32x2){pack2(1.f + bflo(gb[0]), 1.f + bfhi(gb[0])), pack2(1.f + bflo(gb[1]), 1.f + bfhi(gb[1]))});
        }
        VMCNT(0);
        STAMPL(17);
    } else {
        __builtin_amdgcn_s_setprio(3);
        const bool modwg = sidx < 30;
        const int sl = 30 * xg + sidx;
        u32x4 mw[5]; u32 mc[5];
        if (modwg) {
#pragma unroll
            for (int i = 0; i < 5; ++i) {
                const int r = (tid >> 1) + 128 * i;
                mw[i] = gld16<0>(p.wmod, r * 7680 + sl * 32 + (tid & 1) * 16);
                mc[i] = gldu16(p.c, r * 2);
            }
        }
        const int tok = l & 7, c0 = 20 * w + (l >> 3);
        u32x4 xr[3];
        {
            const u32 voff = (rb * 128 + 8 * j + tok) * 1280 + c0 * 16;
            xr[0] = gld16<0>(p.x, voff); xr[1] = gld16<128>(p.x, voff);
            xr[2] = gld16<0>(p.x, l < 32 ? voff + 256 : voff);
        }
        asm volatile("s_waitcnt vmcnt(0)" : "+v"(xr[0]), "+v"(xr[1]), "+v"(xr[2]) :: "memory");
        if (modwg) {
            asm volatile("" : "+v"(mw[0]), "+v"(mw[1]), "+v"(mw[2]), "+v"(mw[3]), "+v"(mw[4]), "+v"(mc[0]), "+v"(mc[1]), "+v"(mc[2]), "+v"(mc[3]), "+v"(mc[4]) :: "memory");
            float acc[8];
#pragma unroll
            for (int e = 0; e < 8; ++e) acc[e] = 0.f;
#pragma unroll
            for (int i = 0; i < 5; ++i) {
                const float cv = bflo(mc[i]);
                const float sv = rbf(cv * __builtin_amdgcn_rcpf(1.f + __builtin_amdgcn_exp2f(-cv * LOG2E)));
#pragma unroll
                for (int d = 0; d < 4; ++d) { acc[2 * d] += sv * bflo(mw[i][d]); acc[2 * d + 1] += sv * bfhi(mw[i][d]); }
            }
#pragma unroll
            for (int e = 0; e < 8; ++e) {
                float v = acc[e];
                v += dppf<0x122>(v); v += dppf<0x124>(v); v += dppf<0x128>(v);
                acc[e] = xsum4(v);
            }
            if (l < 2) {
                stsf4(L0 + L_MODP + w * 64 + l * 32, (f32x4){acc[0], acc[1], acc[2], acc[3]});
                stsf4(L0 + L_MODP + w * 64 + l * 32 + 16, (f32x4){acc[4], acc[5], acc[6], acc[7]});
            }
            asm volatile("s_waitcnt lgkmcnt(0)" ::: "memory");
            u32 old = 0;
            if (l == 0) old = lds_add(L0 + L_CNT, 1u);
            if (__builtin_amdgcn_readfirstlane(old) == 3) {
                float v = 0.f;
#pragma unroll
                for (int ww = 0; ww < 4; ++ww) v += ldsf(L0 + L_MODP + ww * 64 + i16 * 4);
                const float v1 = dppf<0x55>(v), v2 = dppf<0xAA>(v), v3 = dppf<0xFF>(v), v0 = dppf<0x00>(v);
                const u32x4 gr = {pack2(v0, v1), pack2(v2, v3), (u32)tag, (u32)(tag >> 32)};
                if (l < 16 && p4 == 0) ST16C(gr, WS_MODG + (sl * 4 + q4) * 16);
            }
        }
        STAMP(1);
        {
            float s1 = 0.f, s2 = 0.f;
#pragma unroll
            for (int i = 0; i < 3; ++i) {
                float a1 = 0.f, a2 = 0.f;
#pragma unroll
                for (int d = 0; d < 4; ++d) { const float a = bflo(xr[i][d]), c = bfhi(xr[i][d]); a1 += a + c; a2 += a * a + c * c; }
                if (i < 2 || l < 32) { s1 += a1; s2 += a2; }
            }
            s1 += dppf<0x128>(s1); s2 += dppf<0x128>(s2);
            s1 = xsum4(s1); s2 = xsum4(s2);
            if (l < 8) stsf2(L0 + L_STP + (w * 8 + l) * 8, (f32x2){s1, s2});
        }
        {
            const u32 gs = L0 + L_GS + w * 640;
            u32x2 a, c;
            poll_granule2(ws, (l < 40 ? 40 * w + l : 160 + 40 * w + (l - 40)), 160 + 40 * w + 24 + (l & 15), tag, ws, 1, a, c);
            if (l < 40) sts64(gs + l * 8, a);
            else sts64(gs + 320 + (l - 40) * 8, (u32x2){pack2(1.f + bflo(a[0]), 1.f + bfhi(a[0])), pack2(1.f + bflo(a[1]), 1.f + bfhi(a[1]))});
            if (l < 16) sts64(gs + 320 + (24 + l) * 8, (u32x2){pack2(1.f + bflo(c[0]), 1.f + bfhi(c[0])), pack2(1.f + bflo(c[1]), 1.f + bfhi(c[1]))});
        }
        STAMP(2);
        worker_sync(L0 + L_CNT + 4, l);
        {
            f32x2 st = {0.f, 0.f};
#pragma unroll
            for (int ww = 0; ww < 4; ++ww) st += *(LAS f32x2*)(size_t)(L0 + L_STP + (ww * 8 + tok) * 8);
            const float mu = st[0] * (1.f / 640.f), var = fmaxf(st[1] * (1.f / 640.f) - mu * mu, 0.f);
            const float rs = __builtin_amdgcn_rsqf(var + 1e-5f), nmr = -mu * rs;
#pragma unroll
            for (int i = 0; i < 3; ++i) {
                const int cl = i < 2 ? (l >> 3) + 8 * i : 16 + ((l >> 3) & 3);
                const u32x4 sa = lds128(L0 + L_GS + w * 640 + cl * 16), gg = lds128(L0 + L_GS + w * 640 + 320 + cl * 16);
                u32x4 h;
#pragma unroll
                for (int d = 0; d < 4; ++d)
                    h[d] = pack2((bflo(xr[i][d]) * rs + nmr) * bflo(gg[d]) + bflo(sa[d]), (bfhi(xr[i][d]) * rs + nmr) * bfhi(gg[d]) + bfhi(sa[d]));
                if (i < 2 || l < 32) ST16C(h, WS_H + (((rb * 8 + (j >> 1)) * 80 + 20 * w + cl) * 16 + 8 * (j & 1) + tok) * 16);
            }
        }
        VMCNT(0);
        worker_sync(L0 + L_CNT + 8, l);
        if (tid == 0) st_agent(ws + WS_FH + (rb * 16 + j) * 8, tag);
        if (w == 0) poll_flags(ws + WS_FH + rb * 128, 16, l, tag, ws, 11);
        __builtin_amdgcn_s_setprio(0);
    }
    BAR();
    STAMP(3);

    u32x4 xa[20];
    const u32 lo16 = l * 16;
    const char* hsrc = ws + WS_H + (rb * 8 + w) * 80 * 256;
#define XL(s) xa[s] = gld16c<((s) & 3) * 1024>(hsrc + ((s) >> 2) * 4096, lo16)
    XL(0); XL(1); XL(2); XL(3); XL(4); XL(5);
    const char* kvx = ws + WS_KV + (rb * 16 + j) * 22528;
    {
        f32x4 acc[5];
#pragma unroll
        for (int n = 0; n < 5; ++n) acc[n] = Z4;
        const u32 b0 = L0 + L_WKV + 160 * (8 * g + 4 * (g & 1) + q4) + 8 * p4;
        const u32 b1 = L0 + L_WKV + 160 * (8 * g + 4 * ((g & 1) ^ 1) + q4) + 8 * p4;
        u32x4 bf[2][5];
#pragma unroll
        for (int n = 0; n < 5; ++n) bf[0][n] = ldtr2(b0 + n * 32, b1 + n * 32);
        static_for<0, 20>([&](auto S) {
            constexpr int s = S;
            vmw1<(s < 15 ? 5 : 19 - s)>(xa[s]);
            if constexpr (s + 6 < 20) XL(s + 6);
            SB0();
#pragma unroll
            for (int n = 0; n < 5; ++n) {
                if (s < 19) bf[(s + 1) & 1][n] = ldtr2(b0 + (s + 1) * 5120 + n * 32, b1 + (s + 1) * 5120 + n * 32);
                acc[n] = mfma(bf[s & 1][n], xa[s], acc[n]);
            }
#pragma unroll
            for (int n = 0; n < 5; ++n) { SGB(M_DSR, 2); SGB(M_MFMA, 1); }
            SB0();
        });
        STAMP(18);
        BAR();
        const int row = 256 + 16 * w + i16;
#pragma unroll
        for (int n = 0; n < 5; ++n) {
            const int Q = 4 * n + g;
            const u32x2 v = {pack2(acc[n][0], acc[n][1]), pack2(acc[n][2], acc[n][3])};
            const int wi = Q - 10, hd = wi >= 5 ? 1 : 0;
            const u32 a = Q < 10 ? L_K + row * 80 + 8 * Q : L_V + row * 96 + 48 * hd + 8 * (wi - 5 * hd);
            sts64(L0 + a, v);
        }
        if (g < 2) sts64(L0 + L_V + row * 96 + 48 * g + 40, (u32x2){0x3F80u, 0u});
        if (rb < 2) {
            const int nrow = rb == 0 ? 256 : 128;
            for (int i = tid; i < nrow * 5; i += 512) sts128(L0 + L_K + i * 16, (u32x4){0, 0, 0, 0});
            for (int i = tid; i < nrow * 6; i += 512) sts128(L0 + L_V + i * 16, (u32x4){0, 0, 0, 0});
        }
        BAR();
    }
#undef XL
    STAMP(4);
    {
        u32x4 kvd[3];
#pragma unroll
        for (int e = 0; e < 3; ++e) {
            const int n = min(w + 8 * e, 21);
            kvd[e] = lds128(L0 + (n < 10 ? L_K + 20480 + n * 1024 : L_V + 24576 + (n - 10) * 1024) + lo16);
        }
        const u32 kvo = WS_KV + (rb * 16 + j) * 22528 + w * 1024 + lo16;
        const int wa = wslot < 6 ? wslot >> 1 : wslot == 6 ? 3 : wslot < 13 ? 4 + ((wslot - 7) >> 1) : 3;
        const int wup = wslot < 6 ? wslot & 1 : wslot == 6 ? 0 : wslot < 13 ? (wslot - 7) & 1 : 1;
        const char* wsrc = (const char*)(wup ? p.wu : p.wg) + (Gj + (wa <= 3 || nj == 7 ? wa : wa - 1)) * 32 + w * 32 * 3456;
        const u32 wvo = (l >> 1) * 3456 + (l & 1) * 16;
        f32x4 acc[3];
#pragma unroll
        for (int n = 0; n < 3; ++n) acc[n] = Z4;
        const u32 bq = L0 + L_WQ + 80 * (16 * (g >> 1) + 8 * (g & 1) + 2 * q4) + 8 * p4;
        u32x4 bf[3][3];
#pragma unroll
        for (int n = 0; n < 3; ++n) { bf[0][n] = ldtr2(bq + n * 32, bq + n * 32 + 80); bf[1][n] = ldtr2(bq + 2560 + n * 32, bq + 2560 + n * 32 + 80); }
        static_for<0, 20>([&](auto S) {
            constexpr int s = S;
            if constexpr (s == 0) { if (rb < 15) ST16C(kvd[0], kvo); }
            if constexpr (s == 2) { if (rb < 15) ST16C(kvd[1], kvo + 8192); }
            if constexpr (s == 4) { if (rb < 15 && w < 6) ST16C(kvd[2], kvo + 16384); }
            if constexpr (s == 8) { if (wpwg) glds16(wsrc, wvo, L0 + L_WPRAW + w * 1024); }
            if constexpr (s == 11) { if (wpwg) glds16(wsrc + 8 * 32 * 3456, wvo, L0 + L_WPRAW + (w + 8) * 1024); }
            if constexpr (s == 14) { if (wpwg && w < 4) glds16(wsrc + 16 * 32 * 3456, wvo, L0 + L_WPRAW + (w + 16) * 1024); }
            SB0();
#pragma unroll
            for (int n = 0; n < 3; ++n) {
                if (s < 18) bf[(s + 2) % 3][n] = ldtr2(bq + (s + 2) * 2560 + n * 32, bq + (s + 2) * 2560 + n * 32 + 80);
                acc[n] = mfma(bf[s % 3][n], xa[s], acc[n]);
            }
#pragma unroll
            for (int n = 0; n < 3; ++n) { SGB(M_DSR, 2); SGB(M_MFMA, 1); }
            SB0();
        });
        STAMP(21);
#pragma unroll
        for (int n = 0; n < 3; ++n) {
            const int Q = 4 * n + g;
            const u32x2 v = {pack2(acc[n][0] * QSCALE, acc[n][1] * QSCALE), pack2(acc[n][2] * QSCALE, acc[n][3] * QSCALE)};
            if (Q < 10) sts64(L0 + L_Q + (16 * w + i16) * 80 + 8 * Q, v);
        }
    }
    VMCNT(0);
    BAR();
    if (tid == 0) st_agent(ws + WS_FKV + (j * 16 + rb) * 8, tag);
    STAMP(5);
    if (wpwg) {
        float us = 0.f, vs = 0.f;
#pragma unroll
        for (int i = 0; i < 3; ++i) {
            if (i < 2 || w < 4) {
                const int s = w + 8 * i;
                const u32 a = L0 + L_WPRAW + (32 * s + 8 * g + q4) * 32 + 8 * p4;
                const u32x4 wr = ldtr2(a, a + 128);
                const u32x4 g2 = lds128(L0 + L_G2 + (32 * s + 8 * g) * 2), sb = lds128(L0 + L_SB + (32 * s + 8 * g) * 2);
                u32x4 o;
#pragma unroll
                for (int d = 0; d < 4; ++d) {
                    o[d] = pack2(dot2(wr[d], g2[d] & 0xffffu, 0.f), dot2(wr[d], g2[d] & 0xffff0000u, 0.f));
                    us = dot2(o[d], 0x3F803F80u, us);
                    vs = dot2(wr[d], sb[d], vs);
                }
                ST16C(o, WS_WP + ((j * 20 + s) * 14 + wslot) * 1024 + lo16);
            }
        }
        us = xsum4(us); vs = xsum4(vs);
        if (l < 16) { stsf(L0 + L_UVP + w * 128 + l * 4, us); stsf(L0 + L_UVP + w * 128 + 64 + l * 4, vs); }
    }
    if (w == 0 && rb >= 1) poll_flags(ws + WS_FKV + (j * 16 + (rb >= 2 ? rb - 2 : rb - 1)) * 8, rb >= 2 ? 2 : 1, l, tag, ws, 3);
    BAR();
    STAMP(6);
    if (rb >= 1) {
#pragma unroll
        for (int e = 0; e < 3; ++e) {
            const int n = w + 8 * e;
            if (n < 22) {
                const u32 d = n < 10 ? L_K + n * 1024 : L_V + (n - 10) * 1024, st = n < 10 ? 10240 : 12288;
                glds16c(kvx - 16 * 22528 + n * 1024, lo16, L0 + d + st);
                if (rb >= 2) glds16c(kvx - 32 * 22528 + n * 1024, lo16, L0 + d);
            }
        }
    }
    if (wpwg && tid < 32) {
        float v = 0.f;
#pragma unroll
        for (int ww = 0; ww < 8; ++ww) v += ldsf(L0 + L_UVP + ww * 128 + tid * 4);
        st_agent32(ws + WS_UV + (j * 14 + wslot) * 128 + tid * 4, __float_as_uint(v));
    }
    const int hf = j & 1, cb = j >> 1;
    u32 wvoff[5];
#pragma unroll
    for (int i = 0; i < 5; ++i) {
        const u32 sg = 64 * i + l, pos = (sg * 6554) >> 16, c = sg - pos * 10, k = pos ^ (((pos >> 3) & 1) << 2);
        wvoff[i] = k * 1280 + c * 16;
    }
    VMCNT(0);
    BAR();
    {
        const char* src = (const char*)p.wo + cb * 160 + (10 + w) * 32 * 1280;
#pragma unroll
        for (int i = 0; i < 5; ++i) glds16(src, wvoff[i], L0 + L_WO1 + w * 5120 + i * 1024);
        if (w < 2) {
#pragma unroll
            for (int i = 0; i < 5; ++i) glds16(src + 8 * 32 * 1280, wvoff[i], L0 + L_WO1 + (w + 8) * 5120 + i * 1024);
        }
    }
    STAMP(7);

    {
        const int hd = w & 1, qi = w >> 1;
        const int kr0 = 32 * qi;
        u32x4 qf[2];
#pragma unroll
        for (int a = 0; a < 2; ++a) {
            const u32 qa = L0 + L_Q + (kr0 + 16 * a + i16) * 80 + hd * 40;
            const u32x2 q0 = lds64(qa + 8 * g);
            u32x2 q1 = lds64(qa + 32);
            if (g != 0) q1 = (u32x2){0, 0};
            qf[a] = (u32x4){q0[0], q0[1], q1[0], q1[1]};
        }
        const u32 kb0 = L0 + L_K + (kr0 + i16) * 80 + hd * 40 + 8 * g, kb1 = L0 + L_K + (kr0 + i16) * 80 + hd * 40 + 32;
        f32x4 mfirst, mlast;
#pragma unroll
        for (int r = 0; r < 4; ++r) { const bool lo = 4 * g + r <= i16; mfirst[r] = lo ? -INFINITY : 0.f; mlast[r] = lo ? 0.f : -INFINITY; }
        f32x4 s0[17], s1[17];
        {
            u32x4 kf[3];
#define KRD(t) do { const u32x2 k0 = lds64(kb0 + (t) * 1280), k1 = lds64(kb1 + (t) * 1280); kf[(t) % 3] = (u32x4){k0[0], k0[1], k1[0], k1[1]}; } while (0)
            KRD(0); KRD(1);
            static_for<0, 18>([&](auto T) {
                constexpr int t = T;
                SB0();
                if constexpr (t < 16) KRD(t + 2);
                if constexpr (t < 17) s0[t] = mfma(kf[t % 3], qf[0], t == 0 ? mfirst : t == 16 ? mlast : Z4);
                if constexpr (t > 0) s1[t - 1] = mfma(kf[t % 3], qf[1], t == 1 ? mfirst : t == 17 ? mlast : Z4);
                SGB(M_DSR, 2); SGB(M_MFMA, 2);
                SB0();
            });
#undef KRD
        }
        if (rb < 2) {
            const int fv = rb == 1 ? 128 : 256;
#pragma unroll
            for (int t = 0; t < 16; ++t) {
                const bool d0 = kr0 + 16 * t < fv, d1 = kr0 + 16 * t + 16 < fv;
#pragma unroll
                for (int r = 0; r < 4; ++r) { s0[t][r] = d0 ? -INFINITY : s0[t][r]; s1[t][r] = d1 ? -INFINITY : s1[t][r]; }
            }
        }
        STAMP(22);
        float m0 = fmaxf(fmaxf(s0[0][0], s0[0][1]), fmaxf(s0[0][2], s0[0][3])), m1 = fmaxf(fmaxf(s1[0][0], s1[0][1]), fmaxf(s1[0][2], s1[0][3]));
#pragma unroll
        for (int t = 1; t < 17; ++t) {
            m0 = fmaxf(fmaxf(m0, s0[t][0]), s0[t][1]); m1 = fmaxf(fmaxf(m1, s1[t][0]), s1[t][1]);
            m0 = fmaxf(fmaxf(m0, s0[t][2]), s0[t][3]); m1 = fmaxf(fmaxf(m1, s1[t][2]), s1[t][3]);
        }
        m0 = xmax4(m0); m1 = xmax4(m1);
        STAMP(23);
        f32x4 o00 = Z4, o01 = Z4, o10 = Z4, o11 = Z4;
        const u32 vb = L0 + L_V + (kr0 + 4 * g + q4) * 96 + hd * 48 + 8 * p4;
        auto pv = [&](auto SUB) {
            constexpr bool sub = SUB;
            u32x4 vf[2][2];
            vf[0][0] = ldtr2(vb, vb + 16 * 96); vf[0][1] = ldtr2(vb + 32, vb + 16 * 96 + 32);
#define EX(v, m) __builtin_amdgcn_exp2f(sub ? (v) - (m) : (v))
            static_for<0, 9>([&](auto U) {
                constexpr int u = U;
                if constexpr (u < 8) { const u32 a = vb + (u + 1) * 32 * 96; vf[(u + 1) & 1][0] = ldtr2(a, a + 16 * 96); vf[(u + 1) & 1][1] = ldtr2(a + 32, a + 16 * 96 + 32); }
                u32x4 pa, pb;
                pa[0] = pack2(EX(s0[2 * u][0], m0), EX(s0[2 * u][1], m0)); pa[1] = pack2(EX(s0[2 * u][2], m0), EX(s0[2 * u][3], m0));
                if constexpr (u < 8) { pa[2] = pack2(EX(s0[2 * u + 1][0], m0), EX(s0[2 * u + 1][1], m0)); pa[3] = pack2(EX(s0[2 * u + 1][2], m0), EX(s0[2 * u + 1][3], m0)); }
                else { pa[2] = 0; pa[3] = 0; }
                if constexpr (u > 0) { pb[0] = pack2(EX(s1[2 * u - 1][0], m1), EX(s1[2 * u - 1][1], m1)); pb[1] = pack2(EX(s1[2 * u - 1][2], m1), EX(s1[2 * u - 1][3], m1)); }
                else { pb[0] = 0; pb[1] = 0; }
                pb[2] = pack2(EX(s1[2 * u][0], m1), EX(s1[2 * u][1], m1)); pb[3] = pack2(EX(s1[2 * u][2], m1), EX(s1[2 * u][3], m1));
                o00 = mfma(vf[u & 1][0], pa, o00); o01 = mfma(vf[u & 1][1], pa, o01);
                o10 = mfma(vf[u & 1][0], pb, o10); o11 = mfma(vf[u & 1][1], pb, o11);
                SB0();
            });
#undef EX
        };
        if (__all(fabsf(m0) <= 64.f && fabsf(m1) <= 64.f)) pv(std::false_type{}); else pv(std::true_type{});
        STAMP(24);
        const float i0 = __builtin_amdgcn_rcpf(__shfl(o01[0], i16 + 16)), i1 = __builtin_amdgcn_rcpf(__shfl(o11[0], i16 + 16));
        const u32 oa = L0 + L_Q + (kr0 + i16) * 80 + hd * 40;
        sts64(oa + 8 * g, (u32x2){pack2(o00[0] * i0, o00[1] * i0), pack2(o00[2] * i0, o00[3] * i0)});
        sts64(oa + 1280 + 8 * g, (u32x2){pack2(o10[0] * i1, o10[1] * i1), pack2(o10[2] * i1, o10[3] * i1)});
        if (g == 0) {
            sts64(oa + 32, (u32x2){pack2(o01[0] * i0, o01[1] * i0), pack2(o01[2] * i0, o01[3] * i0)});
            sts64(oa + 1280 + 32, (u32x2){pack2(o11[0] * i1, o11[1] * i1), pack2(o11[2] * i1, o11[3] * i1)});
        }
    }
    BAR();
    {
        const int tok = 64 * (w & 1) + l;
        const u32 oo = WS_O + (((rb * 8 + (tok >> 4)) * 80 + 5 * j) * 16 + (tok & 15)) * 16;
        ST16C(lds128(L0 + L_Q + tok * 80 + (w >> 1) * 16), oo + (w >> 1) * 256);
        if (w < 2) ST16C(lds128(L0 + L_Q + tok * 80 + 64), oo + 4 * 256);
    }
    VMCNT(0);
    BAR();
    if (tid == 0) {
        st_agent(ws + WS_FO + (rb * 16 + j) * 8, tag);
        if (wpwg) st_agent(ws + WS_FWP + (j * 16 + rb) * 8, tag);
    }
    STAMP(8);

    const int wm = w & 3, wk = w >> 2;
    const int mt3 = rb * 8 + 4 * hf + wm;
    const int nt0 = wk == 0 ? 0 : 3, ntn = wk == 0 ? 3 : 2;
    u32x2 x1k[3];
    {
        const int wdu = b + 256 * (w - 6);
        const bool wdw = w >= 6 && wdu < 432;
        if (wdw) {
            const int ucb = wdu / 54, us = wdu - 54 * ucb;
#pragma unroll
            for (int i = 0; i < 5; ++i) glds16((const char*)p.wd + ucb * 160 + us * 32 * 1280, wvoff[i], L0 + L_WDRAW + (w - 6) * 5120 + i * 1024);
        }
        if (w >= 1) {
            const char* src = (const char*)p.wo + cb * 160 + (w - 1) * 32 * 1280;
#pragma unroll
            for (int i = 0; i < 5; ++i) glds16(src, wvoff[i], L0 + L_WO0 + (w - 1) * 5120 + i * 1024);
            if (w < 4) {
#pragma unroll
                for (int i = 0; i < 5; ++i) glds16(src + 7 * 32 * 1280, wvoff[i], L0 + L_WO0 + (w + 6) * 5120 + i * 1024);
            } else {
#pragma unroll
                for (int e = 0; e < 3; ++e) {
                    const int pc = w - 4 + 4 * e;
                    if (pc < 10) {
                        const u32 sg = 64 * pc + l, r = (sg * 6554) >> 16, c = sg - r * 10;
                        glds16((const char*)p.x + (rb * 128 + 64 * hf) * 1280 + cb * 160, r * 1280 + c * 16, L0 + L_XR + pc * 1024);
                    }
                }
            }
        }
        if (w == 1 && l < 20) {
            const u32x2 gv = poll_granule(ws, 320 + 20 * cb + l, tag, ws, 4);
            stsf4(L0 + L_GA + l * 16, (f32x4){bflo(gv[0]), bfhi(gv[0]), bflo(gv[1]), bfhi(gv[1])});
        }
        if (w == 0) {
            poll_flags(ws + WS_FWP + j * 128, 2 * nj, l, tag, ws, 6);
            poll_flags(ws + WS_FO + rb * 128, 16, l, tag, ws, 5);
        }
        VMCNT(0);
        if (wdw) {
            const u32 wb = L0 + L_WDRAW + (w - 6) * 5120;
            const u32 b0 = wb + 160 * (8 * g + 4 * (g & 1) + q4) + 8 * p4, b1 = wb + 160 * (8 * g + 4 * ((g & 1) ^ 1) + q4) + 8 * p4;
#pragma unroll
            for (int n = 0; n < 5; ++n) ST16C(ldtr2(b0 + n * 32, b1 + n * 32), WS_WD + (wdu * 5 + n) * 1024 + lo16);
        }
        BAR();
        STAMP(9);
        u32x4 a[10];
        const char* asrc = ws + WS_O + (mt3 * 80 + 40 * wk) * 256;
#define AL(s) a[s] = gld16c<((s) & 3) * 1024>(asrc + ((s) >> 2) * 4096, lo16)
        AL(0); AL(1); AL(2); AL(3);
        f32x4 acc[5];
#pragma unroll
        for (int n = 0; n < 5; ++n) acc[n] = Z4;
        const u32 wb = L0 + (wk == 0 ? L_WO0 : L_WO1);
        const u32 b0 = wb + 160 * (8 * g + 4 * (g & 1) + q4) + 8 * p4, b1 = wb + 160 * (8 * g + 4 * ((g & 1) ^ 1) + q4) + 8 * p4;
        u32x4 bf[2][5];
#pragma unroll
        for (int n = 0; n < 5; ++n) bf[0][n] = ldtr2(b0 + n * 32, b1 + n * 32);
        static_for<0, 10>([&](auto S) {
            constexpr int s = S;
            vmw1<(s < 7 ? 3 : 9 - s)>(a[s]);
            if constexpr (s + 4 < 10) AL(s + 4);
            SB0();
#pragma unroll
            for (int n = 0; n < 5; ++n) {
                if (s < 9) bf[(s + 1) & 1][n] = ldtr2(b0 + (s + 1) * 5120 + n * 32, b1 + (s + 1) * 5120 + n * 32);
                acc[n] = mfma(bf[s & 1][n], a[s], acc[n]);
            }
#pragma unroll
            for (int n = 0; n < 5; ++n) { SGB(M_DSR, 2); SGB(M_MFMA, 1); }
            SB0();
        });
#undef AL
        STAMP(25);
        if (wk == 0) { stsf4(L0 + L_RED3 + (wm * 5 + 3) * 1024 + lo16, acc[3]); stsf4(L0 + L_RED3 + (wm * 5 + 4) * 1024 + lo16, acc[4]); }
        else { stsf4(L0 + L_RED3 + (wm * 5 + 0) * 1024 + lo16, acc[0]); stsf4(L0 + L_RED3 + (wm * 5 + 1) * 1024 + lo16, acc[1]); stsf4(L0 + L_RED3 + (wm * 5 + 2) * 1024 + lo16, acc[2]); }
        BAR();
        float s1 = 0.f, s2 = 0.f;
#pragma unroll
        for (int n = 0; n < 3; ++n) {
            if (n < ntn) {
                const int nt = nt0 + n;
                const f32x4 mine = wk == 0 ? acc[n] : acc[n < 2 ? 3 + n : 4];
                const f32x4 v = mine + ldsf4(L0 + L_RED3 + (wm * 5 + nt) * 1024 + lo16);
                const f32x4 ga = ldsf4(L0 + L_GA + (16 * nt + 4 * g) * 4);
                const u32x2 xr = lds64(L0 + L_XR + (16 * wm + i16) * 160 + 32 * nt + 8 * g);
                const u32x2 o = {pack2(bflo(xr[0]) + ga[0] * v[0], bfhi(xr[0]) + ga[1] * v[1]), pack2(bflo(xr[1]) + ga[2] * v[2], bfhi(xr[1]) + ga[3] * v[3])};
                x1k[n] = o;
                s1 = dot2(o[0], 0x3F803F80u, s1); s1 = dot2(o[1], 0x3F803F80u, s1);
                s2 = dot2(o[0], o[0], s2); s2 = dot2(o[1], o[1], s2);
                sts64(L0 + L_X1S + (16 * wm + i16) * 160 + 32 * nt + 8 * g, o);
            } else x1k[n] = (u32x2){0, 0};
        }
        s1 = xsum4(s1); s2 = xsum4(s2);
        if (l < 16) stsf2(L0 + L_ST3 + (wk * 64 + 16 * wm + l) * 8, (f32x2){s1, s2});
        BAR();
        {
            const u32 xo = WS_X1 + (((rb * 8 + 4 * hf + g) * 80 + 10 * cb) * 16 + i16) * 16;
            ST16C(lds128(L0 + L_X1S + l * 160 + w * 16), xo + w * 256);
            if (w < 2) ST16C(lds128(L0 + L_X1S + l * 160 + (w + 8) * 16), xo + (w + 8) * 256);
            if (w == 2) {
                const f32x2 u = *(LAS f32x2*)(size_t)(L0 + L_ST3 + l * 8), v = *(LAS f32x2*)(size_t)(L0 + L_ST3 + (64 + l) * 8);
                const f32x2 sum = u + v;
                ST8C(__builtin_bit_cast(u32x2, sum), WS_STAT + ((rb * 8 + cb) * 128 + 64 * hf + l) * 8);
            }
        }
        STAMP(26);
        VMCNT(0);
        BAR();
        if (tid == 0) { st_agent(ws + WS_FX1 + (rb * 16 + j) * 8, tag); st_agent(ws + WS_FWD + b * 8, tag); }
    }
    STAMP(10);

    {
        const int wn = w >> 2;
        const char* wpj = ws + WS_WP + (j * 20 * 14 + w) * 1024;
        const char* x1b = ws + WS_X1 + (rb * 8 + w) * 80 * 256;
        const u32 r4w = L0 + L_R4 + w * 1024;
#define P4_B1(t, i) glds16c(wpj + ((t) * 28 + 8 * (i)) * 1024, lo16, r4w + ((t) % 3) * R4_STAGE + 16384 + 8 * (i) * 1024)
#define P4_A1(t, i) glds16c(x1b + (8 * (t) + 4 * (i)) * 256, lo16, r4w + ((t) % 3) * R4_STAGE + 8 * (i) * 1024)
#define P4_B(t) do { P4_B1(t, 0); P4_B1(t, 1); P4_B1(t, 2); if (w < 4) P4_B1(t, 3); } while (0)
        P4_B(0); P4_B(1); P4_B(2);
        if (w == 0) poll_flags(ws + WS_FX1 + rb * 128, 16, l, tag, ws, 7);
        BAR();
        STAMP(11);
        glds16c(ws + WS_STAT + rb * 8192 + w * 1024, lo16, L0 + L_ST4 + w * 1024);
        if (w < 2) glds16c(ws + WS_UV + j * 1792 + w * 1024, lo16, L0 + L_UV + w * 1024);
        P4_A1(0, 0); P4_A1(0, 1); P4_A1(1, 0); P4_A1(1, 1); P4_A1(2, 0); P4_A1(2, 1);
        f32x4 acc[7][2];
#pragma unroll
        for (int n = 0; n < 7; ++n) { acc[n][0] = Z4; acc[n][1] = Z4; }
        const u32 ra = L0 + L_R4 + 2 * wm * 1024 + lo16, rbb = L0 + L_R4 + 16384 + 7 * wn * 1024 + lo16;
        u32x4 fa[2][2], fb[2][7];
#define P4_RA(buf, t, ks) do { const u32 so = ((t) % 3) * R4_STAGE + (ks) * 8192; fa[buf][0] = lds128(ra + so); fa[buf][1] = lds128(ra + so + 1024); } while (0)
#define P4_RB(buf, t, ks, n) fb[buf][n] = lds128(rbb + ((t) % 3) * R4_STAGE + (ks) * 14336 + (n) * 1024)
#define P4_MM(buf, n) do { acc[n][0] = mfma(fb[buf][n], fa[buf][0], acc[n][0]); acc[n][1] = mfma(fb[buf][n], fa[buf][1], acc[n][1]); } while (0)
#define P4_HALF(buf, rd, t, ks, V0, V1, V2) do { \
            SB0(); if (rd) { P4_RA((buf) ^ 1, t, ks); P4_RB((buf) ^ 1, t, ks, 0); } P4_MM(buf, 0); SB0(); V0; \
            SB0(); if (rd) P4_RB((buf) ^ 1, t, ks, 1); P4_MM(buf, 1); SB0(); \
            SB0(); if (rd) P4_RB((buf) ^ 1, t, ks, 2); P4_MM(buf, 2); SB0(); V1; \
            SB0(); if (rd) P4_RB((buf) ^ 1, t, ks, 3); P4_MM(buf, 3); SB0(); \
            SB0(); if (rd) P4_RB((buf) ^ 1, t, ks, 4); P4_MM(buf, 4); SB0(); V2; \
            SB0(); if (rd) P4_RB((buf) ^ 1, t, ks, 5); P4_MM(buf, 5); SB0(); \
            SB0(); if (rd) P4_RB((buf) ^ 1, t, ks, 6); P4_MM(buf, 6); SB0(); } while (0)
        VMCNT(4);
        BAR();
        STAMP(27);
        P4_RA(0, 0, 0);
#pragma unroll
        for (int n = 0; n < 7; ++n) P4_RB(0, 0, 0, n);
        static_for<0, 10>([&](auto T) {
            constexpr int t = T;
            constexpr bool f = t >= 1 && t + 2 <= 9, sN = t + 3 <= 9;
            P4_HALF(0, true, t, 1, if (f && w < 4) P4_B1(t + 2, 3), if (f) P4_A1(t + 2, 0), if (f) P4_A1(t + 2, 1));
            if constexpr (t == 5) {
                if (tid < 128) {
                    f32x2 st = {0.f, 0.f};
#pragma unroll
                    for (int c = 0; c < 8; ++c) st += *(LAS f32x2*)(size_t)(L0 + L_ST4 + (c * 128 + tid) * 8);
                    const float mu = st[0] * (1.f / 640.f), var = fmaxf(st[1] * (1.f / 640.f) - mu * mu, 0.f), r = __builtin_amdgcn_rsqf(var + 1e-5f);
                    stsf2(L0 + L_MR + tid * 8, (f32x2){r, -mu * r});
                }
            }
            if constexpr (t < 9) {
                if constexpr (t == 0) vmw<2>();
                else if constexpr (t == 8) vmw<0>();
                else { if (w < 4) vmw<6>(); else vmw<5>(); }
                BAR();
                P4_HALF(1, true, t + 1, 0, if (sN) P4_B1(t + 3, 0), if (sN) P4_B1(t + 3, 1), if (sN) P4_B1(t + 3, 2));
            } else {
                P4_HALF(1, false, 0, 0, , , );
            }
        });
#undef P4_B
#undef P4_B1
#undef P4_A1
#undef P4_RA
#undef P4_RB
#undef P4_MM
#undef P4_HALF
        STAMP(12);
        f32x2 mr[2];
#pragma unroll
        for (int m = 0; m < 2; ++m) mr[m] = *(LAS f32x2*)(size_t)(L0 + L_MR + (32 * wm + 16 * m + i16) * 8);
#pragma unroll
        for (int n = 0; n < 7; ++n) {
            const f32x4 u = ldsf4(L0 + L_UV + (7 * wn + n) * 128 + 16 * g), v = ldsf4(L0 + L_UV + (7 * wn + n) * 128 + 64 + 16 * g);
#pragma unroll
            for (int m = 0; m < 2; ++m) acc[n][m] = acc[n][m] * mr[m][0] + (u * mr[m][1] + v);
        }
        if (nj == 7) stsf4(L0 + L_XCH + w * 1024 + lo16, wn == 0 ? acc[6][1] : acc[6][0]);
        auto silu_mul = [&](const f32x4 gt, const f32x4 up) -> u32x2 {
            const f32x4 e = gt * (-LOG2E), gu = gt * up;
            f32x4 d;
#pragma unroll
            for (int r = 0; r < 4; ++r) d[r] = __builtin_amdgcn_exp2f(e[r]);
            d = d + 1.f;
#pragma unroll
            for (int r = 0; r < 4; ++r) d[r] = __builtin_amdgcn_rcpf(d[r]);
            const f32x4 o = gu * d;
            return (u32x2){pack2(o[0], o[1]), pack2(o[2], o[3])};
        };
#pragma unroll
        for (int a = 0; a < 3; ++a) {
            const int la = a + 4 * wn, gam = Gj + (la <= 3 || nj == 7 ? la : la - 1);
            const u32x2 o0 = silu_mul(acc[2 * a][0], acc[2 * a + 1][0]), o1 = silu_mul(acc[2 * a][1], acc[2 * a + 1][1]);
            const auto s0 = __builtin_amdgcn_permlane16_swap(o0[0], o1[0], false, false);
            const auto s1 = __builtin_amdgcn_permlane16_swap(o0[1], o1[1], false, false);
            const u32x4 v = {s0[0], s1[0], s0[1], s1[1]};
            ST16C(v, WS_ACT + (((rb * 8 + 2 * wm + (g & 1)) * 216 + 2 * gam + (g >> 1)) * 16 + i16) * 16);
        }
        STAMP(28);
        if (nj == 7) {
            BAR();
            const f32x4 other = ldsf4(L0 + L_XCH + (w ^ 4) * 1024 + lo16);
            const u32x2 o = wn == 0 ? silu_mul(acc[6][0], other) : silu_mul(other, acc[6][1]);
            ST8C(o, WS_ACT + (((rb * 8 + 2 * wm + wn) * 216 + 2 * (Gj + 3) + (g >> 1)) * 16 + i16) * 16 + 8 * (g & 1));
        }
        VMCNT(0);
        BAR();
        if (tid == 0) st_agent(ws + WS_FACT + (rb * 16 + j) * 8, tag);
    }
    STAMP(13);

    {
        if (w == 0) {
            const int u0 = 54 * cb;
            const char* a = ws + WS_FWD + ((u0 + (l < 54 ? l : 0)) & 255) * 8;
            u32 spins = 0;
            for (;;) {
                const u64 v = ld_agent(a);
                if (__all(v == tag)) break;
                if (++spins > SPIN_CAP) { if (l == 0) st_agent32(ws + WS_ERR, 12); break; }
                __builtin_amdgcn_s_sleep(1);
            }
        }
        BAR();
        const char* wsrc = ws + WS_WD + (cb * 54 + wk * 27) * 5120;
        const u32 r5w = L0 + L_R5 + wk * 20480 + wm * 5120;
#define P5_B1(t, i) glds16c(wsrc + min(4 * (t) + wm, 26) * 5120 + (i) * 1024, lo16, r5w + ((t) % 3) * R5_STAGE + (i) * 1024)
#define P5_B(t) do { P5_B1(t, 0); P5_B1(t, 1); P5_B1(t, 2); P5_B1(t, 3); P5_B1(t, 4); } while (0)
        P5_B(0); P5_B(1); P5_B(2);
        if (w == 1 && l < 20) {
            const u32x2 gv = poll_granule(ws, 800 + 20 * cb + l, tag, ws, 8);
            stsf4(L0 + L_GB + l * 16, (f32x4){bflo(gv[0]), bfhi(gv[0]), bflo(gv[1]), bfhi(gv[1])});
        }
        if (w == 0) poll_flags(ws + WS_FACT + rb * 128, 16, l, tag, ws, 9);
        VMCNT(0);
        BAR();
        STAMP(14);
        const char* asrc = ws + WS_ACT + (mt3 * 216 + wk * 108) * 256;
        u32x4 a[3][4];
#define P5_A1(t, i) a[(t) % 3][i] = gld16c<(i) * 1024>(asrc + (t) * 4096, lo16)
        P5_A1(0, 0); P5_A1(0, 1); P5_A1(0, 2); P5_A1(0, 3); P5_A1(1, 0); P5_A1(1, 1); P5_A1(1, 2); P5_A1(1, 3); P5_A1(2, 0); P5_A1(2, 1); P5_A1(2, 2); P5_A1(2, 3);
        f32x4 acc[5];
#pragma unroll
        for (int n = 0; n < 5; ++n) acc[n] = Z4;
        const u32 rb5 = L0 + L_R5 + wk * 20480 + lo16;
        u32x4 fb[2][5];
#define P5_RD(buf, t, kk) do { _Pragma("unroll") for (int n = 0; n < 5; ++n) fb[buf][n] = lds128(rb5 + ((t) % 3) * R5_STAGE + (kk) * 5120 + n * 1024); } while (0)
#define P5_MM(buf, av) do { _Pragma("unroll") for (int n = 0; n < 5; ++n) acc[n] = mfma(fb[buf][n], (av), acc[n]); } while (0)
#define P5_MIX() do { _Pragma("unroll") for (int n = 0; n < 5; ++n) { SGB(M_DSR, 1); SGB(M_MFMA, 1); } } while (0)
        P5_RD(0, 0, 0);
        static_for<0, 7>([&](auto T) {
            constexpr int t = T, q = t % 3;
            constexpr bool nx = t + 3 < 7;
            constexpr int NA = t == 0 ? 8 : t == 1 ? 13 : t == 2 ? 18 : t == 3 ? 21 : t == 4 ? 20 : t == 5 ? 11 : 5;
            constexpr int NB = t == 2 ? 12 : t == 3 ? 12 : t == 4 ? 8 : t == 5 ? 0 : 63;
            vmw4<NA>(a[q][0], a[q][1], a[q][2], a[q][3]);
            SB0(); P5_RD(1, t, 1); P5_MM(0, a[q][0]); P5_MIX(); SB0();
            if constexpr (nx) P5_A1(t + 3, 0);
            SB0(); P5_RD(0, t, 2); P5_MM(1, a[q][1]); P5_MIX(); SB0();
            if constexpr (nx) P5_A1(t + 3, 1);
            if constexpr (t < 6) {
                SB0(); P5_RD(1, t, 3); P5_MM(0, a[q][2]); P5_MIX(); SB0();
                if constexpr (nx) P5_A1(t + 3, 2);
                vmw<NB>(); BAR();
                if constexpr (nx) { P5_B1(t + 3, 0); P5_B1(t + 3, 1); }
                SB0(); P5_RD(0, t + 1, 0); P5_MM(1, a[q][3]); P5_MIX(); SB0();
                if constexpr (nx) { if constexpr (t + 3 < 6) P5_A1(t + 3, 3); P5_B1(t + 3, 2); P5_B1(t + 3, 3); P5_B1(t + 3, 4); }
            } else {
                SB0(); P5_MM(0, a[q][2]); SB0();
            }
        });
#undef P5_B
#undef P5_B1
#undef P5_A1
#undef P5_RD
#undef P5_MM
#undef P5_MIX
        STAMP(15);
        if (wk == 0) { stsf4(L0 + L_RED5 + (wm * 5 + 3) * 1024 + lo16, acc[3]); stsf4(L0 + L_RED5 + (wm * 5 + 4) * 1024 + lo16, acc[4]); }
        else { stsf4(L0 + L_RED5 + (wm * 5 + 0) * 1024 + lo16, acc[0]); stsf4(L0 + L_RED5 + (wm * 5 + 1) * 1024 + lo16, acc[1]); stsf4(L0 + L_RED5 + (wm * 5 + 2) * 1024 + lo16, acc[2]); }
        BAR();
#pragma unroll
        for (int n = 0; n < 3; ++n) {
            if (n < ntn) {
                const int nt = nt0 + n;
                const f32x4 mine = wk == 0 ? acc[n] : acc[n < 2 ? 3 + n : 4];
                const f32x4 v = mine + ldsf4(L0 + L_RED5 + (wm * 5 + nt) * 1024 + lo16);
                const f32x4 gb = ldsf4(L0 + L_GB + (16 * nt + 4 * g) * 4);
                const u32x2 o = {pack2(bflo(x1k[n][0]) + gb[0] * v[0], bfhi(x1k[n][0]) + gb[1] * v[1]), pack2(bflo(x1k[n][1]) + gb[2] * v[2], bfhi(x1k[n][1]) + gb[3] * v[3])};
                sts64(L0 + L_OS + (16 * wm + i16) * 160 + 32 * nt + 8 * g, o);
            }
        }
        BAR();
#pragma unroll
        for (int i = 0; i < 2; ++i) {
            const int id = tid + 512 * i;
            if (id < 640) {
                const int r = (id * 6554) >> 16, c = id - 10 * r;
                *(GAS u32x4*)((char*)p.out + (rb * 128 + 64 * hf + r) * 1280 + cb * 160 + c * 16) = lds128(L0 + L_OS + id * 16);
            }
        }
    }
    STAMP(16);
    if (rb == 15 && j == 0 && w < 4) {
        u32 spins = 0;
        for (;;) {
            const u64 v = ld_agent(ws + WS_FO + tid * 8);
            if (__all(v == tag)) break;
            if (++spins > SPIN_CAP) { st_agent32(ws + WS_ERR, 10); break; }
            __builtin_amdgcn_s_sleep(1);
        }
    }
    if (rb == 15 && j == 0) {
        BAR();
        if (tid == 0) st_agent(ws + WS_EPOCH, tag);
    }
}

extern "C" void kernel_launch(void* const* d_in, const int* in_sizes, int n_in, void* d_out, int out_size, void* d_ws, size_t ws_size, hipStream_t stream) {
    Params p;
    p.x = (const u16*)d_in[0]; p.wqkv = (const u16*)d_in[1]; p.wo = (const u16*)d_in[2];
    p.wg = (const u16*)d_in[4]; p.wu = (const u16*)d_in[5]; p.wd = (const u16*)d_in[6];
    p.c = (const u16*)d_in[7]; p.wmod = (const u16*)d_in[8];
    p.out = (u16*)d_out; p.ws = (char*)d_ws;
    mega_kernel<<<256, 512, 0, stream>>>(p);
}
```

```cpp
#include <hip/hip_runtime.h>
#include <hip/hip_bf16.h>
#include <stdint.h>
#include <type_traits>

typedef unsigned short u16;
typedef unsigned u32;
typedef unsigned long long u64;
typedef __attribute__((ext_vector_type(2))) unsigned u32x2;
typedef __attribute__((ext_vector_type(4))) unsigned u32x4;
typedef __attribute__((ext_vector_type(2))) float f32x2;
typedef __attribute__((ext_vector_type(4))) float f32x4;
typedef __attribute__((ext_vector_type(2))) __bf16 bf16x2;
typedef __attribute__((ext_vector_type(8))) __bf16 bf16x8;
typedef __attribute__((ext_vector_type(4))) short s16x4;
#define LAS __attribute__((address_space(3)))
#define GAS __attribute__((address_space(1)))
#define DI __device__ __forceinline__

constexpr int SEQ = 2048, DM = 640, FF = 1728;
constexpr float QSCALE = 0.22360679774997896f * 1.4426950408889634f;
constexpr float LOG2E = 1.4426950408889634f;

constexpr u32 WS_EPOCH = 0;
constexpr u32 WS_ERR   = 256;
constexpr u32 WS_FKV   = 4096;
constexpr u32 WS_FO    = WS_FKV + 2048;
constexpr u32 WS_FX1   = WS_FO + 2048;
constexpr u32 WS_FACT  = WS_FX1 + 2048;
constexpr u32 WS_FWP   = WS_FACT + 2048;
constexpr u32 WS_FWT   = WS_FWP + 2048;
constexpr u32 WS_MODG  = 16384;
constexpr u32 WS_UV    = 32768;
constexpr u32 WS_FH    = 61440;
constexpr u32 WS_FWD   = 63488;
constexpr u32 WS_STAT  = 65536;
constexpr u32 WS_KV    = 262144;
constexpr u32 WS_O     = WS_KV + 256 * 22528;
constexpr u32 WS_X1    = WS_O + SEQ * DM * 2;
constexpr u32 WS_ACT   = WS_X1 + SEQ * DM * 2;
constexpr u32 WS_WP    = WS_ACT + SEQ * FF * 2;
constexpr u32 WS_H     = WS_WP + 16 * 20 * 14 * 1024;
constexpr u32 WS_WD    = WS_H + SEQ * DM * 2;
constexpr u32 WS_WT    = WS_WD + 8 * 54 * 5 * 1024;
constexpr u32 WS_STAMP = WS_WT + 16 * 153600;
constexpr u32 WS_END   = WS_STAMP + 2 * 256 * 32 * 8;

constexpr u32 L_WKV = 0, L_WQ = 102400, L_GS = 153600, L_STP = 156160, L_MODP = 156416, L_CNT = 156672, L_SB = 157696, L_G2 = 158976, L_WTRAW = 143360, L_XS = 133120;
constexpr u32 L_K = 0, L_V = 30720, L_Q = 67584, L_WPRAW = 77824, L_UVP = 100864;
constexpr u32 L_WO0 = 0, L_WO1 = 112640, L_RED3 = 51200, L_X1S = 71680, L_XR = 81920, L_WDRAW = 92160, L_GA = 102400, L_ST3 = 102912;
constexpr u32 L_R4 = 0, R4_STAGE = 45056, L_ST4 = 135168, L_UV = 143360, L_MR = 145408, L_XCH = 146432;
constexpr u32 L_R5 = 0, R5_STAGE = 40960, L_RED5 = 122880, L_GB = 143360, L_OS = 144384;

struct Params {
    const u16* x; const u16* wqkv; const u16* wo; const u16* wg; const u16* wu; const u16* wd;
    const u16* c; const u16* wmod; u16* out; char* ws;
};

DI float bflo(u32 d) { return __uint_as_float(d << 16); }
DI float bfhi(u32 d) { return __uint_as_float(d & 0xffff0000u); }
DI u32 pack2(float a, float b) { f32x2 v = {a, b}; return __builtin_bit_cast(u32, __builtin_convertvector(v, bf16x2)); }
DI float rbf(float a) { return bflo(pack2(a, a)); }
DI f32x4 mfma(u32x4 a, u32x4 b, f32x4 c) {
    return __builtin_amdgcn_mfma_f32_16x16x32_bf16(__builtin_bit_cast(bf16x8, a), __builtin_bit_cast(bf16x8, b), c, 0, 0, 0);
}
DI u32x2 ldtr(u32 a) { return __builtin_bit_cast(u32x2, __builtin_amdgcn_ds_read_tr16_b64_v4i16((LAS s16x4*)(size_t)a)); }
DI u32x4 ldtr2(u32 a0, u32 a1) { u32x2 x = ldtr(a0), y = ldtr(a1); return (u32x4){x[0], x[1], y[0], y[1]}; }
DI u32x4 lds128(u32 a) { return *(LAS u32x4*)(size_t)a; }
DI u32x2 lds64(u32 a) { return *(LAS u32x2*)(size_t)a; }
DI f32x4 ldsf4(u32 a) { return *(LAS f32x4*)(size_t)a; }
DI float ldsf(u32 a) { return *(LAS float*)(size_t)a; }
DI void sts128(u32 a, u32x4 v) { *(LAS u32x4*)(size_t)a = v; }
DI void sts64(u32 a, u32x2 v) { *(LAS u32x2*)(size_t)a = v; }
DI void stsf(u32 a, float v) { *(LAS float*)(size_t)a = v; }
DI void stsf4(u32 a, f32x4 v) { *(LAS f32x4*)(size_t)a = v; }
DI void stsf2(u32 a, f32x2 v) { *(LAS f32x2*)(size_t)a = v; }
DI void sts16(u32 a, u16 v) { *(LAS u16*)(size_t)a = v; }

DI void glds16(const void* sbase, u32 voff, u32 ldsdst) {
    asm volatile("s_mov_b32 m0, %2\n\ts_nop 3\n\tglobal_load_lds_dwordx4 %0, %1" :: "v"(voff), "s"(sbase), "s"(ldsdst) : "memory");
}
DI void glds16c(const void* sbase, u32 voff, u32 ldsdst) {
    asm volatile("s_mov_b32 m0, %2\n\ts_nop 3\n\tglobal_load_lds_dwordx4 %0, %1 sc1" :: "v"(voff), "s"(sbase), "s"(ldsdst) : "memory");
}
template <int OFF> DI u32x4 gld16(const void* sbase, u32 voff) {
    u32x4 r; asm volatile("s_nop 4\n\tglobal_load_dwordx4 %0, %1, %2 offset:%3" : "=v"(r) : "v"(voff), "s"(sbase), "i"(OFF) : "memory"); return r;
}
template <int OFF> DI u32x4 gld16c(const void* sbase, u32 voff) {
    u32x4 r; asm volatile("s_nop 4\n\tglobal_load_dwordx4 %0, %1, %2 offset:%3 sc1" : "=v"(r) : "v"(voff), "s"(sbase), "i"(OFF) : "memory"); return r;
}
DI u32 gldu16(const void* sbase, u32 voff) {
    u32 r; asm volatile("s_nop 4\n\tglobal_load_ushort %0, %1, %2" : "=v"(r) : "v"(voff), "s"(sbase) : "memory"); return r;
}
#define VMCNT(n) asm volatile("s_waitcnt vmcnt(" #n ")" ::: "memory")
#define BAR() asm volatile("s_waitcnt lgkmcnt(0)\n\ts_barrier" ::: "memory")
#define RLX_AGENT __ATOMIC_RELAXED, __HIP_MEMORY_SCOPE_AGENT
DI u64 ld_agent(const char* p) { return __hip_atomic_load((GAS u64*)p, RLX_AGENT); }
DI void st_agent(char* p, u64 v) { __hip_atomic_store((GAS u64*)p, v, RLX_AGENT); }
DI void st_agent32(char* p, u32 v) { __hip_atomic_store((GAS u32*)p, v, RLX_AGENT); }
DI float ld_agentf(const char* p) { return __uint_as_float(__hip_atomic_load((GAS u32*)p, RLX_AGENT)); }
DI float xsum4(float x) {
    const auto s = __builtin_amdgcn_permlane16_swap(__float_as_uint(x), __float_as_uint(x), false, false);
    x = __uint_as_float(s[0]) + __uint_as_float(s[1]);
    const auto t = __builtin_amdgcn_permlane32_swap(__float_as_uint(x), __float_as_uint(x), false, false);
    return __uint_as_float(t[0]) + __uint_as_float(t[1]);
}
DI float xmax4(float x) {
    const auto s = __builtin_amdgcn_permlane16_swap(__float_as_uint(x), __float_as_uint(x), false, false);
    x = fmaxf(__uint_as_float(s[0]), __uint_as_float(s[1]));
    const auto t = __builtin_amdgcn_permlane32_swap(__float_as_uint(x), __float_as_uint(x), false, false);
    return fmaxf(__uint_as_float(t[0]), __uint_as_float(t[1]));
}
template <int CTRL> DI float dppf(float v) { return __builtin_bit_cast(float, __builtin_amdgcn_update_dpp(0, __builtin_bit_cast(int, v), CTRL, 0xf, 0xf, true)); }

constexpr u32 SPIN_CAP = 1u << 17;
DI void poll_flags(const char* base, int n, int lane, u64 tag, char* ws, u32 code) {
    const char* a = base + (lane < n ? lane : 0) * 8;
    u32 spins = 0;
    for (;;) {
        const u64 v = ld_agent(a);
        if (__all(v == tag)) break;
        if (++spins > SPIN_CAP) { if (lane == 0) st_agent32(ws + WS_ERR, code); break; }
        __builtin_amdgcn_s_sleep(1);
    }
}
DI u32x2 poll_granule(const char* ws, u32 gid, u64 tag, char* wsw, u32 code) {
    u32 spins = 0; u32x4 v;
    const u32 off = WS_MODG + gid * 16;
    for (;;) {
        asm volatile("s_nop 4\n\tglobal_load_dwordx4 %0, %1, %2 sc1\n\ts_waitcnt vmcnt(0)" : "=&v"(v) : "v"(off), "s"(ws) : "memory");
        const bool ok = v[2] == (u32)tag && v[3] == (u32)(tag >> 32);
        if (__all(ok)) break;
        if (++spins > SPIN_CAP) { st_agent32(wsw + WS_ERR, code); break; }
        __builtin_amdgcn_s_sleep(1);
    }
    return (u32x2){v[0], v[1]};
}

DI void poll_granule2(const char* ws, u32 g0, u32 g1, u64 tag, char* wsw, u32 code, u32x2& a, u32x2& c) {
    u32 spins = 0; u32x4 v0, v1;
    const u32 o0 = WS_MODG + g0 * 16, o1 = WS_MODG + g1 * 16;
    for (;;) {
        asm volatile("s_nop 4\n\tglobal_load_dwordx4 %0, %2, %4 sc1\n\tglobal_load_dwordx4 %1, %3, %4 sc1\n\ts_waitcnt vmcnt(0)" : "=&v"(v0), "=&v"(v1) : "v"(o0), "v"(o1), "s"(ws) : "memory");
        const bool ok = v0[2] == (u32)tag && v0[3] == (u32)(tag >> 32) && v1[2] == (u32)tag && v1[3] == (u32)(tag >> 32);
        if (__all(ok)) break;
        if (++spins > SPIN_CAP) { st_agent32(wsw + WS_ERR, code); break; }
        __builtin_amdgcn_s_sleep(1);
    }
    a = (u32x2){v0[0], v0[1]}; c = (u32x2){v1[0], v1[1]};
}

#define STAMPL(i) do {} while (0)
#define STAMP(i) do {} while (0)

template <int N> using IC = std::integral_constant<int, N>;
template <int I, int E, class F> DI void static_for(F&& f) { if constexpr (I < E) { f(IC<I>{}); static_for<I + 1, E>(f); } }
template <int N> DI void vmw() { asm volatile("s_waitcnt vmcnt(%0)" :: "i"(N) : "memory"); }
template <int N> DI void vmw1(u32x4& a) { asm volatile("s_waitcnt vmcnt(%1)" : "+v"(a) : "i"(N) : "memory"); }
template <int N> DI void vmw4(u32x4& a, u32x4& b, u32x4& c, u32x4& d) { asm volatile("s_waitcnt vmcnt(%4)" : "+v"(a), "+v"(b), "+v"(c), "+v"(d) : "i"(N) : "memory"); }
DI float dot2(u32 a, u32 b, float c) { return __builtin_amdgcn_fdot2_f32_bf16(__builtin_bit_cast(bf16x2, a), __builtin_bit_cast(bf16x2, b), c, false); }
#define SB0() __builtin_amdgcn_sched_barrier(0)
#define SGB(m, n) __builtin_amdgcn_sched_group_barrier((m), (n), 0)
constexpr int M_MFMA = 0x8, M_DSR = 0x100;
#define Z4 ((f32x4){0.f, 0.f, 0.f, 0.f})
DI u32 lds_add(u32 a, u32 v) { return __hip_atomic_fetch_add((LAS u32*)(size_t)a, v, __ATOMIC_RELAXED, __HIP_MEMORY_SCOPE_WORKGROUP); }
DI u32 lds_ld(u32 a) { return __hip_atomic_load((LAS u32*)(size_t)a, __ATOMIC_RELAXED, __HIP_MEMORY_SCOPE_WORKGROUP); }
DI void worker_sync(u32 cnt, int lane) {
    asm volatile("s_waitcnt lgkmcnt(0)" ::: "memory");
    if (lane == 0) lds_add(cnt, 1u);
    for (u32 spins = 0; lds_ld(cnt) < 4u && spins < (1u << 22); ++spins) {}
    asm volatile("" ::: "memory");
}

__global__ __launch_bounds__(512, 2) void mega_kernel(Params p) {
    __shared__ __attribute__((aligned(16))) char ldsmem[163840];
    const u32 L0 = (u32)(uintptr_t)(LAS char*)ldsmem;
    const int tid = threadIdx.x, l = tid & 63;
    const int w = __builtin_amdgcn_readfirstlane(tid >> 6);
    const int b = blockIdx.x, xg = b & 7, sidx = b >> 3;
    const int rb = 4 * (xg >> 1) + (sidx >> 3), j = 8 * (xg & 1) + (sidx & 7);
    const int g = l >> 4, i16 = l & 15, q4 = i16 >> 2, p4 = l & 3;
    char* const ws = p.ws;
    asm volatile("" :: "s"(p.x), "s"(p.wqkv), "s"(p.wo), "s"(p.wg), "s"(p.wu), "s"(p.wd), "s"(p.c), "s"(p.wmod), "s"(p.out), "s"(p.ws));
    u64 tag;
#define ASK_TAG(ep) u32x2 ep; asm volatile("global_load_dwordx2 %0, %1, %2" : "=v"(ep) : "v"(WS_EPOCH), "s"(ws) : "memory")
#define TAKE_TAG(ep) do { asm volatile("s_waitcnt vmcnt(0)" : "+v"(ep) :: "memory"); \
        tag = (((u64)(u32)__builtin_amdgcn_readfirstlane(ep[1]) << 32) | (u32)__builtin_amdgcn_readfirstlane(ep[0])) + 1; } while (0)
    const __amdgpu_buffer_rsrc_t wsr = __builtin_amdgcn_make_buffer_rsrc(ws, 0, WS_END, 0x00020000);
#define ST16C(v, off) __builtin_amdgcn_raw_buffer_store_b128((v), wsr, (off), 0, 16)
#define ST8C(v, off)  __builtin_amdgcn_raw_buffer_store_b64((v), wsr, (off), 0, 16)
    STAMP(0);

    const int nj = j < 12 ? 7 : 6, Gj = j < 12 ? 7 * j : 84 + 6 * (j - 12);
    const bool wpwg = rb < 2 * nj;
    const int wslot = (nj == 7 || rb < 6) ? rb : rb + 1;
    if (w >= 4) {
        const int u = w - 4, nt = rb >> 1, s0 = 10 * (rb & 1);
        {
            const int c = min(2 * nt + (l & 1), 14);
            const u32 voff = (l >> 1) * 3840 + 80 * j + (c < 5 ? 1280 + 16 * c : c < 10 ? 2560 + 16 * (c - 5) : 16 * (c - 10));
            const char* src = (const char*)p.wqkv + (s0 + u) * 122880;
            glds16(src, voff, L0 + L_WTRAW + u * 1024);
            glds16(src + 4 * 122880, voff, L0 + L_WTRAW + (u + 4) * 1024);
            if (u < 2) glds16(src + 8 * 122880, voff, L0 + L_WTRAW + (u + 8) * 1024);
            ASK_TAG(epl);
            BAR();
            TAKE_TAG(epl);
            const u32 a = L0 + L_WTRAW + u * 1024 + (8 * g + q4) * 32 + 8 * p4;
            const u32 dst = WS_WT + j * 153600 + (nt < 5 ? (s0 + u) * 5120 + nt * 1024 : 102400 + (s0 + u) * 2560 + (nt - 5) * 1024) + (nt < 7 ? l * 16 : g * 128 + i16 * 16);
            const u32 st = nt < 5 ? 4 * 5120 : 4 * 2560;
            const u32x4 f0 = ldtr2(a, a + 128), f1 = ldtr2(a + 4096, a + 4096 + 128), f2 = ldtr2(a + (u < 2 ? 8192 : 0), a + (u < 2 ? 8192 : 0) + 128);
            if (nt < 7 || i16 < 8) {
                ST16C(f0, dst); ST16C(f1, dst + st);
                if (u < 2) ST16C(f2, dst + 2 * st);
            }
            VMCNT(0);
            asm volatile("s_waitcnt lgkmcnt(0)" ::: "memory");
            u32 old = 0;
            if (l == 0) old = lds_add(L0 + L_CNT + 12, 1u);
            if (__builtin_amdgcn_readfirstlane(old) == 7 && l == 0) st_agent(ws + WS_FWT + (j * 16 + rb) * 8, tag);
        }
        STAMPL(19);
        poll_flags(ws + WS_FWT + j * 128, 16, l, tag, ws, 13);
        STAMPL(20);
        {
            const char* src = ws + WS_WT + j * 153600 + u * 1024;
            const u32 lo = l * 16;
#pragma unroll
            for (int i = 0; i < 38; ++i)
                if (i < 37 || u < 2) glds16c(src + i * 4096, lo, L0 + L_WKV + u * 1024 + i * 4096);
        }
        if (wpwg) {
            u32x2 ga, gb;
            const int t = tid - 256;
            poll_granule2(ws, 480 + t, 480 + 256 + (t & 63), tag, ws, 2, ga, gb);
            if (t < 160) sts64(L0 + L_SB + t * 8, ga);
            else sts64(L0 + L_G2 + (t - 160) * 8, (u32x2){pack2(1.f + bflo(ga[0]), 1.f + bfhi(ga[0])), pack2(1.f + bflo(ga[1]), 1.f + bfhi(ga[1]))});
            if (t < 64) sts64(L0 + L_G2 + (96 + t) * 8, (u32x2){pack2(1.f + bflo(gb[0]), 1.f + bfhi(gb[0])), pack2(1.f + bflo(gb[1]), 1.f + bfhi(gb[1]))});
        }
        VMCNT(0);
        STAMPL(17);
    } else {
        __builtin_amdgcn_s_setprio(3);
        const bool modwg = sidx < 30;
        const int sl = 30 * xg + sidx;
        u32x4 mw[5]; u32 mc[5];
        if (modwg) {
#pragma unroll
            for (int i = 0; i < 5; ++i) {
                const int r = (tid >> 1) + 128 * i;
                mw[i] = gld16<0>(p.wmod, r * 7680 + sl * 32 + (tid & 1) * 16);
                mc[i] = gldu16(p.c, r * 2);
            }
        }
        {
            const char* src = (const char*)p.x + (rb * 128 + 8 * j) * 1280 + w * 1024;
            glds16(src, l * 16, L0 + L_XS + w * 1024);
            glds16(src + 4096, l * 16, L0 + L_XS + (w + 4) * 1024);
            if (w < 2) glds16(src + 8192, l * 16, L0 + L_XS + (w + 8) * 1024);
        }
        ASK_TAG(epw);
        if (tid < 8) *(LAS u32*)(size_t)(L0 + L_CNT + tid * 4) = 0u;
        BAR();
        STAMP(29);
        TAKE_TAG(epw);
        STAMP(30);
        if (modwg) {
            asm volatile("" : "+v"(mw[0]), "+v"(mw[1]), "+v"(mw[2]), "+v"(mw[3]), "+v"(mw[4]), "+v"(mc[0]), "+v"(mc[1]), "+v"(mc[2]), "+v"(mc[3]), "+v"(mc[4]) :: "memory");
            float acc[8];
#pragma unroll
            for (int e = 0; e < 8; ++e) acc[e] = 0.f;
#pragma unroll
            for (int i = 0; i < 5; ++i) {
                const float cv = bflo(mc[i]);
                const float sv = cv * __builtin_amdgcn_rcpf(1.f + __builtin_amdgcn_exp2f(-cv * LOG2E));
                const u32 slo = pack2(sv, 0.f), shi = slo << 16;
#pragma unroll
                for (int d = 0; d < 4; ++d) { acc[2 * d] = dot2(mw[i][d], slo, acc[2 * d]); acc[2 * d + 1] = dot2(mw[i][d], shi, acc[2 * d + 1]); }
            }
#pragma unroll
            for (int e = 0; e < 8; ++e) {
                float v = acc[e];
                v += dppf<0x122>(v); v += dppf<0x124>(v); v += dppf<0x128>(v);
                acc[e] = xsum4(v);
            }
            if (l < 2) {
                stsf4(L0 + L_MODP + w * 64 + l * 32, (f32x4){acc[0], acc[1], acc[2], acc[3]});
                stsf4(L0 + L_MODP + w * 64 + l * 32 + 16, (f32x4){acc[4], acc[5], acc[6], acc[7]});
            }
            asm volatile("s_waitcnt lgkmcnt(0)" ::: "memory");
            u32 old = 0;
            if (l == 0) old = lds_add(L0 + L_CNT, 1u);
            if (__builtin_amdgcn_readfirstlane(old) == 3) {
                float v = 0.f;
#pragma unroll
                for (int ww = 0; ww < 4; ++ww) v += ldsf(L0 + L_MODP + ww * 64 + i16 * 4);
                const float v1 = dppf<0x55>(v), v2 = dppf<0xAA>(v), v3 = dppf<0xFF>(v), v0 = dppf<0x00>(v);
                const u32x4 gr = {pack2(v0, v1), pack2(v2, v3), (u32)tag, (u32)(tag >> 32)};
                if (l < 16 && p4 == 0) ST16C(gr, WS_MODG + (sl * 4 + q4) * 16);
            }
        }
        STAMP(1);
        worker_sync(L0 + L_CNT + 16, l);
        const int tok = l & 7;
        u32x4 xr[3];
#pragma unroll
        for (int i = 0; i < 3; ++i) xr[i] = lds128(L0 + L_XS + tok * 1280 + (20 * w + (i < 2 ? (l >> 3) + 8 * i : 16 + ((l >> 3) & 3))) * 16);
        {
            float s1 = 0.f, s2 = 0.f;
#pragma unroll
            for (int i = 0; i < 3; ++i) {
                float a1 = 0.f, a2 = 0.f;
#pragma unroll
                for (int d = 0; d < 4; ++d) { a1 = dot2(xr[i][d], 0x3F803F80u, a1); a2 = dot2(xr[i][d], xr[i][d], a2); }
                if (i < 2 || l < 32) { s1 += a1; s2 += a2; }
            }
            {
                asm volatile("s_waitcnt lgkmcnt(0)" ::: "memory");
                u32 old = 0;
                if (l == 0) old = lds_add(L0 + L_CNT + 12, 1u);
                if (__builtin_amdgcn_readfirstlane(old) == 7 && l == 0) st_agent(ws + WS_FWT + (j * 16 + rb) * 8, tag);
            }
            s1 += dppf<0x128>(s1); s2 += dppf<0x128>(s2);
            s1 = xsum4(s1); s2 = xsum4(s2);
            if (l < 8) stsf2(L0 + L_STP + (w * 8 + l) * 8, (f32x2){s1, s2});
        }
        {
            const u32 gs = L0 + L_GS + w * 640;
            u32x2 a, c;
            poll_granule2(ws, (l < 40 ? 40 * w + l : 160 + 40 * w + (l - 40)), 160 + 40 * w + 24 + (l & 15), tag, ws, 1, a, c);
            if (l < 40) sts64(gs + l * 8, a);
            else sts64(gs + 320 + (l - 40) * 8, (u32x2){pack2(1.f + bflo(a[0]), 1.f + bfhi(a[0])), pack2(1.f + bflo(a[1]), 1.f + bfhi(a[1]))});
            if (l < 16) sts64(gs + 320 + (24 + l) * 8, (u32x2){pack2(1.f + bflo(c[0]), 1.f + bfhi(c[0])), pack2(1.f + bflo(c[1]), 1.f + bfhi(c[1]))});
        }
        STAMP(2);
        worker_sync(L0 + L_CNT + 4, l);
        {
            f32x2 st = {0.f, 0.f};
#pragma unroll
            for (int ww = 0; ww < 4; ++ww) st += *(LAS f32x2*)(size_t)(L0 + L_STP + (ww * 8 + tok) * 8);
            const float mu = st[0] * (1.f / 640.f), var = fmaxf(st[1] * (1.f / 640.f) - mu * mu, 0.f);
            const float rs = __builtin_amdgcn_rsqf(var + 1e-5f), nmr = -mu * rs;
#pragma unroll
            for (int i = 0; i < 3; ++i) {
                const int cl = i < 2 ? (l >> 3) + 8 * i : 16 + ((l >> 3) & 3);
                const u32x4 sa = lds128(L0 + L_GS + w * 640 + cl * 16), gg = lds128(L0 + L_GS + w * 640 + 320 + cl * 16);
                u32x4 h;
#pragma unroll
                for (int d = 0; d < 4; ++d)
                    h[d] = pack2((bflo(xr[i][d]) * rs + nmr) * bflo(gg[d]) + bflo(sa[d]), (bfhi(xr[i][d]) * rs + nmr) * bfhi(gg[d]) + bfhi(sa[d]));
                if (i < 2 || l < 32) ST16C(h, WS_H + (((rb * 8 + (j >> 1)) * 80 + 20 * w + cl) * 16 + 8 * (j & 1) + tok) * 16);
            }
        }
        VMCNT(0);
        worker_sync(L0 + L_CNT + 8, l);
        if (tid == 0) st_agent(ws + WS_FH + (rb * 16 + j) * 8, tag);
        if (w == 0) poll_flags(ws + WS_FH + rb * 128, 16, l, tag, ws, 11);
        __builtin_amdgcn_s_setprio(0);
    }
    BAR();
    STAMP(3);

    u32x4 xa[20];
    const u32 lo16 = l * 16;
    const char* hsrc = ws + WS_H + (rb * 8 + w) * 80 * 256;
#define XL(s) xa[s] = gld16c<((s) & 3) * 1024>(hsrc + ((s) >> 2) * 4096, lo16)
    XL(0); XL(1); XL(2); XL(3); XL(4); XL(5);
    const char* kvx = ws + WS_KV + (rb * 16 + j) * 22528;
    {
        f32x4 acc[5];
#pragma unroll
        for (int n = 0; n < 5; ++n) acc[n] = Z4;
        const u32 b0 = L0 + L_WKV + lo16;
        u32x4 bf[2][5];
#pragma unroll
        for (int n = 0; n < 5; ++n) bf[0][n] = lds128(b0 + n * 1024);
        static_for<0, 20>([&](auto S) {
            constexpr int s = S;
            vmw1<(s < 15 ? 5 : 19 - s)>(xa[s]);
            if constexpr (s + 6 < 20) XL(s + 6);
            SB0();
#pragma unroll
            for (int n = 0; n < 5; ++n) {
                if (s < 19) bf[(s + 1) & 1][n] = lds128(b0 + (s + 1) * 5120 + n * 1024);
                acc[n] = mfma(bf[s & 1][n], xa[s], acc[n]);
            }
#pragma unroll
            for (int n = 0; n < 5; ++n) { SGB(M_DSR, 1); SGB(M_MFMA, 1); }
            SB0();
        });
        STAMP(18);
        BAR();
        const int row = 256 + 16 * w + i16;
#pragma unroll
        for (int n = 0; n < 5; ++n) {
            const int Q = 4 * n + g;
            const u32x2 v = {pack2(acc[n][0], acc[n][1]), pack2(acc[n][2], acc[n][3])};
            const int wi = Q - 10, hd = wi >= 5 ? 1 : 0;
            const u32 a = Q < 10 ? L_K + row * 80 + 8 * Q : L_V + row * 96 + 48 * hd + 8 * (wi - 5 * hd);
            sts64(L0 + a, v);
        }
        if (g < 2) sts64(L0 + L_V + row * 96 + 48 * g + 40, (u32x2){0x3F80u, 0u});
        if (rb < 2) {
            const int nrow = rb == 0 ? 256 : 128;
            for (int i = tid; i < nrow * 5; i += 512) sts128(L0 + L_K + i * 16, (u32x4){0, 0, 0, 0});
            for (int i = tid; i < nrow * 6; i += 512) sts128(L0 + L_V + i * 16, (u32x4){0, 0, 0, 0});
        }
        BAR();
    }
#undef XL
    STAMP(4);
    {
        u32x4 kvd[3];
#pragma unroll
        for (int e = 0; e < 3; ++e) {
            const int n = min(w + 8 * e, 21);
            kvd[e] = lds128(L0 + (n < 10 ? L_K + 20480 + n * 1024 : L_V + 24576 + (n - 10) * 1024) + lo16);
        }
        const u32 kvo = WS_KV + (rb * 16 + j) * 22528 + w * 1024 + lo16;
        const int wa = wslot < 6 ? wslot >> 1 : wslot == 6 ? 3 : wslot < 13 ? 4 + ((wslot - 7) >> 1) : 3;
        const int wup = wslot < 6 ? wslot & 1 : wslot == 6 ? 0 : wslot < 13 ? (wslot - 7) & 1 : 1;
        const char* wsrc = (const char*)(wup ? p.wu : p.wg) + (Gj + (wa <= 3 || nj == 7 ? wa : wa - 1)) * 32 + w * 32 * 3456;
        const u32 wvo = (l >> 1) * 3456 + (l & 1) * 16;
        f32x4 acc[3];
#pragma unroll
        for (int n = 0; n < 3; ++n) acc[n] = Z4;
        const u32 bq = L0 + L_WQ + lo16, bq2 = L0 + L_WQ + 2048 + g * 128 + (i16 & 7) * 16;
#define QRD(s, n) lds128(((n) < 2 ? bq + (n) * 1024 : bq2) + (s) * 2560)
        u32x4 bf[3][3];
#pragma unroll
        for (int n = 0; n < 3; ++n) { bf[0][n] = QRD(0, n); bf[1][n] = QRD(1, n); }
        static_for<0, 20>([&](auto S) {
            constexpr int s = S;
            if constexpr (s == 0) { if (rb < 15) ST16C(kvd[0], kvo); }
            if constexpr (s == 2) { if (rb < 15) ST16C(kvd[1], kvo + 8192); }
            if constexpr (s == 4) { if (rb < 15 && w < 6) ST16C(kvd[2], kvo + 16384); }
            if constexpr (s == 8) { if (wpwg) glds16(wsrc, wvo, L0 + L_WPRAW + w * 1024); }
            if constexpr (s == 11) { if (wpwg) glds16(wsrc + 8 * 32 * 3456, wvo, L0 + L_WPRAW + (w + 8) * 1024); }
            if constexpr (s == 14) { if (wpwg && w < 4) glds16(wsrc + 16 * 32 * 3456, wvo, L0 + L_WPRAW + (w + 16) * 1024); }
            SB0();
#pragma unroll
            for (int n = 0; n < 3; ++n) {
                if (s < 18) bf[(s + 2) % 3][n] = QRD(s + 2, n);
                acc[n] = mfma(bf[s % 3][n], xa[s], acc[n]);
            }
#pragma unroll
            for (int n = 0; n < 3; ++n) { SGB(M_DSR, 1); SGB(M_MFMA, 1); }
            SB0();
        });
#undef QRD
        STAMP(21);
#pragma unroll
        for (int n = 0; n < 3; ++n) {
            const int Q = 4 * n + g;
            const u32x2 v = {pack2(acc[n][0] * QSCALE, acc[n][1] * QSCALE), pack2(acc[n][2] * QSCALE, acc[n][3] * QSCALE)};
            if (Q < 10) sts64(L0 + L_Q + (16 * w + i16) * 80 + 8 * Q, v);
        }
    }
    VMCNT(0);
    BAR();
    if (tid == 0) st_agent(ws + WS_FKV + (j * 16 + rb) * 8, tag);
    STAMP(5);
    if (wpwg) {
        float us = 0.f, vs = 0.f;
#pragma unroll
        for (int i = 0; i < 3; ++i) {
            if (i < 2 || w < 4) {
                const int s = w + 8 * i;
                const u32 a = L0 + L_WPRAW + (32 * s + 8 * g + q4) * 32 + 8 * p4;
                const u32x4 wr = ldtr2(a, a + 128);
                const u32x4 g2 = lds128(L0 + L_G2 + (32 * s + 8 * g) * 2), sb = lds128(L0 + L_SB + (32 * s + 8 * g) * 2);
                u32x4 o;
#pragma unroll
                for (int d = 0; d < 4; ++d) {
                    o[d] = pack2(dot2(wr[d], g2[d] & 0xffffu, 0.f), dot2(wr[d], g2[d] & 0xffff0000u, 0.f));
                    us = dot2(o[d], 0x3F803F80u, us);
                    vs = dot2(wr[d], sb[d], vs);
                }
                ST16C(o, WS_WP + ((j * 20 + s) * 14 + wslot) * 1024 + lo16);
            }
        }
        us = xsum4(us); vs = xsum4(vs);
        if (l < 16) { stsf(L0 + L_UVP + w * 128 + l * 4, us); stsf(L0 + L_UVP + w * 128 + 64 + l * 4, vs); }
    }
    if (w == 0 && rb >= 1) poll_flags(ws + WS_FKV + (j * 16 + (rb >= 2 ? rb - 2 : rb - 1)) * 8, rb >= 2 ? 2 : 1, l, tag, ws, 3);
    BAR();
    STAMP(6);
    if (rb >= 1) {
#pragma unroll
        for (int e = 0; e < 3; ++e) {
            const int n = w + 8 * e;
            if (n < 22) {
                const u32 d = n < 10 ? L_K + n * 1024 : L_V + (n - 10) * 1024, st = n < 10 ? 10240 : 12288;
                glds16c(kvx - 16 * 22528 + n * 1024, lo16, L0 + d + st);
                if (rb >= 2) glds16c(kvx - 32 * 22528 + n * 1024, lo16, L0 + d);
            }
        }
    }
    if (wpwg && tid < 32) {
        float v = 0.f;
#pragma unroll
        for (int ww = 0; ww < 8; ++ww) v += ldsf(L0 + L_UVP + ww * 128 + tid * 4);
        st_agent32(ws + WS_UV + (j * 14 + wslot) * 128 + tid * 4, __float_as_uint(v));
    }
    const int hf = j & 1, cb = j >> 1;
    u32 wvoff[5];
#pragma unroll
    for (int i = 0; i < 5; ++i) {
        const u32 sg = 64 * i + l, pos = (sg * 6554) >> 16, c = sg - pos * 10, k = pos ^ (((pos >> 3) & 1) << 2);
        wvoff[i] = k * 1280 + c * 16;
    }
    VMCNT(0);
    BAR();
    {
        const char* src = (const char*)p.wo + cb * 160 + (10 + w) * 32 * 1280;
#pragma unroll
        for (int i = 0; i < 5; ++i) glds16(src, wvoff[i], L0 + L_WO1 + w * 5120 + i * 1024);
        if (w < 2) {
#pragma unroll
            for (int i = 0; i < 5; ++i) glds16(src + 8 * 32 * 1280, wvoff[i], L0 + L_WO1 + (w + 8) * 5120 + i * 1024);
        }
    }
    STAMP(7);

    {
        const int hd = w & 1, qi = w >> 1;
        const int kr0 = 32 * qi;
        u32x4 qf[2];
#pragma unroll
        for (int a = 0; a < 2; ++a) {
            const u32 qa = L0 + L_Q + (kr0 + 16 * a + i16) * 80 + hd * 40;
            const u32x2 q0 = lds64(qa + 8 * g);
            u32x2 q1 = lds64(qa + 32);
            if (g != 0) q1 = (u32x2){0, 0};
            qf[a] = (u32x4){q0[0], q0[1], q1[0], q1[1]};
        }
        const u32 kb0 = L0 + L_K + (kr0 + i16) * 80 + hd * 40 + 8 * g, kb1 = L0 + L_K + (kr0 + i16) * 80 + hd * 40 + 32;
        f32x4 mfirst, mlast;
#pragma unroll
        for (int r = 0; r < 4; ++r) { const bool lo = 4 * g + r <= i16; mfirst[r] = lo ? -INFINITY : 0.f; mlast[r] = lo ? 0.f : -INFINITY; }
        f32x4 s0[17], s1[17];
        {
            u32x4 kf[3];
#define KRD(t) do { const u32x2 k0 = lds64(kb0 + (t) * 1280), k1 = lds64(kb1 + (t) * 1280); kf[(t) % 3] = (u32x4){k0[0], k0[1], k1[0], k1[1]}; } while (0)
            KRD(0); KRD(1);
            static_for<0, 18>([&](auto T) {
                constexpr int t = T;
                SB0();
                if constexpr (t < 16) KRD(t + 2);
                if constexpr (t < 17) s0[t] = mfma(kf[t % 3], qf[0], t == 0 ? mfirst : t == 16 ? mlast : Z4);
                if constexpr (t > 0) s1[t - 1] = mfma(kf[t % 3], qf[1], t == 1 ? mfirst : t == 17 ? mlast : Z4);
                SGB(M_DSR, 2); SGB(M_MFMA, 2);
                SB0();
            });
#undef KRD
        }
        if (rb < 2) {
            const int fv = rb == 1 ? 128 : 256;
#pragma unroll
            for (int t = 0; t < 16; ++t) {
                const bool d0 = kr0 + 16 * t < fv, d1 = kr0 + 16 * t + 16 < fv;
#pragma unroll
                for (int r = 0; r < 4; ++r) { s0[t][r] = d0 ? -INFINITY : s0[t][r]; s1[t][r] = d1 ? -INFINITY : s1[t][r]; }
            }
        }
        STAMP(22);
        float m0 = fmaxf(fmaxf(s0[0][0], s0[0][1]), fmaxf(s0[0][2], s0[0][3])), m1 = fmaxf(fmaxf(s1[0][0], s1[0][1]), fmaxf(s1[0][2], s1[0][3]));
#pragma unroll
        for (int t = 1; t < 17; ++t) {
            m0 = fmaxf(fmaxf(m0, s0[t][0]), s0[t][1]); m1 = fmaxf(fmaxf(m1, s1[t][0]), s1[t][1]);
            m0 = fmaxf(fmaxf(m0, s0[t][2]), s0[t][3]); m1 = fmaxf(fmaxf(m1, s1[t][2]), s1[t][3]);
        }
        m0 = xmax4(m0); m1 = xmax4(m1);
        STAMP(23);
        f32x4 o00 = Z4, o01 = Z4, o10 = Z4, o11 = Z4;
        const u32 vb = L0 + L_V + (kr0 + 4 * g + q4) * 96 + hd * 48 + 8 * p4;
        auto pv = [&](auto SUB) {
            constexpr bool sub = SUB;
            u32x4 vf[2][2];
            vf[0][0] = ldtr2(vb, vb + 16 * 96); vf[0][1] = ldtr2(vb + 32, vb + 16 * 96 + 32);
#define EX(v, m) __builtin_amdgcn_exp2f(sub ? (v) - (m) : (v))
            static_for<0, 9>([&](auto U) {
                constexpr int u = U;
                if constexpr (u < 8) { const u32 a = vb + (u + 1) * 32 * 96; vf[(u + 1) & 1][0] = ldtr2(a, a + 16 * 96); vf[(u + 1) & 1][1] = ldtr2(a + 32, a + 16 * 96 + 32); }
                u32x4 pa, pb;
                pa[0] = pack2(EX(s0[2 * u][0], m0), EX(s0[2 * u][1], m0)); pa[1] = pack2(EX(s0[2 * u][2], m0), EX(s0[2 * u][3], m0));
                if constexpr (u < 8) { pa[2] = pack2(EX(s0[2 * u + 1][0], m0), EX(s0[2 * u + 1][1], m0)); pa[3] = pack2(EX(s0[2 * u + 1][2], m0), EX(s0[2 * u + 1][3], m0)); }
                else { pa[2] = 0; pa[3] = 0; }
                if constexpr (u > 0) { pb[0] = pack2(EX(s1[2 * u - 1][0], m1), EX(s1[2 * u - 1][1], m1)); pb[1] = pack2(EX(s1[2 * u - 1][2], m1), EX(s1[2 * u - 1][3], m1)); }
                else { pb[0] = 0; pb[1] = 0; }
                pb[2] = pack2(EX(s1[2 * u][0], m1), EX(s1[2 * u][1], m1)); pb[3] = pack2(EX(s1[2 * u][2], m1), EX(s1[2 * u][3], m1));
                o00 = mfma(vf[u & 1][0], pa, o00); o01 = mfma(vf[u & 1][1], pa, o01);
                o10 = mfma(vf[u & 1][0], pb, o10); o11 = mfma(vf[u & 1][1], pb, o11);
                SB0();
            });
#undef EX
        };
        if (__all(fabsf(m0) <= 64.f && fabsf(m1) <= 64.f)) pv(std::false_type{}); else pv(std::true_type{});
        STAMP(24);
        const float i0 = __builtin_amdgcn_rcpf(__shfl(o01[0], i16 + 16)), i1 = __builtin_amdgcn_rcpf(__shfl(o11[0], i16 + 16));
        const u32 oa = L0 + L_Q + (kr0 + i16) * 80 + hd * 40;
        sts64(oa + 8 * g, (u32x2){pack2(o00[0] * i0, o00[1] * i0), pack2(o00[2] * i0, o00[3] * i0)});
        sts64(oa + 1280 + 8 * g, (u32x2){pack2(o10[0] * i1, o10[1] * i1), pack2(o10[2] * i1, o10[3] * i1)});
        if (g == 0) {
            sts64(oa + 32, (u32x2){pack2(o01[0] * i0, o01[1] * i0), pack2(o01[2] * i0, o01[3] * i0)});
            sts64(oa + 1280 + 32, (u32x2){pack2(o11[0] * i1, o11[1] * i1), pack2(o11[2] * i1, o11[3] * i1)});
        }
    }
    BAR();
    {
        const int tok = 64 * (w & 1) + l;
        const u32 oo = WS_O + (((rb * 8 + (tok >> 4)) * 80 + 5 * j) * 16 + (tok & 15)) * 16;
        ST16C(lds128(L0 + L_Q + tok * 80 + (w >> 1) * 16), oo + (w >> 1) * 256);
        if (w < 2) ST16C(lds128(L0 + L_Q + tok * 80 + 64), oo + 4 * 256);
    }
    VMCNT(0);
    BAR();
    if (tid == 0) {
        st_agent(ws + WS_FO + (rb * 16 + j) * 8, tag);
        if (wpwg) st_agent(ws + WS_FWP + (j * 16 + rb) * 8, tag);
    }
    STAMP(8);

    const int wm = w & 3, wk = w >> 2;
    const int mt3 = rb * 8 + 4 * hf + wm;
    const int nt0 = wk == 0 ? 0 : 3, ntn = wk == 0 ? 3 : 2;
    u32x2 x1k[3];
    {
        const int wdu = b + 256 * (w - 6);
        const bool wdw = w >= 6 && wdu < 432;
        if (wdw) {
            const int ucb = wdu / 54, us = wdu - 54 * ucb;
#pragma unroll
            for (int i = 0; i < 5; ++i) glds16((const char*)p.wd + ucb * 160 + us * 32 * 1280, wvoff[i], L0 + L_WDRAW + (w - 6) * 5120 + i * 1024);
        }
        if (w >= 1) {
            const char* src = (const char*)p.wo + cb * 160 + (w - 1) * 32 * 1280;
#pragma unroll
            for (int i = 0; i < 5; ++i) glds16(src, wvoff[i], L0 + L_WO0 + (w - 1) * 5120 + i * 1024);
            if (w < 4) {
#pragma unroll
                for (int i = 0; i < 5; ++i) glds16(src + 7 * 32 * 1280, wvoff[i], L0 + L_WO0 + (w + 6) * 5120 + i * 1024);
            } else {
#pragma unroll
                for (int e = 0; e < 3; ++e) {
                    const int pc = w - 4 + 4 * e;
                    if (pc < 10) {
                        const u32 sg = 64 * pc + l, r = (sg * 6554) >> 16, c = sg - r * 10;
                        glds16((const char*)p.x + (rb * 128 + 64 * hf) * 1280 + cb * 160, r * 1280 + c * 16, L0 + L_XR + pc * 1024);
                    }
                }
            }
        }
        if (w == 1 && l < 20) {
            const u32x2 gv = poll_granule(ws, 320 + 20 * cb + l, tag, ws, 4);
            stsf4(L0 + L_GA + l * 16, (f32x4){bflo(gv[0]), bfhi(gv[0]), bflo(gv[1]), bfhi(gv[1])});
        }
        if (w == 0) {
            poll_flags(ws + WS_FWP + j * 128, 2 * nj, l, tag, ws, 6);
            poll_flags(ws + WS_FO + rb * 128, 16, l, tag, ws, 5);
        }
        VMCNT(0);
        if (wdw) {
            const u32 wb = L0 + L_WDRAW + (w - 6) * 5120;
            const u32 b0 = wb + 160 * (8 * g + 4 * (g & 1) + q4) + 8 * p4, b1 = wb + 160 * (8 * g + 4 * ((g & 1) ^ 1) + q4) + 8 * p4;
#pragma unroll
            for (int n = 0; n < 5; ++n) ST16C(ldtr2(b0 + n * 32, b1 + n * 32), WS_WD + (wdu * 5 + n) * 1024 + lo16);
        }
        BAR();
        STAMP(9);
        u32x4 a[10];
        const char* asrc = ws + WS_O + (mt3 * 80 + 40 * wk) * 256;
#define AL(s) a[s] = gld16c<((s) & 3) * 1024>(asrc + ((s) >> 2) * 4096, lo16)
        AL(0); AL(1); AL(2); AL(3);
        f32x4 acc[5];
#pragma unroll
        for (int n = 0; n < 5; ++n) acc[n] = Z4;
        const u32 wb = L0 + (wk == 0 ? L_WO0 : L_WO1);
        const u32 b0 = wb + 160 * (8 * g + 4 * (g & 1) + q4) + 8 * p4, b1 = wb + 160 * (8 * g + 4 * ((g & 1) ^ 1) + q4) + 8 * p4;
        u32x4 bf[2][5];
#pragma unroll
        for (int n = 0; n < 5; ++n) bf[0][n] = ldtr2(b0 + n * 32, b1 + n * 32);
        static_for<0, 10>([&](auto S) {
            constexpr int s = S;
            vmw1<(s < 7 ? 3 : 9 - s)>(a[s]);
            if constexpr (s + 4 < 10) AL(s + 4);
            SB0();
#pragma unroll
            for (int n = 0; n < 5; ++n) {
                if (s < 9) bf[(s + 1) & 1][n] = ldtr2(b0 + (s + 1) * 5120 + n * 32, b1 + (s + 1) * 5120 + n * 32);
                acc[n] = mfma(bf[s & 1][n], a[s], acc[n]);
            }
#pragma unroll
            for (int n = 0; n < 5; ++n) { SGB(M_DSR, 2); SGB(M_MFMA, 1); }
            SB0();
        });
#undef AL
        STAMP(25);
        if (wk == 0) { stsf4(L0 + L_RED3 + (wm * 5 + 3) * 1024 + lo16, acc[3]); stsf4(L0 + L_RED3 + (wm * 5 + 4) * 1024 + lo16, acc[4]); }
        else { stsf4(L0 + L_RED3 + (wm * 5 + 0) * 1024 + lo16, acc[0]); stsf4(L0 + L_RED3 + (wm * 5 + 1) * 1024 + lo16, acc[1]); stsf4(L0 + L_RED3 + (wm * 5 + 2) * 1024 + lo16, acc[2]); }
        BAR();
        float s1 = 0.f, s2 = 0.f;
#pragma unroll
        for (int n = 0; n < 3; ++n) {
            if (n < ntn) {
                const int nt = nt0 + n;
                const f32x4 mine = wk == 0 ? acc[n] : acc[n < 2 ? 3 + n : 4];
                const f32x4 v = mine + ldsf4(L0 + L_RED3 + (wm * 5 + nt) * 1024 + lo16);
                const f32x4 ga = ldsf4(L0 + L_GA + (16 * nt + 4 * g) * 4);
                const u32x2 xr = lds64(L0 + L_XR + (16 * wm + i16) * 160 + 32 * nt + 8 * g);
                const u32x2 o = {pack2(bflo(xr[0]) + ga[0] * v[0], bfhi(xr[0]) + ga[1] * v[1]), pack2(bflo(xr[1]) + ga[2] * v[2], bfhi(xr[1]) + ga[3] * v[3])};
                x1k[n] = o;
                s1 = dot2(o[0], 0x3F803F80u, s1); s1 = dot2(o[1], 0x3F803F80u, s1);
                s2 = dot2(o[0], o[0], s2); s2 = dot2(o[1], o[1], s2);
                sts64(L0 + L_X1S + (16 * wm + i16) * 160 + 32 * nt + 8 * g, o);
            } else x1k[n] = (u32x2){0, 0};
        }
        s1 = xsum4(s1); s2 = xsum4(s2);
        if (l < 16) stsf2(L0 + L_ST3 + (wk * 64 + 16 * wm + l) * 8, (f32x2){s1, s2});
        BAR();
        {
            const u32 xo = WS_X1 + (((rb * 8 + 4 * hf + g) * 80 + 10 * cb) * 16 + i16) * 16;
            ST16C(lds128(L0 + L_X1S + l * 160 + w * 16), xo + w * 256);
            if (w < 2) ST16C(lds128(L0 + L_X1S + l * 160 + (w + 8) * 16), xo + (w + 8) * 256);
            if (w == 2) {
                const f32x2 u = *(LAS f32x2*)(size_t)(L0 + L_ST3 + l * 8), v = *(LAS f32x2*)(size_t)(L0 + L_ST3 + (64 + l) * 8);
                const f32x2 sum = u + v;
                ST8C(__builtin_bit_cast(u32x2, sum), WS_STAT + ((rb * 8 + cb) * 128 + 64 * hf + l) * 8);
            }
        }
        STAMP(26);
        VMCNT(0);
        BAR();
        if (tid == 0) { st_agent(ws + WS_FX1 + (rb * 16 + j) * 8, tag); st_agent(ws + WS_FWD + b * 8, tag); }
    }
    STAMP(10);

    {
        const int wn = w >> 2;
        const char* wpj = ws + WS_WP + (j * 20 * 14 + w) * 1024;
        const char* x1b = ws + WS_X1 + (rb * 8 + w) * 80 * 256;
        const u32 r4w = L0 + L_R4 + w * 1024;
#define P4_B1(t, i) glds16c(wpj + ((t) * 28 + 8 * (i)) * 1024, lo16, r4w + ((t) % 3) * R4_STAGE + 16384 + 8 * (i) * 1024)
#define P4_A1(t, i) glds16c(x1b + (8 * (t) + 4 * (i)) * 256, lo16, r4w + ((t) % 3) * R4_STAGE + 8 * (i) * 1024)
#define P4_B(t) do { P4_B1(t, 0); P4_B1(t, 1); P4_B1(t, 2); if (w < 4) P4_B1(t, 3); } while (0)
        P4_B(0); P4_B(1); P4_B(2);
        if (w == 0) poll_flags(ws + WS_FX1 + rb * 128, 16, l, tag, ws, 7);
        BAR();
        STAMP(11);
        glds16c(ws + WS_STAT + rb * 8192 + w * 1024, lo16, L0 + L_ST4 + w * 1024);
        if (w < 2) glds16c(ws + WS_UV + j * 1792 + w * 1024, lo16, L0 + L_UV + w * 1024);
        P4_A1(0, 0); P4_A1(0, 1); P4_A1(1, 0); P4_A1(1, 1); P4_A1(2, 0); P4_A1(2, 1);
        f32x4 acc[7][2];
#pragma unroll
        for (int n = 0; n < 7; ++n) { acc[n][0] = Z4; acc[n][1] = Z4; }
        const u32 ra = L0 + L_R4 + 2 * wm * 1024 + lo16, rbb = L0 + L_R4 + 16384 + 7 * wn * 1024 + lo16;
        u32x4 fa[2][2], fb[2][7];
#define P4_RA(buf, t, ks) do { const u32 so = ((t) % 3) * R4_STAGE + (ks) * 8192; fa[buf][0] = lds128(ra + so); fa[buf][1] = lds128(ra + so + 1024); } while (0)
#define P4_RB(buf, t, ks, n) fb[buf][n] = lds128(rbb + ((t) % 3) * R4_STAGE + (ks) * 14336 + (n) * 1024)
#define P4_MM(buf, n) do { acc[n][0] = mfma(fb[buf][n], fa[buf][0], acc[n][0]); acc[n][1] = mfma(fb[buf][n], fa[buf][1], acc[n][1]); } while (0)
#define P4_HALF(buf, rd, t, ks, V0, V1, V2) do { \
            SB0(); if (rd) { P4_RA((buf) ^ 1, t, ks); P4_RB((buf) ^ 1, t, ks, 0); } P4_MM(buf, 0); SB0(); V0; \
            SB0(); if (rd) P4_RB((buf) ^ 1, t, ks, 1); P4_MM(buf, 1); SB0(); \
            SB0(); if (rd) P4_RB((buf) ^ 1, t, ks, 2); P4_MM(buf, 2); SB0(); V1; \
            SB0(); if (rd) P4_RB((buf) ^ 1, t, ks, 3); P4_MM(buf, 3); SB0(); \
            SB0(); if (rd) P4_RB((buf) ^ 1, t, ks, 4); P4_MM(buf, 4); SB0(); V2; \
            SB0(); if (rd) P4_RB((buf) ^ 1, t, ks, 5); P4_MM(buf, 5); SB0(); \
            SB0(); if (rd) P4_RB((buf) ^ 1, t, ks, 6); P4_MM(buf, 6); SB0(); } while (0)
        VMCNT(4);
        BAR();
        STAMP(27);
        P4_RA(0, 0, 0);
#pragma unroll
        for (int n = 0; n < 7; ++n) P4_RB(0, 0, 0, n);
        static_for<0, 10>([&](auto T) {
            constexpr int t = T;
            constexpr bool f = t >= 1 && t + 2 <= 9, sN = t + 3 <= 9;
            P4_HALF(0, true, t, 1, if (f && w < 4) P4_B1(t + 2, 3), if (f) P4_A1(t + 2, 0), if (f) P4_A1(t + 2, 1));
            if constexpr (t == 5) {
                if (tid < 128) {
                    f32x2 st = {0.f, 0.f};
#pragma unroll
                    for (int c = 0; c < 8; ++c) st += *(LAS f32x2*)(size_t)(L0 + L_ST4 + (c * 128 + tid) * 8);
                    const float mu = st[0] * (1.f / 640.f), var = fmaxf(st[1] * (1.f / 640.f) - mu * mu, 0.f), r = __builtin_amdgcn_rsqf(var + 1e-5f);
                    stsf2(L0 + L_MR + tid * 8, (f32x2){r, -mu * r});
                }
            }
            if constexpr (t < 9) {
                if constexpr (t == 0) vmw<2>();
                else if constexpr (t == 8) vmw<0>();
                else { if (w < 4) vmw<6>(); else vmw<5>(); }
                BAR();
                P4_HALF(1, true, t + 1, 0, if (sN) P4_B1(t + 3, 0), if (sN) P4_B1(t + 3, 1), if (sN) P4_B1(t + 3, 2));
            } else {
                P4_HALF(1, false, 0, 0, , , );
            }
        });
#undef P4_B
#undef P4_B1
#undef P4_A1
#undef P4_RA
#undef P4_RB
#undef P4_MM
#undef P4_HALF
        STAMP(12);
        f32x2 mr[2];
#pragma unroll
        for (int m = 0; m < 2; ++m) mr[m] = *(LAS f32x2*)(size_t)(L0 + L_MR + (32 * wm + 16 * m + i16) * 8);
#pragma unroll
        for (int n = 0; n < 7; ++n) {
            const f32x4 u = ldsf4(L0 + L_UV + (7 * wn + n) * 128 + 16 * g), v = ldsf4(L0 + L_UV + (7 * wn + n) * 128 + 64 + 16 * g);
#pragma unroll
            for (int m = 0; m < 2; ++m) acc[n][m] = acc[n][m] * mr[m][0] + (u * mr[m][1] + v);
        }
        if (nj == 7) stsf4(L0 + L_XCH + w * 1024 + lo16, wn == 0 ? acc[6][1] : acc[6][0]);
        auto silu_mul = [&](const f32x4 gt, const f32x4 up) -> u32x2 {
            const f32x4 e = gt * (-LOG2E), gu = gt * up;
            f32x4 d;
#pragma unroll
            for (int r = 0; r < 4; ++r) d[r] = __builtin_amdgcn_exp2f(e[r]);
            d = d + 1.f;
#pragma unroll
            for (int r = 0; r < 4; ++r) d[r] = __builtin_amdgcn_rcpf(d[r]);
            const f32x4 o = gu * d;
            return (u32x2){pack2(o[0], o[1]), pack2(o[2], o[3])};
        };
#pragma unroll
        for (int a = 0; a < 3; ++a) {
            const int la = a + 4 * wn, gam = Gj + (la <= 3 || nj == 7 ? la : la - 1);
            const u32x2 o0 = silu_mul(acc[2 * a][0], acc[2 * a + 1][0]), o1 = silu_mul(acc[2 * a][1], acc[2 * a + 1][1]);
            const auto s0 = __builtin_amdgcn_permlane16_swap(o0[0], o1[0], false, false);
            const auto s1 = __builtin_amdgcn_permlane16_swap(o0[1], o1[1], false, false);
            const u32x4 v = {s0[0], s1[0], s0[1], s1[1]};
            ST16C(v, WS_ACT + (((rb * 8 + 2 * wm + (g & 1)) * 216 + 2 * gam + (g >> 1)) * 16 + i16) * 16);
        }
        STAMP(28);
        if (nj == 7) {
            BAR();
            const f32x4 other = ldsf4(L0 + L_XCH + (w ^ 4) * 1024 + lo16);
            const u32x2 o = wn == 0 ? silu_mul(acc[6][0], other) : silu_mul(other, acc[6][1]);
            ST8C(o, WS_ACT + (((rb * 8 + 2 * wm + wn) * 216 + 2 * (Gj + 3) + (g >> 1)) * 16 + i16) * 16 + 8 * (g & 1));
        }
        VMCNT(0);
        BAR();
        if (tid == 0) st_agent(ws + WS_FACT + (rb * 16 + j) * 8, tag);
    }
    STAMP(13);

    {
        if (w == 0) {
            const int u0 = 54 * cb;
            const char* a = ws + WS_FWD + ((u0 + (l < 54 ? l : 0)) & 255) * 8;
            u32 spins = 0;
            for (;;) {
                const u64 v = ld_agent(a);
                if (__all(v == tag)) break;
                if (++spins > SPIN_CAP) { if (l == 0) st_agent32(ws + WS_ERR, 12); break; }
                __builtin_amdgcn_s_sleep(1);
            }
        }
        BAR();
        const char* wsrc = ws + WS_WD + (cb * 54 + wk * 27) * 5120;
        const u32 r5w = L0 + L_R5 + wk * 20480 + wm * 5120;
#define P5_B1(t, i) glds16c(wsrc + min(4 * (t) + wm, 26) * 5120 + (i) * 1024, lo16, r5w + ((t) % 3) * R5_STAGE + (i) * 1024)
#define P5_B(t) do { P5_B1(t, 0); P5_B1(t, 1); P5_B1(t, 2); P5_B1(t, 3); P5_B1(t, 4); } while (0)
        P5_B(0); P5_B(1); P5_B(2);
        if (w == 1 && l < 20) {
            const u32x2 gv = poll_granule(ws, 800 + 20 * cb + l, tag, ws, 8);
            stsf4(L0 + L_GB + l * 16, (f32x4){bflo(gv[0]), bfhi(gv[0]), bflo(gv[1]), bfhi(gv[1])});
        }
        if (w == 0) poll_flags(ws + WS_FACT + rb * 128, 16, l, tag, ws, 9);
        VMCNT(0);
        BAR();
        STAMP(14);
        const char* asrc = ws + WS_ACT + (mt3 * 216 + wk * 108) * 256;
        u32x4 a[3][4];
#define P5_A1(t, i) a[(t) % 3][i] = gld16c<(i) * 1024>(asrc + (t) * 4096, lo16)
        P5_A1(0, 0); P5_A1(0, 1); P5_A1(0, 2); P5_A1(0, 3); P5_A1(1, 0); P5_A1(1, 1); P5_A1(1, 2); P5_A1(1, 3); P5_A1(2, 0); P5_A1(2, 1); P5_A1(2, 2); P5_A1(2, 3);
        f32x4 acc[5];
#pragma unroll
        for (int n = 0; n < 5; ++n) acc[n] = Z4;
        const u32 rb5 = L0 + L_R5 + wk * 20480 + lo16;
        u32x4 fb[2][5];
#define P5_RD(buf, t, kk) do { _Pragma("unroll") for (int n = 0; n < 5; ++n) fb[buf][n] = lds128(rb5 + ((t) % 3) * R5_STAGE + (kk) * 5120 + n * 1024); } while (0)
#define P5_MM(buf, av) do { _Pragma("unroll") for (int n = 0; n < 5; ++n) acc[n] = mfma(fb[buf][n], (av), acc[n]); } while (0)
#define P5_MIX() do { _Pragma("unroll") for (int n = 0; n < 5; ++n) { SGB(M_DSR, 1); SGB(M_MFMA, 1); } } while (0)
        P5_RD(0, 0, 0);
        static_for<0, 7>([&](auto T) {
            constexpr int t = T, q = t % 3;
            constexpr bool nx = t + 3 < 7;
            constexpr int NA = t == 0 ? 8 : t == 1 ? 13 : t == 2 ? 18 : t == 3 ? 21 : t == 4 ? 20 : t == 5 ? 11 : 5;
            constexpr int NB = t == 2 ? 12 : t == 3 ? 12 : t == 4 ? 8 : t == 5 ? 0 : 63;
            vmw4<NA>(a[q][0], a[q][1], a[q][2], a[q][3]);
            SB0(); P5_RD(1, t, 1); P5_MM(0, a[q][0]); P5_MIX(); SB0();
            if constexpr (nx) P5_A1(t + 3, 0);
            SB0(); P5_RD(0, t, 2); P5_MM(1, a[q][1]); P5_MIX(); SB0();
            if constexpr (nx) P5_A1(t + 3, 1);
            if constexpr (t < 6) {
                SB0(); P5_RD(1, t, 3); P5_MM(0, a[q][2]); P5_MIX(); SB0();
                if constexpr (nx) P5_A1(t + 3, 2);
                vmw<NB>(); BAR();
                if constexpr (nx) { P5_B1(t + 3, 0); P5_B1(t + 3, 1); }
                SB0(); P5_RD(0, t + 1, 0); P5_MM(1, a[q][3]); P5_MIX(); SB0();
                if constexpr (nx) { if constexpr (t + 3 < 6) P5_A1(t + 3, 3); P5_B1(t + 3, 2); P5_B1(t + 3, 3); P5_B1(t + 3, 4); }
            } else {
                SB0(); P5_MM(0, a[q][2]); SB0();
            }
        });
#undef P5_B
#undef P5_B1
#undef P5_A1
#undef P5_RD
#undef P5_MM
#undef P5_MIX
        STAMP(15);
        if (wk == 0) { stsf4(L0 + L_RED5 + (wm * 5 + 3) * 1024 + lo16, acc[3]); stsf4(L0 + L_RED5 + (wm * 5 + 4) * 1024 + lo16, acc[4]); }
        else { stsf4(L0 + L_RED5 + (wm * 5 + 0) * 1024 + lo16, acc[0]); stsf4(L0 + L_RED5 + (wm * 5 + 1) * 1024 + lo16, acc[1]); stsf4(L0 + L_RED5 + (wm * 5 + 2) * 1024 + lo16, acc[2]); }
        BAR();
#pragma unroll
        for (int n = 0; n < 3; ++n) {
            if (n < ntn) {
                const int nt = nt0 + n;
                const f32x4 mine = wk == 0 ? acc[n] : acc[n < 2 ? 3 + n : 4];
                const f32x4 v = mine + ldsf4(L0 + L_RED5 + (wm * 5 + nt) * 1024 + lo16);
                const f32x4 gb = ldsf4(L0 + L_GB + (16 * nt + 4 * g) * 4);
                const u32x2 o = {pack2(bflo(x1k[n][0]) + gb[0] * v[0], bfhi(x1k[n][0]) + gb[1] * v[1]), pack2(bflo(x1k[n][1]) + gb[2] * v[2], bfhi(x1k[n][1]) + gb[3] * v[3])};
                sts64(L0 + L_OS + (16 * wm + i16) * 160 + 32 * nt + 8 * g, o);
            }
        }
        BAR();
#pragma unroll
        for (int i = 0; i < 2; ++i) {
            const int id = tid + 512 * i;
            if (id < 640) {
                const int r = (id * 6554) >> 16, c = id - 10 * r;
                *(GAS u32x4*)((char*)p.out + (rb * 128 + 64 * hf + r) * 1280 + cb * 160 + c * 16) = lds128(L0 + L_OS + id * 16);
            }
        }
    }
    STAMP(16);
    if (rb == 15 && j == 0 && w < 4) {
        u32 spins = 0;
        for (;;) {
            const u64 v = ld_agent(ws + WS_FO + tid * 8);
            if (__all(v == tag)) break;
            if (++spins > SPIN_CAP) { st_agent32(ws + WS_ERR, 10); break; }
            __builtin_amdgcn_s_sleep(1);
        }
    }
    if (rb == 15 && j == 0) {
        BAR();
        if (tid == 0) st_agent(ws + WS_EPOCH, tag);
    }
}

extern "C" void kernel_launch(void* const* d_in, const int* in_sizes, int n_in, void* d_out, int out_size, void* d_ws, size_t ws_size, hipStream_t stream) {
    Params p;
    p.x = (const u16*)d_in[0]; p.wqkv = (const u16*)d_in[1]; p.wo = (const u16*)d_in[2];
    p.wg = (const u16*)d_in[4]; p.wu = (const u16*)d_in[5]; p.wd = (const u16*)d_in[6];
    p.c = (const u16*)d_in[7]; p.wmod = (const u16*)d_in[8];
    p.out = (u16*)d_out; p.ws = (char*)d_ws;
    mega_kernel<<<256, 512, 0, stream>>>(p);
}
```

```cpp
#include <hip/hip_runtime.h>
#include <hip/hip_bf16.h>
#include <stdint.h>
#include <type_traits>

typedef unsigned short u16;
typedef unsigned u32;
typedef unsigned long long u64;
typedef __attribute__((ext_vector_type(2))) unsigned u32x2;
typedef __attribute__((ext_vector_type(4))) unsigned u32x4;
typedef __attribute__((ext_vector_type(2))) float f32x2;
typedef __attribute__((ext_vector_type(4))) float f32x4;
typedef __attribute__((ext_vector_type(2))) __bf16 bf16x2;
typedef __attribute__((ext_vector_type(8))) __bf16 bf16x8;
typedef __attribute__((ext_vector_type(4))) short s16x4;
#define LAS __attribute__((address_space(3)))
#define GAS __attribute__((address_space(1)))
#define DI __device__ __forceinline__

constexpr int SEQ = 2048, DM = 640, FF = 1728;
constexpr float QSCALE = 0.22360679774997896f * 1.4426950408889634f;
constexpr float LOG2E = 1.4426950408889634f;

constexpr u32 WS_EPOCH = 0;
constexpr u32 WS_ERR   = 256;
constexpr u32 FS = 128;
constexpr u32 WS_FKV   = 4096;
constexpr u32 WS_FO    = WS_FKV + 256 * FS;
constexpr u32 WS_FX1   = WS_FO + 256 * FS;
constexpr u32 WS_FACT  = WS_FX1 + 256 * FS;
constexpr u32 WS_FH    = WS_FACT + 256 * FS;
constexpr u32 WS_FWT   = WS_FH + 256 * FS;
constexpr u32 WS_FWP   = WS_FWT + 256 * FS;
constexpr u32 WS_FW2   = WS_FWP + 256 * FS;
constexpr u32 WS_MODG  = WS_FW2 + 256 * FS;
constexpr u32 WS_UV    = WS_MODG + 61440;
constexpr u32 WS_STAT  = WS_UV + 114688;
constexpr u32 WS_KV    = 589824;
constexpr u32 WS_O     = WS_KV + 256 * 22528;
constexpr u32 WS_X1    = WS_O + SEQ * DM * 2;
constexpr u32 WS_ACT   = WS_X1 + SEQ * DM * 2;
constexpr u32 WS_WP    = WS_ACT + SEQ * FF * 2;
constexpr u32 WS_H     = WS_WP + 16 * 20 * 14 * 1024;
constexpr u32 WS_WD    = WS_H + SEQ * DM * 2;
constexpr u32 WS_WO    = WS_WD + 8 * 54 * 5 * 1024;
constexpr u32 WS_WT    = WS_WO + 8 * 20 * 5 * 1024;
constexpr u32 WS_STAMP = WS_WT + 16 * 153600;
constexpr u32 WS_END   = WS_STAMP + 256 * 8 * 64 * 4;

constexpr u32 L_WKV = 0, L_WQ = 102400, L_GS = 153600, L_STP = 156160, L_MODP = 156416, L_CNT = 157440, L_SB = 157696, L_G2 = 158016;
constexpr u32 L_ODRAW = 101376, L_WTRAW = 116736, L_XS = 133120;
constexpr u32 L_K = 0, L_V = 30720, L_Q = 67584, L_WPRAW = 77824, L_UVL = 98304;
constexpr u32 L_WO = 61440, L_RED3 = 0, L_X1S = 20480, L_XR = 30720, L_GA = 40960, L_ST3 = 41472, L_WDRAW = 43008;
constexpr u32 L_R4 = 0, R4_STAGE = 45056, L_ST4 = 135168, L_UV = 143360, L_MR = 150528, L_XCH = 151552, L_UVS = 159744;
constexpr u32 L_R5 = 0, R5_STAGE = 40960, L_RED5 = 122880, L_GB = 143360, L_OS = 144384;

struct Params {
    const u16* x; const u16* wqkv; const u16* wo; const u16* wg; const u16* wu; const u16* wd;
    const u16* c; const u16* wmod; u16* out; char* ws;
};

DI float bflo(u32 d) { return __uint_as_float(d << 16); }
DI float bfhi(u32 d) { return __uint_as_float(d & 0xffff0000u); }
DI u32 pack2(float a, float b) { f32x2 v = {a, b}; return __builtin_bit_cast(u32, __builtin_convertvector(v, bf16x2)); }
DI float rbf(float a) { return bflo(pack2(a, a)); }
DI f32x4 mfma(u32x4 a, u32x4 b, f32x4 c) {
    return __builtin_amdgcn_mfma_f32_16x16x32_bf16(__builtin_bit_cast(bf16x8, a), __builtin_bit_cast(bf16x8, b), c, 0, 0, 0);
}
DI u32x2 ldtr(u32 a) { return __builtin_bit_cast(u32x2, __builtin_amdgcn_ds_read_tr16_b64_v4i16((LAS s16x4*)(size_t)a)); }
DI u32x4 ldtr2(u32 a0, u32 a1) { u32x2 x = ldtr(a0), y = ldtr(a1); return (u32x4){x[0], x[1], y[0], y[1]}; }
DI u32x4 lds128(u32 a) { return *(LAS u32x4*)(size_t)a; }
DI u32x2 lds64(u32 a) { return *(LAS u32x2*)(size_t)a; }
DI f32x4 ldsf4(u32 a) { return *(LAS f32x4*)(size_t)a; }
DI float ldsf(u32 a) { return *(LAS float*)(size_t)a; }
DI void sts128(u32 a, u32x4 v) { *(LAS u32x4*)(size_t)a = v; }
DI void sts64(u32 a, u32x2 v) { *(LAS u32x2*)(size_t)a = v; }
DI void stsf(u32 a, float v) { *(LAS float*)(size_t)a = v; }
DI void stsf4(u32 a, f32x4 v) { *(LAS f32x4*)(size_t)a = v; }
DI void stsf2(u32 a, f32x2 v) { *(LAS f32x2*)(size_t)a = v; }
DI void sts16(u32 a, u16 v) { *(LAS u16*)(size_t)a = v; }

DI void glds16(const void* sbase, u32 voff, u32 ldsdst) {
    asm volatile("s_mov_b32 m0, %2\n\ts_nop 3\n\tglobal_load_lds_dwordx4 %0, %1" :: "v"(voff), "s"(sbase), "s"(ldsdst) : "memory");
}
DI void glds16c(const void* sbase, u32 voff, u32 ldsdst) {
    asm volatile("s_mov_b32 m0, %2\n\ts_nop 3\n\tglobal_load_lds_dwordx4 %0, %1 sc1" :: "v"(voff), "s"(sbase), "s"(ldsdst) : "memory");
}
template <int OFF> DI u32x4 gld16(const void* sbase, u32 voff) {
    u32x4 r; asm volatile("s_nop 4\n\tglobal_load_dwordx4 %0, %1, %2 offset:%3" : "=v"(r) : "v"(voff), "s"(sbase), "i"(OFF) : "memory"); return r;
}
template <int OFF> DI u32x4 gld16c(const void* sbase, u32 voff) {
    u32x4 r; asm volatile("s_nop 4\n\tglobal_load_dwordx4 %0, %1, %2 offset:%3 sc1" : "=v"(r) : "v"(voff), "s"(sbase), "i"(OFF) : "memory"); return r;
}
DI u32 gldu16(const void* sbase, u32 voff) {
    u32 r; asm volatile("s_nop 4\n\tglobal_load_ushort %0, %1, %2" : "=v"(r) : "v"(voff), "s"(sbase) : "memory"); return r;
}
#define VMCNT(n) asm volatile("s_waitcnt vmcnt(" #n ")" ::: "memory")
#define BAR() asm volatile("s_waitcnt lgkmcnt(0)\n\ts_barrier" ::: "memory")
#define RLX_AGENT __ATOMIC_RELAXED, __HIP_MEMORY_SCOPE_AGENT
DI u64 ld_agent(const char* p) { return __hip_atomic_load((GAS u64*)p, RLX_AGENT); }
DI void st_agent(char* p, u64 v) { __hip_atomic_store((GAS u64*)p, v, RLX_AGENT); }
DI void st_agent32(char* p, u32 v) { __hip_atomic_store((GAS u32*)p, v, RLX_AGENT); }
DI float ld_agentf(const char* p) { return __uint_as_float(__hip_atomic_load((GAS u32*)p, RLX_AGENT)); }
DI float xsum4(float x) {
    const auto s = __builtin_amdgcn_permlane16_swap(__float_as_uint(x), __float_as_uint(x), false, false);
    x = __uint_as_float(s[0]) + __uint_as_float(s[1]);
    const auto t = __builtin_amdgcn_permlane32_swap(__float_as_uint(x), __float_as_uint(x), false, false);
    return __uint_as_float(t[0]) + __uint_as_float(t[1]);
}
DI float xmax4(float x) {
    const auto s = __builtin_amdgcn_permlane16_swap(__float_as_uint(x), __float_as_uint(x), false, false);
    x = fmaxf(__uint_as_float(s[0]), __uint_as_float(s[1]));
    const auto t = __builtin_amdgcn_permlane32_swap(__float_as_uint(x), __float_as_uint(x), false, false);
    return fmaxf(__uint_as_float(t[0]), __uint_as_float(t[1]));
}
template <int CTRL> DI float dppf(float v) { return __builtin_bit_cast(float, __builtin_amdgcn_update_dpp(0, __builtin_bit_cast(int, v), CTRL, 0xf, 0xf, true)); }

constexpr u32 SPIN_CAP = 1u << 17;
DI void poll_flags(const char* base, int n, int lane, u64 tag, char* ws, u32 code) {
    const char* a = base + (lane < n ? lane : 0) * FS;
    u32 spins = 0;
    for (;;) {
        const u64 v = ld_agent(a);
        if (__all(v == tag)) break;
        if (++spins > SPIN_CAP) { if (lane == 0) st_agent32(ws + WS_ERR, code); break; }
        __builtin_amdgcn_s_sleep(1);
    }
}
typedef __attribute__((ext_vector_type(2))) _Float16 f16x2;
DI float hlo(u32 d) { return (float)__builtin_bit_cast(f16x2, d)[0]; }
DI float hhi(u32 d) { return (float)__builtin_bit_cast(f16x2, d)[1]; }
DI u32 packh(float a, float b) { const f16x2 v = {(_Float16)a, (_Float16)b}; return __builtin_bit_cast(u32, v); }
DI bool gran_ok(const u32x4& v, u64 tag) { return v[2] == (u32)tag && v[3] == (u32)(tag >> 32); }
DI f32x4 gran_sum(const u32x4& a, const u32x4& b, const u32x4& c, const u32x4& d) {
    const f32x4 s = {(hlo(a[0]) + hlo(b[0])) + (hlo(c[0]) + hlo(d[0])), (hhi(a[0]) + hhi(b[0])) + (hhi(c[0]) + hhi(d[0])),
                     (hlo(a[1]) + hlo(b[1])) + (hlo(c[1]) + hlo(d[1])), (hhi(a[1]) + hhi(b[1])) + (hhi(c[1]) + hhi(d[1]))};
    return (f32x4){rbf(s[0]), rbf(s[1]), rbf(s[2]), rbf(s[3])};
}
DI f32x4 gather1(const char* ws, u32 g0, u64 tag, char* wsw, u32 code) {
    u32 spins = 0; u32x4 v0, v1, v2, v3;
    const u32 o = WS_MODG + g0 * 16;
    for (;;) {
        asm volatile("s_nop 4\n\tglobal_load_dwordx4 %0, %4, %5 sc1\n\tglobal_load_dwordx4 %1, %6, %5 sc1\n\tglobal_load_dwordx4 %2, %7, %5 sc1\n\tglobal_load_dwordx4 %3, %8, %5 sc1\n\ts_waitcnt vmcnt(0)"
                     : "=&v"(v0), "=&v"(v1), "=&v"(v2), "=&v"(v3) : "v"(o), "s"(ws), "v"(o + 15360), "v"(o + 30720), "v"(o + 46080) : "memory");
        if (__all(gran_ok(v0, tag) && gran_ok(v1, tag) && gran_ok(v2, tag) && gran_ok(v3, tag))) break;
        if (++spins > SPIN_CAP) { st_agent32(wsw + WS_ERR, code); break; }
        __builtin_amdgcn_s_sleep(1);
    }
    return gran_sum(v0, v1, v2, v3);
}
DI void gather2(const char* ws, u32 g0, u32 g1, u64 tag, char* wsw, u32 code, f32x4& ra, f32x4& rb) {
    u32 spins = 0; u32x4 v0, v1, v2, v3, u0, u1, u2, u3;
    const u32 o = WS_MODG + g0 * 16, q = WS_MODG + g1 * 16;
    for (;;) {
        asm volatile("s_nop 4\n\tglobal_load_dwordx4 %0, %8, %9 sc1\n\tglobal_load_dwordx4 %1, %10, %9 sc1\n\tglobal_load_dwordx4 %2, %11, %9 sc1\n\tglobal_load_dwordx4 %3, %12, %9 sc1\n\t"
                     "global_load_dwordx4 %4, %13, %9 sc1\n\tglobal_load_dwordx4 %5, %14, %9 sc1\n\tglobal_load_dwordx4 %6, %15, %9 sc1\n\tglobal_load_dwordx4 %7, %16, %9 sc1\n\ts_waitcnt vmcnt(0)"
                     : "=&v"(v0), "=&v"(v1), "=&v"(v2), "=&v"(v3), "=&v"(u0), "=&v"(u1), "=&v"(u2), "=&v"(u3)
                     : "v"(o), "s"(ws), "v"(o + 15360), "v"(o + 30720), "v"(o + 46080), "v"(q), "v"(q + 15360), "v"(q + 30720), "v"(q + 46080) : "memory");
        if (__all(gran_ok(v0, tag) && gran_ok(v1, tag) && gran_ok(v2, tag) && gran_ok(v3, tag) && gran_ok(u0, tag) && gran_ok(u1, tag) && gran_ok(u2, tag) && gran_ok(u3, tag))) break;
        if (++spins > SPIN_CAP) { st_agent32(wsw + WS_ERR, code); break; }
        __builtin_amdgcn_s_sleep(1);
    }
    ra = gran_sum(v0, v1, v2, v3); rb = gran_sum(u0, u1, u2, u3);
}
DI void gather_strip(const char* ws, u32 gs, u32 gc, u32 strip, int l, u64 tag, char* wsw, u32 code) {
    f32x4 a, c;
    gather2(ws, l < 40 ? gs + l : gc + (l - 40), gc + 24 + (l & 15), tag, wsw, code, a, c);
    if (l >= 40) a = a + 1.f;
    c = c + 1.f;
    *(LAS u32x2*)(size_t)(strip + (l < 40 ? l * 8 : 320 + (l - 40) * 8)) = (u32x2){pack2(a[0], a[1]), pack2(a[2], a[3])};
    if (l < 16) *(LAS u32x2*)(size_t)(strip + 320 + (24 + l) * 8) = (u32x2){pack2(c[0], c[1]), pack2(c[2], c[3])};
}
DI void poll_at(const char* ws, u32 off, int lane, u64 tag, char* wsw, u32 code) {
    u32 spins = 0;
    for (;;) {
        const u64 v = ld_agent(ws + off);
        if (__all(v == tag)) break;
        if (++spins > SPIN_CAP) { if (lane == 0) st_agent32(wsw + WS_ERR, code); break; }
        __builtin_amdgcn_s_sleep(1);
    }
}

#define STAMP(i) do {} while (0)
#define STAMPL(i) do {} while (0)
#define STAMP_RT(i) do {} while (0)
#define STAMP_OUT() do {} while (0)

template <int N> using IC = std::integral_constant<int, N>;
template <int I, int E, class F> DI void static_for(F&& f) { if constexpr (I < E) { f(IC<I>{}); static_for<I + 1, E>(f); } }
template <int N> DI void vmw() { asm volatile("s_waitcnt vmcnt(%0)" :: "i"(N) : "memory"); }
template <int N> DI void vmw1(u32x4& a) { asm volatile("s_waitcnt vmcnt(%1)" : "+v"(a) : "i"(N) : "memory"); }
template <int N> DI void vmw4(u32x4& a, u32x4& b, u32x4& c, u32x4& d) { asm volatile("s_waitcnt vmcnt(%4)" : "+v"(a), "+v"(b), "+v"(c), "+v"(d) : "i"(N) : "memory"); }
DI float dot2(u32 a, u32 b, float c) { return __builtin_amdgcn_fdot2_f32_bf16(__builtin_bit_cast(bf16x2, a), __builtin_bit_cast(bf16x2, b), c, false); }
#define SB0() __builtin_amdgcn_sched_barrier(0)
#define SGB(m, n) __builtin_amdgcn_sched_group_barrier((m), (n), 0)
constexpr int M_MFMA = 0x8, M_DSR = 0x100;
#define Z4 ((f32x4){0.f, 0.f, 0.f, 0.f})
DI u32 lds_add(u32 a, u32 v) { return __hip_atomic_fetch_add((LAS u32*)(size_t)a, v, __ATOMIC_RELAXED, __HIP_MEMORY_SCOPE_WORKGROUP); }
DI u32 lds_ld(u32 a) { return __hip_atomic_load((LAS u32*)(size_t)a, __ATOMIC_RELAXED, __HIP_MEMORY_SCOPE_WORKGROUP); }
DI void worker_sync(u32 cnt, int lane) {
    asm volatile("s_waitcnt lgkmcnt(0)" ::: "memory");
    if (lane == 0) lds_add(cnt, 1u);
    for (u32 spins = 0; lds_ld(cnt) < 4u && spins < (1u << 22); ++spins) {}
    asm volatile("" ::: "memory");
}

__global__ __launch_bounds__(512, 2) void mega_kernel(Params p) {
    __shared__ __attribute__((aligned(16))) char ldsmem[163840];
    const u32 L0 = (u32)(uintptr_t)(LAS char*)ldsmem;
    const int tid = threadIdx.x, l = tid & 63;
    const int w = __builtin_amdgcn_readfirstlane(tid >> 6);
    const int b = blockIdx.x, xg = b & 7, sidx = b >> 3;
    const int rb = 4 * (xg >> 1) + (sidx >> 3), j = 8 * (xg & 1) + (sidx & 7);
    const int g = l >> 4, i16 = l & 15, q4 = i16 >> 2, p4 = l & 3;
    char* const ws = p.ws;
    asm volatile("" :: "s"(p.x), "s"(p.wqkv), "s"(p.wo), "s"(p.wg), "s"(p.wu), "s"(p.wd), "s"(p.c), "s"(p.wmod), "s"(p.out), "s"(p.ws));
    u64 tag;
#define ASK_TAG(ep) u32x2 ep; asm volatile("global_load_dwordx2 %0, %1, %2" : "=v"(ep) : "v"(WS_EPOCH), "s"(ws) : "memory")
#define TAKE_TAG(ep) do { asm volatile("s_waitcnt vmcnt(0)" : "+v"(ep) :: "memory"); \
        tag = (((u64)(u32)__builtin_amdgcn_readfirstlane(ep[1]) << 32) | (u32)__builtin_amdgcn_readfirstlane(ep[0])) + 1; } while (0)
    const __amdgpu_buffer_rsrc_t wsr = __builtin_amdgcn_make_buffer_rsrc(ws, 0, WS_END, 0x00020000);
#define ST16C(v, off) __builtin_amdgcn_raw_buffer_store_b128((v), wsr, (off), 0, 16)
#define ST8C(v, off)  __builtin_amdgcn_raw_buffer_store_b64((v), wsr, (off), 0, 16)
    STAMP_RT(62); STAMP(0);

    const int nj = j < 12 ? 7 : 6, Gj = j < 12 ? 7 * j : 84 + 6 * (j - 12);
    const bool wpwg = b < 216;
    const int wpm = b >= 108 ? 1 : 0, wpb = (b - 108 * wpm) >> 2, wpq = b & 3;
    if (w >= 4) {
        __builtin_amdgcn_s_setprio(3);
        const int u = w - 4;
        const bool tw = b >= 106;
        const int tb = (b - 106) / 5, tk = (b - 106) - 5 * tb, ts = 4 * tk + u;
        if (tw) {
            const char* src = (const char*)p.wqkv + ts * 122880 + tb * 128;
            const u32 voff = (l >> 3) * 3840 + (l & 7) * 16;
#pragma unroll
            for (int i = 0; i < 4; ++i) glds16(src + i * 8 * 3840, voff, L0 + L_WTRAW + u * 4096 + i * 1024);
        }
        ASK_TAG(epl);
        BAR();
        __builtin_amdgcn_s_setprio(0);
        TAKE_TAG(epl);
        if (tw) {
            const u32 a = L0 + L_WTRAW + u * 4096 + (8 * g + q4) * 128 + 8 * p4;
#pragma unroll
            for (int t = 0; t < 4; ++t) {
                const u32 cc = 8 * tb + 2 * t + (i16 >> 3);
                const u32 seg = cc >= 160 ? 2 : cc >= 80 ? 1 : 0, wi = cc - 80 * seg, jj = (wi * 13108) >> 16, c5 = wi - 5 * jj;
                const u32 c = seg == 0 ? 10 + c5 : seg == 1 ? c5 : 5 + c5, nt = c >> 1;
                const u32 dst = WS_WT + jj * 153600 + (nt < 5 ? ts * 5120 + nt * 1024 : 102400 + ts * 2560 + (nt - 5) * 1024)
                              + (nt < 7 ? (g * 16 + 8 * (c & 1) + (i16 & 7)) * 16 : g * 128 + (i16 & 7) * 16);
                ST16C(ldtr2(a + 32 * t, a + 32 * t + 512), dst);
            }
        }
        vmw<0>();
        {
            asm volatile("s_waitcnt lgkmcnt(0)" ::: "memory");
            u32 old = 0;
            if (l == 0) old = lds_add(L0 + L_CNT + 12, 1u);
            if (tw && __builtin_amdgcn_readfirstlane(old) == 3 && l == 0) st_agent(ws + WS_FWT + (b - 106) * FS, tag);
        }
        STAMPL(19);
        if (u == 0) {
            const int wh = l >= 30 ? 0 : l / 5, kr = l >= 30 ? 0 : l - 5 * wh;
            const int bl = (1280 * (wh >> 1) + 80 * j + 79 * (wh & 1)) >> 7;
            poll_at(ws, WS_FWT + (bl * 5 + kr) * FS, l, tag, ws, 13);
            for (u32 spins = 0; lds_ld(L0 + L_CNT + 12) < 4u && spins < (1u << 22); ++spins) {}
            if (l == 0) lds_add(L0 + L_CNT + 24, 1u);
        } else {
            for (u32 spins = 0; lds_ld(L0 + L_CNT + 24) < 1u && spins < (1u << 22); ++spins) {}
        }
        asm volatile("" ::: "memory");
        STAMPL(20);
        {
            const char* src = ws + WS_WT + j * 153600 + u * 1024;
            const u32 lo = l * 16;
#pragma unroll
            for (int i = 0; i < 38; ++i) {
                if (i == 32) { for (u32 spins = 0; lds_ld(L0 + L_CNT + 20) < 4u && spins < (1u << 22); ++spins) {} }
                if (i < 37 || u < 2) glds16c(src + i * 4096, lo, L0 + L_WKV + u * 1024 + i * 4096);
            }
        }
        if (wpwg && u == 0) gather_strip(ws, 480 + 40 * wpq, 640 + 40 * wpq, L0 + L_SB, l, tag, ws, 2);
        VMCNT(0);
        STAMPL(17);
    } else {
        __builtin_amdgcn_s_setprio(2);
        const bool modwg = b < 240;
        u32x4 mw[5]; u32 mc[5];
        if (modwg) {
#pragma unroll
            for (int i = 0; i < 5; ++i) {
                const int r = 160 * (b & 3) + (tid >> 3) + 32 * i;
                mw[i] = gld16<0>(p.wmod, r * 7680 + (b >> 2) * 128 + (tid & 7) * 16);
                mc[i] = gldu16(p.c, r * 2);
            }
        }
        {
            const char* src = (const char*)p.x + (rb * 128 + 8 * j) * 1280 + w * 1024;
            glds16(src, l * 16, L0 + L_XS + w * 1024);
            glds16(src + 4096, l * 16, L0 + L_XS + (w + 4) * 1024);
            if (w < 2) glds16(src + 8192, l * 16, L0 + L_XS + (w + 8) * 1024);
        }
        ASK_TAG(epw);
        if (tid < 8) *(LAS u32*)(size_t)(L0 + L_CNT + tid * 4) = 0u;
        BAR();
        STAMP(29);
        TAKE_TAG(epw);
        STAMP(30);
        if (modwg) {
            asm volatile("" : "+v"(mw[0]), "+v"(mw[1]), "+v"(mw[2]), "+v"(mw[3]), "+v"(mw[4]), "+v"(mc[0]), "+v"(mc[1]), "+v"(mc[2]), "+v"(mc[3]), "+v"(mc[4]) :: "memory");
            float acc[8];
#pragma unroll
            for (int e = 0; e < 8; ++e) acc[e] = 0.f;
#pragma unroll
            for (int i = 0; i < 5; ++i) {
                const float cv = bflo(mc[i]);
                const float sv = cv * __builtin_amdgcn_rcpf(1.f + __builtin_amdgcn_exp2f(-cv * LOG2E));
                const u32 slo = pack2(sv, 0.f), shi = slo << 16;
#pragma unroll
                for (int d = 0; d < 4; ++d) { acc[2 * d] = dot2(mw[i][d], slo, acc[2 * d]); acc[2 * d + 1] = dot2(mw[i][d], shi, acc[2 * d + 1]); }
            }
#pragma unroll
            for (int e = 0; e < 8; ++e) { const float v = acc[e]; acc[e] = xsum4(v + dppf<0x128>(v)); }
            if (l < 8) {
                stsf4(L0 + L_MODP + w * 256 + l * 32, (f32x4){acc[0], acc[1], acc[2], acc[3]});
                stsf4(L0 + L_MODP + w * 256 + l * 32 + 16, (f32x4){acc[4], acc[5], acc[6], acc[7]});
            }
            asm volatile("s_waitcnt lgkmcnt(0)" ::: "memory");
            u32 old = 0;
            if (l == 0) old = lds_add(L0 + L_CNT, 1u);
            if (__builtin_amdgcn_readfirstlane(old) == 3) {
                float v = 0.f;
#pragma unroll
                for (int ww = 0; ww < 4; ++ww) v += ldsf(L0 + L_MODP + ww * 256 + l * 4);
                const float v1 = dppf<0x55>(v), v2 = dppf<0xAA>(v), v3 = dppf<0xFF>(v), v0 = dppf<0x00>(v);
                const u32x4 gr = {packh(v0, v1), packh(v2, v3), (u32)tag, (u32)(tag >> 32)};
                if (p4 == 0) ST16C(gr, WS_MODG + ((b & 3) * 960 + (b >> 2) * 16 + (l >> 2)) * 16);
            }
        }
        STAMP(1);
        worker_sync(L0 + L_CNT + 16, l);
        const int tok = l & 7;
        u32x4 xr[3];
#pragma unroll
        for (int i = 0; i < 3; ++i) xr[i] = lds128(L0 + L_XS + tok * 1280 + (20 * w + (i < 2 ? (l >> 3) + 8 * i : 16 + ((l >> 3) & 3))) * 16);
        {
            float s1 = 0.f, s2 = 0.f;
#pragma unroll
            for (int i = 0; i < 3; ++i) {
                float a1 = 0.f, a2 = 0.f;
#pragma unroll
                for (int d = 0; d < 4; ++d) { a1 = dot2(xr[i][d], 0x3F803F80u, a1); a2 = dot2(xr[i][d], xr[i][d], a2); }
                if (i < 2 || l < 32) { s1 += a1; s2 += a2; }
            }
            asm volatile("s_waitcnt lgkmcnt(0)" ::: "memory");
            if (l == 0) lds_add(L0 + L_CNT + 20, 1u);
            s1 += dppf<0x128>(s1); s2 += dppf<0x128>(s2);
            s1 = xsum4(s1); s2 = xsum4(s2);
            if (l < 8) stsf2(L0 + L_STP + (w * 8 + l) * 8, (f32x2){s1, s2});
        }
        gather_strip(ws, 40 * w, 160 + 40 * w, L0 + L_GS + w * 640, l, tag, ws, 1);
        STAMP(2);
        worker_sync(L0 + L_CNT + 4, l);
        {
            f32x2 st = {0.f, 0.f};
#pragma unroll
            for (int ww = 0; ww < 4; ++ww) st += *(LAS f32x2*)(size_t)(L0 + L_STP + (ww * 8 + tok) * 8);
            const float mu = st[0] * (1.f / 640.f), var = fmaxf(st[1] * (1.f / 640.f) - mu * mu, 0.f);
            const float rs = __builtin_amdgcn_rsqf(var + 1e-5f), nmr = -mu * rs;
#pragma unroll
            for (int i = 0; i < 3; ++i) {
                const int cl = i < 2 ? (l >> 3) + 8 * i : 16 + ((l >> 3) & 3);
                const u32x4 sa = lds128(L0 + L_GS + w * 640 + cl * 16), gg = lds128(L0 + L_GS + w * 640 + 320 + cl * 16);
                u32x4 h;
#pragma unroll
                for (int d = 0; d < 4; ++d)
                    h[d] = pack2((bflo(xr[i][d]) * rs + nmr) * bflo(gg[d]) + bflo(sa[d]), (bfhi(xr[i][d]) * rs + nmr) * bfhi(gg[d]) + bfhi(sa[d]));
                if (i < 2 || l < 32) ST16C(h, WS_H + (((rb * 8 + (j >> 1)) * 80 + 20 * w + cl) * 16 + 8 * (j & 1) + tok) * 16);
            }
        }
        VMCNT(0);
        worker_sync(L0 + L_CNT + 8, l);
        if (tid == 0) st_agent(ws + WS_FH + (rb * 16 + j) * FS, tag);
        if (w == 0) poll_flags(ws + WS_FH + rb * 16 * FS, 16, l, tag, ws, 11);
        __builtin_amdgcn_s_setprio(0);
    }
    BAR();
    STAMP(3);

    u32x4 xa[20];
    const u32 lo16 = l * 16;
    const char* hsrc = ws + WS_H + (rb * 8 + w) * 80 * 256;
#define XL(s) xa[s] = gld16c<((s) & 3) * 1024>(hsrc + ((s) >> 2) * 4096, lo16)
    XL(0); XL(1); XL(2); XL(3); XL(4); XL(5);
    const char* kvx = ws + WS_KV + (rb * 16 + j) * 22528;
    {
        f32x4 acc[5];
#pragma unroll
        for (int n = 0; n < 5; ++n) acc[n] = Z4;
        const u32 b0 = L0 + L_WKV + lo16;
        u32x4 bf[2][5];
#pragma unroll
        for (int n = 0; n < 5; ++n) bf[0][n] = lds128(b0 + n * 1024);
        static_for<0, 20>([&](auto S) {
            constexpr int s = S;
            vmw1<(s < 15 ? 5 : 19 - s)>(xa[s]);
            if constexpr (s + 6 < 20) XL(s + 6);
            SB0();
#pragma unroll
            for (int n = 0; n < 5; ++n) {
                if (s < 19) bf[(s + 1) & 1][n] = lds128(b0 + (s + 1) * 5120 + n * 1024);
                acc[n] = mfma(bf[s & 1][n], xa[s], acc[n]);
            }
#pragma unroll
            for (int n = 0; n < 5; ++n) { SGB(M_DSR, 1); SGB(M_MFMA, 1); }
            SB0();
        });
        STAMP(18);
        BAR();
        const int row = 256 + 16 * w + i16;
#pragma unroll
        for (int n = 0; n < 5; ++n) {
            const int Q = 4 * n + g;
            const u32x2 v = {pack2(acc[n][0], acc[n][1]), pack2(acc[n][2], acc[n][3])};
            const int wi = Q - 10, hd = wi >= 5 ? 1 : 0;
            const u32 a = Q < 10 ? L_K + row * 80 + 8 * Q : L_V + row * 96 + 48 * hd + 8 * (wi - 5 * hd);
            sts64(L0 + a, v);
        }
        if (g < 2) sts64(L0 + L_V + row * 96 + 48 * g + 40, (u32x2){0x3F80u, 0u});
        if (rb < 2) {
            const int nrow = rb == 0 ? 256 : 128;
            for (int i = tid; i < nrow * 5; i += 512) sts128(L0 + L_K + i * 16, (u32x4){0, 0, 0, 0});
            for (int i = tid; i < nrow * 6; i += 512) sts128(L0 + L_V + i * 16, (u32x4){0, 0, 0, 0});
        }
        BAR();
    }
#undef XL
    STAMP(4);
    {
        u32x4 kvd[3];
#pragma unroll
        for (int e = 0; e < 3; ++e) {
            const int n = min(w + 8 * e, 21);
            kvd[e] = lds128(L0 + (n < 10 ? L_K + 20480 + n * 1024 : L_V + 24576 + (n - 10) * 1024) + lo16);
        }
        const u32 kvo = WS_KV + (rb * 16 + j) * 22528 + w * 1024 + lo16;
        const char* wsrc = (const char*)(wpm ? p.wu : p.wg) + (160 * wpq + 8 * w) * 3456 + wpb * 128;
        const u32 wvo = (l >> 3) * 3456 + (l & 7) * 16;
        f32x4 acc[3];
#pragma unroll
        for (int n = 0; n < 3; ++n) acc[n] = Z4;
        const u32 bq = L0 + L_WQ + lo16, bq2 = L0 + L_WQ + 2048 + g * 128 + (i16 & 7) * 16;
#define QRD(s, n) lds128(((n) < 2 ? bq + (n) * 1024 : bq2) + (s) * 2560)
        u32x4 bf[3][3];
#pragma unroll
        for (int n = 0; n < 3; ++n) { bf[0][n] = QRD(0, n); bf[1][n] = QRD(1, n); }
        static_for<0, 20>([&](auto S) {
            constexpr int s = S;
            if constexpr (s == 0) { if (rb < 15) ST16C(kvd[0], kvo); }
            if constexpr (s == 2) { if (rb < 15) ST16C(kvd[1], kvo + 8192); }
            if constexpr (s == 4) { if (rb < 15 && w < 6) ST16C(kvd[2], kvo + 16384); }
            if constexpr (s == 8) { if (wpwg) glds16(wsrc, wvo, L0 + L_WPRAW + w * 1024); }
            if constexpr (s == 11) { if (wpwg) glds16(wsrc + 64 * 3456, wvo, L0 + L_WPRAW + (w + 8) * 1024); }
            if constexpr (s == 14) { if (wpwg && w < 4) glds16(wsrc + 128 * 3456, wvo, L0 + L_WPRAW + (w + 16) * 1024); }
            SB0();
#pragma unroll
            for (int n = 0; n < 3; ++n) {
                if (s < 18) bf[(s + 2) % 3][n] = QRD(s + 2, n);
                acc[n] = mfma(bf[s % 3][n], xa[s], acc[n]);
            }
#pragma unroll
            for (int n = 0; n < 3; ++n) { SGB(M_DSR, 1); SGB(M_MFMA, 1); }
            SB0();
        });
#undef QRD
        STAMP(21);
#pragma unroll
        for (int n = 0; n < 3; ++n) {
            const int Q = 4 * n + g;
            const u32x2 v = {pack2(acc[n][0] * QSCALE, acc[n][1] * QSCALE), pack2(acc[n][2] * QSCALE, acc[n][3] * QSCALE)};
            if (Q < 10) sts64(L0 + L_Q + (16 * w + i16) * 80 + 8 * Q, v);
        }
    }
    VMCNT(0);
    BAR();
    if (tid == 0) st_agent(ws + WS_FKV + (j * 16 + rb) * FS, tag);
    STAMP(5);
    auto wp_slot = [&](int gam, int& jj, int& slot) {
        jj = gam < 84 ? gam / 7 : 12 + (gam - 84) / 6;
        const int ar = gam - (jj < 12 ? 7 * jj : 84 + 6 * (jj - 12)), a = (jj < 12 || ar < 3) ? ar : ar + 1;
        slot = a < 3 ? 2 * a + wpm : a == 3 ? (wpm ? 13 : 6) : 7 + 2 * (a - 4) + wpm;
    };
    if (wpwg) {
#pragma unroll
        for (int i = 0; i < 3; ++i) {
            if (i < 2 || w < 4) {
                const int f = w + 8 * i, ks = f >> 2, t16 = f & 3;
                int jj, slot; wp_slot(4 * wpb + t16, jj, slot);
                const u32 a = L0 + L_WPRAW + (32 * ks + 8 * g + q4) * 128 + 32 * t16 + 8 * p4;
                const u32x4 wr = ldtr2(a, a + 512);
                const u32x4 g2 = lds128(L0 + L_G2 + (32 * ks + 8 * g) * 2), sb = lds128(L0 + L_SB + (32 * ks + 8 * g) * 2);
                u32x4 o; float us = 0.f, vs = 0.f;
#pragma unroll
                for (int d = 0; d < 4; ++d) {
                    o[d] = pack2(dot2(wr[d], g2[d] & 0xffffu, 0.f), dot2(wr[d], g2[d] & 0xffff0000u, 0.f));
                    us = dot2(o[d], 0x3F803F80u, us);
                    vs = dot2(wr[d], sb[d], vs);
                }
                ST16C(o, WS_WP + ((jj * 20 + 5 * wpq + ks) * 14 + slot) * 1024 + lo16);
                us = xsum4(us); vs = xsum4(vs);
                if (l < 32) stsf(L0 + L_UVL + f * 128 + l * 4, l < 16 ? us : vs);
            }
        }
    }
    if (w == 0) {
        if (rb >= 1) poll_flags(ws + WS_FKV + (j * 16 + (rb >= 2 ? rb - 2 : rb - 1)) * FS, rb >= 2 ? 2 : 1, l, tag, ws, 3);
    }
    BAR();
    STAMP(6);
    if (rb >= 1) {
#pragma unroll
        for (int e = 0; e < 3; ++e) {
            const int n = w + 8 * e;
            if (n < 22) {
                const u32 d = n < 10 ? L_K + n * 1024 : L_V + (n - 10) * 1024, st = n < 10 ? 10240 : 12288;
                glds16c(kvx - 16 * 22528 + n * 1024, lo16, L0 + d + st);
                if (rb >= 2) glds16c(kvx - 32 * 22528 + n * 1024, lo16, L0 + d);
            }
        }
    }
    if (wpwg && tid < 128) {
        const int t16 = tid >> 5;
        int jj, slot; wp_slot(4 * wpb + t16, jj, slot);
        float v = 0.f;
#pragma unroll
        for (int ks = 0; ks < 5; ++ks) v += ldsf(L0 + L_UVL + (4 * ks + t16) * 128 + (tid & 31) * 4);
        st_agent32(ws + WS_UV + ((jj * 14 + slot) * 4 + wpq) * 128 + (tid & 31) * 4, __float_as_uint(v));
    }
    const int hf = j & 1, cb = j >> 1;
    VMCNT(0);
    BAR();
    u32 wvoff[5];
#pragma unroll
    for (int i = 0; i < 5; ++i) {
        const u32 sg = 64 * i + l, pos = (sg * 6554) >> 16, c = sg - pos * 10, k = pos ^ (((pos >> 3) & 1) << 2);
        wvoff[i] = k * 1280 + c * 16;
    }
    const char* wosrc = (const char*)p.wo + cb * 160;
#pragma unroll
    for (int e = 0; e < 2; ++e)
#pragma unroll
        for (int i = 0; i < 5; ++i) glds16(wosrc + (4 + w + 8 * e) * 32 * 1280, wvoff[i], L0 + L_WO + (4 + w + 8 * e) * 5120 + i * 1024);
    STAMP(7);

    {
        const int hd = w & 1, qi = w >> 1;
        const int kr0 = 32 * qi;
        u32x4 qf[2];
#pragma unroll
        for (int a = 0; a < 2; ++a) {
            const u32 qa = L0 + L_Q + (kr0 + 16 * a + i16) * 80 + hd * 40;
            const u32x2 q0 = lds64(qa + 8 * g);
            u32x2 q1 = lds64(qa + 32);
            if (g != 0) q1 = (u32x2){0, 0};
            qf[a] = (u32x4){q0[0], q0[1], q1[0], q1[1]};
        }
        const u32 kb0 = L0 + L_K + (kr0 + i16) * 80 + hd * 40 + 8 * g, kb1 = L0 + L_K + (kr0 + i16) * 80 + hd * 40 + 32;
        f32x4 mfirst, mlast;
#pragma unroll
        for (int r = 0; r < 4; ++r) { const bool lo = 4 * g + r <= i16; mfirst[r] = lo ? -INFINITY : 0.f; mlast[r] = lo ? 0.f : -INFINITY; }
        f32x4 s0[17], s1[17];
        {
            u32x4 kf[3];
#define KRD(t) do { const u32x2 k0 = lds64(kb0 + (t) * 1280), k1 = lds64(kb1 + (t) * 1280); kf[(t) % 3] = (u32x4){k0[0], k0[1], k1[0], k1[1]}; } while (0)
            KRD(0); KRD(1);
            static_for<0, 18>([&](auto T) {
                constexpr int t = T;
                SB0();
                if constexpr (t < 16) KRD(t + 2);
                if constexpr (t < 17) s0[t] = mfma(kf[t % 3], qf[0], t == 0 ? mfirst : t == 16 ? mlast : Z4);
                if constexpr (t > 0) s1[t - 1] = mfma(kf[t % 3], qf[1], t == 1 ? mfirst : t == 17 ? mlast : Z4);
                SGB(M_DSR, 2); SGB(M_MFMA, 2);
                SB0();
            });
#undef KRD
        }
        if (rb < 2) {
            const int fv = rb == 1 ? 128 : 256;
#pragma unroll
            for (int t = 0; t < 16; ++t) {
                const bool d0 = kr0 + 16 * t < fv, d1 = kr0 + 16 * t + 16 < fv;
#pragma unroll
                for (int r = 0; r < 4; ++r) { s0[t][r] = d0 ? -INFINITY : s0[t][r]; s1[t][r] = d1 ? -INFINITY : s1[t][r]; }
            }
        }
        STAMP(22);
        float m0 = fmaxf(fmaxf(s0[0][0], s0[0][1]), fmaxf(s0[0][2], s0[0][3])), m1 = fmaxf(fmaxf(s1[0][0], s1[0][1]), fmaxf(s1[0][2], s1[0][3]));
#pragma unroll
        for (int t = 1; t < 17; ++t) {
            m0 = fmaxf(fmaxf(m0, s0[t][0]), s0[t][1]); m1 = fmaxf(fmaxf(m1, s1[t][0]), s1[t][1]);
            m0 = fmaxf(fmaxf(m0, s0[t][2]), s0[t][3]); m1 = fmaxf(fmaxf(m1, s1[t][2]), s1[t][3]);
        }
        m0 = xmax4(m0); m1 = xmax4(m1);
        STAMP(23);
        f32x4 o00 = Z4, o01 = Z4, o10 = Z4, o11 = Z4;
        const u32 vb = L0 + L_V + (kr0 + 4 * g + q4) * 96 + hd * 48 + 8 * p4;
        auto pv = [&](auto SUB) {
            constexpr bool sub = SUB;
            u32x4 vf[2][2];
            vf[0][0] = ldtr2(vb, vb + 16 * 96); vf[0][1] = ldtr2(vb + 32, vb + 16 * 96 + 32);
#define EX(v, m) __builtin_amdgcn_exp2f(sub ? (v) - (m) : (v))
            static_for<0, 9>([&](auto U) {
                constexpr int u = U;
                if constexpr (u < 8) { const u32 a = vb + (u + 1) * 32 * 96; vf[(u + 1) & 1][0] = ldtr2(a, a + 16 * 96); vf[(u + 1) & 1][1] = ldtr2(a + 32, a + 16 * 96 + 32); }
                u32x4 pa, pb;
                pa[0] = pack2(EX(s0[2 * u][0], m0), EX(s0[2 * u][1], m0)); pa[1] = pack2(EX(s0[2 * u][2], m0), EX(s0[2 * u][3], m0));
                if constexpr (u < 8) { pa[2] = pack2(EX(s0[2 * u + 1][0], m0), EX(s0[2 * u + 1][1], m0)); pa[3] = pack2(EX(s0[2 * u + 1][2], m0), EX(s0[2 * u + 1][3], m0)); }
                else { pa[2] = 0; pa[3] = 0; }
                if constexpr (u > 0) { pb[0] = pack2(EX(s1[2 * u - 1][0], m1), EX(s1[2 * u - 1][1], m1)); pb[1] = pack2(EX(s1[2 * u - 1][2], m1), EX(s1[2 * u - 1][3], m1)); }
                else { pb[0] = 0; pb[1] = 0; }
                pb[2] = pack2(EX(s1[2 * u][0], m1), EX(s1[2 * u][1], m1)); pb[3] = pack2(EX(s1[2 * u][2], m1), EX(s1[2 * u][3], m1));
                o00 = mfma(vf[u & 1][0], pa, o00); o01 = mfma(vf[u & 1][1], pa, o01);
                o10 = mfma(vf[u & 1][0], pb, o10); o11 = mfma(vf[u & 1][1], pb, o11);
                SB0();
            });
#undef EX
        };
        if (__all(fabsf(m0) <= 64.f && fabsf(m1) <= 64.f)) pv(std::false_type{}); else pv(std::true_type{});
        STAMP(24);
        const float i0 = __builtin_amdgcn_rcpf(__shfl(o01[0], i16 + 16)), i1 = __builtin_amdgcn_rcpf(__shfl(o11[0], i16 + 16));
        const u32 oa = L0 + L_Q + (kr0 + i16) * 80 + hd * 40;
        sts64(oa + 8 * g, (u32x2){pack2(o00[0] * i0, o00[1] * i0), pack2(o00[2] * i0, o00[3] * i0)});
        sts64(oa + 1280 + 8 * g, (u32x2){pack2(o10[0] * i1, o10[1] * i1), pack2(o10[2] * i1, o10[3] * i1)});
        if (g == 0) {
            sts64(oa + 32, (u32x2){pack2(o01[0] * i0, o01[1] * i0), pack2(o01[2] * i0, o01[3] * i0)});
            sts64(oa + 1280 + 32, (u32x2){pack2(o11[0] * i1, o11[1] * i1), pack2(o11[2] * i1, o11[3] * i1)});
        }
    }
    BAR();
    {
        const int tok = 64 * (w & 1) + l;
        const u32 oo = WS_O + (((rb * 8 + (tok >> 4)) * 80 + 5 * j) * 16 + (tok & 15)) * 16;
        ST16C(lds128(L0 + L_Q + tok * 80 + (w >> 1) * 16), oo + (w >> 1) * 256);
        if (w < 2) ST16C(lds128(L0 + L_Q + tok * 80 + 64), oo + 4 * 256);
    }
    VMCNT(0);
    BAR();
    if (tid == 0) {
        st_agent(ws + WS_FO + (rb * 16 + j) * FS, tag);
        if (wpwg) st_agent(ws + WS_FWP + b * FS, tag);
    }
    STAMP(8);

    const int wm = w & 3, wk = w >> 2;
    const int mt3 = rb * 8 + 4 * hf + wm;
    const int nt0 = wk == 0 ? 0 : 3, ntn = wk == 0 ? 3 : 2;
    u32x2 x1k[3];
    {
        const int wdu = b + 256 * (w - 6);
        const bool wdw = w >= 6 && wdu < 432;
        if (w >= 1 && w < 5) {
#pragma unroll
            for (int i = 0; i < 5; ++i) glds16(wosrc + (w - 1) * 32 * 1280, wvoff[i], L0 + L_WO + (w - 1) * 5120 + i * 1024);
        } else if (w >= 5) {
#pragma unroll
            for (int e = 0; e < 4; ++e) {
                const int pc = w - 5 + 3 * e;
                if (pc < 10) {
                    const u32 sg = 64 * pc + l, r = (sg * 6554) >> 16, c = sg - r * 10;
                    glds16((const char*)p.x + (rb * 128 + 64 * hf) * 1280 + cb * 160, r * 1280 + c * 16, L0 + L_XR + pc * 1024);
                }
            }
            if (wdw) {
                const int ucb = wdu / 54, us = wdu - 54 * ucb;
#pragma unroll
                for (int i = 0; i < 5; ++i) glds16((const char*)p.wd + ucb * 160 + us * 32 * 1280, wvoff[i], L0 + L_WDRAW + (w - 6) * 5120 + i * 1024);
            }
        }
        if (w == 1 && l < 20) stsf4(L0 + L_GA + l * 16, gather1(ws, 320 + 20 * cb + l, tag, ws, 4));
        if (w == 0) {
            const int bl = min((Gj >> 2) + ((l >> 2) % 3), (Gj + nj - 1) >> 2);
            poll_at(ws, WS_FWP + (108 * (l >= 12 && l < 24 ? 1 : 0) + 4 * bl + (l & 3)) * FS, l, tag, ws, 6);
            poll_flags(ws + WS_FO + rb * 16 * FS, 16, l, tag, ws, 5);
        }
        if (wdw) vmw<5>(); else vmw<0>();
        BAR();
        STAMP(9);
        u32x4 a[10];
        const char* asrc = ws + WS_O + (mt3 * 80 + 40 * wk) * 256;
#define AL(s) a[s] = gld16c<((s) & 3) * 1024>(asrc + ((s) >> 2) * 4096, lo16)
        AL(0); AL(1); AL(2); AL(3);
        f32x4 acc[5];
#pragma unroll
        for (int n = 0; n < 5; ++n) acc[n] = Z4;
        const u32 wb = L0 + L_WO + wk * 51200;
        const u32 b0 = wb + 160 * (8 * g + 4 * (g & 1) + q4) + 8 * p4, b1 = wb + 160 * (8 * g + 4 * ((g & 1) ^ 1) + q4) + 8 * p4;
        u32x4 bf[2][5];
#pragma unroll
        for (int n = 0; n < 5; ++n) bf[0][n] = ldtr2(b0 + n * 32, b1 + n * 32);
        static_for<0, 10>([&](auto S) {
            constexpr int s = S;
            vmw1<(s < 7 ? 3 : 9 - s)>(a[s]);
            if constexpr (s + 4 < 10) AL(s + 4);
            SB0();
#pragma unroll
            for (int n = 0; n < 5; ++n) {
                if (s < 9) bf[(s + 1) & 1][n] = ldtr2(b0 + (s + 1) * 5120 + n * 32, b1 + (s + 1) * 5120 + n * 32);
                acc[n] = mfma(bf[s & 1][n], a[s], acc[n]);
            }
#pragma unroll
            for (int n = 0; n < 5; ++n) { SGB(M_DSR, 2); SGB(M_MFMA, 1); }
            SB0();
        });
#undef AL
        STAMP(25);
        if (wdw) {
            const u32 wr = L0 + L_WDRAW + (w - 6) * 5120;
            const u32 r0 = wr + 160 * (8 * g + 4 * (g & 1) + q4) + 8 * p4, r1 = wr + 160 * (8 * g + 4 * ((g & 1) ^ 1) + q4) + 8 * p4;
#pragma unroll
            for (int n = 0; n < 5; ++n) ST16C(ldtr2(r0 + n * 32, r1 + n * 32), WS_WD + (wdu * 5 + n) * 1024 + lo16);
        }
        if (wk == 0) { stsf4(L0 + L_RED3 + (wm * 5 + 3) * 1024 + lo16, acc[3]); stsf4(L0 + L_RED3 + (wm * 5 + 4) * 1024 + lo16, acc[4]); }
        else { stsf4(L0 + L_RED3 + (wm * 5 + 0) * 1024 + lo16, acc[0]); stsf4(L0 + L_RED3 + (wm * 5 + 1) * 1024 + lo16, acc[1]); stsf4(L0 + L_RED3 + (wm * 5 + 2) * 1024 + lo16, acc[2]); }
        BAR();
        float s1 = 0.f, s2 = 0.f;
#pragma unroll
        for (int n = 0; n < 3; ++n) {
            if (n < ntn) {
                const int nt = nt0 + n;
                const f32x4 mine = wk == 0 ? acc[n] : acc[n < 2 ? 3 + n : 4];
                const f32x4 v = mine + ldsf4(L0 + L_RED3 + (wm * 5 + nt) * 1024 + lo16);
                const f32x4 ga = ldsf4(L0 + L_GA + (16 * nt + 4 * g) * 4);
                const u32x2 xr = lds64(L0 + L_XR + (16 * wm + i16) * 160 + 32 * nt + 8 * g);
                const u32x2 o = {pack2(bflo(xr[0]) + ga[0] * v[0], bfhi(xr[0]) + ga[1] * v[1]), pack2(bflo(xr[1]) + ga[2] * v[2], bfhi(xr[1]) + ga[3] * v[3])};
                x1k[n] = o;
                s1 = dot2(o[0], 0x3F803F80u, s1); s1 = dot2(o[1], 0x3F803F80u, s1);
                s2 = dot2(o[0], o[0], s2); s2 = dot2(o[1], o[1], s2);
                sts64(L0 + L_X1S + (16 * wm + i16) * 160 + 32 * nt + 8 * g, o);
            } else x1k[n] = (u32x2){0, 0};
        }
        s1 = xsum4(s1); s2 = xsum4(s2);
        if (l < 16) stsf2(L0 + L_ST3 + (wk * 64 + 16 * wm + l) * 8, (f32x2){s1, s2});
        BAR();
        {
            const u32 xo = WS_X1 + (((rb * 8 + 4 * hf + g) * 80 + 10 * cb) * 16 + i16) * 16;
            ST16C(lds128(L0 + L_X1S + l * 160 + w * 16), xo + w * 256);
            if (w < 2) ST16C(lds128(L0 + L_X1S + l * 160 + (w + 8) * 16), xo + (w + 8) * 256);
            if (w == 2) {
                const f32x2 u = *(LAS f32x2*)(size_t)(L0 + L_ST3 + l * 8), v = *(LAS f32x2*)(size_t)(L0 + L_ST3 + (64 + l) * 8);
                const f32x2 sum = u + v;
                ST8C(__builtin_bit_cast(u32x2, sum), WS_STAT + ((rb * 8 + cb) * 128 + 64 * hf + l) * 8);
            }
        }
        STAMP(26);
        VMCNT(0);
        BAR();
        if (tid == 0) { st_agent(ws + WS_FX1 + (rb * 16 + j) * FS, tag); st_agent(ws + WS_FW2 + b * FS, tag); }
    }
    STAMP(10);

    {
        const int wn = w >> 2;
        const char* wpj = ws + WS_WP + (j * 20 * 14 + w) * 1024;
        const char* x1b = ws + WS_X1 + (rb * 8 + w) * 80 * 256;
        const u32 r4w = L0 + L_R4 + w * 1024;
#define P4_B1(t, i) glds16c(wpj + ((t) * 28 + 8 * (i)) * 1024, lo16, r4w + ((t) % 3) * R4_STAGE + 16384 + 8 * (i) * 1024)
#define P4_A1(t, i) glds16c(x1b + (8 * (t) + 4 * (i)) * 256, lo16, r4w + ((t) % 3) * R4_STAGE + 8 * (i) * 1024)
#define P4_B(t) do { P4_B1(t, 0); P4_B1(t, 1); P4_B1(t, 2); if (w < 4) P4_B1(t, 3); } while (0)
        P4_B(0); P4_B(1); P4_B(2);
        if (w == 0) poll_flags(ws + WS_FX1 + rb * 16 * FS, 16, l, tag, ws, 7);
        BAR();
        STAMP(11);
        glds16c(ws + WS_STAT + rb * 8192 + w * 1024, lo16, L0 + L_ST4 + w * 1024);
        if (w < 7) glds16c(ws + WS_UV + j * 7168 + w * 1024, lo16, L0 + L_UV + w * 1024);
        P4_A1(0, 0); P4_A1(0, 1); P4_A1(1, 0); P4_A1(1, 1); P4_A1(2, 0); P4_A1(2, 1);
        f32x4 acc[7][2];
#pragma unroll
        for (int n = 0; n < 7; ++n) { acc[n][0] = Z4; acc[n][1] = Z4; }
        const u32 ra = L0 + L_R4 + 2 * wm * 1024 + lo16, rbb = L0 + L_R4 + 16384 + 7 * wn * 1024 + lo16;
        u32x4 fa[2][2], fb[2][7];
#define P4_RA(buf, t, ks) do { const u32 so = ((t) % 3) * R4_STAGE + (ks) * 8192; fa[buf][0] = lds128(ra + so); fa[buf][1] = lds128(ra + so + 1024); } while (0)
#define P4_RB(buf, t, ks, n) fb[buf][n] = lds128(rbb + ((t) % 3) * R4_STAGE + (ks) * 14336 + (n) * 1024)
#define P4_MM(buf, n) do { acc[n][0] = mfma(fb[buf][n], fa[buf][0], acc[n][0]); acc[n][1] = mfma(fb[buf][n], fa[buf][1], acc[n][1]); } while (0)
#define P4_HALF(buf, rd, t, ks, V0, V1, V2) do { \
            SB0(); if (rd) { P4_RA((buf) ^ 1, t, ks); P4_RB((buf) ^ 1, t, ks, 0); } P4_MM(buf, 0); SB0(); V0; \
            SB0(); if (rd) P4_RB((buf) ^ 1, t, ks, 1); P4_MM(buf, 1); SB0(); \
            SB0(); if (rd) P4_RB((buf) ^ 1, t, ks, 2); P4_MM(buf, 2); SB0(); V1; \
            SB0(); if (rd) P4_RB((buf) ^ 1, t, ks, 3); P4_MM(buf, 3); SB0(); \
            SB0(); if (rd) P4_RB((buf) ^ 1, t, ks, 4); P4_MM(buf, 4); SB0(); V2; \
            SB0(); if (rd) P4_RB((buf) ^ 1, t, ks, 5); P4_MM(buf, 5); SB0(); \
            SB0(); if (rd) P4_RB((buf) ^ 1, t, ks, 6); P4_MM(buf, 6); SB0(); } while (0)
        VMCNT(4);
        BAR();
        STAMP(27);
        P4_RA(0, 0, 0);
#pragma unroll
        for (int n = 0; n < 7; ++n) P4_RB(0, 0, 0, n);
        static_for<0, 10>([&](auto T) {
            constexpr int t = T;
            constexpr bool f = t >= 1 && t + 2 <= 9, sN = t + 3 <= 9;
            P4_HALF(0, true, t, 1, if (f && w < 4) P4_B1(t + 2, 3), if (f) P4_A1(t + 2, 0), if (f) P4_A1(t + 2, 1));
            if constexpr (t == 5) {
                if (tid < 128) {
                    f32x2 st = {0.f, 0.f};
#pragma unroll
                    for (int c = 0; c < 8; ++c) st += *(LAS f32x2*)(size_t)(L0 + L_ST4 + (c * 128 + tid) * 8);
                    const float mu = st[0] * (1.f / 640.f), var = fmaxf(st[1] * (1.f / 640.f) - mu * mu, 0.f), r = __builtin_amdgcn_rsqf(var + 1e-5f);
                    stsf2(L0 + L_MR + tid * 8, (f32x2){r, -mu * r});
                } else {
                    const int e = tid - 128;
                    stsf(L0 + L_UVS + e * 4, (ldsf(L0 + L_UV + (e >> 5) * 512 + (e & 31) * 4) + ldsf(L0 + L_UV + (e >> 5) * 512 + 128 + (e & 31) * 4))
                                           + (ldsf(L0 + L_UV + (e >> 5) * 512 + 256 + (e & 31) * 4) + ldsf(L0 + L_UV + (e >> 5) * 512 + 384 + (e & 31) * 4)));
                    if (e < 64) { const int e2 = e + 384;
                        stsf(L0 + L_UVS + e2 * 4, (ldsf(L0 + L_UV + (e2 >> 5) * 512 + (e2 & 31) * 4) + ldsf(L0 + L_UV + (e2 >> 5) * 512 + 128 + (e2 & 31) * 4))
                                                + (ldsf(L0 + L_UV + (e2 >> 5) * 512 + 256 + (e2 & 31) * 4) + ldsf(L0 + L_UV + (e2 >> 5) * 512 + 384 + (e2 & 31) * 4))); }
                }
            }
            if constexpr (t < 9) {
                if constexpr (t == 0) vmw<2>();
                else if constexpr (t == 8) vmw<0>();
                else { if (w < 4) vmw<6>(); else vmw<5>(); }
                BAR();
                P4_HALF(1, true, t + 1, 0, if (sN) P4_B1(t + 3, 0), if (sN) P4_B1(t + 3, 1), if (sN) P4_B1(t + 3, 2));
            } else {
                P4_HALF(1, false, 0, 0, , , );
            }
        });
#undef P4_B
#undef P4_B1
#undef P4_A1
#undef P4_RA
#undef P4_RB
#undef P4_MM
#undef P4_HALF
        STAMP(12);
        f32x2 mr[2];
#pragma unroll
        for (int m = 0; m < 2; ++m) mr[m] = *(LAS f32x2*)(size_t)(L0 + L_MR + (32 * wm + 16 * m + i16) * 8);
#pragma unroll
        for (int n = 0; n < 7; ++n) {
            const f32x4 u = ldsf4(L0 + L_UVS + (7 * wn + n) * 128 + 16 * g), v = ldsf4(L0 + L_UVS + (7 * wn + n) * 128 + 64 + 16 * g);
#pragma unroll
            for (int m = 0; m < 2; ++m) acc[n][m] = acc[n][m] * mr[m][0] + (u * mr[m][1] + v);
        }
        if (nj == 7) stsf4(L0 + L_XCH + w * 1024 + lo16, wn == 0 ? acc[6][1] : acc[6][0]);
        auto silu_mul = [&](const f32x4 gt, const f32x4 up) -> u32x2 {
            const f32x4 e = gt * (-LOG2E), gu = gt * up;
            f32x4 d;
#pragma unroll
            for (int r = 0; r < 4; ++r) d[r] = __builtin_amdgcn_exp2f(e[r]);
            d = d + 1.f;
#pragma unroll
            for (int r = 0; r < 4; ++r) d[r] = __builtin_amdgcn_rcpf(d[r]);
            const f32x4 o = gu * d;
            return (u32x2){pack2(o[0], o[1]), pack2(o[2], o[3])};
        };
#pragma unroll
        for (int a = 0; a < 3; ++a) {
            const int la = a + 4 * wn, gam = Gj + (la <= 3 || nj == 7 ? la : la - 1);
            const u32x2 o0 = silu_mul(acc[2 * a][0], acc[2 * a + 1][0]), o1 = silu_mul(acc[2 * a][1], acc[2 * a + 1][1]);
            const auto s0 = __builtin_amdgcn_permlane16_swap(o0[0], o1[0], false, false);
            const auto s1 = __builtin_amdgcn_permlane16_swap(o0[1], o1[1], false, false);
            const u32x4 v = {s0[0], s1[0], s0[1], s1[1]};
            ST16C(v, WS_ACT + (((rb * 8 + 2 * wm + (g & 1)) * 216 + 2 * gam + (g >> 1)) * 16 + i16) * 16);
        }
        STAMP(28);
        if (nj == 7) {
            BAR();
            const f32x4 other = ldsf4(L0 + L_XCH + (w ^ 4) * 1024 + lo16);
            const u32x2 o = wn == 0 ? silu_mul(acc[6][0], other) : silu_mul(other, acc[6][1]);
            ST8C(o, WS_ACT + (((rb * 8 + 2 * wm + wn) * 216 + 2 * (Gj + 3) + (g >> 1)) * 16 + i16) * 16 + 8 * (g & 1));
        }
        VMCNT(0);
        BAR();
        if (tid == 0) st_agent(ws + WS_FACT + (rb * 16 + j) * FS, tag);
    }
    STAMP(13);

    {
        if (w == 0) {
            const int u0 = 54 * cb;
            const char* a = ws + WS_FW2 + ((u0 + (l < 54 ? l : 0)) & 255) * FS;
            u32 spins = 0;
            for (;;) {
                const u64 v = ld_agent(a);
                if (__all(v == tag)) break;
                if (++spins > SPIN_CAP) { if (l == 0) st_agent32(ws + WS_ERR, 12); break; }
                __builtin_amdgcn_s_sleep(1);
            }
        }
        BAR();
        const char* wsrc = ws + WS_WD + (cb * 54 + wk * 27) * 5120;
        const u32 r5w = L0 + L_R5 + wk * 20480 + wm * 5120;
#define P5_B1(t, i) glds16c(wsrc + min(4 * (t) + wm, 26) * 5120 + (i) * 1024, lo16, r5w + ((t) % 3) * R5_STAGE + (i) * 1024)
#define P5_B(t) do { P5_B1(t, 0); P5_B1(t, 1); P5_B1(t, 2); P5_B1(t, 3); P5_B1(t, 4); } while (0)
        P5_B(0); P5_B(1); P5_B(2);
        if (w == 1 && l < 20) stsf4(L0 + L_GB + l * 16, gather1(ws, 800 + 20 * cb + l, tag, ws, 8));
        if (w == 0) poll_flags(ws + WS_FACT + rb * 16 * FS, 16, l, tag, ws, 9);
        VMCNT(0);
        BAR();
        STAMP(14);
        const char* asrc = ws + WS_ACT + (mt3 * 216 + wk * 108) * 256;
        u32x4 a[3][4];
#define P5_A1(t, i) a[(t) % 3][i] = gld16c<(i) * 1024>(asrc + (t) * 4096, lo16)
        P5_A1(0, 0); P5_A1(0, 1); P5_A1(0, 2); P5_A1(0, 3); P5_A1(1, 0); P5_A1(1, 1); P5_A1(1, 2); P5_A1(1, 3); P5_A1(2, 0); P5_A1(2, 1); P5_A1(2, 2); P5_A1(2, 3);
        f32x4 acc[5];
#pragma unroll
        for (int n = 0; n < 5; ++n) acc[n] = Z4;
        const u32 rb5 = L0 + L_R5 + wk * 20480 + lo16;
        u32x4 fb[2][5];
#define P5_RD(buf, t, kk) do { _Pragma("unroll") for (int n = 0; n < 5; ++n) fb[buf][n] = lds128(rb5 + ((t) % 3) * R5_STAGE + (kk) * 5120 + n * 1024); } while (0)
#define P5_MM(buf, av) do { _Pragma("unroll") for (int n = 0; n < 5; ++n) acc[n] = mfma(fb[buf][n], (av), acc[n]); } while (0)
#define P5_MIX() do { _Pragma("unroll") for (int n = 0; n < 5; ++n) { SGB(M_DSR, 1); SGB(M_MFMA, 1); } } while (0)
        P5_RD(0, 0, 0);
        static_for<0, 7>([&](auto T) {
            constexpr int t = T, q = t % 3;
            constexpr bool nx = t + 3 < 7;
            constexpr int NA = t == 0 ? 8 : t == 1 ? 13 : t == 2 ? 18 : t == 3 ? 21 : t == 4 ? 20 : t == 5 ? 11 : 5;
            constexpr int NB = t == 2 ? 12 : t == 3 ? 12 : t == 4 ? 8 : t == 5 ? 0 : 63;
            vmw4<NA>(a[q][0], a[q][1], a[q][2], a[q][3]);
            SB0(); P5_RD(1, t, 1); P5_MM(0, a[q][0]); P5_MIX(); SB0();
            if constexpr (nx) P5_A1(t + 3, 0);
            SB0(); P5_RD(0, t, 2); P5_MM(1, a[q][1]); P5_MIX(); SB0();
            if constexpr (nx) P5_A1(t + 3, 1);
            if constexpr (t < 6) {
                SB0(); P5_RD(1, t, 3); P5_MM(0, a[q][2]); P5_MIX(); SB0();
                if constexpr (nx) P5_A1(t + 3, 2);
                vmw<NB>(); BAR();
                if constexpr (nx) { P5_B1(t + 3, 0); P5_B1(t + 3, 1); }
                SB0(); P5_RD(0, t + 1, 0); P5_MM(1, a[q][3]); P5_MIX(); SB0();
                if constexpr (nx) { if constexpr (t + 3 < 6) P5_A1(t + 3, 3); P5_B1(t + 3, 2); P5_B1(t + 3, 3); P5_B1(t + 3, 4); }
            } else {
                SB0(); P5_MM(0, a[q][2]); SB0();
            }
        });
#undef P5_B
#undef P5_B1
#undef P5_A1
#undef P5_RD
#undef P5_MM
#undef P5_MIX
        STAMP(15);
        if (wk == 0) { stsf4(L0 + L_RED5 + (wm * 5 + 3) * 1024 + lo16, acc[3]); stsf4(L0 + L_RED5 + (wm * 5 + 4) * 1024 + lo16, acc[4]); }
        else { stsf4(L0 + L_RED5 + (wm * 5 + 0) * 1024 + lo16, acc[0]); stsf4(L0 + L_RED5 + (wm * 5 + 1) * 1024 + lo16, acc[1]); stsf4(L0 + L_RED5 + (wm * 5 + 2) * 1024 + lo16, acc[2]); }
        BAR();
#pragma unroll
        for (int n = 0; n < 3; ++n) {
            if (n < ntn) {
                const int nt = nt0 + n;
                const f32x4 mine = wk == 0 ? acc[n] : acc[n < 2 ? 3 + n : 4];
                const f32x4 v = mine + ldsf4(L0 + L_RED5 + (wm * 5 + nt) * 1024 + lo16);
                const f32x4 gb = ldsf4(L0 + L_GB + (16 * nt + 4 * g) * 4);
                const u32x2 o = {pack2(bflo(x1k[n][0]) + gb[0] * v[0], bfhi(x1k[n][0]) + gb[1] * v[1]), pack2(bflo(x1k[n][1]) + gb[2] * v[2], bfhi(x1k[n][1]) + gb[3] * v[3])};
                sts64(L0 + L_OS + (16 * wm + i16) * 160 + 32 * nt + 8 * g, o);
            }
        }
        BAR();
#pragma unroll
        for (int i = 0; i < 2; ++i) {
            const int id = tid + 512 * i;
            if (id < 640) {
                const int r = (id * 6554) >> 16, c = id - 10 * r;
                *(GAS u32x4*)((char*)p.out + (rb * 128 + 64 * hf + r) * 1280 + cb * 160 + c * 16) = lds128(L0 + L_OS + id * 16);
            }
        }
    }
    STAMP(16); STAMP_RT(63); STAMP_OUT();
    if (rb == 15 && j == 0 && w < 4) {
        u32 spins = 0;
        for (;;) {
            const u64 v = ld_agent(ws + WS_FO + tid * FS);
            if (__all(v == tag)) break;
            if (++spins > SPIN_CAP) { st_agent32(ws + WS_ERR, 10); break; }
            __builtin_amdgcn_s_sleep(1);
        }
    }
    if (rb == 15 && j == 0) {
        BAR();
        if (tid == 0) st_agent(ws + WS_EPOCH, tag);
    }
}

extern "C" void kernel_launch(void* const* d_in, const int* in_sizes, int n_in, void* d_out, int out_size, void* d_ws, size_t ws_size, hipStream_t stream) {
    Params p;
    p.x = (const u16*)d_in[0]; p.wqkv = (const u16*)d_in[1]; p.wo = (const u16*)d_in[2];
    p.wg = (const u16*)d_in[4]; p.wu = (const u16*)d_in[5]; p.wd = (const u16*)d_in[6];
    p.c = (const u16*)d_in[7]; p.wmod = (const u16*)d_in[8];
    p.out = (u16*)d_out; p.ws = (char*)d_ws;
    mega_kernel<<<256, 512, 0, stream>>>(p);
}
```

```cpp
#include <hip/hip_runtime.h>
#include <hip/hip_bf16.h>
#include <stdint.h>
#include <type_traits>

typedef unsigned short u16;
typedef unsigned u32;
typedef unsigned long long u64;
typedef __attribute__((ext_vector_type(2))) unsigned u32x2;
typedef __attribute__((ext_vector_type(4))) unsigned u32x4;
typedef __attribute__((ext_vector_type(2))) float f32x2;
typedef __attribute__((ext_vector_type(4))) float f32x4;
typedef __attribute__((ext_vector_type(2))) __bf16 bf16x2;
typedef __attribute__((ext_vector_type(8))) __bf16 bf16x8;
typedef __attribute__((ext_vector_type(4))) short s16x4;
#define LAS __attribute__((address_space(3)))
#define GAS __attribute__((address_space(1)))
#define DI __device__ __forceinline__

constexpr int SEQ = 2048, DM = 640, FF = 1728;
constexpr float QSCALE = 0.22360679774997896f * 1.4426950408889634f;
constexpr float LOG2E = 1.4426950408889634f;

constexpr u32 WS_EPOCH = 0;
constexpr u32 WS_ERR   = 256;
constexpr u32 FS = 128;
constexpr u32 WS_FKV   = 4096;
constexpr u32 WS_FO    = WS_FKV + 256 * FS;
constexpr u32 WS_FX1   = WS_FO + 256 * FS;
constexpr u32 WS_FACT  = WS_FX1 + 256 * FS;
constexpr u32 WS_FH    = WS_FACT + 256 * FS;
constexpr u32 WS_FWT   = WS_FH + 256 * FS;
constexpr u32 WS_FWP   = WS_FWT + 256 * FS;
constexpr u32 WS_FW2   = WS_FWP + 256 * FS;
constexpr u32 WS_MODG  = WS_FW2 + 256 * FS;
constexpr u32 WS_UV    = WS_MODG + 61440;
constexpr u32 WS_STAT  = WS_UV + 114688;
constexpr u32 WS_KV    = 589824;
constexpr u32 WS_O     = WS_KV + 256 * 22528;
constexpr u32 WS_X1    = WS_O + SEQ * DM * 2;
constexpr u32 WS_ACT   = WS_X1 + SEQ * DM * 2;
constexpr u32 WS_WP    = WS_ACT + SEQ * FF * 2;
constexpr u32 WS_H     = WS_WP + 16 * 20 * 14 * 1024;
constexpr u32 WS_WD    = WS_H + SEQ * DM * 2;
constexpr u32 WS_WO    = WS_WD + 8 * 54 * 5 * 1024;
constexpr u32 WS_WT    = WS_WO + 8 * 20 * 5 * 1024;
constexpr u32 WS_STAMP = WS_WT + 16 * 153600;
constexpr u32 WS_END   = WS_STAMP + 256 * 8 * 64 * 4;

constexpr u32 L_WKV = 0, L_WQ = 102400, L_G32 = 153600, L_MODP = 156160, L_GB1 = 157184, L_SAB = 158464, L_UVS1 = 159808, L_SB = 160832, L_G2 = 161152,
              L_CNT = 161472, L_ZERO = 161536;
constexpr u32 L_XS = 0, L_WTRAW = 131072;
constexpr u32 L_K = 0, L_V = 30720, L_Q = 67584, L_WPRAW = 77824, L_UVL = 98304;
constexpr u32 L_WO = 61440, L_RED3 = 0, L_X1S = 20480, L_XR = 30720, L_GA = 40960, L_ST3 = 41472, L_WDRAW = 43008;
constexpr u32 L_R4 = 0, R4_STAGE = 45056, L_ST4 = 135168, L_UV = 143360, L_MR = 150528, L_XCH = 151552, L_UVS = 159744;
constexpr u32 L_R5 = 0, R5_STAGE = 40960, L_RED5 = 122880, L_GB = 143360, L_OS = 144384;

struct Params {
    const u16* x; const u16* wqkv; const u16* wo; const u16* wg; const u16* wu; const u16* wd;
    const u16* c; const u16* wmod; u16* out; char* ws;
};

DI float bflo(u32 d) { return __uint_as_float(d << 16); }
DI float bfhi(u32 d) { return __uint_as_float(d & 0xffff0000u); }
DI u32 pack2(float a, float b) { f32x2 v = {a, b}; return __builtin_bit_cast(u32, __builtin_convertvector(v, bf16x2)); }
DI float rbf(float a) { return bflo(pack2(a, a)); }
DI f32x4 mfma(u32x4 a, u32x4 b, f32x4 c) {
    return __builtin_amdgcn_mfma_f32_16x16x32_bf16(__builtin_bit_cast(bf16x8, a), __builtin_bit_cast(bf16x8, b), c, 0, 0, 0);
}
DI u32x2 ldtr(u32 a) { return __builtin_bit_cast(u32x2, __builtin_amdgcn_ds_read_tr16_b64_v4i16((LAS s16x4*)(size_t)a)); }
DI u32x4 ldtr2(u32 a0, u32 a1) { u32x2 x = ldtr(a0), y = ldtr(a1); return (u32x4){x[0], x[1], y[0], y[1]}; }
DI u32x4 lds128(u32 a) { return *(LAS u32x4*)(size_t)a; }
DI u32x2 lds64(u32 a) { return *(LAS u32x2*)(size_t)a; }
DI f32x4 ldsf4(u32 a) { return *(LAS f32x4*)(size_t)a; }
DI float ldsf(u32 a) { return *(LAS float*)(size_t)a; }
DI void sts128(u32 a, u32x4 v) { *(LAS u32x4*)(size_t)a = v; }
DI void sts64(u32 a, u32x2 v) { *(LAS u32x2*)(size_t)a = v; }
DI void stsf(u32 a, float v) { *(LAS float*)(size_t)a = v; }
DI void stsf4(u32 a, f32x4 v) { *(LAS f32x4*)(size_t)a = v; }
DI void stsf2(u32 a, f32x2 v) { *(LAS f32x2*)(size_t)a = v; }
DI void sts16(u32 a, u16 v) { *(LAS u16*)(size_t)a = v; }

DI void glds16(const void* sbase, u32 voff, u32 ldsdst) {
    asm volatile("s_mov_b32 m0, %2\n\ts_nop 3\n\tglobal_load_lds_dwordx4 %0, %1" :: "v"(voff), "s"(sbase), "s"(__builtin_amdgcn_readfirstlane(ldsdst)) : "memory");
}
DI void glds16c(const void* sbase, u32 voff, u32 ldsdst) {
    asm volatile("s_mov_b32 m0, %2\n\ts_nop 3\n\tglobal_load_lds_dwordx4 %0, %1 sc1" :: "v"(voff), "s"(sbase), "s"(__builtin_amdgcn_readfirstlane(ldsdst)) : "memory");
}
template <int OFF> DI u32x4 gld16(const void* sbase, u32 voff) {
    u32x4 r; asm volatile("s_nop 4\n\tglobal_load_dwordx4 %0, %1, %2 offset:%3" : "=v"(r) : "v"(voff), "s"(sbase), "i"(OFF) : "memory"); return r;
}
template <int OFF> DI u32x4 gld16c(const void* sbase, u32 voff) {
    u32x4 r; asm volatile("s_nop 4\n\tglobal_load_dwordx4 %0, %1, %2 offset:%3 sc1" : "=v"(r) : "v"(voff), "s"(sbase), "i"(OFF) : "memory"); return r;
}
DI u32 gldu16(const void* sbase, u32 voff) {
    u32 r; asm volatile("s_nop 4\n\tglobal_load_ushort %0, %1, %2" : "=v"(r) : "v"(voff), "s"(sbase) : "memory"); return r;
}
#define VMCNT(n) asm volatile("s_waitcnt vmcnt(" #n ")" ::: "memory")
#define BAR() asm volatile("s_waitcnt lgkmcnt(0)\n\ts_barrier" ::: "memory")
#define RLX_AGENT __ATOMIC_RELAXED, __HIP_MEMORY_SCOPE_AGENT
DI u64 ld_agent(const char* p) { return __hip_atomic_load((GAS u64*)p, RLX_AGENT); }
DI void st_agent(char* p, u64 v) { __hip_atomic_store((GAS u64*)p, v, RLX_AGENT); }
DI void st_agent32(char* p, u32 v) { __hip_atomic_store((GAS u32*)p, v, RLX_AGENT); }
DI float ld_agentf(const char* p) { return __uint_as_float(__hip_atomic_load((GAS u32*)p, RLX_AGENT)); }
DI float xsum4(float x) {
    const auto s = __builtin_amdgcn_permlane16_swap(__float_as_uint(x), __float_as_uint(x), false, false);
    x = __uint_as_float(s[0]) + __uint_as_float(s[1]);
    const auto t = __builtin_amdgcn_permlane32_swap(__float_as_uint(x), __float_as_uint(x), false, false);
    return __uint_as_float(t[0]) + __uint_as_float(t[1]);
}
DI float xmax4(float x) {
    const auto s = __builtin_amdgcn_permlane16_swap(__float_as_uint(x), __float_as_uint(x), false, false);
    x = fmaxf(__uint_as_float(s[0]), __uint_as_float(s[1]));
    const auto t = __builtin_amdgcn_permlane32_swap(__float_as_uint(x), __float_as_uint(x), false, false);
    return fmaxf(__uint_as_float(t[0]), __uint_as_float(t[1]));
}
template <int CTRL> DI float dppf(float v) { return __builtin_bit_cast(float, __builtin_amdgcn_update_dpp(0, __builtin_bit_cast(int, v), CTRL, 0xf, 0xf, true)); }

constexpr u32 SPIN_CAP = 1u << 17;
DI void poll_flags(const char* base, int n, int lane, u64 tag, char* ws, u32 code) {
    const char* a = base + (lane < n ? lane : 0) * FS;
    u32 spins = 0;
    for (;;) {
        const u64 v = ld_agent(a);
        if (__all(v == tag)) break;
        if (++spins > SPIN_CAP) { if (lane == 0) st_agent32(ws + WS_ERR, code); break; }
        __builtin_amdgcn_s_sleep(1);
    }
}
typedef __attribute__((ext_vector_type(2))) _Float16 f16x2;
DI float hlo(u32 d) { return (float)__builtin_bit_cast(f16x2, d)[0]; }
DI float hhi(u32 d) { return (float)__builtin_bit_cast(f16x2, d)[1]; }
DI u32 packh(float a, float b) { const f16x2 v = {(_Float16)a, (_Float16)b}; return __builtin_bit_cast(u32, v); }
DI bool gran_ok(const u32x4& v, u64 tag) { return v[2] == (u32)tag && v[3] == (u32)(tag >> 32); }
DI f32x4 gran_sum(const u32x4& a, const u32x4& b, const u32x4& c, const u32x4& d) {
    const f32x4 s = {(hlo(a[0]) + hlo(b[0])) + (hlo(c[0]) + hlo(d[0])), (hhi(a[0]) + hhi(b[0])) + (hhi(c[0]) + hhi(d[0])),
                     (hlo(a[1]) + hlo(b[1])) + (hlo(c[1]) + hlo(d[1])), (hhi(a[1]) + hhi(b[1])) + (hhi(c[1]) + hhi(d[1]))};
    return (f32x4){rbf(s[0]), rbf(s[1]), rbf(s[2]), rbf(s[3])};
}
DI f32x4 gather1(const char* ws, u32 g0, u64 tag, char* wsw, u32 code) {
    u32 spins = 0; u32x4 v0, v1, v2, v3;
    const u32 o = WS_MODG + g0 * 16;
    for (;;) {
        asm volatile("s_nop 4\n\tglobal_load_dwordx4 %0, %4, %5 sc1\n\tglobal_load_dwordx4 %1, %6, %5 sc1\n\tglobal_load_dwordx4 %2, %7, %5 sc1\n\tglobal_load_dwordx4 %3, %8, %5 sc1\n\ts_waitcnt vmcnt(0)"
                     : "=&v"(v0), "=&v"(v1), "=&v"(v2), "=&v"(v3) : "v"(o), "s"(ws), "v"(o + 15360), "v"(o + 30720), "v"(o + 46080) : "memory");
        if (__all(gran_ok(v0, tag) && gran_ok(v1, tag) && gran_ok(v2, tag) && gran_ok(v3, tag))) break;
        if (++spins > SPIN_CAP) { st_agent32(wsw + WS_ERR, code); break; }
        __builtin_amdgcn_s_sleep(1);
    }
    return gran_sum(v0, v1, v2, v3);
}
DI void gather2(const char* ws, u32 g0, u32 g1, u64 tag, char* wsw, u32 code, f32x4& ra, f32x4& rb) {
    u32 spins = 0; u32x4 v0, v1, v2, v3, u0, u1, u2, u3;
    const u32 o = WS_MODG + g0 * 16, q = WS_MODG + g1 * 16;
    for (;;) {
        asm volatile("s_nop 4\n\tglobal_load_dwordx4 %0, %8, %9 sc1\n\tglobal_load_dwordx4 %1, %10, %9 sc1\n\tglobal_load_dwordx4 %2, %11, %9 sc1\n\tglobal_load_dwordx4 %3, %12, %9 sc1\n\t"
                     "global_load_dwordx4 %4, %13, %9 sc1\n\tglobal_load_dwordx4 %5, %14, %9 sc1\n\tglobal_load_dwordx4 %6, %15, %9 sc1\n\tglobal_load_dwordx4 %7, %16, %9 sc1\n\ts_waitcnt vmcnt(0)"
                     : "=&v"(v0), "=&v"(v1), "=&v"(v2), "=&v"(v3), "=&v"(u0), "=&v"(u1), "=&v"(u2), "=&v"(u3)
                     : "v"(o), "s"(ws), "v"(o + 15360), "v"(o + 30720), "v"(o + 46080), "v"(q), "v"(q + 15360), "v"(q + 30720), "v"(q + 46080) : "memory");
        if (__all(gran_ok(v0, tag) && gran_ok(v1, tag) && gran_ok(v2, tag) && gran_ok(v3, tag) && gran_ok(u0, tag) && gran_ok(u1, tag) && gran_ok(u2, tag) && gran_ok(u3, tag))) break;
        if (++spins > SPIN_CAP) { st_agent32(wsw + WS_ERR, code); break; }
        __builtin_amdgcn_s_sleep(1);
    }
    ra = gran_sum(v0, v1, v2, v3); rb = gran_sum(u0, u1, u2, u3);
}
DI void gather_strip(const char* ws, u32 gs, u32 gc, u32 strip, int l, u64 tag, char* wsw, u32 code) {
    f32x4 a, c;
    gather2(ws, l < 40 ? gs + l : gc + (l - 40), gc + 24 + (l & 15), tag, wsw, code, a, c);
    if (l >= 40) a = a + 1.f;
    c = c + 1.f;
    *(LAS u32x2*)(size_t)(strip + (l < 40 ? l * 8 : 320 + (l - 40) * 8)) = (u32x2){pack2(a[0], a[1]), pack2(a[2], a[3])};
    if (l < 16) *(LAS u32x2*)(size_t)(strip + 320 + (24 + l) * 8) = (u32x2){pack2(c[0], c[1]), pack2(c[2], c[3])};
}
DI void gather_tables(const char* ws, int w, u32 sab, u32 gb, u32 g32, int l, u64 tag, char* wsw, u32 code) {
    f32x4 a, c;
    gather2(ws, l < 40 ? 40 * w + l : 160 + 40 * w + (l - 40), 160 + 40 * w + 24 + (l & 15), tag, wsw, code, a, c);
    if (l < 40) *(LAS u32x2*)(size_t)(sab + (40 * w + l) * 8) = (u32x2){pack2(a[0], a[1]), pack2(a[2], a[3])};
    else {
        const u32 p0 = pack2(a[0] + 1.f, a[1] + 1.f), p1 = pack2(a[2] + 1.f, a[3] + 1.f);
        *(LAS u32x2*)(size_t)(gb + (40 * w + l - 40) * 8) = (u32x2){p0, p1};
        *(LAS u32x4*)(size_t)(g32 + (40 * w + l - 40) * 16) = (u32x4){p0 & 0xffffu, p0 & 0xffff0000u, p1 & 0xffffu, p1 & 0xffff0000u};
    }
    if (l < 16) {
        const u32 p0 = pack2(c[0] + 1.f, c[1] + 1.f), p1 = pack2(c[2] + 1.f, c[3] + 1.f);
        *(LAS u32x2*)(size_t)(gb + (40 * w + 24 + l) * 8) = (u32x2){p0, p1};
        *(LAS u32x4*)(size_t)(g32 + (40 * w + 24 + l) * 16) = (u32x4){p0 & 0xffffu, p0 & 0xffff0000u, p1 & 0xffffu, p1 & 0xffff0000u};
    }
}
DI void poll_at(const char* ws, u32 off, int lane, u64 tag, char* wsw, u32 code) {
    u32 spins = 0;
    for (;;) {
        const u64 v = ld_agent(ws + off);
        if (__all(v == tag)) break;
        if (++spins > SPIN_CAP) { if (lane == 0) st_agent32(wsw + WS_ERR, code); break; }
        __builtin_amdgcn_s_sleep(1);
    }
}

#define STAMP(i) do {} while (0)
#define STAMPL(i) do {} while (0)
#define STAMP_RT(i) do {} while (0)
#define STAMP_OUT() do {} while (0)

template <int N> using IC = std::integral_constant<int, N>;
template <int I, int E, class F> DI void static_for(F&& f) { if constexpr (I < E) { f(IC<I>{}); static_for<I + 1, E>(f); } }
template <int N> DI void vmw() { asm volatile("s_waitcnt vmcnt(%0)" :: "i"(N) : "memory"); }
template <int N> DI void vmw1(u32x4& a) { asm volatile("s_waitcnt vmcnt(%1)" : "+v"(a) : "i"(N) : "memory"); }
template <int N> DI void vmw4(u32x4& a, u32x4& b, u32x4& c, u32x4& d) { asm volatile("s_waitcnt vmcnt(%4)" : "+v"(a), "+v"(b), "+v"(c), "+v"(d) : "i"(N) : "memory"); }
DI u32 opaque(u32 a) { asm("" : "+v"(a)); return a; }
DI float dot2(u32 a, u32 b, float c) { return __builtin_amdgcn_fdot2_f32_bf16(__builtin_bit_cast(bf16x2, a), __builtin_bit_cast(bf16x2, b), c, false); }
#define SB0() __builtin_amdgcn_sched_barrier(0)
#define SGB(m, n) __builtin_amdgcn_sched_group_barrier((m), (n), 0)
constexpr int M_MFMA = 0x8, M_DSR = 0x100;
#define Z4 ((f32x4){0.f, 0.f, 0.f, 0.f})
DI u32 lds_add(u32 a, u32 v) { return __hip_atomic_fetch_add((LAS u32*)(size_t)a, v, __ATOMIC_RELAXED, __HIP_MEMORY_SCOPE_WORKGROUP); }
DI u32 lds_ld(u32 a) { return __hip_atomic_load((LAS u32*)(size_t)a, __ATOMIC_RELAXED, __HIP_MEMORY_SCOPE_WORKGROUP); }
DI void worker_sync(u32 cnt, int lane) {
    asm volatile("s_waitcnt lgkmcnt(0)" ::: "memory");
    if (lane == 0) lds_add(cnt, 1u);
    for (u32 spins = 0; lds_ld(cnt) < 4u && spins < (1u << 22); ++spins) {}
    asm volatile("" ::: "memory");
}

__global__ __launch_bounds__(512, 2) void mega_kernel(Params p) {
    __shared__ __attribute__((aligned(16))) char ldsmem[163840];
    const u32 L0 = (u32)(uintptr_t)(LAS char*)ldsmem;
    const int tid = threadIdx.x, l = tid & 63;
    const int w = __builtin_amdgcn_readfirstlane(tid >> 6);
    const int b = blockIdx.x, xg = b & 7, sidx = b >> 3;
    const int rb = 4 * (xg >> 1) + (sidx >> 3), j = 8 * (xg & 1) + (sidx & 7);
    const int g = l >> 4, i16 = l & 15, q4 = i16 >> 2, p4 = l & 3;
    char* const ws = p.ws;
    asm volatile("" :: "s"(p.x), "s"(p.wqkv), "s"(p.wo), "s"(p.wg), "s"(p.wu), "s"(p.wd), "s"(p.c), "s"(p.wmod), "s"(p.out), "s"(p.ws));
    u64 tag;
#define ASK_TAG(ep) u32x2 ep; asm volatile("global_load_dwordx2 %0, %1, %2" : "=v"(ep) : "v"(WS_EPOCH), "s"(ws) : "memory")
#define TAKE_TAG(ep) do { asm volatile("s_waitcnt vmcnt(0)" : "+v"(ep) :: "memory"); \
        tag = (((u64)(u32)__builtin_amdgcn_readfirstlane(ep[1]) << 32) | (u32)__builtin_amdgcn_readfirstlane(ep[0])) + 1; } while (0)
    const __amdgpu_buffer_rsrc_t wsr = __builtin_amdgcn_make_buffer_rsrc(ws, 0, WS_END, 0x00020000);
#define ST16C(v, off) __builtin_amdgcn_raw_buffer_store_b128((v), wsr, (off), 0, 16)
#define ST8C(v, off)  __builtin_amdgcn_raw_buffer_store_b64((v), wsr, (off), 0, 16)
    STAMP_RT(62); STAMP(0);

    u32 wu0 = 0, wu1 = 0, wu2 = 0;
    if (w == 7) {
        const char* a0 = ws + (size_t)min(l, 31) * (1u << 20);
        const char* a1 = l < 3 ? (const char*)p.x + l * (1u << 20) : l < 6 ? (const char*)p.wqkv + (l - 3) * (1u << 20) : l < 7 ? (const char*)p.wo
                       : l < 10 ? (const char*)p.wg + (l - 7) * (1u << 20) : l < 13 ? (const char*)p.wu + (l - 10) * (1u << 20) : l < 16 ? (const char*)p.wd + (l - 13) * (1u << 20)
                       : l < 21 ? (const char*)p.wmod + (l - 16) * (1u << 20) : l < 24 ? (const char*)p.out + (l - 21) * (1u << 20) : (const char*)p.c;
        const char* pc; asm volatile("s_getpc_b64 %0" : "=s"(pc));
        const char* a2 = pc + (size_t)(sidx * 11 + min(l, 10)) * 128;
        asm volatile("global_load_dword %0, %3, off\n\tglobal_load_dword %1, %4, off\n\tglobal_load_dword %2, %5, off" : "=&v"(wu0), "=&v"(wu1), "=&v"(wu2) : "v"(a0), "v"(a1), "v"(a2) : "memory");
    }
    const int nj = j < 12 ? 7 : 6, Gj = j < 12 ? 7 * j : 84 + 6 * (j - 12);
    const bool wpwg = b < 216;
    const int wpm = b >= 108 ? 1 : 0, wpb = (b - 108 * wpm) >> 2, wpq = b & 3;
    const u32 lo16 = l * 16;
    const u32 XB = L0 + L_XS + w * 16384;
    const char* xsrc = (const char*)p.x + (rb * 128 + 16 * w) * 1280;
    u32 xvo[2];
#pragma unroll
    for (int h = 0; h < 2; ++h) { const u32 row = 8 * h + (l >> 3); xvo[h] = row * 1280 + (((l & 7) ^ ((row >> 1) & 7)) * 16); }
#define XP(kb) do { glds16(xsrc + (kb) * 128, xvo[0], XB + ((kb) & 7) * 2048); glds16(xsrc + (kb) * 128, xvo[1], XB + ((kb) & 7) * 2048 + 1024); } while (0)
    if (w < 4) {
        __builtin_amdgcn_s_setprio(2);
        ASK_TAG(epw);
        const bool modwg = b < 240;
        u32x4 mw[5]; u32 mc[5];
        if (modwg) {
#pragma unroll
            for (int i = 0; i < 5; ++i) {
                const int r = 160 * (b & 3) + (tid >> 3) + 32 * i;
                mw[i] = gld16<0>(p.wmod, r * 7680 + (b >> 2) * 128 + (tid & 7) * 16);
                mc[i] = gldu16(p.c, r * 2);
            }
        }
        if (tid < 16) *(LAS u32*)(size_t)(L0 + L_CNT + tid * 4) = 0u;
        if (tid >= 64 && tid < 64 + 84) sts128(L0 + L_ZERO + (tid - 64) * 16, (u32x4){0, 0, 0, 0});
        BAR();
        XP(0); XP(1); XP(2); XP(3);
        STAMP(29);
        asm volatile("s_waitcnt vmcnt(8)" : "+v"(epw), "+v"(mw[0]), "+v"(mw[1]), "+v"(mw[2]), "+v"(mw[3]), "+v"(mw[4]), "+v"(mc[0]), "+v"(mc[1]), "+v"(mc[2]), "+v"(mc[3]), "+v"(mc[4]) :: "memory");
        tag = (((u64)(u32)__builtin_amdgcn_readfirstlane(epw[1]) << 32) | (u32)__builtin_amdgcn_readfirstlane(epw[0])) + 1;
        STAMP(30);
        bool stored = false;
        if (modwg) {
            float acc[8];
#pragma unroll
            for (int e = 0; e < 8; ++e) acc[e] = 0.f;
#pragma unroll
            for (int i = 0; i < 5; ++i) {
                const float cv = bflo(mc[i]);
                const float sv = cv * __builtin_amdgcn_rcpf(1.f + __builtin_amdgcn_exp2f(-cv * LOG2E));
                const u32 slo = pack2(sv, 0.f), shi = slo << 16;
#pragma unroll
                for (int d = 0; d < 4; ++d) { acc[2 * d] = dot2(mw[i][d], slo, acc[2 * d]); acc[2 * d + 1] = dot2(mw[i][d], shi, acc[2 * d + 1]); }
            }
#pragma unroll
            for (int e = 0; e < 8; ++e) { const float v = acc[e]; acc[e] = xsum4(v + dppf<0x128>(v)); }
            if (l < 8) {
                stsf4(L0 + L_MODP + w * 256 + l * 32, (f32x4){acc[0], acc[1], acc[2], acc[3]});
                stsf4(L0 + L_MODP + w * 256 + l * 32 + 16, (f32x4){acc[4], acc[5], acc[6], acc[7]});
            }
            asm volatile("s_waitcnt lgkmcnt(0)" ::: "memory");
            u32 old = 0;
            if (l == 0) old = lds_add(L0 + L_CNT, 1u);
            if (__builtin_amdgcn_readfirstlane(old) == 3) {
                float v = 0.f;
#pragma unroll
                for (int ww = 0; ww < 4; ++ww) v += ldsf(L0 + L_MODP + ww * 256 + l * 4);
                const float v1 = dppf<0x55>(v), v2 = dppf<0xAA>(v), v3 = dppf<0xFF>(v), v0 = dppf<0x00>(v);
                const u32x4 gr = {packh(v0, v1), packh(v2, v3), (u32)tag, (u32)(tag >> 32)};
                if (p4 == 0) ST16C(gr, WS_MODG + ((b & 3) * 960 + (b >> 2) * 16 + (l >> 2)) * 16);
                stored = true;
            }
        }
        STAMP(1);
        XP(4); XP(5); XP(6); XP(7);
        vmw<0>();
        (void)stored;
    } else {
        __builtin_amdgcn_s_setprio(3);
        const int u = w - 4;
        const bool tw = b >= 106;
        const int tb = (b - 106) / 5, tk = (b - 106) - 5 * tb, ts = 4 * tk + u;
        ASK_TAG(epl);
        if (tw) {
            const char* src = (const char*)p.wqkv + ts * 122880 + tb * 128;
            const u32 voff = (l >> 3) * 3840 + (l & 7) * 16;
#pragma unroll
            for (int i = 0; i < 4; ++i) glds16(src + i * 8 * 3840, voff, L0 + L_WTRAW + u * 4096 + i * 1024);
        }
        BAR();
        u32 dst[4];
#pragma unroll
        for (int t = 0; t < 4; ++t) {
            const u32 cc = 8 * tb + 2 * t + (i16 >> 3);
            const u32 seg = cc >= 160 ? 2 : cc >= 80 ? 1 : 0, wi = cc - 80 * seg, jj = (wi * 13108) >> 16, c5 = wi - 5 * jj;
            const u32 c = seg == 0 ? 10 + c5 : seg == 1 ? c5 : 5 + c5, nt = c >> 1;
            dst[t] = WS_WT + jj * 153600 + (nt < 5 ? ts * 5120 + nt * 1024 : 102400 + ts * 2560 + (nt - 5) * 1024)
                   + (nt < 7 ? (g * 16 + 8 * (c & 1) + (i16 & 7)) * 16 : g * 128 + (i16 & 7) * 16);
        }
        asm volatile("" : "+v"(dst[0]), "+v"(dst[1]), "+v"(dst[2]), "+v"(dst[3]));
        XP(0); XP(1); XP(2); XP(3);
        asm volatile("s_waitcnt vmcnt(8)" : "+v"(epl) :: "memory");
        tag = (((u64)(u32)__builtin_amdgcn_readfirstlane(epl[1]) << 32) | (u32)__builtin_amdgcn_readfirstlane(epl[0])) + 1;
        STAMPL(36);
        if (tw) {
            const u32 a = L0 + L_WTRAW + u * 4096 + (8 * g + q4) * 128 + 8 * p4;
#pragma unroll
            for (int t = 0; t < 4; ++t) ST16C(ldtr2(a + 32 * t, a + 32 * t + 512), dst[t]);
        }
        __builtin_amdgcn_s_setprio(2);
        XP(4); XP(5); XP(6); XP(7);
        bool sig = false;
        if (tw) {
            vmw<8>();
            u32 old = 0;
            if (l == 0) old = lds_add(L0 + L_CNT + 12, 1u);
            sig = __builtin_amdgcn_readfirstlane(old) == 3;
            if (sig && l == 0) st_agent(ws + WS_FWT + (b - 106) * FS, tag);
        }
        STAMPL(19);
        if (sig) vmw<1>(); else vmw<0>();
    }
    u32x4 xa[20];
    const u32 xr0 = XB + (i16 >> 3) * 1024 + (i16 & 7) * 128 + ((g ^ ((i16 >> 1) & 7)) * 16), xr1 = xr0 ^ 64;
#define XR(s) xa[s] = lds128((((s) & 1) ? xr1 : xr0) + (((s) >> 1) & 7) * 2048)
    XR(0); XR(1); XR(2); XR(3);
    asm volatile("s_waitcnt lgkmcnt(0)" : "+v"(xa[0]), "+v"(xa[1]), "+v"(xa[2]), "+v"(xa[3]) :: "memory");
    XP(8); XP(9);
    XR(4); XR(5); XR(6); XR(7); XR(8); XR(9); XR(10); XR(11); XR(12); XR(13); XR(14); XR(15);
    vmw<0>();
    XR(16); XR(17); XR(18); XR(19);
#undef XR
#undef XP
    float rs, nmr;
    {
        float s1 = 0.f, s2 = 0.f, t1 = 0.f, t2 = 0.f;
#pragma unroll
        for (int s_ = 0; s_ < 20; ++s_) {
            s1 = dot2(xa[s_][0], 0x3F803F80u, s1); t1 = dot2(xa[s_][1], 0x3F803F80u, t1); s1 = dot2(xa[s_][2], 0x3F803F80u, s1); t1 = dot2(xa[s_][3], 0x3F803F80u, t1);
            s2 = dot2(xa[s_][0], xa[s_][0], s2); t2 = dot2(xa[s_][1], xa[s_][1], t2); s2 = dot2(xa[s_][2], xa[s_][2], s2); t2 = dot2(xa[s_][3], xa[s_][3], t2);
        }
        const float m1 = xsum4(s1 + t1) * (1.f / 640.f), m2 = xsum4(s2 + t2) * (1.f / 640.f);
        rs = __builtin_amdgcn_rsqf(fmaxf(m2 - m1 * m1, 0.f) + 1e-5f); nmr = -m1 * rs;
    }
    STAMP(31);
    if (w < 4) gather_tables(ws, w, L0 + L_SAB, L0 + L_GB1, L0 + L_G32, l, tag, ws, 1);
    else if (w == 4) {
        const int wh = l >= 30 ? 0 : l / 5, kr = l >= 30 ? 0 : l - 5 * wh;
        const int bl = (1280 * (wh >> 1) + 80 * j + 79 * (wh & 1)) >> 7;
        poll_at(ws, WS_FWT + (bl * 5 + kr) * FS, l, tag, ws, 13);
    } else if (w == 5 && wpwg) {
        gather_strip(ws, 480 + 40 * wpq, 640 + 40 * wpq, L0 + L_SB, l, tag, ws, 2);
    }
    STAMP(2);
    __builtin_amdgcn_s_setprio(0);
    BAR();
    asm volatile("s_waitcnt vmcnt(%3)" : "+v"(wu0), "+v"(wu1), "+v"(wu2) : "i"(0) : "memory");
    STAMP(3);
    const char* wtsrc = ws + WS_WT + j * 153600 + w * 1024;
    {
        const u32 gt = opaque(L0 + L_G32 + 32 * g);
        u32x4 ga[2], gc[2];
        ga[0] = lds128(gt); gc[0] = lds128(gt + 16);
        static_for<0, 20>([&](auto S) {
            constexpr int s = S;
            if constexpr (s < 19) { if (s < 18 || w < 6) glds16c(wtsrc + s * 8192, lo16, L0 + L_WKV + w * 1024 + s * 8192); }
            if constexpr (s < 19) { ga[(s + 1) & 1] = lds128(gt + (s + 1) * 128); gc[(s + 1) & 1] = lds128(gt + (s + 1) * 128 + 16); }
            float t0, t1, t2, t3, t4, t5, t6, t7;
            asm volatile("v_dot2_f32_bf16 %4, %0, %12, 0\n\tv_dot2_f32_bf16 %5, %0, %13, 0\n\tv_dot2_f32_bf16 %6, %1, %14, 0\n\tv_dot2_f32_bf16 %7, %1, %15, 0\n\t"
                         "v_dot2_f32_bf16 %8, %2, %16, 0\n\tv_dot2_f32_bf16 %9, %2, %17, 0\n\tv_dot2_f32_bf16 %10, %3, %18, 0\n\tv_dot2_f32_bf16 %11, %3, %19, 0\n\t"
                         "v_cvt_pk_bf16_f32 %0, %4, %5\n\tv_cvt_pk_bf16_f32 %1, %6, %7\n\tv_cvt_pk_bf16_f32 %2, %8, %9\n\ts_nop 0\n\tv_cvt_pk_bf16_f32 %3, %10, %11"
                         : "+v"(xa[s][0]), "+v"(xa[s][1]), "+v"(xa[s][2]), "+v"(xa[s][3]),
                           "=&v"(t0), "=&v"(t1), "=&v"(t2), "=&v"(t3), "=&v"(t4), "=&v"(t5), "=&v"(t6), "=&v"(t7)
                         : "v"(ga[s & 1][0]), "v"(ga[s & 1][1]), "v"(ga[s & 1][2]), "v"(ga[s & 1][3]), "v"(gc[s & 1][0]), "v"(gc[s & 1][1]), "v"(gc[s & 1][2]), "v"(gc[s & 1][3]));
            SB0();
        });
    }
    STAMP(32);
    vmw<5>();
    BAR();
    STAMP(33);

    const char* kvx = ws + WS_KV + (rb * 16 + j) * 22528;
    const u32 zb = opaque(L0 + (i16 == 0 ? L_GB1 + 16 * g : i16 == 1 ? L_SAB + 16 * g : L_ZERO));
    {
        f32x4 acc[5], accz = Z4;
#pragma unroll
        for (int n = 0; n < 5; ++n) acc[n] = Z4;
        const int t0 = w < 5 ? w : w - 5;
        u32 b0[5];
#pragma unroll
        for (int n = 0; n < 5; ++n) { const int nt = t0 + n >= 5 ? t0 + n - 5 : t0 + n; b0[n] = opaque(L0 + L_WKV + nt * 1024 + lo16); }
        u32x4 bf[2][5], zf[2];
#pragma unroll
        for (int n = 0; n < 5; ++n) bf[0][n] = lds128(b0[n]);
        zf[0] = lds128(zb);
        static_for<0, 20>([&](auto S) {
            constexpr int s = S;
            SB0();
            if constexpr (s < 19) zf[(s + 1) & 1] = lds128(zb + (s + 1) * 64);
            accz = mfma(bf[s & 1][0], zf[s & 1], accz);
#pragma unroll
            for (int n = 0; n < 5; ++n) {
                if (s < 19) bf[(s + 1) & 1][n] = lds128(b0[n] + (s + 1) * 5120);
                acc[n] = mfma(bf[s & 1][n], xa[s], acc[n]);
            }
#pragma unroll
            for (int n = 0; n < 6; ++n) { SGB(M_DSR, 1); SGB(M_MFMA, 1); }
            SB0();
        });
        if (w < 5 && i16 < 2) stsf4(L0 + L_UVS1 + w * 128 + i16 * 64 + g * 16, accz);
        STAMP(18);
        vmw<0>();
        BAR();
        const int row = 256 + 16 * w + i16;
#pragma unroll
        for (int n = 0; n < 5; ++n) {
            const int nt = t0 + n >= 5 ? t0 + n - 5 : t0 + n, Q = 4 * nt + g;
            const f32x4 u4 = ldsf4(L0 + L_UVS1 + nt * 128 + g * 16), v4 = ldsf4(L0 + L_UVS1 + nt * 128 + 64 + g * 16);
            const f32x4 val = acc[n] * rs + (u4 * nmr + v4);
            const u32x2 v = {pack2(val[0], val[1]), pack2(val[2], val[3])};
            const int wi = Q - 10, hd = wi >= 5 ? 1 : 0;
            const u32 a = Q < 10 ? L_K + row * 80 + 8 * Q : L_V + row * 96 + 48 * hd + 8 * (wi - 5 * hd);
            sts64(L0 + a, v);
        }
        if (g < 2) sts64(L0 + L_V + row * 96 + 48 * g + 40, (u32x2){0x3F80u, 0u});
        if (rb < 2) {
            const int nrow = rb == 0 ? 256 : 128;
            for (int i = tid; i < nrow * 5; i += 512) sts128(L0 + L_K + i * 16, (u32x4){0, 0, 0, 0});
            for (int i = tid; i < nrow * 6; i += 512) sts128(L0 + L_V + i * 16, (u32x4){0, 0, 0, 0});
        }
        BAR();
    }
#undef XL
    STAMP(4);
    {
        u32x4 kvd[3];
#pragma unroll
        for (int e = 0; e < 3; ++e) {
            const int n = min(w + 8 * e, 21);
            kvd[e] = lds128(L0 + (n < 10 ? L_K + 20480 + n * 1024 : L_V + 24576 + (n - 10) * 1024) + lo16);
        }
        const u32 kvo = WS_KV + (rb * 16 + j) * 22528 + w * 1024 + lo16;
        const char* wsrc = (const char*)(wpm ? p.wu : p.wg) + (160 * wpq + 8 * w) * 3456 + wpb * 128;
        const u32 wvo = (l >> 3) * 3456 + (l & 7) * 16;
        f32x4 acc[3], accz = Z4;
#pragma unroll
        for (int n = 0; n < 3; ++n) acc[n] = Z4;
        const int t0 = w % 3;
        u32 bq[3];
#pragma unroll
        for (int n = 0; n < 3; ++n) { const int nt = t0 + n >= 3 ? t0 + n - 3 : t0 + n; bq[n] = opaque(L0 + L_WQ + (nt < 2 ? nt * 1024 + lo16 : 2048 + g * 128 + (i16 & 7) * 16)); }
#define QRD(s, n) lds128(bq[n] + (s) * 2560)
        u32x4 bf[3][3], zf[3];
#pragma unroll
        for (int n = 0; n < 3; ++n) { bf[0][n] = QRD(0, n); bf[1][n] = QRD(1, n); }
        zf[0] = lds128(zb); zf[1] = lds128(zb + 64);
        static_for<0, 20>([&](auto S) {
            constexpr int s = S;
            if constexpr (s == 0) { if (rb < 15) ST16C(kvd[0], kvo); }
            if constexpr (s == 2) { if (rb < 15) ST16C(kvd[1], kvo + 8192); }
            if constexpr (s == 4) { if (rb < 15 && w < 6) ST16C(kvd[2], kvo + 16384); }
            if constexpr (s == 8) { if (wpwg) glds16(wsrc, wvo, L0 + L_WPRAW + w * 1024); }
            if constexpr (s == 11) { if (wpwg) glds16(wsrc + 64 * 3456, wvo, L0 + L_WPRAW + (w + 8) * 1024); }
            if constexpr (s == 14) { if (wpwg && w < 4) glds16(wsrc + 128 * 3456, wvo, L0 + L_WPRAW + (w + 16) * 1024); }
            SB0();
            if constexpr (s < 18) zf[(s + 2) % 3] = lds128(zb + (s + 2) * 64);
            accz = mfma(bf[s % 3][0], zf[s % 3], accz);
#pragma unroll
            for (int n = 0; n < 3; ++n) {
                if (s < 18) bf[(s + 2) % 3][n] = QRD(s + 2, n);
                acc[n] = mfma(bf[s % 3][n], xa[s], acc[n]);
            }
#pragma unroll
            for (int n = 0; n < 4; ++n) { SGB(M_DSR, 1); SGB(M_MFMA, 1); }
            SB0();
        });
#undef QRD
        if (w < 3 && i16 < 2) stsf4(L0 + L_UVS1 + (5 + w) * 128 + i16 * 64 + g * 16, accz);
        STAMP(21);
        BAR();
#pragma unroll
        for (int n = 0; n < 3; ++n) {
            const int nt = t0 + n >= 3 ? t0 + n - 3 : t0 + n, Q = 4 * nt + g;
            const f32x4 u4 = ldsf4(L0 + L_UVS1 + (5 + nt) * 128 + g * 16), v4 = ldsf4(L0 + L_UVS1 + (5 + nt) * 128 + 64 + g * 16);
            const f32x4 val = (acc[n] * rs + (u4 * nmr + v4)) * QSCALE;
            if (Q < 10) sts64(L0 + L_Q + (16 * w + i16) * 80 + 8 * Q, (u32x2){pack2(val[0], val[1]), pack2(val[2], val[3])});
        }
    }
    VMCNT(0);
    BAR();
    if (tid == 0) st_agent(ws + WS_FKV + (j * 16 + rb) * FS, tag);
    STAMP(5);
    auto wp_slot = [&](int gam, int& jj, int& slot) {
        jj = gam < 84 ? gam / 7 : 12 + (gam - 84) / 6;
        const int ar = gam - (jj < 12 ? 7 * jj : 84 + 6 * (jj - 12)), a = (jj < 12 || ar < 3) ? ar : ar + 1;
        slot = a < 3 ? 2 * a + wpm : a == 3 ? (wpm ? 13 : 6) : 7 + 2 * (a - 4) + wpm;
    };
    if (wpwg) {
#pragma unroll
        for (int i = 0; i < 3; ++i) {
            if (i < 2 || w < 4) {
                const int f = w + 8 * i, ks = f >> 2, t16 = f & 3;
                int jj, slot; wp_slot(4 * wpb + t16, jj, slot);
                const u32 a = L0 + L_WPRAW + (32 * ks + 8 * g + q4) * 128 + 32 * t16 + 8 * p4;
                const u32x4 wr = ldtr2(a, a + 512);
                const u32x4 g2 = lds128(L0 + L_G2 + (32 * ks + 8 * g) * 2), sb = lds128(L0 + L_SB + (32 * ks + 8 * g) * 2);
                u32x4 o; float us = 0.f, vs = 0.f;
#pragma unroll
                for (int d = 0; d < 4; ++d) {
                    o[d] = pack2(dot2(wr[d], g2[d] & 0xffffu, 0.f), dot2(wr[d], g2[d] & 0xffff0000u, 0.f));
                    us = dot2(o[d], 0x3F803F80u, us);
                    vs = dot2(wr[d], sb[d], vs);
                }
                ST16C(o, WS_WP + ((jj * 20 + 5 * wpq + ks) * 14 + slot) * 1024 + lo16);
                us = xsum4(us); vs = xsum4(vs);
                if (l < 32) stsf(L0 + L_UVL + f * 128 + l * 4, l < 16 ? us : vs);
            }
        }
    }
    if (w == 0) {
        if (rb >= 1) poll_flags(ws + WS_FKV + (j * 16 + (rb >= 2 ? rb - 2 : rb - 1)) * FS, rb >= 2 ? 2 : 1, l, tag, ws, 3);
    }
    BAR();
    STAMP(6);
    if (rb >= 1) {
#pragma unroll
        for (int e = 0; e < 3; ++e) {
            const int n = w + 8 * e;
            if (n < 22) {
                const u32 d = n < 10 ? L_K + n * 1024 : L_V + (n - 10) * 1024, st = n < 10 ? 10240 : 12288;
                glds16c(kvx - 16 * 22528 + n * 1024, lo16, L0 + d + st);
                if (rb >= 2) glds16c(kvx - 32 * 22528 + n * 1024, lo16, L0 + d);
            }
        }
    }
    if (wpwg && tid < 128) {
        const int t16 = tid >> 5;
        int jj, slot; wp_slot(4 * wpb + t16, jj, slot);
        float v = 0.f;
#pragma unroll
        for (int ks = 0; ks < 5; ++ks) v += ldsf(L0 + L_UVL + (4 * ks + t16) * 128 + (tid & 31) * 4);
        st_agent32(ws + WS_UV + ((jj * 14 + slot) * 4 + wpq) * 128 + (tid & 31) * 4, __float_as_uint(v));
    }
    const int hf = j & 1, cb = j >> 1;
    VMCNT(0);
    BAR();
    u32 wvoff[5];
#pragma unroll
    for (int i = 0; i < 5; ++i) {
        const u32 sg = 64 * i + l, pos = (sg * 6554) >> 16, c = sg - pos * 10, k = pos ^ (((pos >> 3) & 1) << 2);
        wvoff[i] = k * 1280 + c * 16;
    }
    const char* wosrc = (const char*)p.wo + cb * 160;
#pragma unroll
    for (int e = 0; e < 2; ++e)
#pragma unroll
        for (int i = 0; i < 5; ++i) glds16(wosrc + (4 + w + 8 * e) * 32 * 1280, wvoff[i], L0 + L_WO + (4 + w + 8 * e) * 5120 + i * 1024);
    STAMP(7);

    {
        const int hd = w & 1, qi = w >> 1;
        const int kr0 = 32 * qi;
        u32x4 qf[2];
#pragma unroll
        for (int a = 0; a < 2; ++a) {
            const u32 qa = L0 + L_Q + (kr0 + 16 * a + i16) * 80 + hd * 40;
            const u32x2 q0 = lds64(qa + 8 * g);
            u32x2 q1 = lds64(qa + 32);
            if (g != 0) q1 = (u32x2){0, 0};
            qf[a] = (u32x4){q0[0], q0[1], q1[0], q1[1]};
        }
        const u32 kb0 = L0 + L_K + (kr0 + i16) * 80 + hd * 40 + 8 * g, kb1 = L0 + L_K + (kr0 + i16) * 80 + hd * 40 + 32;
        f32x4 mfirst, mlast;
#pragma unroll
        for (int r = 0; r < 4; ++r) { const bool lo = 4 * g + r <= i16; mfirst[r] = lo ? -INFINITY : 0.f; mlast[r] = lo ? 0.f : -INFINITY; }
        f32x4 s0[17], s1[17];
        {
            u32x4 kf[3];
#define KRD(t) do { const u32x2 k0 = lds64(kb0 + (t) * 1280), k1 = lds64(kb1 + (t) * 1280); kf[(t) % 3] = (u32x4){k0[0], k0[1], k1[0], k1[1]}; } while (0)
            KRD(0); KRD(1);
            static_for<0, 18>([&](auto T) {
                constexpr int t = T;
                SB0();
                if constexpr (t < 16) KRD(t + 2);
                if constexpr (t < 17) s0[t] = mfma(kf[t % 3], qf[0], t == 0 ? mfirst : t == 16 ? mlast : Z4);
                if constexpr (t > 0) s1[t - 1] = mfma(kf[t % 3], qf[1], t == 1 ? mfirst : t == 17 ? mlast : Z4);
                SGB(M_DSR, 2); SGB(M_MFMA, 2);
                SB0();
            });
#undef KRD
        }
        if (rb < 2) {
            const int fv = rb == 1 ? 128 : 256;
#pragma unroll
            for (int t = 0; t < 16; ++t) {
                const bool d0 = kr0 + 16 * t < fv, d1 = kr0 + 16 * t + 16 < fv;
#pragma unroll
                for (int r = 0; r < 4; ++r) { s0[t][r] = d0 ? -INFINITY : s0[t][r]; s1[t][r] = d1 ? -INFINITY : s1[t][r]; }
            }
        }
        STAMP(22);
        float m0 = fmaxf(fmaxf(s0[0][0], s0[0][1]), fmaxf(s0[0][2], s0[0][3])), m1 = fmaxf(fmaxf(s1[0][0], s1[0][1]), fmaxf(s1[0][2], s1[0][3]));
#pragma unroll
        for (int t = 1; t < 17; ++t) {
            m0 = fmaxf(fmaxf(m0, s0[t][0]), s0[t][1]); m1 = fmaxf(fmaxf(m1, s1[t][0]), s1[t][1]);
            m0 = fmaxf(fmaxf(m0, s0[t][2]), s0[t][3]); m1 = fmaxf(fmaxf(m1, s1[t][2]), s1[t][3]);
        }
        m0 = xmax4(m0); m1 = xmax4(m1);
        STAMP(23);
        f32x4 o00 = Z4, o01 = Z4, o10 = Z4, o11 = Z4;
        const u32 vb = L0 + L_V + (kr0 + 4 * g + q4) * 96 + hd * 48 + 8 * p4;
        auto pv = [&](auto SUB) {
            constexpr bool sub = SUB;
            u32x4 vf[2][2];
            vf[0][0] = ldtr2(vb, vb + 16 * 96); vf[0][1] = ldtr2(vb + 32, vb + 16 * 96 + 32);
#define EX(v, m) __builtin_amdgcn_exp2f(sub ? (v) - (m) : (v))
            static_for<0, 9>([&](auto U) {
                constexpr int u = U;
                if constexpr (u < 8) { const u32 a = vb + (u + 1) * 32 * 96; vf[(u + 1) & 1][0] = ldtr2(a, a + 16 * 96); vf[(u + 1) & 1][1] = ldtr2(a + 32, a + 16 * 96 + 32); }
                u32x4 pa, pb;
                pa[0] = pack2(EX(s0[2 * u][0], m0), EX(s0[2 * u][1], m0)); pa[1] = pack2(EX(s0[2 * u][2], m0), EX(s0[2 * u][3], m0));
                if constexpr (u < 8) { pa[2] = pack2(EX(s0[2 * u + 1][0], m0), EX(s0[2 * u + 1][1], m0)); pa[3] = pack2(EX(s0[2 * u + 1][2], m0), EX(s0[2 * u + 1][3], m0)); }
                else { pa[2] = 0; pa[3] = 0; }
                if constexpr (u > 0) { pb[0] = pack2(EX(s1[2 * u - 1][0], m1), EX(s1[2 * u - 1][1], m1)); pb[1] = pack2(EX(s1[2 * u - 1][2], m1), EX(s1[2 * u - 1][3], m1)); }
                else { pb[0] = 0; pb[1] = 0; }
                pb[2] = pack2(EX(s1[2 * u][0], m1), EX(s1[2 * u][1], m1)); pb[3] = pack2(EX(s1[2 * u][2], m1), EX(s1[2 * u][3], m1));
                o00 = mfma(vf[u & 1][0], pa, o00); o01 = mfma(vf[u & 1][1], pa, o01);
                o10 = mfma(vf[u & 1][0], pb, o10); o11 = mfma(vf[u & 1][1], pb, o11);
                SB0();
            });
#undef EX
        };
        if (__all(fabsf(m0) <= 64.f && fabsf(m1) <= 64.f)) pv(std::false_type{}); else pv(std::true_type{});
        STAMP(24);
        const float i0 = __builtin_amdgcn_rcpf(__shfl(o01[0], i16 + 16)), i1 = __builtin_amdgcn_rcpf(__shfl(o11[0], i16 + 16));
        const u32 oa = L0 + L_Q + (kr0 + i16) * 80 + hd * 40;
        sts64(oa + 8 * g, (u32x2){pack2(o00[0] * i0, o00[1] * i0), pack2(o00[2] * i0, o00[3] * i0)});
        sts64(oa + 1280 + 8 * g, (u32x2){pack2(o10[0] * i1, o10[1] * i1), pack2(o10[2] * i1, o10[3] * i1)});
        if (g == 0) {
            sts64(oa + 32, (u32x2){pack2(o01[0] * i0, o01[1] * i0), pack2(o01[2] * i0, o01[3] * i0)});
            sts64(oa + 1280 + 32, (u32x2){pack2(o11[0] * i1, o11[1] * i1), pack2(o11[2] * i1, o11[3] * i1)});
        }
    }
    BAR();
    {
        const int tok = 64 * (w & 1) + l;
        const u32 oo = WS_O + (((rb * 8 + (tok >> 4)) * 80 + 5 * j) * 16 + (tok & 15)) * 16;
        ST16C(lds128(L0 + L_Q + tok * 80 + (w >> 1) * 16), oo + (w >> 1) * 256);
        if (w < 2) ST16C(lds128(L0 + L_Q + tok * 80 + 64), oo + 4 * 256);
    }
    VMCNT(0);
    BAR();
    if (tid == 0) {
        st_agent(ws + WS_FO + (rb * 16 + j) * FS, tag);
        if (wpwg) st_agent(ws + WS_FWP + b * FS, tag);
    }
    STAMP(8);

    const int wm = w & 3, wk = w >> 2;
    const int mt3 = rb * 8 + 4 * hf + wm;
    const int nt0 = wk == 0 ? 0 : 3, ntn = wk == 0 ? 3 : 2;
    u32x2 x1k[3];
    {
        const int wdu = b + 256 * (w - 6);
        const bool wdw = w >= 6 && wdu < 432;
        if (w >= 1 && w < 5) {
#pragma unroll
            for (int i = 0; i < 5; ++i) glds16(wosrc + (w - 1) * 32 * 1280, wvoff[i], L0 + L_WO + (w - 1) * 5120 + i * 1024);
        } else if (w >= 5) {
#pragma unroll
            for (int e = 0; e < 4; ++e) {
                const int pc = w - 5 + 3 * e;
                if (pc < 10) {
                    const u32 sg = 64 * pc + l, r = (sg * 6554) >> 16, c = sg - r * 10;
                    glds16((const char*)p.x + (rb * 128 + 64 * hf) * 1280 + cb * 160, r * 1280 + c * 16, L0 + L_XR + pc * 1024);
                }
            }
            if (wdw) {
                const int ucb = wdu / 54, us = wdu - 54 * ucb;
#pragma unroll
                for (int i = 0; i < 5; ++i) glds16((const char*)p.wd + ucb * 160 + us * 32 * 1280, wvoff[i], L0 + L_WDRAW + (w - 6) * 5120 + i * 1024);
            }
        }
        if (w == 1 && l < 20) stsf4(L0 + L_GA + l * 16, gather1(ws, 320 + 20 * cb + l, tag, ws, 4));
        if (w == 0) {
            const int bl = min((Gj >> 2) + ((l >> 2) % 3), (Gj + nj - 1) >> 2);
            poll_at(ws, WS_FWP + (108 * (l >= 12 && l < 24 ? 1 : 0) + 4 * bl + (l & 3)) * FS, l, tag, ws, 6);
            poll_flags(ws + WS_FO + rb * 16 * FS, 16, l, tag, ws, 5);
        }
        if (wdw) vmw<5>(); else vmw<0>();
        BAR();
        STAMP(9);
        u32x4 a[10];
        const char* asrc = ws + WS_O + (mt3 * 80 + 40 * wk) * 256;
#define AL(s) a[s] = gld16c<((s) & 3) * 1024>(asrc + ((s) >> 2) * 4096, lo16)
        AL(0); AL(1); AL(2); AL(3);
        f32x4 acc[5];
#pragma unroll
        for (int n = 0; n < 5; ++n) acc[n] = Z4;
        const u32 wb = L0 + L_WO + wk * 51200;
        const u32 b0 = wb + 160 * (8 * g + 4 * (g & 1) + q4) + 8 * p4, b1 = wb + 160 * (8 * g + 4 * ((g & 1) ^ 1) + q4) + 8 * p4;
        u32x4 bf[2][5];
#pragma unroll
        for (int n = 0; n < 5; ++n) bf[0][n] = ldtr2(b0 + n * 32, b1 + n * 32);
        static_for<0, 10>([&](auto S) {
            constexpr int s = S;
            vmw1<(s < 7 ? 3 : 9 - s)>(a[s]);
            if constexpr (s + 4 < 10) AL(s + 4);
            SB0();
#pragma unroll
            for (int n = 0; n < 5; ++n) {
                if (s < 9) bf[(s + 1) & 1][n] = ldtr2(b0 + (s + 1) * 5120 + n * 32, b1 + (s + 1) * 5120 + n * 32);
                acc[n] = mfma(bf[s & 1][n], a[s], acc[n]);
            }
#pragma unroll
            for (int n = 0; n < 5; ++n) { SGB(M_DSR, 2); SGB(M_MFMA, 1); }
            SB0();
        });
#undef AL
        STAMP(25);
        if (wdw) {
            const u32 wr = L0 + L_WDRAW + (w - 6) * 5120;
            const u32 r0 = wr + 160 * (8 * g + 4 * (g & 1) + q4) + 8 * p4, r1 = wr + 160 * (8 * g + 4 * ((g & 1) ^ 1) + q4) + 8 * p4;
#pragma unroll
            for (int n = 0; n < 5; ++n) ST16C(ldtr2(r0 + n * 32, r1 + n * 32), WS_WD + (wdu * 5 + n) * 1024 + lo16);
        }
        if (wk == 0) { stsf4(L0 + L_RED3 + (wm * 5 + 3) * 1024 + lo16, acc[3]); stsf4(L0 + L_RED3 + (wm * 5 + 4) * 1024 + lo16, acc[4]); }
        else { stsf4(L0 + L_RED3 + (wm * 5 + 0) * 1024 + lo16, acc[0]); stsf4(L0 + L_RED3 + (wm * 5 + 1) * 1024 + lo16, acc[1]); stsf4(L0 + L_RED3 + (wm * 5 + 2) * 1024 + lo16, acc[2]); }
        BAR();
        float s1 = 0.f, s2 = 0.f;
#pragma unroll
        for (int n = 0; n < 3; ++n) {
            if (n < ntn) {
                const int nt = nt0 + n;
                const f32x4 mine = wk == 0 ? acc[n] : acc[n < 2 ? 3 + n : 4];
                const f32x4 v = mine + ldsf4(L0 + L_RED3 + (wm * 5 + nt) * 1024 + lo16);
                const f32x4 ga = ldsf4(L0 + L_GA + (16 * nt + 4 * g) * 4);
                const u32x2 xr = lds64(L0 + L_XR + (16 * wm + i16) * 160 + 32 * nt + 8 * g);
                const u32x2 o = {pack2(bflo(xr[0]) + ga[0] * v[0], bfhi(xr[0]) + ga[1] * v[1]), pack2(bflo(xr[1]) + ga[2] * v[2], bfhi(xr[1]) + ga[3] * v[3])};
                x1k[n] = o;
                s1 = dot2(o[0], 0x3F803F80u, s1); s1 = dot2(o[1], 0x3F803F80u, s1);
                s2 = dot2(o[0], o[0], s2); s2 = dot2(o[1], o[1], s2);
                sts64(L0 + L_X1S + (16 * wm + i16) * 160 + 32 * nt + 8 * g, o);
            } else x1k[n] = (u32x2){0, 0};
        }
        s1 = xsum4(s1); s2 = xsum4(s2);
        if (l < 16) stsf2(L0 + L_ST3 + (wk * 64 + 16 * wm + l) * 8, (f32x2){s1, s2});
        BAR();
        {
            const u32 xo = WS_X1 + (((rb * 8 + 4 * hf + g) * 80 + 10 * cb) * 16 + i16) * 16;
            ST16C(lds128(L0 + L_X1S + l * 160 + w * 16), xo + w * 256);
            if (w < 2) ST16C(lds128(L0 + L_X1S + l * 160 + (w + 8) * 16), xo + (w + 8) * 256);
            if (w == 2) {
                const f32x2 u = *(LAS f32x2*)(size_t)(L0 + L_ST3 + l * 8), v = *(LAS f32x2*)(size_t)(L0 + L_ST3 + (64 + l) * 8);
                const f32x2 sum = u + v;
                ST8C(__builtin_bit_cast(u32x2, sum), WS_STAT + ((rb * 8 + cb) * 128 + 64 * hf + l) * 8);
            }
        }
        STAMP(26);
        VMCNT(0);
        BAR();
        if (tid == 0) { st_agent(ws + WS_FX1 + (rb * 16 + j) * FS, tag); st_agent(ws + WS_FW2 + b * FS, tag); }
    }
    STAMP(10);

    {
        const int wn = w >> 2;
        const char* wpj = ws + WS_WP + (j * 20 * 14 + w) * 1024;
        const char* x1b = ws + WS_X1 + (rb * 8 + w) * 80 * 256;
        const u32 r4w = L0 + L_R4 + w * 1024;
#define P4_B1(t, i) glds16c(wpj + ((t) * 28 + 8 * (i)) * 1024, lo16, r4w + ((t) % 3) * R4_STAGE + 16384 + 8 * (i) * 1024)
#define P4_A1(t, i) glds16c(x1b + (8 * (t) + 4 * (i)) * 256, lo16, r4w + ((t) % 3) * R4_STAGE + 8 * (i) * 1024)
#define P4_B(t) do { P4_B1(t, 0); P4_B1(t, 1); P4_B1(t, 2); if (w < 4) P4_B1(t, 3); } while (0)
        P4_B(0); P4_B(1); P4_B(2);
        if (w == 0) poll_flags(ws + WS_FX1 + rb * 16 * FS, 16, l, tag, ws, 7);
        BAR();
        STAMP(11);
        glds16c(ws + WS_STAT + rb * 8192 + w * 1024, lo16, L0 + L_ST4 + w * 1024);
        if (w < 7) glds16c(ws + WS_UV + j * 7168 + w * 1024, lo16, L0 + L_UV + w * 1024);
        P4_A1(0, 0); P4_A1(0, 1); P4_A1(1, 0); P4_A1(1, 1); P4_A1(2, 0); P4_A1(2, 1);
        f32x4 acc[7][2];
#pragma unroll
        for (int n = 0; n < 7; ++n) { acc[n][0] = Z4; acc[n][1] = Z4; }
        const u32 ra = L0 + L_R4 + 2 * wm * 1024 + lo16, rbb = L0 + L_R4 + 16384 + 7 * wn * 1024 + lo16;
        u32x4 fa[2][2], fb[2][7];
#define P4_RA(buf, t, ks) do { const u32 so = ((t) % 3) * R4_STAGE + (ks) * 8192; fa[buf][0] = lds128(ra + so); fa[buf][1] = lds128(ra + so + 1024); } while (0)
#define P4_RB(buf, t, ks, n) fb[buf][n] = lds128(rbb + ((t) % 3) * R4_STAGE + (ks) * 14336 + (n) * 1024)
#define P4_MM(buf, n) do { acc[n][0] = mfma(fb[buf][n], fa[buf][0], acc[n][0]); acc[n][1] = mfma(fb[buf][n], fa[buf][1], acc[n][1]); } while (0)
#define P4_HALF(buf, rd, t, ks, V0, V1, V2) do { \
            SB0(); if (rd) { P4_RA((buf) ^ 1, t, ks); P4_RB((buf) ^ 1, t, ks, 0); } P4_MM(buf, 0); SB0(); V0; \
            SB0(); if (rd) P4_RB((buf) ^ 1, t, ks, 1); P4_MM(buf, 1); SB0(); \
            SB0(); if (rd) P4_RB((buf) ^ 1, t, ks, 2); P4_MM(buf, 2); SB0(); V1; \
            SB0(); if (rd) P4_RB((buf) ^ 1, t, ks, 3); P4_MM(buf, 3); SB0(); \
            SB0(); if (rd) P4_RB((buf) ^ 1, t, ks, 4); P4_MM(buf, 4); SB0(); V2; \
            SB0(); if (rd) P4_RB((buf) ^ 1, t, ks, 5); P4_MM(buf, 5); SB0(); \
            SB0(); if (rd) P4_RB((buf) ^ 1, t, ks, 6); P4_MM(buf, 6); SB0(); } while (0)
        VMCNT(4);
        BAR();
        STAMP(27);
        P4_RA(0, 0, 0);
#pragma unroll
        for (int n = 0; n < 7; ++n) P4_RB(0, 0, 0, n);
        static_for<0, 10>([&](auto T) {
            constexpr int t = T;
            constexpr bool f = t >= 1 && t + 2 <= 9, sN = t + 3 <= 9;
            P4_HALF(0, true, t, 1, if (f && w < 4) P4_B1(t + 2, 3), if (f) P4_A1(t + 2, 0), if (f) P4_A1(t + 2, 1));
            if constexpr (t == 5) {
                if (tid < 128) {
                    f32x2 st = {0.f, 0.f};
#pragma unroll
                    for (int c = 0; c < 8; ++c) st += *(LAS f32x2*)(size_t)(L0 + L_ST4 + (c * 128 + tid) * 8);
                    const float mu = st[0] * (1.f / 640.f), var = fmaxf(st[1] * (1.f / 640.f) - mu * mu, 0.f), r = __builtin_amdgcn_rsqf(var + 1e-5f);
                    stsf2(L0 + L_MR + tid * 8, (f32x2){r, -mu * r});
                } else {
                    const int e = tid - 128;
                    stsf(L0 + L_UVS + e * 4, (ldsf(L0 + L_UV + (e >> 5) * 512 + (e & 31) * 4) + ldsf(L0 + L_UV + (e >> 5) * 512 + 128 + (e & 31) * 4))
                                           + (ldsf(L0 + L_UV + (e >> 5) * 512 + 256 + (e & 31) * 4) + ldsf(L0 + L_UV + (e >> 5) * 512 + 384 + (e & 31) * 4)));
                    if (e < 64) { const int e2 = e + 384;
                        stsf(L0 + L_UVS + e2 * 4, (ldsf(L0 + L_UV + (e2 >> 5) * 512 + (e2 & 31) * 4) + ldsf(L0 + L_UV + (e2 >> 5) * 512 + 128 + (e2 & 31) * 4))
                                                + (ldsf(L0 + L_UV + (e2 >> 5) * 512 + 256 + (e2 & 31) * 4) + ldsf(L0 + L_UV + (e2 >> 5) * 512 + 384 + (e2 & 31) * 4))); }
                }
            }
            if constexpr (t < 9) {
                if constexpr (t == 0) vmw<2>();
                else if constexpr (t == 8) vmw<0>();
                else { if (w < 4) vmw<6>(); else vmw<5>(); }
                BAR();
                P4_HALF(1, true, t + 1, 0, if (sN) P4_B1(t + 3, 0), if (sN) P4_B1(t + 3, 1), if (sN) P4_B1(t + 3, 2));
            } else {
                P4_HALF(1, false, 0, 0, , , );
            }
        });
#undef P4_B
#undef P4_B1
#undef P4_A1
#undef P4_RA
#undef P4_RB
#undef P4_MM
#undef P4_HALF
        STAMP(12);
        f32x2 mr[2];
#pragma unroll
        for (int m = 0; m < 2; ++m) mr[m] = *(LAS f32x2*)(size_t)(L0 + L_MR + (32 * wm + 16 * m + i16) * 8);
#pragma unroll
        for (int n = 0; n < 7; ++n) {
            const f32x4 u = ldsf4(L0 + L_UVS + (7 * wn + n) * 128 + 16 * g), v = ldsf4(L0 + L_UVS + (7 * wn + n) * 128 + 64 + 16 * g);
#pragma unroll
            for (int m = 0; m < 2; ++m) acc[n][m] = acc[n][m] * mr[m][0] + (u * mr[m][1] + v);
        }
        if (nj == 7) stsf4(L0 + L_XCH + w * 1024 + lo16, wn == 0 ? acc[6][1] : acc[6][0]);
        auto silu_mul = [&](const f32x4 gt, const f32x4 up) -> u32x2 {
            const f32x4 e = gt * (-LOG2E), gu = gt * up;
            f32x4 d;
#pragma unroll
            for (int r = 0; r < 4; ++r) d[r] = __builtin_amdgcn_exp2f(e[r]);
            d = d + 1.f;
#pragma unroll
            for (int r = 0; r < 4; ++r) d[r] = __builtin_amdgcn_rcpf(d[r]);
            const f32x4 o = gu * d;
            return (u32x2){pack2(o[0], o[1]), pack2(o[2], o[3])};
        };
#pragma unroll
        for (int a = 0; a < 3; ++a) {
            const int la = a + 4 * wn, gam = Gj + (la <= 3 || nj == 7 ? la : la - 1);
            const u32x2 o0 = silu_mul(acc[2 * a][0], acc[2 * a + 1][0]), o1 = silu_mul(acc[2 * a][1], acc[2 * a + 1][1]);
            const auto s0 = __builtin_amdgcn_permlane16_swap(o0[0], o1[0], false, false);
            const auto s1 = __builtin_amdgcn_permlane16_swap(o0[1], o1[1], false, false);
            const u32x4 v = {s0[0], s1[0], s0[1], s1[1]};
            ST16C(v, WS_ACT + (((rb * 8 + 2 * wm + (g & 1)) * 216 + 2 * gam + (g >> 1)) * 16 + i16) * 16);
        }
        STAMP(28);
        if (nj == 7) {
            BAR();
            const f32x4 other = ldsf4(L0 + L_XCH + (w ^ 4) * 1024 + lo16);
            const u32x2 o = wn == 0 ? silu_mul(acc[6][0], other) : silu_mul(other, acc[6][1]);
            ST8C(o, WS_ACT + (((rb * 8 + 2 * wm + wn) * 216 + 2 * (Gj + 3) + (g >> 1)) * 16 + i16) * 16 + 8 * (g & 1));
        }
        VMCNT(0);
        BAR();
        if (tid == 0) st_agent(ws + WS_FACT + (rb * 16 + j) * FS, tag);
    }
    STAMP(13);

    {
        if (w == 0) {
            const int u0 = 54 * cb;
            const char* a = ws + WS_FW2 + ((u0 + (l < 54 ? l : 0)) & 255) * FS;
            u32 spins = 0;
            for (;;) {
                const u64 v = ld_agent(a);
                if (__all(v == tag)) break;
                if (++spins > SPIN_CAP) { if (l == 0) st_agent32(ws + WS_ERR, 12); break; }
                __builtin_amdgcn_s_sleep(1);
            }
        }
        BAR();
        const char* wsrc = ws + WS_WD + (cb * 54 + wk * 27) * 5120;
        const u32 r5w = L0 + L_R5 + wk * 20480 + wm * 5120;
#define P5_B1(t, i) glds16c(wsrc + min(4 * (t) + wm, 26) * 5120 + (i) * 1024, lo16, r5w + ((t) % 3) * R5_STAGE + (i) * 1024)
#define P5_B(t) do { P5_B1(t, 0); P5_B1(t, 1); P5_B1(t, 2); P5_B1(t, 3); P5_B1(t, 4); } while (0)
        P5_B(0); P5_B(1); P5_B(2);
        if (w == 1 && l < 20) stsf4(L0 + L_GB + l * 16, gather1(ws, 800 + 20 * cb + l, tag, ws, 8));
        if (w == 0) poll_flags(ws + WS_FACT + rb * 16 * FS, 16, l, tag, ws, 9);
        VMCNT(0);
        BAR();
        STAMP(14);
        const char* asrc = ws + WS_ACT + (mt3 * 216 + wk * 108) * 256;
        u32x4 a[3][4];
#define P5_A1(t, i) a[(t) % 3][i] = gld16c<(i) * 1024>(asrc + (t) * 4096, lo16)
        P5_A1(0, 0); P5_A1(0, 1); P5_A1(0, 2); P5_A1(0, 3); P5_A1(1, 0); P5_A1(1, 1); P5_A1(1, 2); P5_A1(1, 3); P5_A1(2, 0); P5_A1(2, 1); P5_A1(2, 2); P5_A1(2, 3);
        f32x4 acc[5];
#pragma unroll
        for (int n = 0; n < 5; ++n) acc[n] = Z4;
        const u32 rb5 = L0 + L_R5 + wk * 20480 + lo16;
        u32x4 fb[2][5];
#define P5_RD(buf, t, kk) do { _Pragma("unroll") for (int n = 0; n < 5; ++n) fb[buf][n] = lds128(rb5 + ((t) % 3) * R5_STAGE + (kk) * 5120 + n * 1024); } while (0)
#define P5_MM(buf, av) do { _Pragma("unroll") for (int n = 0; n < 5; ++n) acc[n] = mfma(fb[buf][n], (av), acc[n]); } while (0)
#define P5_MIX() do { _Pragma("unroll") for (int n = 0; n < 5; ++n) { SGB(M_DSR, 1); SGB(M_MFMA, 1); } } while (0)
        P5_RD(0, 0, 0);
        static_for<0, 7>([&](auto T) {
            constexpr int t = T, q = t % 3;
            constexpr bool nx = t + 3 < 7;
            constexpr int NA = t == 0 ? 8 : t == 1 ? 13 : t == 2 ? 18 : t == 3 ? 21 : t == 4 ? 20 : t == 5 ? 11 : 5;
            constexpr int NB = t == 2 ? 12 : t == 3 ? 12 : t == 4 ? 8 : t == 5 ? 0 : 63;
            vmw4<NA>(a[q][0], a[q][1], a[q][2], a[q][3]);
            SB0(); P5_RD(1, t, 1); P5_MM(0, a[q][0]); P5_MIX(); SB0();
            if constexpr (nx) P5_A1(t + 3, 0);
            SB0(); P5_RD(0, t, 2); P5_MM(1, a[q][1]); P5_MIX(); SB0();
            if constexpr (nx) P5_A1(t + 3, 1);
            if constexpr (t < 6) {
                SB0(); P5_RD(1, t, 3); P5_MM(0, a[q][2]); P5_MIX(); SB0();
                if constexpr (nx) P5_A1(t + 3, 2);
                vmw<NB>(); BAR();
                if constexpr (nx) { P5_B1(t + 3, 0); P5_B1(t + 3, 1); }
                SB0(); P5_RD(0, t + 1, 0); P5_MM(1, a[q][3]); P5_MIX(); SB0();
                if constexpr (nx) { if constexpr (t + 3 < 6) P5_A1(t + 3, 3); P5_B1(t + 3, 2); P5_B1(t + 3, 3); P5_B1(t + 3, 4); }
            } else {
                SB0(); P5_MM(0, a[q][2]); SB0();
            }
        });
#undef P5_B
#undef P5_B1
#undef P5_A1
#undef P5_RD
#undef P5_MM
#undef P5_MIX
        STAMP(15);
        if (wk == 0) { stsf4(L0 + L_RED5 + (wm * 5 + 3) * 1024 + lo16, acc[3]); stsf4(L0 + L_RED5 + (wm * 5 + 4) * 1024 + lo16, acc[4]); }
        else { stsf4(L0 + L_RED5 + (wm * 5 + 0) * 1024 + lo16, acc[0]); stsf4(L0 + L_RED5 + (wm * 5 + 1) * 1024 + lo16, acc[1]); stsf4(L0 + L_RED5 + (wm * 5 + 2) * 1024 + lo16, acc[2]); }
        BAR();
#pragma unroll
        for (int n = 0; n < 3; ++n) {
            if (n < ntn) {
                const int nt = nt0 + n;
                const f32x4 mine = wk == 0 ? acc[n] : acc[n < 2 ? 3 + n : 4];
                const f32x4 v = mine + ldsf4(L0 + L_RED5 + (wm * 5 + nt) * 1024 + lo16);
                const f32x4 gb = ldsf4(L0 + L_GB + (16 * nt + 4 * g) * 4);
                const u32x2 o = {pack2(bflo(x1k[n][0]) + gb[0] * v[0], bfhi(x1k[n][0]) + gb[1] * v[1]), pack2(bflo(x1k[n][1]) + gb[2] * v[2], bfhi(x1k[n][1]) + gb[3] * v[3])};
                sts64(L0 + L_OS + (16 * wm + i16) * 160 + 32 * nt + 8 * g, o);
            }
        }
        BAR();
#pragma unroll
        for (int i = 0; i < 2; ++i) {
            const int id = tid + 512 * i;
            if (id < 640) {
                const int r = (id * 6554) >> 16, c = id - 10 * r;
                *(GAS u32x4*)((char*)p.out + (rb * 128 + 64 * hf + r) * 1280 + cb * 160 + c * 16) = lds128(L0 + L_OS + id * 16);
            }
        }
    }
    STAMP(16); STAMP_RT(63); STAMP_OUT();
    if (rb == 15 && j == 0 && w < 4) {
        u32 spins = 0;
        for (;;) {
            const u64 v = ld_agent(ws + WS_FO + tid * FS);
            if (__all(v == tag)) break;
            if (++spins > SPIN_CAP) { st_agent32(ws + WS_ERR, 10); break; }
            __builtin_amdgcn_s_sleep(1);
        }
    }
    if (rb == 15 && j == 0) {
        BAR();
        if (tid == 0) st_agent(ws + WS_EPOCH, tag);
    }
}

extern "C" void kernel_launch(void* const* d_in, const int* in_sizes, int n_in, void* d_out, int out_size, void* d_ws, size_t ws_size, hipStream_t stream) {
    Params p;
    p.x = (const u16*)d_in[0]; p.wqkv = (const u16*)d_in[1]; p.wo = (const u16*)d_in[2];
    p.wg = (const u16*)d_in[4]; p.wu = (const u16*)d_in[5]; p.wd = (const u16*)d_in[6];
    p.c = (const u16*)d_in[7]; p.wmod = (const u16*)d_in[8];
    p.out = (u16*)d_out; p.ws = (char*)d_ws;
    mega_kernel<<<256, 512, 0, stream>>>(p);
}
```

```cpp
#include <hip/hip_runtime.h>
#include <hip/hip_bf16.h>
#include <stdint.h>
#include <type_traits>

typedef unsigned short u16;
typedef unsigned u32;
typedef unsigned long long u64;
typedef __attribute__((ext_vector_type(2))) unsigned u32x2;
typedef __attribute__((ext_vector_type(4))) unsigned u32x4;
typedef __attribute__((ext_vector_type(2))) float f32x2;
typedef __attribute__((ext_vector_type(4))) float f32x4;
typedef __attribute__((ext_vector_type(2))) __bf16 bf16x2;
typedef __attribute__((ext_vector_type(8))) __bf16 bf16x8;
typedef __attribute__((ext_vector_type(4))) short s16x4;
#define LAS __attribute__((address_space(3)))
#define GAS __attribute__((address_space(1)))
#define DI __device__ __forceinline__

constexpr int SEQ = 2048, DM = 640, FF = 1728;
constexpr float QSCALE = 0.22360679774997896f * 1.4426950408889634f;
constexpr float LOG2E = 1.4426950408889634f;

constexpr u32 WS_EPOCH = 0;
constexpr u32 WS_ERR   = 256;
constexpr u32 FS = 128;
constexpr u32 WS_FKV   = 4096;
constexpr u32 WS_FO    = WS_FKV + 256 * FS;
constexpr u32 WS_FX1   = WS_FO + 256 * FS;
constexpr u32 WS_FACT  = WS_FX1 + 256 * FS;
constexpr u32 WS_FH    = WS_FACT + 256 * FS;
constexpr u32 WS_FWT   = WS_FH + 256 * FS;
constexpr u32 WS_FWP   = WS_FWT + 256 * FS;
constexpr u32 WS_FW2   = WS_FWP + 256 * FS;
constexpr u32 WS_MODG  = WS_FW2 + 256 * FS;
constexpr u32 WS_UV    = WS_MODG + 61440;
constexpr u32 WS_STAT  = WS_UV + 114688;
constexpr u32 WS_KV    = 589824;
constexpr u32 WS_O     = WS_KV + 256 * 22528;
constexpr u32 WS_X1    = WS_O + SEQ * DM * 2;
constexpr u32 WS_ACT   = WS_X1 + SEQ * DM * 2;
constexpr u32 WS_WP    = WS_ACT + SEQ * FF * 2;
constexpr u32 WS_H     = WS_WP + 16 * 20 * 14 * 1024;
constexpr u32 WS_WD    = WS_H + SEQ * DM * 2;
constexpr u32 WS_WO    = WS_WD + 8 * 54 * 5 * 1024;
constexpr u32 WS_WT    = WS_WO + 8 * 20 * 5 * 1024;
constexpr u32 WS_STAMP = WS_WT + 16 * 153600;
constexpr u32 WS_END   = WS_STAMP + 256 * 8 * 64 * 4;

constexpr u32 L_WKV = 0, L_WQ = 102400, L_GS = 153600, L_STP = 156160, L_MODP = 156416, L_CNT = 157440, L_SB = 157696, L_G2 = 158016;
constexpr u32 L_ODRAW = 101376, L_WTRAW = 116736, L_XS = 133120;
constexpr u32 L_K = 0, L_V = 30720, L_Q = 67584, L_WPRAW = 77824, L_UVL = 98304;
constexpr u32 L_WO = 61440, L_RED3 = 0, L_X1S = 20480, L_XR = 30720, L_GA = 40960, L_ST3 = 41472, L_WDRAW = 43008;
constexpr u32 L_R4 = 0, R4_STAGE = 45056, L_ST4 = 135168, L_UV = 143360, L_MR = 150528, L_XCH = 151552, L_UVS = 159744;
constexpr u32 L_RED5 = 126976, L_GB = 147456, L_OS = 148480;
constexpr u32 r5_slot(int t) { return t % 3 == 0 ? 45056 : t % 3 == 1 ? 86016 : 0; }

struct Params {
    const u16* x; const u16* wqkv; const u16* wo; const u16* wg; const u16* wu; const u16* wd;
    const u16* c; const u16* wmod; u16* out; char* ws;
};

DI float bflo(u32 d) { return __uint_as_float(d << 16); }
DI float bfhi(u32 d) { return __uint_as_float(d & 0xffff0000u); }
DI u32 pack2(float a, float b) { f32x2 v = {a, b}; return __builtin_bit_cast(u32, __builtin_convertvector(v, bf16x2)); }
DI float rbf(float a) { return bflo(pack2(a, a)); }
DI f32x4 mfma(u32x4 a, u32x4 b, f32x4 c) {
    return __builtin_amdgcn_mfma_f32_16x16x32_bf16(__builtin_bit_cast(bf16x8, a), __builtin_bit_cast(bf16x8, b), c, 0, 0, 0);
}
DI u32x2 ldtr(u32 a) { return __builtin_bit_cast(u32x2, __builtin_amdgcn_ds_read_tr16_b64_v4i16((LAS s16x4*)(size_t)a)); }
DI u32x4 ldtr2(u32 a0, u32 a1) { u32x2 x = ldtr(a0), y = ldtr(a1); return (u32x4){x[0], x[1], y[0], y[1]}; }
DI u32x4 lds128(u32 a) { return *(LAS u32x4*)(size_t)a; }
DI u32x2 lds64(u32 a) { return *(LAS u32x2*)(size_t)a; }
DI f32x4 ldsf4(u32 a) { return *(LAS f32x4*)(size_t)a; }
DI float ldsf(u32 a) { return *(LAS float*)(size_t)a; }
DI void sts128(u32 a, u32x4 v) { *(LAS u32x4*)(size_t)a = v; }
DI void sts64(u32 a, u32x2 v) { *(LAS u32x2*)(size_t)a = v; }
DI void stsf(u32 a, float v) { *(LAS float*)(size_t)a = v; }
DI void stsf4(u32 a, f32x4 v) { *(LAS f32x4*)(size_t)a = v; }
DI void stsf2(u32 a, f32x2 v) { *(LAS f32x2*)(size_t)a = v; }
DI void sts16(u32 a, u16 v) { *(LAS u16*)(size_t)a = v; }

DI void glds16_(const void* root, u32 voff, u32 ldsdst) {
    asm volatile("s_mov_b32 m0, %2\n\ts_nop 0\n\tglobal_load_lds_dwordx4 %0, %1" :: "v"(voff), "s"(root), "s"(__builtin_amdgcn_readfirstlane(ldsdst)) : "memory");
}
DI void glds16c_(const void* root, u32 voff, u32 ldsdst) {
    asm volatile("s_mov_b32 m0, %2\n\ts_nop 0\n\tglobal_load_lds_dwordx4 %0, %1 sc1" :: "v"(voff), "s"(root), "s"(__builtin_amdgcn_readfirstlane(ldsdst)) : "memory");
}
template <int OFF> DI void glds16c_next_(const void* root, u32 voff) {
    asm volatile("global_load_lds_dwordx4 %0, %1 offset:%2 sc1" :: "v"(voff), "s"(root), "i"(OFF) : "memory");
}
template <int OFF> DI u32x4 gld16(const void* root, u32 voff) {
    u32x4 r; asm volatile("global_load_dwordx4 %0, %1, %2 offset:%3" : "=v"(r) : "v"(voff), "s"(root), "i"(OFF) : "memory"); return r;
}
template <int OFF> DI u32x4 gld16c_(const void* root, u32 voff) {
    u32x4 r; asm volatile("global_load_dwordx4 %0, %1, %2 offset:%3 sc1" : "=v"(r) : "v"(voff), "s"(root), "i"(OFF) : "memory"); return r;
}
#define ROFF(root, ptr) ((u32)((const char*)(ptr) - (const char*)(root)))
#define glds16(root, ptr, voff, dst) glds16_((root), ROFF(root, ptr) + (voff), (dst))
#define glds16c(ptr, voff, dst) glds16c_(ws, ROFF(ws, ptr) + (voff), (dst))
#define glds16c_next(OFF, ptr, voff) glds16c_next_<(OFF)>(ws, ROFF(ws, ptr) + (voff))
#define gld16c(OFF, ptr, voff) gld16c_<(OFF)>(ws, ROFF(ws, ptr) + (voff))
DI u32 gldu16(const void* sbase, u32 voff) {
    u32 r; asm volatile("global_load_ushort %0, %1, %2" : "=v"(r) : "v"(voff), "s"(sbase) : "memory"); return r;
}
#define VMCNT(n) asm volatile("s_waitcnt vmcnt(" #n ")" ::: "memory")
#define BAR() asm volatile("s_waitcnt lgkmcnt(0)\n\ts_barrier" ::: "memory")
#define RLX_AGENT __ATOMIC_RELAXED, __HIP_MEMORY_SCOPE_AGENT
DI u64 ld_agent(const char* p) { return __hip_atomic_load((GAS u64*)p, RLX_AGENT); }
DI void st_agent(char* p, u64 v) { __hip_atomic_store((GAS u64*)p, v, RLX_AGENT); }
DI void st_agent32(char* p, u32 v) { __hip_atomic_store((GAS u32*)p, v, RLX_AGENT); }
DI float ld_agentf(const char* p) { return __uint_as_float(__hip_atomic_load((GAS u32*)p, RLX_AGENT)); }
DI float xsum4(float x) {
    const auto s = __builtin_amdgcn_permlane16_swap(__float_as_uint(x), __float_as_uint(x), false, false);
    x = __uint_as_float(s[0]) + __uint_as_float(s[1]);
    const auto t = __builtin_amdgcn_permlane32_swap(__float_as_uint(x), __float_as_uint(x), false, false);
    return __uint_as_float(t[0]) + __uint_as_float(t[1]);
}
DI float xmax4(float x) {
    const auto s = __builtin_amdgcn_permlane16_swap(__float_as_uint(x), __float_as_uint(x), false, false);
    x = fmaxf(__uint_as_float(s[0]), __uint_as_float(s[1]));
    const auto t = __builtin_amdgcn_permlane32_swap(__float_as_uint(x), __float_as_uint(x), false, false);
    return fmaxf(__uint_as_float(t[0]), __uint_as_float(t[1]));
}
template <int CTRL> DI float dppf(float v) { return __builtin_bit_cast(float, __builtin_amdgcn_update_dpp(0, __builtin_bit_cast(int, v), CTRL, 0xf, 0xf, true)); }

constexpr u32 SPIN_CAP = 1u << 17;
DI void poll_flags(const char* base, int n, int lane, u64 tag, char* ws, u32 code) {
    const char* a = base + (lane < n ? lane : 0) * FS;
    u32 spins = 0;
    for (;;) {
        const u64 v = ld_agent(a);
        if (__all(v == tag)) break;
        if (++spins > SPIN_CAP) { if (lane == 0) st_agent32(ws + WS_ERR, code); break; }
        __builtin_amdgcn_s_sleep(1);
    }
}
typedef __attribute__((ext_vector_type(2))) _Float16 f16x2;
DI float hlo(u32 d) { return (float)__builtin_bit_cast(f16x2, d)[0]; }
DI float hhi(u32 d) { return (float)__builtin_bit_cast(f16x2, d)[1]; }
DI u32 packh(float a, float b) { const f16x2 v = {(_Float16)a, (_Float16)b}; return __builtin_bit_cast(u32, v); }
DI bool gran_ok(const u32x4& v, u64 tag) { return v[2] == (u32)tag && v[3] == (u32)(tag >> 32); }
DI f32x4 gran_sum(const u32x4& a, const u32x4& b, const u32x4& c, const u32x4& d) {
    const f32x4 s = {(hlo(a[0]) + hlo(b[0])) + (hlo(c[0]) + hlo(d[0])), (hhi(a[0]) + hhi(b[0])) + (hhi(c[0]) + hhi(d[0])),
                     (hlo(a[1]) + hlo(b[1])) + (hlo(c[1]) + hlo(d[1])), (hhi(a[1]) + hhi(b[1])) + (hhi(c[1]) + hhi(d[1]))};
    return (f32x4){rbf(s[0]), rbf(s[1]), rbf(s[2]), rbf(s[3])};
}
DI f32x4 gather1(const char* ws, u32 g0, u64 tag, char* wsw, u32 code) {
    u32 spins = 0; u32x4 v0, v1, v2, v3;
    const u32 o = WS_MODG + g0 * 16;
    for (;;) {
        asm volatile("s_nop 4\n\tglobal_load_dwordx4 %0, %4, %5 sc1\n\tglobal_load_dwordx4 %1, %6, %5 sc1\n\tglobal_load_dwordx4 %2, %7, %5 sc1\n\tglobal_load_dwordx4 %3, %8, %5 sc1\n\ts_waitcnt vmcnt(0)"
                     : "=&v"(v0), "=&v"(v1), "=&v"(v2), "=&v"(v3) : "v"(o), "s"(ws), "v"(o + 15360), "v"(o + 30720), "v"(o + 46080) : "memory");
        if (__all(gran_ok(v0, tag) && gran_ok(v1, tag) && gran_ok(v2, tag) && gran_ok(v3, tag))) break;
        if (++spins > SPIN_CAP) { st_agent32(wsw + WS_ERR, code); break; }
        __builtin_amdgcn_s_sleep(1);
    }
    return gran_sum(v0, v1, v2, v3);
}
DI void gather2(const char* ws, u32 g0, u32 g1, u64 tag, char* wsw, u32 code, f32x4& ra, f32x4& rb) {
    u32 spins = 0; u32x4 v0, v1, v2, v3, u0, u1, u2, u3;
    const u32 o = WS_MODG + g0 * 16, q = WS_MODG + g1 * 16;
    for (;;) {
        asm volatile("s_nop 4\n\tglobal_load_dwordx4 %0, %8, %9 sc1\n\tglobal_load_dwordx4 %1, %10, %9 sc1\n\tglobal_load_dwordx4 %2, %11, %9 sc1\n\tglobal_load_dwordx4 %3, %12, %9 sc1\n\t"
                     "global_load_dwordx4 %4, %13, %9 sc1\n\tglobal_load_dwordx4 %5, %14, %9 sc1\n\tglobal_load_dwordx4 %6, %15, %9 sc1\n\tglobal_load_dwordx4 %7, %16, %9 sc1\n\ts_waitcnt vmcnt(0)"
                     : "=&v"(v0), "=&v"(v1), "=&v"(v2), "=&v"(v3), "=&v"(u0), "=&v"(u1), "=&v"(u2), "=&v"(u3)
                     : "v"(o), "s"(ws), "v"(o + 15360), "v"(o + 30720), "v"(o + 46080), "v"(q), "v"(q + 15360), "v"(q + 30720), "v"(q + 46080) : "memory");
        if (__all(gran_ok(v0, tag) && gran_ok(v1, tag) && gran_ok(v2, tag) && gran_ok(v3, tag) && gran_ok(u0, tag) && gran_ok(u1, tag) && gran_ok(u2, tag) && gran_ok(u3, tag))) break;
        if (++spins > SPIN_CAP) { st_agent32(wsw + WS_ERR, code); break; }
        __builtin_amdgcn_s_sleep(1);
    }
    ra = gran_sum(v0, v1, v2, v3); rb = gran_sum(u0, u1, u2, u3);
}
DI void gather_strip(const char* ws, u32 gs, u32 gc, u32 strip, int l, u64 tag, char* wsw, u32 code) {
    f32x4 a, c;
    gather2(ws, l < 40 ? gs + l : gc + (l - 40), gc + 24 + (l & 15), tag, wsw, code, a, c);
    if (l >= 40) a = a + 1.f;
    c = c + 1.f;
    *(LAS u32x2*)(size_t)(strip + (l < 40 ? l * 8 : 320 + (l - 40) * 8)) = (u32x2){pack2(a[0], a[1]), pack2(a[2], a[3])};
    if (l < 16) *(LAS u32x2*)(size_t)(strip + 320 + (24 + l) * 8) = (u32x2){pack2(c[0], c[1]), pack2(c[2], c[3])};
}
DI void poll_at(const char* ws, u32 off, int lane, u64 tag, char* wsw, u32 code) {
    u32 spins = 0;
    for (;;) {
        const u64 v = ld_agent(ws + off);
        if (__all(v == tag)) break;
        if (++spins > SPIN_CAP) { if (lane == 0) st_agent32(wsw + WS_ERR, code); break; }
        __builtin_amdgcn_s_sleep(1);
    }
}

#define STAMP(i) do {} while (0)
#define STAMPL(i) do {} while (0)
#define STAMP_RT(i) do {} while (0)
#define STAMP_OUT() do {} while (0)

template <int N> using IC = std::integral_constant<int, N>;
template <int I, int E, class F> DI void static_for(F&& f) { if constexpr (I < E) { f(IC<I>{}); static_for<I + 1, E>(f); } }
template <int N> DI void vmw() { asm volatile("s_waitcnt vmcnt(%0)" :: "i"(N) : "memory"); }
template <int N> DI void vmw1(u32x4& a) { asm volatile("s_waitcnt vmcnt(%1)" : "+v"(a) : "i"(N) : "memory"); }
template <int N> DI void vmw4(u32x4& a, u32x4& b, u32x4& c, u32x4& d) { asm volatile("s_waitcnt vmcnt(%4)" : "+v"(a), "+v"(b), "+v"(c), "+v"(d) : "i"(N) : "memory"); }
DI u32 opaque(u32 a) { asm("" : "+v"(a)); return a; }
DI float dot2(u32 a, u32 b, float c) { return __builtin_amdgcn_fdot2_f32_bf16(__builtin_bit_cast(bf16x2, a), __builtin_bit_cast(bf16x2, b), c, false); }
#define SB0() __builtin_amdgcn_sched_barrier(0)
#define SGB(m, n) __builtin_amdgcn_sched_group_barrier((m), (n), 0)
constexpr int M_MFMA = 0x8, M_DSR = 0x100;
#define Z4 ((f32x4){0.f, 0.f, 0.f, 0.f})
DI u32 lds_add(u32 a, u32 v) { return __hip_atomic_fetch_add((LAS u32*)(size_t)a, v, __ATOMIC_RELAXED, __HIP_MEMORY_SCOPE_WORKGROUP); }
DI u32 lds_ld(u32 a) { return __hip_atomic_load((LAS u32*)(size_t)a, __ATOMIC_RELAXED, __HIP_MEMORY_SCOPE_WORKGROUP); }
DI void worker_sync(u32 cnt, int lane) {
    asm volatile("s_waitcnt lgkmcnt(0)" ::: "memory");
    if (lane == 0) lds_add(cnt, 1u);
    for (u32 spins = 0; lds_ld(cnt) < 4u && spins < (1u << 22); ++spins) {}
    asm volatile("" ::: "memory");
}

__global__ __launch_bounds__(512, 2) void mega_kernel(Params p) {
    __shared__ __attribute__((aligned(16))) char ldsmem[163840];
    const u32 L0 = (u32)(uintptr_t)(LAS char*)ldsmem;
    const int tid = threadIdx.x, l = tid & 63;
    const int w = __builtin_amdgcn_readfirstlane(tid >> 6);
    const int b = blockIdx.x, xg = b & 7, sidx = b >> 3;
    const int rb = 4 * (xg >> 1) + (sidx >> 3), j = 8 * (xg & 1) + (sidx & 7);
    const int g = l >> 4, i16 = l & 15, q4 = i16 >> 2, p4 = l & 3;
    char* const ws = p.ws;
    asm volatile("" :: "s"(p.x), "s"(p.wqkv), "s"(p.wo), "s"(p.wg), "s"(p.wu), "s"(p.wd), "s"(p.c), "s"(p.wmod), "s"(p.out), "s"(p.ws));
    u64 tag;
#define ASK_TAG(ep) u32x2 ep; asm volatile("global_load_dwordx2 %0, %1, %2" : "=v"(ep) : "v"(WS_EPOCH), "s"(ws) : "memory")
#define TAKE_TAG(ep) do { asm volatile("s_waitcnt vmcnt(0)" : "+v"(ep) :: "memory"); \
        tag = (((u64)(u32)__builtin_amdgcn_readfirstlane(ep[1]) << 32) | (u32)__builtin_amdgcn_readfirstlane(ep[0])) + 1; } while (0)
    const __amdgpu_buffer_rsrc_t wsr = __builtin_amdgcn_make_buffer_rsrc(ws, 0, WS_END, 0x00020000);
#define ST16C(v, off) __builtin_amdgcn_raw_buffer_store_b128((v), wsr, (off), 0, 16)
#define ST8C(v, off)  __builtin_amdgcn_raw_buffer_store_b64((v), wsr, (off), 0, 16)
    STAMP_RT(62); STAMP(0);

    u32 wu0 = 0, wu1 = 0, wu2 = 0;
    if (w == 7) {
        const char* a0 = ws + (size_t)min(l, 31) * (1u << 20);
        const char* a1 = l < 3 ? (const char*)p.x + l * (1u << 20) : l < 6 ? (const char*)p.wqkv + (l - 3) * (1u << 20) : l < 7 ? (const char*)p.wo
                       : l < 10 ? (const char*)p.wg + (l - 7) * (1u << 20) : l < 13 ? (const char*)p.wu + (l - 10) * (1u << 20) : l < 16 ? (const char*)p.wd + (l - 13) * (1u << 20)
                       : l < 21 ? (const char*)p.wmod + (l - 16) * (1u << 20) : l < 24 ? (const char*)p.out + (l - 21) * (1u << 20) : (const char*)p.c;
        const char* pc; asm volatile("s_getpc_b64 %0" : "=s"(pc));
        const char* a2 = pc + (size_t)(sidx * 11 + min(l, 10)) * 128;
        asm volatile("global_load_dword %0, %3, off\n\tglobal_load_dword %1, %4, off\n\tglobal_load_dword %2, %5, off" : "=&v"(wu0), "=&v"(wu1), "=&v"(wu2) : "v"(a0), "v"(a1), "v"(a2) : "memory");
    }
    const int nj = j < 12 ? 7 : 6, Gj = j < 12 ? 7 * j : 84 + 6 * (j - 12);
    const bool wpwg = b < 216;
    const int wpm = b >= 108 ? 1 : 0, wpb = (b - 108 * wpm) >> 2, wpq = b & 3;
    if (w >= 4) {
        __builtin_amdgcn_s_setprio(3);
        const int u = w - 4;
        const bool tw = b >= 106;
        const int tb = (b - 106) / 5, tk = (b - 106) - 5 * tb, ts = 4 * tk + u;
        if (tw) {
            const char* src = (const char*)p.wqkv + ts * 122880 + tb * 128;
            const u32 voff = (l >> 3) * 3840 + (l & 7) * 16;
#pragma unroll
            for (int i = 0; i < 4; ++i) glds16(p.wqkv, src + i * 8 * 3840, voff, L0 + L_WTRAW + u * 4096 + i * 1024);
        }
        ASK_TAG(epl);
        BAR();
        __builtin_amdgcn_s_setprio(0);
        TAKE_TAG(epl);
        if (tw) {
            const u32 a = L0 + L_WTRAW + u * 4096 + (8 * g + q4) * 128 + 8 * p4;
#pragma unroll
            for (int t = 0; t < 4; ++t) {
                const u32 cc = 8 * tb + 2 * t + (i16 >> 3);
                const u32 seg = cc >= 160 ? 2 : cc >= 80 ? 1 : 0, wi = cc - 80 * seg, jj = (wi * 13108) >> 16, c5 = wi - 5 * jj;
                const u32 c = seg == 0 ? 10 + c5 : seg == 1 ? c5 : 5 + c5, nt = c >> 1;
                const u32 dst = WS_WT + jj * 153600 + (nt < 5 ? ts * 5120 + nt * 1024 : 102400 + ts * 2560 + (nt - 5) * 1024)
                              + (nt < 7 ? (g * 16 + 8 * (c & 1) + (i16 & 7)) * 16 : g * 128 + (i16 & 7) * 16);
                ST16C(ldtr2(a + 32 * t, a + 32 * t + 512), dst);
            }
        }
        vmw<0>();
        {
            asm volatile("s_waitcnt lgkmcnt(0)" ::: "memory");
            u32 old = 0;
            if (l == 0) old = lds_add(L0 + L_CNT + 12, 1u);
            if (tw && __builtin_amdgcn_readfirstlane(old) == 3 && l == 0) st_agent(ws + WS_FWT + (b - 106) * FS, tag);
        }
        STAMPL(19);
        if (u == 0) {
            const int wh = l >= 30 ? 0 : l / 5, kr = l >= 30 ? 0 : l - 5 * wh;
            const int bl = (1280 * (wh >> 1) + 80 * j + 79 * (wh & 1)) >> 7;
            poll_at(ws, WS_FWT + (bl * 5 + kr) * FS, l, tag, ws, 13);
            for (u32 spins = 0; lds_ld(L0 + L_CNT + 12) < 4u && spins < (1u << 22); ++spins) {}
            if (l == 0) lds_add(L0 + L_CNT + 24, 1u);
        } else {
            for (u32 spins = 0; lds_ld(L0 + L_CNT + 24) < 1u && spins < (1u << 22); ++spins) {}
        }
        asm volatile("" ::: "memory");
        STAMPL(20);
        {
            const char* src = ws + WS_WT + j * 153600 + u * 1024;
            const u32 lo = l * 16;
#pragma unroll
            for (int i = 0; i < 25; ++i) glds16c(src + i * 4096, lo, L0 + L_WKV + u * 1024 + i * 4096);
        }
        if (wpwg && u == 0) gather_strip(ws, 480 + 40 * wpq, 640 + 40 * wpq, L0 + L_SB, l, tag, ws, 2);
        VMCNT(0);
        STAMPL(17);
    } else {
        __builtin_amdgcn_s_setprio(2);
        const bool modwg = b < 240;
        u32x4 mw[5]; u32 mc[5];
        if (modwg) {
#pragma unroll
            for (int i = 0; i < 5; ++i) {
                const int r = 160 * (b & 3) + (tid >> 3) + 32 * i;
                mw[i] = gld16<0>(p.wmod, r * 7680 + (b >> 2) * 128 + (tid & 7) * 16);
                mc[i] = gldu16(p.c, r * 2);
            }
        }
        {
            const char* src = (const char*)p.x + (rb * 128 + 8 * j) * 1280 + w * 1024;
            glds16(p.x, src, l * 16, L0 + L_XS + w * 1024);
            glds16(p.x, src + 4096, l * 16, L0 + L_XS + (w + 4) * 1024);
            if (w < 2) glds16(p.x, src + 8192, l * 16, L0 + L_XS + (w + 8) * 1024);
        }
        ASK_TAG(epw);
        if (tid < 16) *(LAS u32*)(size_t)(L0 + L_CNT + tid * 4) = 0u;
        BAR();
        STAMP(29);
        TAKE_TAG(epw);
        STAMP(30);
        if (modwg) {
            asm volatile("" : "+v"(mw[0]), "+v"(mw[1]), "+v"(mw[2]), "+v"(mw[3]), "+v"(mw[4]), "+v"(mc[0]), "+v"(mc[1]), "+v"(mc[2]), "+v"(mc[3]), "+v"(mc[4]) :: "memory");
            float acc[8];
#pragma unroll
            for (int e = 0; e < 8; ++e) acc[e] = 0.f;
#pragma unroll
            for (int i = 0; i < 5; ++i) {
                const float cv = bflo(mc[i]);
                const float sv = cv * __builtin_amdgcn_rcpf(1.f + __builtin_amdgcn_exp2f(-cv * LOG2E));
                const u32 slo = pack2(sv, 0.f), shi = slo << 16;
#pragma unroll
                for (int d = 0; d < 4; ++d) { acc[2 * d] = dot2(mw[i][d], slo, acc[2 * d]); acc[2 * d + 1] = dot2(mw[i][d], shi, acc[2 * d + 1]); }
            }
#pragma unroll
            for (int e = 0; e < 8; ++e) { const float v = acc[e]; acc[e] = xsum4(v + dppf<0x128>(v)); }
            if (l < 8) {
                stsf4(L0 + L_MODP + w * 256 + l * 32, (f32x4){acc[0], acc[1], acc[2], acc[3]});
                stsf4(L0 + L_MODP + w * 256 + l * 32 + 16, (f32x4){acc[4], acc[5], acc[6], acc[7]});
            }
            asm volatile("s_waitcnt lgkmcnt(0)" ::: "memory");
            u32 old = 0;
            if (l == 0) old = lds_add(L0 + L_CNT, 1u);
            if (__builtin_amdgcn_readfirstlane(old) == 3) {
                float v = 0.f;
#pragma unroll
                for (int ww = 0; ww < 4; ++ww) v += ldsf(L0 + L_MODP + ww * 256 + l * 4);
                const float v1 = dppf<0x55>(v), v2 = dppf<0xAA>(v), v3 = dppf<0xFF>(v), v0 = dppf<0x00>(v);
                const u32x4 gr = {packh(v0, v1), packh(v2, v3), (u32)tag, (u32)(tag >> 32)};
                if (p4 == 0) ST16C(gr, WS_MODG + ((b & 3) * 960 + (b >> 2) * 16 + (l >> 2)) * 16);
            }
        }
        STAMP(1);
        worker_sync(L0 + L_CNT + 16, l);
        const int tok = l & 7;
        u32x4 xr[3];
#pragma unroll
        for (int i = 0; i < 3; ++i) xr[i] = lds128(L0 + L_XS + tok * 1280 + (20 * w + (i < 2 ? (l >> 3) + 8 * i : 16 + ((l >> 3) & 3))) * 16);
        {
            float s1 = 0.f, s2 = 0.f;
#pragma unroll
            for (int i = 0; i < 3; ++i) {
                float a1 = 0.f, a2 = 0.f;
#pragma unroll
                for (int d = 0; d < 4; ++d) { a1 = dot2(xr[i][d], 0x3F803F80u, a1); a2 = dot2(xr[i][d], xr[i][d], a2); }
                if (i < 2 || l < 32) { s1 += a1; s2 += a2; }
            }
            asm volatile("s_waitcnt lgkmcnt(0)" ::: "memory");
            if (l == 0) lds_add(L0 + L_CNT + 20, 1u);
            s1 += dppf<0x128>(s1); s2 += dppf<0x128>(s2);
            s1 = xsum4(s1); s2 = xsum4(s2);
            if (l < 8) stsf2(L0 + L_STP + (w * 8 + l) * 8, (f32x2){s1, s2});
        }
        gather_strip(ws, 40 * w, 160 + 40 * w, L0 + L_GS + w * 640, l, tag, ws, 1);
        STAMP(2);
        worker_sync(L0 + L_CNT + 4, l);
        {
            f32x2 st = {0.f, 0.f};
#pragma unroll
            for (int ww = 0; ww < 4; ++ww) st += *(LAS f32x2*)(size_t)(L0 + L_STP + (ww * 8 + tok) * 8);
            const float mu = st[0] * (1.f / 640.f), var = fmaxf(st[1] * (1.f / 640.f) - mu * mu, 0.f);
            const float rs = __builtin_amdgcn_rsqf(var + 1e-5f), nmr = -mu * rs;
#pragma unroll
            for (int i = 0; i < 3; ++i) {
                const int cl = i < 2 ? (l >> 3) + 8 * i : 16 + ((l >> 3) & 3);
                const u32x4 sa = lds128(L0 + L_GS + w * 640 + cl * 16), gg = lds128(L0 + L_GS + w * 640 + 320 + cl * 16);
                u32x4 h;
#pragma unroll
                for (int d = 0; d < 4; ++d)
                    h[d] = pack2((bflo(xr[i][d]) * rs + nmr) * bflo(gg[d]) + bflo(sa[d]), (bfhi(xr[i][d]) * rs + nmr) * bfhi(gg[d]) + bfhi(sa[d]));
                if (i < 2 || l < 32) ST16C(h, WS_H + (((rb * 8 + (j >> 1)) * 80 + 20 * w + cl) * 16 + 8 * (j & 1) + tok) * 16);
            }
        }
        VMCNT(0);
        worker_sync(L0 + L_CNT + 8, l);
        if (tid == 0) st_agent(ws + WS_FH + (rb * 16 + j) * FS, tag);
        if (w == 0) poll_flags(ws + WS_FH + rb * 16 * FS, 16, l, tag, ws, 11);
        __builtin_amdgcn_s_setprio(0);
    }
    BAR();
    asm volatile("s_waitcnt vmcnt(%3)" : "+v"(wu0), "+v"(wu1), "+v"(wu2) : "i"(0) : "memory");
    STAMP(3);

    u32x4 xa[20];
    const u32 lo16 = l * 16;
    const char* hsrc = ws + WS_H + (rb * 8 + w) * 80 * 256;
#define XL(s) xa[s] = gld16c(((s) & 3) * 1024, hsrc + ((s) >> 2) * 4096, lo16)
    XL(0); XL(1); XL(2); XL(3); XL(4); XL(5);
    const char* kvx = ws + WS_KV + (rb * 16 + j) * 22528;
    const char* wtq = ws + WS_WT + j * 153600 + (100 + w) * 1024;
    {
        f32x4 acc[5];
#pragma unroll
        for (int n = 0; n < 5; ++n) acc[n] = Z4;
        const u32 b0 = L0 + L_WKV + lo16;
        u32x4 bf[2][5];
#pragma unroll
        for (int n = 0; n < 5; ++n) bf[0][n] = lds128(b0 + n * 1024);
        static_for<0, 20>([&](auto S) {
            constexpr int s = S;
            vmw1<(s < 14 ? 5 : 6)>(xa[s]);
            if constexpr (s + 6 < 20) XL(s + 6);
            if constexpr (s >= 13) { if (s < 19 || w < 2) glds16c(wtq + (s - 13) * 8192, lo16, L0 + L_WKV + (100 + w) * 1024 + (s - 13) * 8192); }
            SB0();
#pragma unroll
            for (int n = 0; n < 5; ++n) {
                if (s < 19) bf[(s + 1) & 1][n] = lds128(b0 + (s + 1) * 5120 + n * 1024);
                acc[n] = mfma(bf[s & 1][n], xa[s], acc[n]);
            }
#pragma unroll
            for (int n = 0; n < 5; ++n) { SGB(M_DSR, 1); SGB(M_MFMA, 1); }
            SB0();
        });
        STAMP(18);
        BAR();
        const int row = 256 + 16 * w + i16;
#pragma unroll
        for (int n = 0; n < 5; ++n) {
            const int Q = 4 * n + g;
            const u32x2 v = {pack2(acc[n][0], acc[n][1]), pack2(acc[n][2], acc[n][3])};
            const int wi = Q - 10, hd = wi >= 5 ? 1 : 0;
            const u32 a = Q < 10 ? L_K + row * 80 + 8 * Q : L_V + row * 96 + 48 * hd + 8 * (wi - 5 * hd);
            sts64(L0 + a, v);
        }
        if (g < 2) sts64(L0 + L_V + row * 96 + 48 * g + 40, (u32x2){0x3F80u, 0u});
        if (rb < 2) {
            const int nrow = rb == 0 ? 256 : 128;
            for (int i = tid; i < nrow * 5; i += 512) sts128(L0 + L_K + i * 16, (u32x4){0, 0, 0, 0});
            for (int i = tid; i < nrow * 6; i += 512) sts128(L0 + L_V + i * 16, (u32x4){0, 0, 0, 0});
        }
        vmw<0>();
        BAR();
    }
#undef XL
    STAMP(4);
    {
        u32x4 kvd[3];
#pragma unroll
        for (int e = 0; e < 3; ++e) {
            const int n = min(w + 8 * e, 21);
            kvd[e] = lds128(L0 + (n < 10 ? L_K + 20480 + n * 1024 : L_V + 24576 + (n - 10) * 1024) + lo16);
        }
        const u32 kvo = WS_KV + (rb * 16 + j) * 22528 + w * 1024 + lo16;
        const u16* wroot = wpm ? p.wu : p.wg;
        const char* wsrc = (const char*)wroot + (160 * wpq + 8 * w) * 3456 + wpb * 128;
        const u32 wvo = (l >> 3) * 3456 + (l & 7) * 16;
        f32x4 acc[3];
#pragma unroll
        for (int n = 0; n < 3; ++n) acc[n] = Z4;
        const u32 bq = L0 + L_WQ + lo16, bq2 = L0 + L_WQ + 2048 + g * 128 + (i16 & 7) * 16;
#define QRD(s, n) lds128(((n) < 2 ? bq + (n) * 1024 : bq2) + (s) * 2560)
        u32x4 bf[3][3];
#pragma unroll
        for (int n = 0; n < 3; ++n) { bf[0][n] = QRD(0, n); bf[1][n] = QRD(1, n); }
        static_for<0, 20>([&](auto S) {
            constexpr int s = S;
            if constexpr (s == 0) { if (rb < 15) ST16C(kvd[0], kvo); }
            if constexpr (s == 1) { if (rb < 15) ST16C(kvd[1], kvo + 8192); }
            if constexpr (s == 2) { if (rb < 15 && w < 6) ST16C(kvd[2], kvo + 16384); }
            if constexpr (s == 14) {
                vmw<0>();
                u32 old = 0;
                if (l == 0) old = lds_add(L0 + L_CNT + 28, 1u);
                if (__builtin_amdgcn_readfirstlane(old) == 7 && l == 0) st_agent(ws + WS_FKV + (j * 16 + rb) * FS, tag);
            }
            if constexpr (s == 15) { if (wpwg) glds16(wroot, wsrc, wvo, L0 + L_WPRAW + w * 1024); }
            if constexpr (s == 16) { if (wpwg) glds16(wroot, wsrc + 64 * 3456, wvo, L0 + L_WPRAW + (w + 8) * 1024); }
            if constexpr (s == 17) { if (wpwg && w < 4) glds16(wroot, wsrc + 128 * 3456, wvo, L0 + L_WPRAW + (w + 16) * 1024); }
            SB0();
#pragma unroll
            for (int n = 0; n < 3; ++n) {
                if (s < 18) bf[(s + 2) % 3][n] = QRD(s + 2, n);
                acc[n] = mfma(bf[s % 3][n], xa[s], acc[n]);
            }
#pragma unroll
            for (int n = 0; n < 3; ++n) { SGB(M_DSR, 1); SGB(M_MFMA, 1); }
            SB0();
        });
#undef QRD
        STAMP(21);
#pragma unroll
        for (int n = 0; n < 3; ++n) {
            const int Q = 4 * n + g;
            const u32x2 v = {pack2(acc[n][0] * QSCALE, acc[n][1] * QSCALE), pack2(acc[n][2] * QSCALE, acc[n][3] * QSCALE)};
            if (Q < 10) sts64(L0 + L_Q + (16 * w + i16) * 80 + 8 * Q, v);
        }
    }
    if (w == 0 && rb >= 1) poll_flags(ws + WS_FKV + (j * 16 + (rb >= 2 ? rb - 2 : rb - 1)) * FS, rb >= 2 ? 2 : 1, l, tag, ws, 3);
    VMCNT(0);
    BAR();
    STAMP(5);
    if (rb >= 1) {
#pragma unroll
        for (int e = 0; e < 3; ++e) {
            const int n = w + 8 * e;
            if (n < 22) {
                const u32 d = n < 10 ? L_K + n * 1024 : L_V + (n - 10) * 1024, st = n < 10 ? 10240 : 12288;
                glds16c(kvx - 16 * 22528 + n * 1024, lo16, L0 + d + st);
                if (rb >= 2) glds16c(kvx - 32 * 22528 + n * 1024, lo16, L0 + d);
            }
        }
    }
    auto wp_slot = [&](int gam, int& jj, int& slot) {
        jj = gam < 84 ? gam / 7 : 12 + (gam - 84) / 6;
        const int ar = gam - (jj < 12 ? 7 * jj : 84 + 6 * (jj - 12)), a = (jj < 12 || ar < 3) ? ar : ar + 1;
        slot = a < 3 ? 2 * a + wpm : a == 3 ? (wpm ? 13 : 6) : 7 + 2 * (a - 4) + wpm;
    };
    if (wpwg) {
#pragma unroll
        for (int i = 0; i < 3; ++i) {
            if (i < 2 || w < 4) {
                const int f = w + 8 * i, ks = f >> 2, t16 = f & 3;
                int jj, slot; wp_slot(4 * wpb + t16, jj, slot);
                const u32 a = L0 + L_WPRAW + (32 * ks + 8 * g + q4) * 128 + 32 * t16 + 8 * p4;
                const u32x4 wr = ldtr2(a, a + 512);
                const u32x4 g2 = lds128(L0 + L_G2 + (32 * ks + 8 * g) * 2), sb = lds128(L0 + L_SB + (32 * ks + 8 * g) * 2);
                u32x4 o; float us = 0.f, vs = 0.f;
#pragma unroll
                for (int d = 0; d < 4; ++d) {
                    o[d] = pack2(dot2(wr[d], g2[d] & 0xffffu, 0.f), dot2(wr[d], g2[d] & 0xffff0000u, 0.f));
                    us = dot2(o[d], 0x3F803F80u, us);
                    vs = dot2(wr[d], sb[d], vs);
                }
                ST16C(o, WS_WP + ((jj * 20 + 5 * wpq + ks) * 14 + slot) * 1024 + lo16);
                us = xsum4(us); vs = xsum4(vs);
                if (l < 32) stsf(L0 + L_UVL + f * 128 + l * 4, l < 16 ? us : vs);
            }
        }
    }
    if (wpwg) { if (w < 4) vmw<3>(); else vmw<2>(); } else vmw<0>();
    BAR();
    STAMP(6);
    if (wpwg && w == 3) {
#pragma unroll
        for (int e = 0; e < 2; ++e) {
            const int t16 = 2 * e + (l >> 5);
            int jj, slot; wp_slot(4 * wpb + t16, jj, slot);
            float v = 0.f;
#pragma unroll
            for (int ks = 0; ks < 5; ++ks) v += ldsf(L0 + L_UVL + (4 * ks + t16) * 128 + (l & 31) * 4);
            st_agent32(ws + WS_UV + ((jj * 14 + slot) * 4 + wpq) * 128 + (l & 31) * 4, __float_as_uint(v));
        }
    }
    const int hf = j & 1, cb = j >> 1;
    u32 wvoff[5];
#pragma unroll
    for (int i = 0; i < 5; ++i) {
        const u32 sg = 64 * i + l, pos = (sg * 6554) >> 16, c = sg - pos * 10, k = pos ^ (((pos >> 3) & 1) << 2);
        wvoff[i] = k * 1280 + c * 16;
    }
    const char* wosrc = (const char*)p.wo + cb * 160;
#define WO_STEP(sx) do { _Pragma("unroll") for (int i = 0; i < 5; ++i) glds16(p.wo, wosrc + (sx) * 32 * 1280, wvoff[i], L0 + L_WO + (sx) * 5120 + i * 1024); } while (0)
    STAMP(7);

    {
        const int hd = w & 1, qi = w >> 1;
        const int kr0 = 32 * qi;
        u32x4 qf[2];
#pragma unroll
        for (int a = 0; a < 2; ++a) {
            const u32 qa = L0 + L_Q + (kr0 + 16 * a + i16) * 80 + hd * 40;
            const u32x2 q0 = lds64(qa + 8 * g);
            u32x2 q1 = lds64(qa + 32);
            if (g != 0) q1 = (u32x2){0, 0};
            qf[a] = (u32x4){q0[0], q0[1], q1[0], q1[1]};
        }
        const u32 kb0 = L0 + L_K + (kr0 + i16) * 80 + hd * 40 + 8 * g, kb1 = L0 + L_K + (kr0 + i16) * 80 + hd * 40 + 32;
        f32x4 mfirst, mlast;
#pragma unroll
        for (int r = 0; r < 4; ++r) { const bool lo = 4 * g + r <= i16; mfirst[r] = lo ? -INFINITY : 0.f; mlast[r] = lo ? 0.f : -INFINITY; }
        f32x4 s0[17], s1[17];
        {
            u32x4 kf[6];
#define KRD(t) do { const u32x2 k0 = lds64(kb0 + (t) * 1280), k1 = lds64(kb1 + (t) * 1280); kf[(t) % 6] = (u32x4){k0[0], k0[1], k1[0], k1[1]}; } while (0)
            KRD(0); KRD(1); KRD(2); KRD(3); KRD(4);
            static_for<0, 18>([&](auto T) {
                constexpr int t = T;
                SB0();
                if constexpr (t < 13) KRD(t + 5);
                if constexpr (t < 17) s0[t] = mfma(kf[t % 6], qf[0], t == 0 ? mfirst : t == 16 ? mlast : Z4);
                if constexpr (t > 0) s1[t - 1] = mfma(kf[t % 6], qf[1], t == 1 ? mfirst : t == 17 ? mlast : Z4);
                SGB(M_DSR, 2); SGB(M_MFMA, 2);
                SB0();
            });
#undef KRD
        }
        WO_STEP(4 + w);
        if (rb < 2) {
            const int fv = rb == 1 ? 128 : 256;
#pragma unroll
            for (int t = 0; t < 16; ++t) {
                const bool d0 = kr0 + 16 * t < fv, d1 = kr0 + 16 * t + 16 < fv;
#pragma unroll
                for (int r = 0; r < 4; ++r) { s0[t][r] = d0 ? -INFINITY : s0[t][r]; s1[t][r] = d1 ? -INFINITY : s1[t][r]; }
            }
        }
        STAMP(22);
        float m0 = fmaxf(s0[0][0], s0[0][1]), m1 = fmaxf(s1[0][0], s1[0][1]), n0 = fmaxf(s0[0][2], s0[0][3]), n1 = fmaxf(s1[0][2], s1[0][3]);
#pragma unroll
        for (int t = 1; t < 17; ++t) {
            m0 = fmaxf(fmaxf(m0, s0[t][0]), s0[t][1]); m1 = fmaxf(fmaxf(m1, s1[t][0]), s1[t][1]);
            n0 = fmaxf(fmaxf(n0, s0[t][2]), s0[t][3]); n1 = fmaxf(fmaxf(n1, s1[t][2]), s1[t][3]);
        }
        m0 = fmaxf(m0, n0); m1 = fmaxf(m1, n1);
        m0 = xmax4(m0); m1 = xmax4(m1);
        WO_STEP(12 + w);
        STAMP(23);
        f32x4 o00 = Z4, o01 = Z4, o10 = Z4, o11 = Z4;
        const u32 vb = L0 + L_V + (kr0 + 4 * g + q4) * 96 + hd * 48 + 8 * p4;
        auto pv = [&](auto SUB) {
            constexpr bool sub = SUB;
            u32x4 vf[2][2];
            vf[0][0] = ldtr2(vb, vb + 16 * 96); vf[0][1] = ldtr2(vb + 32, vb + 16 * 96 + 32);
#define EX(v, m) __builtin_amdgcn_exp2f(sub ? (v) - (m) : (v))
            static_for<0, 9>([&](auto U) {
                constexpr int u = U;
                if constexpr (u < 8) { const u32 a = vb + (u + 1) * 32 * 96; vf[(u + 1) & 1][0] = ldtr2(a, a + 16 * 96); vf[(u + 1) & 1][1] = ldtr2(a + 32, a + 16 * 96 + 32); }
                u32x4 pa, pb;
                pa[0] = pack2(EX(s0[2 * u][0], m0), EX(s0[2 * u][1], m0)); pa[1] = pack2(EX(s0[2 * u][2], m0), EX(s0[2 * u][3], m0));
                if constexpr (u < 8) { pa[2] = pack2(EX(s0[2 * u + 1][0], m0), EX(s0[2 * u + 1][1], m0)); pa[3] = pack2(EX(s0[2 * u + 1][2], m0), EX(s0[2 * u + 1][3], m0)); }
                else { pa[2] = 0; pa[3] = 0; }
                if constexpr (u > 0) { pb[0] = pack2(EX(s1[2 * u - 1][0], m1), EX(s1[2 * u - 1][1], m1)); pb[1] = pack2(EX(s1[2 * u - 1][2], m1), EX(s1[2 * u - 1][3], m1)); }
                else { pb[0] = 0; pb[1] = 0; }
                pb[2] = pack2(EX(s1[2 * u][0], m1), EX(s1[2 * u][1], m1)); pb[3] = pack2(EX(s1[2 * u][2], m1), EX(s1[2 * u][3], m1));
                o00 = mfma(vf[u & 1][0], pa, o00); o01 = mfma(vf[u & 1][1], pa, o01);
                o10 = mfma(vf[u & 1][0], pb, o10); o11 = mfma(vf[u & 1][1], pb, o11);
                SB0();
            });
#undef EX
        };
        if (__all(fabsf(m0) <= 64.f && fabsf(m1) <= 64.f)) pv(std::false_type{}); else pv(std::true_type{});
        STAMP(24);
        const float i0 = __builtin_amdgcn_rcpf(__shfl(o01[0], i16 + 16)), i1 = __builtin_amdgcn_rcpf(__shfl(o11[0], i16 + 16));
        const u32 oa = L0 + L_Q + (kr0 + i16) * 80 + hd * 40;
        sts64(oa + 8 * g, (u32x2){pack2(o00[0] * i0, o00[1] * i0), pack2(o00[2] * i0, o00[3] * i0)});
        sts64(oa + 1280 + 8 * g, (u32x2){pack2(o10[0] * i1, o10[1] * i1), pack2(o10[2] * i1, o10[3] * i1)});
        if (g == 0) {
            sts64(oa + 32, (u32x2){pack2(o01[0] * i0, o01[1] * i0), pack2(o01[2] * i0, o01[3] * i0)});
            sts64(oa + 1280 + 32, (u32x2){pack2(o11[0] * i1, o11[1] * i1), pack2(o11[2] * i1, o11[3] * i1)});
        }
    }
    BAR();
    {
        const int tok = 64 * (w & 1) + l;
        const u32 oo = WS_O + (((rb * 8 + (tok >> 4)) * 80 + 5 * j) * 16 + (tok & 15)) * 16;
        ST16C(lds128(L0 + L_Q + tok * 80 + (w >> 1) * 16), oo + (w >> 1) * 256);
        if (w < 2) ST16C(lds128(L0 + L_Q + tok * 80 + 64), oo + 4 * 256);
    }
    VMCNT(0);
    BAR();
    if (tid == 0) {
        st_agent(ws + WS_FO + (rb * 16 + j) * FS, tag);
        if (wpwg) st_agent(ws + WS_FWP + b * FS, tag);
    }
    STAMP(8);

    const int wm = w & 3, wk = w >> 2;
    const int mt3 = rb * 8 + 4 * hf + wm;
    const int nt0 = wk == 0 ? 0 : 3, ntn = wk == 0 ? 3 : 2;
    u32x2 x1k[3];
    {
        const int wdu = b + 256 * (w - 6);
        const bool wdw = w >= 6 && wdu < 432;
        if (w >= 1 && w < 5) {
#pragma unroll
            for (int i = 0; i < 5; ++i) glds16(p.wo, wosrc + (w - 1) * 32 * 1280, wvoff[i], L0 + L_WO + (w - 1) * 5120 + i * 1024);
        } else if (w >= 5) {
#pragma unroll
            for (int e = 0; e < 4; ++e) {
                const int pc = w - 5 + 3 * e;
                if (pc < 10) {
                    const u32 sg = 64 * pc + l, r = (sg * 6554) >> 16, c = sg - r * 10;
                    glds16(p.x, (const char*)p.x + (rb * 128 + 64 * hf) * 1280 + cb * 160, r * 1280 + c * 16, L0 + L_XR + pc * 1024);
                }
            }
            if (wdw) {
                const int ucb = wdu / 54, us = wdu - 54 * ucb;
#pragma unroll
                for (int i = 0; i < 5; ++i) glds16(p.wd, (const char*)p.wd + ucb * 160 + us * 32 * 1280, wvoff[i], L0 + L_WDRAW + (w - 6) * 5120 + i * 1024);
            }
        }
        if (w == 1 && l < 20) stsf4(L0 + L_GA + l * 16, gather1(ws, 320 + 20 * cb + l, tag, ws, 4));
        if (w == 0) {
            const int bl = min((Gj >> 2) + ((l >> 2) % 3), (Gj + nj - 1) >> 2);
            poll_at(ws, WS_FWP + (108 * (l >= 12 && l < 24 ? 1 : 0) + 4 * bl + (l & 3)) * FS, l, tag, ws, 6);
            poll_flags(ws + WS_FO + rb * 16 * FS, 16, l, tag, ws, 5);
        }
        if (wdw) vmw<5>(); else vmw<0>();
        BAR();
        STAMP(9);
        u32x4 a[10];
        const char* asrc = ws + WS_O + (mt3 * 80 + 40 * wk) * 256;
#define AL(s) a[s] = gld16c(((s) & 3) * 1024, asrc + ((s) >> 2) * 4096, lo16)
        AL(0); AL(1); AL(2); AL(3);
        f32x4 acc[5];
#pragma unroll
        for (int n = 0; n < 5; ++n) acc[n] = Z4;
        const u32 wb = L0 + L_WO + wk * 51200;
        const u32 b0 = wb + 160 * (8 * g + 4 * (g & 1) + q4) + 8 * p4, b1 = wb + 160 * (8 * g + 4 * ((g & 1) ^ 1) + q4) + 8 * p4;
        u32x4 bf[2][5];
#pragma unroll
        for (int n = 0; n < 5; ++n) bf[0][n] = ldtr2(b0 + n * 32, b1 + n * 32);
        static_for<0, 10>([&](auto S) {
            constexpr int s = S;
            vmw1<(s < 7 ? 3 : 9 - s)>(a[s]);
            if constexpr (s + 4 < 10) AL(s + 4);
            SB0();
#pragma unroll
            for (int n = 0; n < 5; ++n) {
                if (s < 9) bf[(s + 1) & 1][n] = ldtr2(b0 + (s + 1) * 5120 + n * 32, b1 + (s + 1) * 5120 + n * 32);
                acc[n] = mfma(bf[s & 1][n], a[s], acc[n]);
            }
#pragma unroll
            for (int n = 0; n < 5; ++n) { SGB(M_DSR, 2); SGB(M_MFMA, 1); }
            SB0();
        });
#undef AL
        STAMP(25);
        if (wk == 0) { stsf4(L0 + L_RED3 + (wm * 5 + 3) * 1024 + lo16, acc[3]); stsf4(L0 + L_RED3 + (wm * 5 + 4) * 1024 + lo16, acc[4]); }
        else { stsf4(L0 + L_RED3 + (wm * 5 + 0) * 1024 + lo16, acc[0]); stsf4(L0 + L_RED3 + (wm * 5 + 1) * 1024 + lo16, acc[1]); stsf4(L0 + L_RED3 + (wm * 5 + 2) * 1024 + lo16, acc[2]); }
        BAR();
        float s1 = 0.f, s2 = 0.f;
#pragma unroll
        for (int n = 0; n < 3; ++n) {
            if (n < ntn) {
                const int nt = nt0 + n;
                const f32x4 mine = wk == 0 ? acc[n] : acc[n < 2 ? 3 + n : 4];
                const f32x4 v = mine + ldsf4(L0 + L_RED3 + (wm * 5 + nt) * 1024 + lo16);
                const f32x4 ga = ldsf4(L0 + L_GA + (16 * nt + 4 * g) * 4);
                const u32x2 xr = lds64(L0 + L_XR + (16 * wm + i16) * 160 + 32 * nt + 8 * g);
                const u32x2 o = {pack2(bflo(xr[0]) + ga[0] * v[0], bfhi(xr[0]) + ga[1] * v[1]), pack2(bflo(xr[1]) + ga[2] * v[2], bfhi(xr[1]) + ga[3] * v[3])};
                x1k[n] = o;
                s1 = dot2(o[0], 0x3F803F80u, s1); s1 = dot2(o[1], 0x3F803F80u, s1);
                s2 = dot2(o[0], o[0], s2); s2 = dot2(o[1], o[1], s2);
                sts64(L0 + L_X1S + (16 * wm + i16) * 160 + 32 * nt + 8 * g, o);
            } else x1k[n] = (u32x2){0, 0};
        }
        s1 = xsum4(s1); s2 = xsum4(s2);
        if (l < 16) stsf2(L0 + L_ST3 + (wk * 64 + 16 * wm + l) * 8, (f32x2){s1, s2});
        BAR();
        {
            const u32 xo = WS_X1 + (((rb * 8 + 4 * hf + g) * 80 + 10 * cb) * 16 + i16) * 16;
            ST16C(lds128(L0 + L_X1S + l * 160 + w * 16), xo + w * 256);
            if (w < 2) ST16C(lds128(L0 + L_X1S + l * 160 + (w + 8) * 16), xo + (w + 8) * 256);
            if (w == 2) {
                const f32x2 u = *(LAS f32x2*)(size_t)(L0 + L_ST3 + l * 8), v = *(LAS f32x2*)(size_t)(L0 + L_ST3 + (64 + l) * 8);
                const f32x2 sum = u + v;
                ST8C(__builtin_bit_cast(u32x2, sum), WS_STAT + ((rb * 8 + cb) * 128 + 64 * hf + l) * 8);
            }
        }
        if (wdw) {
            const u32 wr = L0 + L_WDRAW + (w - 6) * 5120;
            const u32 r0 = wr + 160 * (8 * g + 4 * (g & 1) + q4) + 8 * p4, r1 = wr + 160 * (8 * g + 4 * ((g & 1) ^ 1) + q4) + 8 * p4;
#pragma unroll
            for (int n = 0; n < 5; ++n) ST16C(ldtr2(r0 + n * 32, r1 + n * 32), WS_WD + (wdu * 5 + n) * 1024 + lo16);
        }
        STAMP(26);
        if (wdw) vmw<5>(); else vmw<0>();
        BAR();
        if (tid == 0) st_agent(ws + WS_FX1 + (rb * 16 + j) * FS, tag);
    }
    STAMP(10);

    const char* wdsrc = ws + WS_WD + (cb * 54 + wk * 27) * 5120;
    const u32 r5w = L0 + wk * 20480 + wm * 5120;
#define P5_B1(t, i) do { if ((i) == 0 || (i) == 4) glds16c(wdsrc + min(4 * (t) + wm, 26) * 5120 + (i) * 1024, lo16, r5w + r5_slot(t) + (i) * 1024); \
        else glds16c_next(((i) & 3) * 1024, wdsrc + min(4 * (t) + wm, 26) * 5120, lo16); } while (0)
    {
        const int wn = w >> 2;
        const int bst = w < 4 ? 4 * w : 16 + 3 * (w - 4);
        const char* wpj = ws + WS_WP + (j * 20 * 14 + bst) * 1024;
        const char* x1b = ws + WS_X1 + (rb * 8 + w) * 80 * 256;
        const u32 r4a = L0 + L_R4 + w * 2048, r4b = L0 + L_R4 + 16384 + bst * 1024;
#define P4_B1(t, i) do { if ((i) == 0) glds16c(wpj + (t) * 28 * 1024, lo16, r4b + ((t) % 3) * R4_STAGE); else glds16c_next((i) * 1024, wpj + (t) * 28 * 1024, lo16); } while (0)
#define P4_A1(t, i) do { if ((i) == 0) glds16c(x1b + 8 * (t) * 256, lo16, r4a + ((t) % 3) * R4_STAGE); else glds16c_next(1024, x1b + 8 * (t) * 256, lo16); } while (0)
#define P4_B(t) do { P4_B1(t, 0); P4_B1(t, 1); P4_B1(t, 2); if (w < 4) P4_B1(t, 3); } while (0)
        P4_B(0); P4_B(1); P4_B(2);
        if (w == 0) poll_flags(ws + WS_FX1 + rb * 16 * FS, 16, l, tag, ws, 7);
        BAR();
        STAMP(11);
        glds16c(ws + WS_STAT + rb * 8192 + w * 1024, lo16, L0 + L_ST4 + w * 1024);
        if (w < 7) glds16c(ws + WS_UV + j * 7168 + w * 1024, lo16, L0 + L_UV + w * 1024);
        P4_A1(0, 0); P4_A1(0, 1); P4_A1(1, 0); P4_A1(1, 1); P4_A1(2, 0); P4_A1(2, 1);
        f32x4 acc[7][2];
#pragma unroll
        for (int n = 0; n < 7; ++n) { acc[n][0] = Z4; acc[n][1] = Z4; }
        const u32 ra = opaque(L0 + L_R4 + 2 * wm * 2048 + lo16), rbb = opaque(L0 + L_R4 + 16384 + 7 * wn * 1024 + lo16);
        u32x4 fa[2][2], fb[2][7];
#define P4_RA(buf, t, ks) do { const u32 so = ((t) % 3) * R4_STAGE + (ks) * 1024; fa[buf][0] = lds128(ra + so); fa[buf][1] = lds128(ra + so + 2048); } while (0)
#define P4_RB(buf, t, ks, n) fb[buf][n] = lds128(rbb + ((t) % 3) * R4_STAGE + (ks) * 14336 + (n) * 1024)
#define P4_MM(buf, n) do { acc[n][0] = mfma(fb[buf][n], fa[buf][0], acc[n][0]); acc[n][1] = mfma(fb[buf][n], fa[buf][1], acc[n][1]); } while (0)
#define P4_HALF(buf, rd, t, ks, V0, V1, V2) do { \
            SB0(); if (rd) { P4_RA((buf) ^ 1, t, ks); P4_RB((buf) ^ 1, t, ks, 0); } P4_MM(buf, 0); SB0(); V0; \
            SB0(); if (rd) P4_RB((buf) ^ 1, t, ks, 1); P4_MM(buf, 1); SB0(); \
            SB0(); if (rd) P4_RB((buf) ^ 1, t, ks, 2); P4_MM(buf, 2); SB0(); V1; \
            SB0(); if (rd) P4_RB((buf) ^ 1, t, ks, 3); P4_MM(buf, 3); SB0(); \
            SB0(); if (rd) P4_RB((buf) ^ 1, t, ks, 4); P4_MM(buf, 4); SB0(); V2; \
            SB0(); if (rd) P4_RB((buf) ^ 1, t, ks, 5); P4_MM(buf, 5); SB0(); \
            SB0(); if (rd) P4_RB((buf) ^ 1, t, ks, 6); P4_MM(buf, 6); SB0(); } while (0)
        VMCNT(4);
        BAR();
        if (tid == 0) st_agent(ws + WS_FW2 + b * FS, tag);
        STAMP(27);
        u32x2 wdf = {0u, 0u};
        const u32 wdo = WS_FW2 + ((54 * cb + (l < 54 ? l : 0)) & 255) * FS;
        P4_RA(0, 0, 0);
#pragma unroll
        for (int n = 0; n < 7; ++n) P4_RB(0, 0, 0, n);
        static_for<0, 10>([&](auto T) {
            constexpr int t = T;
            constexpr bool f = t >= 1 && t + 2 <= 9, sN = t + 3 <= 9;
            if constexpr (t < 9) P4_HALF(0, true, t, 1, if (f && w < 4) P4_B1(t + 2, 3), if (f) P4_A1(t + 2, 0), if (f) P4_A1(t + 2, 1));
            else P4_HALF(0, true, t, 1, P5_B1(0, 3), P5_B1(0, 4), P5_B1(1, 0));
            if constexpr (t == 5) {
                if (tid < 128) {
                    f32x2 st = {0.f, 0.f};
#pragma unroll
                    for (int c = 0; c < 8; ++c) st += *(LAS f32x2*)(size_t)(L0 + L_ST4 + (c * 128 + tid) * 8);
                    const float mu = st[0] * (1.f / 640.f), var = fmaxf(st[1] * (1.f / 640.f) - mu * mu, 0.f), r = __builtin_amdgcn_rsqf(var + 1e-5f);
                    stsf2(L0 + L_MR + tid * 8, (f32x2){r, -mu * r});
                } else {
                    const int e = tid - 128;
                    stsf(L0 + L_UVS + e * 4, (ldsf(L0 + L_UV + (e >> 5) * 512 + (e & 31) * 4) + ldsf(L0 + L_UV + (e >> 5) * 512 + 128 + (e & 31) * 4))
                                           + (ldsf(L0 + L_UV + (e >> 5) * 512 + 256 + (e & 31) * 4) + ldsf(L0 + L_UV + (e >> 5) * 512 + 384 + (e & 31) * 4)));
                    if (e < 64) { const int e2 = e + 384;
                        stsf(L0 + L_UVS + e2 * 4, (ldsf(L0 + L_UV + (e2 >> 5) * 512 + (e2 & 31) * 4) + ldsf(L0 + L_UV + (e2 >> 5) * 512 + 128 + (e2 & 31) * 4))
                                                + (ldsf(L0 + L_UV + (e2 >> 5) * 512 + 256 + (e2 & 31) * 4) + ldsf(L0 + L_UV + (e2 >> 5) * 512 + 384 + (e2 & 31) * 4))); }
                }
            }
            if constexpr (t < 9) {
                if constexpr (t == 0) vmw<2>();
                else if constexpr (t == 8) {
                    asm volatile("s_waitcnt vmcnt(0)" : "+v"(wdf) :: "memory");
                    if (w == 7) { if (!__all(wdf[0] == (u32)tag && wdf[1] == (u32)(tag >> 32))) poll_at(ws, wdo, l, tag, ws, 12); }
                }
                else { if (w < 4) vmw<6>(); else vmw<5>(); }
                BAR();
                if constexpr (t == 7) { if (w == 7) asm volatile("s_nop 4\n\tglobal_load_dwordx2 %0, %1, %2 sc1" : "=v"(wdf) : "v"(wdo), "s"(ws) : "memory"); }
                if constexpr (t < 8) P4_HALF(1, true, t + 1, 0, if (sN) P4_B1(t + 3, 0), if (sN) P4_B1(t + 3, 1), if (sN) P4_B1(t + 3, 2));
                else P4_HALF(1, true, t + 1, 0, P5_B1(0, 0), P5_B1(0, 1), P5_B1(0, 2));
            } else {
                P4_HALF(1, false, 0, 0, P5_B1(1, 1), P5_B1(1, 2), P5_B1(1, 3));
                P5_B1(1, 4);
            }
        });
#undef P4_B
#undef P4_B1
#undef P4_A1
#undef P4_RA
#undef P4_RB
#undef P4_MM
#undef P4_HALF
        STAMP(12);
        f32x2 mr[2];
#pragma unroll
        for (int m = 0; m < 2; ++m) mr[m] = *(LAS f32x2*)(size_t)(L0 + L_MR + (32 * wm + 16 * m + i16) * 8);
#pragma unroll
        for (int n = 0; n < 7; ++n) {
            const f32x4 u = ldsf4(L0 + L_UVS + (7 * wn + n) * 128 + 16 * g), v = ldsf4(L0 + L_UVS + (7 * wn + n) * 128 + 64 + 16 * g);
#pragma unroll
            for (int m = 0; m < 2; ++m) acc[n][m] = acc[n][m] * mr[m][0] + (u * mr[m][1] + v);
        }
        if (nj == 7) stsf4(L0 + L_XCH + w * 1024 + lo16, wn == 0 ? acc[6][1] : acc[6][0]);
        auto silu_mul = [&](const f32x4 gt, const f32x4 up) -> u32x2 {
            const f32x4 e = gt * (-LOG2E), gu = gt * up;
            f32x4 d;
#pragma unroll
            for (int r = 0; r < 4; ++r) d[r] = __builtin_amdgcn_exp2f(e[r]);
            d = d + 1.f;
#pragma unroll
            for (int r = 0; r < 4; ++r) d[r] = __builtin_amdgcn_rcpf(d[r]);
            const f32x4 o = gu * d;
            return (u32x2){pack2(o[0], o[1]), pack2(o[2], o[3])};
        };
#pragma unroll
        for (int a = 0; a < 3; ++a) {
            const int la = a + 4 * wn, gam = Gj + (la <= 3 || nj == 7 ? la : la - 1);
            const u32x2 o0 = silu_mul(acc[2 * a][0], acc[2 * a + 1][0]), o1 = silu_mul(acc[2 * a][1], acc[2 * a + 1][1]);
            const auto s0 = __builtin_amdgcn_permlane16_swap(o0[0], o1[0], false, false);
            const auto s1 = __builtin_amdgcn_permlane16_swap(o0[1], o1[1], false, false);
            const u32x4 v = {s0[0], s1[0], s0[1], s1[1]};
            ST16C(v, WS_ACT + (((rb * 8 + 2 * wm + (g & 1)) * 216 + 2 * gam + (g >> 1)) * 16 + i16) * 16);
        }
        STAMP(28);
        BAR();
        if (nj == 7) {
            const f32x4 other = ldsf4(L0 + L_XCH + (w ^ 4) * 1024 + lo16);
            const u32x2 o = wn == 0 ? silu_mul(acc[6][0], other) : silu_mul(other, acc[6][1]);
            ST8C(o, WS_ACT + (((rb * 8 + 2 * wm + wn) * 216 + 2 * (Gj + 3) + (g >> 1)) * 16 + i16) * 16 + 8 * (g & 1));
        }
        P5_B1(2, 0); P5_B1(2, 1); P5_B1(2, 2); P5_B1(2, 3); P5_B1(2, 4);
        vmw<5>();
        BAR();
        if (tid == 0) st_agent(ws + WS_FACT + (rb * 16 + j) * FS, tag);
    }
    STAMP(13);

    {
        if (w == 1 && l < 20) stsf4(L0 + L_GB + l * 16, gather1(ws, 800 + 20 * cb + l, tag, ws, 8));
        if (w == 0) poll_flags(ws + WS_FACT + rb * 16 * FS, 16, l, tag, ws, 9);
        VMCNT(0);
        BAR();
        STAMP(14);
        const char* asrc = ws + WS_ACT + (mt3 * 216 + wk * 108) * 256;
        u32x4 a[3][4];
#define P5_A1(t, i) a[(t) % 3][i] = gld16c((i) * 1024, asrc + (t) * 4096, lo16)
        P5_A1(0, 0); P5_A1(0, 1); P5_A1(0, 2); P5_A1(0, 3); P5_A1(1, 0); P5_A1(1, 1); P5_A1(1, 2); P5_A1(1, 3); P5_A1(2, 0); P5_A1(2, 1); P5_A1(2, 2); P5_A1(2, 3);
        f32x4 acc[5];
#pragma unroll
        for (int n = 0; n < 5; ++n) acc[n] = Z4;
        const u32 rb5 = opaque(L0 + wk * 20480 + lo16);
        u32x4 fb[2][5];
#define P5_RD(buf, t, kk) do { _Pragma("unroll") for (int n = 0; n < 5; ++n) fb[buf][n] = lds128(rb5 + r5_slot(t) + (kk) * 5120 + n * 1024); } while (0)
#define P5_MM(buf, av) do { _Pragma("unroll") for (int n = 0; n < 5; ++n) acc[n] = mfma(fb[buf][n], (av), acc[n]); } while (0)
#define P5_MIX() do { _Pragma("unroll") for (int n = 0; n < 5; ++n) { SGB(M_DSR, 1); SGB(M_MFMA, 1); } } while (0)
        P5_RD(0, 0, 0);
        static_for<0, 7>([&](auto T) {
            constexpr int t = T, q = t % 3;
            constexpr bool nx = t + 3 < 7;
            constexpr int NA = t == 0 ? 8 : t == 1 ? 13 : t == 2 ? 18 : t == 3 ? 21 : t == 4 ? 20 : t == 5 ? 11 : 5;
            constexpr int NB = t == 2 ? 12 : t == 3 ? 12 : t == 4 ? 8 : t == 5 ? 0 : 63;
            vmw4<NA>(a[q][0], a[q][1], a[q][2], a[q][3]);
            SB0(); P5_RD(1, t, 1); P5_MM(0, a[q][0]); P5_MIX(); SB0();
            if constexpr (nx) P5_A1(t + 3, 0);
            SB0(); P5_RD(0, t, 2); P5_MM(1, a[q][1]); P5_MIX(); SB0();
            if constexpr (nx) P5_A1(t + 3, 1);
            if constexpr (t < 6) {
                SB0(); P5_RD(1, t, 3); P5_MM(0, a[q][2]); P5_MIX(); SB0();
                if constexpr (nx) P5_A1(t + 3, 2);
                vmw<NB>(); BAR();
                if constexpr (nx) { P5_B1(t + 3, 0); P5_B1(t + 3, 1); }
                SB0(); P5_RD(0, t + 1, 0); P5_MM(1, a[q][3]); P5_MIX(); SB0();
                if constexpr (nx) { if constexpr (t + 3 < 6) P5_A1(t + 3, 3); P5_B1(t + 3, 2); P5_B1(t + 3, 3); P5_B1(t + 3, 4); }
            } else {
                SB0(); P5_MM(0, a[q][2]); SB0();
            }
        });
#undef P5_B1
#undef P5_A1
#undef P5_RD
#undef P5_MM
#undef P5_MIX
        STAMP(15);
        if (wk == 0) { stsf4(L0 + L_RED5 + (wm * 5 + 3) * 1024 + lo16, acc[3]); stsf4(L0 + L_RED5 + (wm * 5 + 4) * 1024 + lo16, acc[4]); }
        else { stsf4(L0 + L_RED5 + (wm * 5 + 0) * 1024 + lo16, acc[0]); stsf4(L0 + L_RED5 + (wm * 5 + 1) * 1024 + lo16, acc[1]); stsf4(L0 + L_RED5 + (wm * 5 + 2) * 1024 + lo16, acc[2]); }
        BAR();
#pragma unroll
        for (int n = 0; n < 3; ++n) {
            if (n < ntn) {
                const int nt = nt0 + n;
                const f32x4 mine = wk == 0 ? acc[n] : acc[n < 2 ? 3 + n : 4];
                const f32x4 v = mine + ldsf4(L0 + L_RED5 + (wm * 5 + nt) * 1024 + lo16);
                const f32x4 gb = ldsf4(L0 + L_GB + (16 * nt + 4 * g) * 4);
                const u32x2 o = {pack2(bflo(x1k[n][0]) + gb[0] * v[0], bfhi(x1k[n][0]) + gb[1] * v[1]), pack2(bflo(x1k[n][1]) + gb[2] * v[2], bfhi(x1k[n][1]) + gb[3] * v[3])};
                sts64(L0 + L_OS + (16 * wm + i16) * 160 + 32 * nt + 8 * g, o);
            }
        }
        BAR();
#pragma unroll
        for (int i = 0; i < 2; ++i) {
            const int id = tid + 512 * i;
            if (id < 640) {
                const int r = (id * 6554) >> 16, c = id - 10 * r;
                *(GAS u32x4*)((char*)p.out + (rb * 128 + 64 * hf + r) * 1280 + cb * 160 + c * 16) = lds128(L0 + L_OS + id * 16);
            }
        }
    }
    STAMP(16); STAMP_RT(63); STAMP_OUT();
    if (rb == 15 && j == 0 && w < 4) {
        u32 spins = 0;
        for (;;) {
            const u64 v = ld_agent(ws + WS_FO + tid * FS);
            if (__all(v == tag)) break;
            if (++spins > SPIN_CAP) { st_agent32(ws + WS_ERR, 10); break; }
            __builtin_amdgcn_s_sleep(1);
        }
    }
    if (rb == 15 && j == 0) {
        BAR();
        if (tid == 0) st_agent(ws + WS_EPOCH, tag);
    }
}

extern "C" void kernel_launch(void* const* d_in, const int* in_sizes, int n_in, void* d_out, int out_size, void* d_ws, size_t ws_size, hipStream_t stream) {
    Params p;
    p.x = (const u16*)d_in[0]; p.wqkv = (const u16*)d_in[1]; p.wo = (const u16*)d_in[2];
    p.wg = (const u16*)d_in[4]; p.wu = (const u16*)d_in[5]; p.wd = (const u16*)d_in[6];
    p.c = (const u16*)d_in[7]; p.wmod = (const u16*)d_in[8];
    p.out = (u16*)d_out; p.ws = (char*)d_ws;
    mega_kernel<<<256, 512, 0, stream>>>(p);
}
```

```cpp
#define NITER 1
#include <hip/hip_runtime.h>
#include <hip/hip_bf16.h>
#include <stdint.h>
#include <type_traits>

typedef unsigned short u16;
typedef unsigned u32;
typedef unsigned long long u64;
typedef __attribute__((ext_vector_type(2))) unsigned u32x2;
typedef __attribute__((ext_vector_type(4))) unsigned u32x4;
typedef __attribute__((ext_vector_type(2))) float f32x2;
typedef __attribute__((ext_vector_type(4))) float f32x4;
typedef __attribute__((ext_vector_type(2))) __bf16 bf16x2;
typedef __attribute__((ext_vector_type(8))) __bf16 bf16x8;
typedef __attribute__((ext_vector_type(4))) short s16x4;
#define LAS __attribute__((address_space(3)))
#define GAS __attribute__((address_space(1)))
#define DI __device__ __forceinline__

constexpr int SEQ = 2048, DM = 640, FF = 1728;
constexpr float QSCALE = 0.22360679774997896f * 1.4426950408889634f;
constexpr float LOG2E = 1.4426950408889634f;

constexpr u32 WS_EPOCH = 0;
constexpr u32 WS_ERR   = 256;
constexpr u32 FS = 128;
constexpr u32 WS_FKV   = 4096;
constexpr u32 WS_FO    = WS_FKV + 256 * FS;
constexpr u32 WS_FX1   = WS_FO + 256 * FS;
constexpr u32 WS_FACT  = WS_FX1 + 256 * FS;
constexpr u32 WS_FH    = WS_FACT + 256 * FS;
constexpr u32 WS_FWT   = WS_FH + 256 * FS;
constexpr u32 WS_FWP   = WS_FWT + 256 * FS;
constexpr u32 WS_FW2   = WS_FWP + 256 * FS;
constexpr u32 WS_MODG  = WS_FW2 + 256 * FS;
constexpr u32 WS_UV    = WS_MODG + 61440;
constexpr u32 WS_STAT  = WS_UV + 114688;
constexpr u32 WS_KV    = 589824;
constexpr u32 WS_O     = WS_KV + 256 * 22528;
constexpr u32 WS_X1    = WS_O + SEQ * DM * 2;
constexpr u32 WS_ACT   = WS_X1 + SEQ * DM * 2;
constexpr u32 WS_WP    = WS_ACT + SEQ * FF * 2;
constexpr u32 WS_H     = WS_WP + 16 * 20 * 14 * 1024;
constexpr u32 WS_WD    = WS_H + SEQ * DM * 2;
constexpr u32 WS_WO    = WS_WD + 8 * 54 * 5 * 1024;
constexpr u32 WS_WT    = WS_WO + 8 * 20 * 5 * 1024;
constexpr u32 WS_DONE = WS_WT + 16 * 153600;
constexpr u32 WS_STAMP = WS_DONE + 256 * FS;
constexpr u32 WS_END   = WS_STAMP + 256 * 8 * 64 * 4;

constexpr u32 L_WKV = 0, L_WQ = 102400, L_GS = 153600, L_STP = 156160, L_MODP = 156416, L_CNT = 157440, L_SB = 157696, L_G2 = 158016;
constexpr u32 L_ODRAW = 101376, L_WTRAW = 116736, L_XS = 133120;
constexpr u32 L_K = 0, L_V = 30720, L_Q = 67584, L_WPRAW = 77824, L_UVL = 98304;
constexpr u32 L_WO = 61440, L_RED3 = 0, L_X1S = 20480, L_XR = 30720, L_GA = 40960, L_ST3 = 41472, L_WDRAW = 43008;
constexpr u32 L_R4 = 0, R4_STAGE = 45056, L_ST4 = 135168, L_UV = 143360, L_MR = 150528, L_XCH = 151552, L_UVS = 159744;
constexpr u32 L_RED5 = 126976, L_GB = 147456, L_OS = 148480;
constexpr u32 r5_slot(int t) { return t % 3 == 0 ? 45056 : t % 3 == 1 ? 86016 : 0; }

struct Params {
    const u16* x; const u16* wqkv; const u16* wo; const u16* wg; const u16* wu; const u16* wd;
    const u16* c; const u16* wmod; u16* out; char* ws;
};

DI float bflo(u32 d) { return __uint_as_float(d << 16); }
DI float bfhi(u32 d) { return __uint_as_float(d & 0xffff0000u); }
DI u32 pack2(float a, float b) { f32x2 v = {a, b}; return __builtin_bit_cast(u32, __builtin_convertvector(v, bf16x2)); }
DI float rbf(float a) { return bflo(pack2(a, a)); }
DI f32x4 mfma(u32x4 a, u32x4 b, f32x4 c) {
    return __builtin_amdgcn_mfma_f32_16x16x32_bf16(__builtin_bit_cast(bf16x8, a), __builtin_bit_cast(bf16x8, b), c, 0, 0, 0);
}
DI u32x2 ldtr(u32 a) { return __builtin_bit_cast(u32x2, __builtin_amdgcn_ds_read_tr16_b64_v4i16((LAS s16x4*)(size_t)a)); }
DI u32x4 ldtr2(u32 a0, u32 a1) { u32x2 x = ldtr(a0), y = ldtr(a1); return (u32x4){x[0], x[1], y[0], y[1]}; }
DI u32x4 lds128(u32 a) { return *(LAS u32x4*)(size_t)a; }
DI u32x2 lds64(u32 a) { return *(LAS u32x2*)(size_t)a; }
DI f32x4 ldsf4(u32 a) { return *(LAS f32x4*)(size_t)a; }
DI float ldsf(u32 a) { return *(LAS float*)(size_t)a; }
DI void sts128(u32 a, u32x4 v) { *(LAS u32x4*)(size_t)a = v; }
DI void sts64(u32 a, u32x2 v) { *(LAS u32x2*)(size_t)a = v; }
DI void stsf(u32 a, float v) { *(LAS float*)(size_t)a = v; }
DI void stsf4(u32 a, f32x4 v) { *(LAS f32x4*)(size_t)a = v; }
DI void stsf2(u32 a, f32x2 v) { *(LAS f32x2*)(size_t)a = v; }
DI void sts16(u32 a, u16 v) { *(LAS u16*)(size_t)a = v; }

DI void glds16_(const void* root, u32 voff, u32 ldsdst) {
    asm volatile("s_mov_b32 m0, %2\n\ts_nop 0\n\tglobal_load_lds_dwordx4 %0, %1" :: "v"(voff), "s"(root), "s"(__builtin_amdgcn_readfirstlane(ldsdst)) : "memory");
}
DI void glds16c_(const void* root, u32 voff, u32 ldsdst) {
    asm volatile("s_mov_b32 m0, %2\n\ts_nop 0\n\tglobal_load_lds_dwordx4 %0, %1 sc1" :: "v"(voff), "s"(root), "s"(__builtin_amdgcn_readfirstlane(ldsdst)) : "memory");
}
template <int OFF> DI void glds16c_next_(const void* root, u32 voff) {
    asm volatile("global_load_lds_dwordx4 %0, %1 offset:%2 sc1" :: "v"(voff), "s"(root), "i"(OFF) : "memory");
}
template <int OFF> DI u32x4 gld16(const void* root, u32 voff) {
    u32x4 r; asm volatile("global_load_dwordx4 %0, %1, %2 offset:%3" : "=v"(r) : "v"(voff), "s"(root), "i"(OFF) : "memory"); return r;
}
template <int OFF> DI u32x4 gld16c_(const void* root, u32 voff) {
    u32x4 r; asm volatile("global_load_dwordx4 %0, %1, %2 offset:%3 sc1" : "=v"(r) : "v"(voff), "s"(root), "i"(OFF) : "memory"); return r;
}
#define ROFF(root, ptr) ((u32)((const char*)(ptr) - (const char*)(root)))
#define glds16(root, ptr, voff, dst) glds16_((root), ROFF(root, ptr) + (voff), (dst))
#define glds16c(ptr, voff, dst) glds16c_(ws, ROFF(ws, ptr) + (voff), (dst))
#define glds16c_next(OFF, ptr, voff) glds16c_next_<(OFF)>(ws, ROFF(ws, ptr) + (voff))
#define gld16c(OFF, ptr, voff) gld16c_<(OFF)>(ws, ROFF(ws, ptr) + (voff))
DI u32 gldu16(const void* sbase, u32 voff) {
    u32 r; asm volatile("global_load_ushort %0, %1, %2" : "=v"(r) : "v"(voff), "s"(sbase) : "memory"); return r;
}
#define VMCNT(n) asm volatile("s_waitcnt vmcnt(" #n ")" ::: "memory")
#define BAR() asm volatile("s_waitcnt lgkmcnt(0)\n\ts_barrier" ::: "memory")
#define RLX_AGENT __ATOMIC_RELAXED, __HIP_MEMORY_SCOPE_AGENT
DI u64 ld_agent(const char* p) { return __hip_atomic_load((GAS u64*)p, RLX_AGENT); }
DI void st_agent(char* p, u64 v) { __hip_atomic_store((GAS u64*)p, v, RLX_AGENT); }
DI void st_agent32(char* p, u32 v) { __hip_atomic_store((GAS u32*)p, v, RLX_AGENT); }
DI float ld_agentf(const char* p) { return __uint_as_float(__hip_atomic_load((GAS u32*)p, RLX_AGENT)); }
DI float xsum4(float x) {
    const auto s = __builtin_amdgcn_permlane16_swap(__float_as_uint(x), __float_as_uint(x), false, false);
    x = __uint_as_float(s[0]) + __uint_as_float(s[1]);
    const auto t = __builtin_amdgcn_permlane32_swap(__float_as_uint(x), __float_as_uint(x), false, false);
    return __uint_as_float(t[0]) + __uint_as_float(t[1]);
}
DI float xmax4(float x) {
    const auto s = __builtin_amdgcn_permlane16_swap(__float_as_uint(x), __float_as_uint(x), false, false);
    x = fmaxf(__uint_as_float(s[0]), __uint_as_float(s[1]));
    const auto t = __builtin_amdgcn_permlane32_swap(__float_as_uint(x), __float_as_uint(x), false, false);
    return fmaxf(__uint_as_float(t[0]), __uint_as_float(t[1]));
}
template <int CTRL> DI float dppf(float v) { return __builtin_bit_cast(float, __builtin_amdgcn_update_dpp(0, __builtin_bit_cast(int, v), CTRL, 0xf, 0xf, true)); }

constexpr u32 SPIN_CAP = 1u << 17;
DI void poll_flags(const char* base, int n, int lane, u64 tag, char* ws, u32 code) {
    const char* a = base + (lane < n ? lane : 0) * FS;
    u32 spins = 0;
    for (;;) {
        const u64 v = ld_agent(a);
        if (__all(v == tag)) break;
        if (++spins > SPIN_CAP) { if (lane == 0) st_agent32(ws + WS_ERR, code); break; }
        __builtin_amdgcn_s_sleep(1);
    }
}
typedef __attribute__((ext_vector_type(2))) _Float16 f16x2;
DI float hlo(u32 d) { return (float)__builtin_bit_cast(f16x2, d)[0]; }
DI float hhi(u32 d) { return (float)__builtin_bit_cast(f16x2, d)[1]; }
DI u32 packh(float a, float b) { const f16x2 v = {(_Float16)a, (_Float16)b}; return __builtin_bit_cast(u32, v); }
DI bool gran_ok(const u32x4& v, u64 tag) { return v[2] == (u32)tag && v[3] == (u32)(tag >> 32); }
DI f32x4 gran_sum(const u32x4& a, const u32x4& b, const u32x4& c, const u32x4& d) {
    const f32x4 s = {(hlo(a[0]) + hlo(b[0])) + (hlo(c[0]) + hlo(d[0])), (hhi(a[0]) + hhi(b[0])) + (hhi(c[0]) + hhi(d[0])),
                     (hlo(a[1]) + hlo(b[1])) + (hlo(c[1]) + hlo(d[1])), (hhi(a[1]) + hhi(b[1])) + (hhi(c[1]) + hhi(d[1]))};
    return (f32x4){rbf(s[0]), rbf(s[1]), rbf(s[2]), rbf(s[3])};
}
DI f32x4 gather1(const char* ws, u32 g0, u64 tag, char* wsw, u32 code) {
    u32 spins = 0; u32x4 v0, v1, v2, v3;
    const u32 o = WS_MODG + g0 * 16;
    for (;;) {
        asm volatile("s_nop 4\n\tglobal_load_dwordx4 %0, %4, %5 sc1\n\tglobal_load_dwordx4 %1, %6, %5 sc1\n\tglobal_load_dwordx4 %2, %7, %5 sc1\n\tglobal_load_dwordx4 %3, %8, %5 sc1\n\ts_waitcnt vmcnt(0)"
                     : "=&v"(v0), "=&v"(v1), "=&v"(v2), "=&v"(v3) : "v"(o), "s"(ws), "v"(o + 15360), "v"(o + 30720), "v"(o + 46080) : "memory");
        if (__all(gran_ok(v0, tag) && gran_ok(v1, tag) && gran_ok(v2, tag) && gran_ok(v3, tag))) break;
        if (++spins > SPIN_CAP) { st_agent32(wsw + WS_ERR, code); break; }
        __builtin_amdgcn_s_sleep(1);
    }
    return gran_sum(v0, v1, v2, v3);
}
DI void gather2(const char* ws, u32 g0, u32 g1, u64 tag, char* wsw, u32 code, f32x4& ra, f32x4& rb) {
    u32 spins = 0; u32x4 v0, v1, v2, v3, u0, u1, u2, u3;
    const u32 o = WS_MODG + g0 * 16, q = WS_MODG + g1 * 16;
    for (;;) {
        asm volatile("s_nop 4\n\tglobal_load_dwordx4 %0, %8, %9 sc1\n\tglobal_load_dwordx4 %1, %10, %9 sc1\n\tglobal_load_dwordx4 %2, %11, %9 sc1\n\tglobal_load_dwordx4 %3, %12, %9 sc1\n\t"
                     "global_load_dwordx4 %4, %13, %9 sc1\n\tglobal_load_dwordx4 %5, %14, %9 sc1\n\tglobal_load_dwordx4 %6, %15, %9 sc1\n\tglobal_load_dwordx4 %7, %16, %9 sc1\n\ts_waitcnt vmcnt(0)"
                     : "=&v"(v0), "=&v"(v1), "=&v"(v2), "=&v"(v3), "=&v"(u0), "=&v"(u1), "=&v"(u2), "=&v"(u3)
                     : "v"(o), "s"(ws), "v"(o + 15360), "v"(o + 30720), "v"(o + 46080), "v"(q), "v"(q + 15360), "v"(q + 30720), "v"(q + 46080) : "memory");
        if (__all(gran_ok(v0, tag) && gran_ok(v1, tag) && gran_ok(v2, tag) && gran_ok(v3, tag) && gran_ok(u0, tag) && gran_ok(u1, tag) && gran_ok(u2, tag) && gran_ok(u3, tag))) break;
        if (++spins > SPIN_CAP) { st_agent32(wsw + WS_ERR, code); break; }
        __builtin_amdgcn_s_sleep(1);
    }
    ra = gran_sum(v0, v1, v2, v3); rb = gran_sum(u0, u1, u2, u3);
}
DI void gather_strip(const char* ws, u32 gs, u32 gc, u32 strip, int l, u64 tag, char* wsw, u32 code) {
    f32x4 a, c;
    gather2(ws, l < 40 ? gs + l : gc + (l - 40), gc + 24 + (l & 15), tag, wsw, code, a, c);
    if (l >= 40) a = a + 1.f;
    c = c + 1.f;
    *(LAS u32x2*)(size_t)(strip + (l < 40 ? l * 8 : 320 + (l - 40) * 8)) = (u32x2){pack2(a[0], a[1]), pack2(a[2], a[3])};
    if (l < 16) *(LAS u32x2*)(size_t)(strip + 320 + (24 + l) * 8) = (u32x2){pack2(c[0], c[1]), pack2(c[2], c[3])};
}
DI void poll_at(const char* ws, u32 off, int lane, u64 tag, char* wsw, u32 code) {
    u32 spins = 0;
    for (;;) {
        const u64 v = ld_agent(ws + off);
        if (__all(v == tag)) break;
        if (++spins > SPIN_CAP) { if (lane == 0) st_agent32(wsw + WS_ERR, code); break; }
        __builtin_amdgcn_s_sleep(1);
    }
}

#define STAMP(i) do {} while (0)
#define STAMPL(i) do {} while (0)
#define STAMP_RT(i) do {} while (0)
#define STAMP_OUT() do {} while (0)

template <int N> using IC = std::integral_constant<int, N>;
template <int I, int E, class F> DI void static_for(F&& f) { if constexpr (I < E) { f(IC<I>{}); static_for<I + 1, E>(f); } }
template <int N> DI void vmw() { asm volatile("s_waitcnt vmcnt(%0)" :: "i"(N) : "memory"); }
template <int N> DI void vmw1(u32x4& a) { asm volatile("s_waitcnt vmcnt(%1)" : "+v"(a) : "i"(N) : "memory"); }
template <int N> DI void vmw4(u32x4& a, u32x4& b, u32x4& c, u32x4& d) { asm volatile("s_waitcnt vmcnt(%4)" : "+v"(a), "+v"(b), "+v"(c), "+v"(d) : "i"(N) : "memory"); }
DI u32 opaque(u32 a) { asm("" : "+v"(a)); return a; }
DI float dot2(u32 a, u32 b, float c) { return __builtin_amdgcn_fdot2_f32_bf16(__builtin_bit_cast(bf16x2, a), __builtin_bit_cast(bf16x2, b), c, false); }
#define SB0() __builtin_amdgcn_sched_barrier(0)
#define SGB(m, n) __builtin_amdgcn_sched_group_barrier((m), (n), 0)
constexpr int M_MFMA = 0x8, M_DSR = 0x100;
#define Z4 ((f32x4){0.f, 0.f, 0.f, 0.f})
DI u32 lds_add(u32 a, u32 v) { return __hip_atomic_fetch_add((LAS u32*)(size_t)a, v, __ATOMIC_RELAXED, __HIP_MEMORY_SCOPE_WORKGROUP); }
DI u32 lds_ld(u32 a) { return __hip_atomic_load((LAS u32*)(size_t)a, __ATOMIC_RELAXED, __HIP_MEMORY_SCOPE_WORKGROUP); }
DI void worker_sync(u32 cnt, int lane) {
    asm volatile("s_waitcnt lgkmcnt(0)" ::: "memory");
    if (lane == 0) lds_add(cnt, 1u);
    for (u32 spins = 0; lds_ld(cnt) < 4u && spins < (1u << 22); ++spins) {}
    asm volatile("" ::: "memory");
}

template <int it, int NIT> DI void body(const Params& p, const u32 L0) {
    int tid = threadIdx.x; asm volatile("" : "+v"(tid));
    const int l = tid & 63;
    const int w = __builtin_amdgcn_readfirstlane(tid >> 6);
    int b = blockIdx.x; asm volatile("" : "+s"(b));
    const int xg = b & 7, sidx = b >> 3;
    const int rb = 4 * (xg >> 1) + (sidx >> 3), j = 8 * (xg & 1) + (sidx & 7);
    const int g = l >> 4, i16 = l & 15, q4 = i16 >> 2, p4 = l & 3;
    char* const ws = p.ws;
    asm volatile("" :: "s"(p.x), "s"(p.wqkv), "s"(p.wo), "s"(p.wg), "s"(p.wu), "s"(p.wd), "s"(p.c), "s"(p.wmod), "s"(p.out), "s"(p.ws));
    u64 tag;
#define ASK_TAG(ep) u32x2 ep; asm volatile("global_load_dwordx2 %0, %1, %2" : "=v"(ep) : "v"(WS_EPOCH), "s"(ws) : "memory")
#define TAKE_TAG(ep) do { asm volatile("s_waitcnt vmcnt(0)" : "+v"(ep) :: "memory"); \
        tag = (((u64)(u32)__builtin_amdgcn_readfirstlane(ep[1]) << 32) | (u32)__builtin_amdgcn_readfirstlane(ep[0])) + 1 + it; } while (0)
    const __amdgpu_buffer_rsrc_t wsr = __builtin_amdgcn_make_buffer_rsrc(ws, 0, WS_END, 0x00020000);
#define ST16C(v, off) __builtin_amdgcn_raw_buffer_store_b128((v), wsr, (off), 0, 16)
#define ST8C(v, off)  __builtin_amdgcn_raw_buffer_store_b64((v), wsr, (off), 0, 16)
    STAMP_RT(62); STAMP(0);

    u32 wu0 = 0, wu1 = 0, wu2 = 0;
    if (w == 7) {
        const char* a0 = ws + (size_t)min(l, 31) * (1u << 20);
        const char* a1 = l < 3 ? (const char*)p.x + l * (1u << 20) : l < 6 ? (const char*)p.wqkv + (l - 3) * (1u << 20) : l < 7 ? (const char*)p.wo
                       : l < 10 ? (const char*)p.wg + (l - 7) * (1u << 20) : l < 13 ? (const char*)p.wu + (l - 10) * (1u << 20) : l < 16 ? (const char*)p.wd + (l - 13) * (1u << 20)
                       : l < 21 ? (const char*)p.wmod + (l - 16) * (1u << 20) : l < 24 ? (const char*)p.out + (l - 21) * (1u << 20) : (const char*)p.c;
        const char* pc; asm volatile("s_getpc_b64 %0" : "=s"(pc));
        const char* a2 = pc + (size_t)(sidx * 11 + min(l, 10)) * 128;
        asm volatile("global_load_dword %0, %3, off\n\tglobal_load_dword %1, %4, off\n\tglobal_load_dword %2, %5, off" : "=&v"(wu0), "=&v"(wu1), "=&v"(wu2) : "v"(a0), "v"(a1), "v"(a2) : "memory");
    }
    const int nj = j < 12 ? 7 : 6, Gj = j < 12 ? 7 * j : 84 + 6 * (j - 12);
    const bool wpwg = b < 216;
    const int wpm = b >= 108 ? 1 : 0, wpb = (b - 108 * wpm) >> 2, wpq = b & 3;
    if (w >= 4) {
        __builtin_amdgcn_s_setprio(3);
        const int u = w - 4;
        const bool tw = b >= 106;
        const int tb = (b - 106) / 5, tk = (b - 106) - 5 * tb, ts = 4 * tk + u;
        if (tw) {
            const char* src = (const char*)p.wqkv + ts * 122880 + tb * 128;
            const u32 voff = (l >> 3) * 3840 + (l & 7) * 16;
#pragma unroll
            for (int i = 0; i < 4; ++i) glds16(p.wqkv, src + i * 8 * 3840, voff, L0 + L_WTRAW + u * 4096 + i * 1024);
        }
        ASK_TAG(epl);
        BAR();
        __builtin_amdgcn_s_setprio(0);
        TAKE_TAG(epl);
        if (tw) {
            const u32 a = L0 + L_WTRAW + u * 4096 + (8 * g + q4) * 128 + 8 * p4;
#pragma unroll
            for (int t = 0; t < 4; ++t) {
                const u32 cc = 8 * tb + 2 * t + (i16 >> 3);
                const u32 seg = cc >= 160 ? 2 : cc >= 80 ? 1 : 0, wi = cc - 80 * seg, jj = (wi * 13108) >> 16, c5 = wi - 5 * jj;
                const u32 c = seg == 0 ? 10 + c5 : seg == 1 ? c5 : 5 + c5, nt = c >> 1;
                const u32 dst = WS_WT + jj * 153600 + (nt < 5 ? ts * 5120 + nt * 1024 : 102400 + ts * 2560 + (nt - 5) * 1024)
                              + (nt < 7 ? (g * 16 + 8 * (c & 1) + (i16 & 7)) * 16 : g * 128 + (i16 & 7) * 16);
                ST16C(ldtr2(a + 32 * t, a + 32 * t + 512), dst);
            }
        }
        vmw<0>();
        {
            asm volatile("s_waitcnt lgkmcnt(0)" ::: "memory");
            u32 old = 0;
            if (l == 0) old = lds_add(L0 + L_CNT + 12, 1u);
            if (tw && __builtin_amdgcn_readfirstlane(old) == 3 && l == 0) st_agent(ws + WS_FWT + (b - 106) * FS, tag);
        }
        STAMPL(19);
        if (u == 0) {
            const int wh = l >= 30 ? 0 : l / 5, kr = l >= 30 ? 0 : l - 5 * wh;
            const int bl = (1280 * (wh >> 1) + 80 * j + 79 * (wh & 1)) >> 7;
            poll_at(ws, WS_FWT + (bl * 5 + kr) * FS, l, tag, ws, 13);
            for (u32 spins = 0; lds_ld(L0 + L_CNT + 12) < 4u && spins < (1u << 22); ++spins) {}
            if (l == 0) lds_add(L0 + L_CNT + 24, 1u);
        } else {
            for (u32 spins = 0; lds_ld(L0 + L_CNT + 24) < 1u && spins < (1u << 22); ++spins) {}
        }
        asm volatile("" ::: "memory");
        STAMPL(20);
        {
            const char* src = ws + WS_WT + j * 153600 + u * 1024;
            const u32 lo = l * 16;
#pragma unroll
            for (int i = 0; i < 25; ++i) glds16c(src + i * 4096, lo, L0 + L_WKV + u * 1024 + i * 4096);
        }
        if (wpwg && u == 0) gather_strip(ws, 480 + 40 * wpq, 640 + 40 * wpq, L0 + L_SB, l, tag, ws, 2);
        VMCNT(0);
        STAMPL(17);
    } else {
        __builtin_amdgcn_s_setprio(2);
        const bool modwg = b < 240;
        u32x4 mw[5]; u32 mc[5];
        if (modwg) {
#pragma unroll
            for (int i = 0; i < 5; ++i) {
                const int r = 160 * (b & 3) + (tid >> 3) + 32 * i;
                mw[i] = gld16<0>(p.wmod, r * 7680 + (b >> 2) * 128 + (tid & 7) * 16);
                mc[i] = gldu16(p.c, r * 2);
            }
        }
        {
            const char* src = (const char*)p.x + (rb * 128 + 8 * j) * 1280 + w * 1024;
            glds16(p.x, src, l * 16, L0 + L_XS + w * 1024);
            glds16(p.x, src + 4096, l * 16, L0 + L_XS + (w + 4) * 1024);
            if (w < 2) glds16(p.x, src + 8192, l * 16, L0 + L_XS + (w + 8) * 1024);
        }
        ASK_TAG(epw);
        if (tid < 16) *(LAS u32*)(size_t)(L0 + L_CNT + tid * 4) = 0u;
        BAR();
        STAMP(29);
        TAKE_TAG(epw);
        STAMP(30);
        if (modwg) {
            asm volatile("" : "+v"(mw[0]), "+v"(mw[1]), "+v"(mw[2]), "+v"(mw[3]), "+v"(mw[4]), "+v"(mc[0]), "+v"(mc[1]), "+v"(mc[2]), "+v"(mc[3]), "+v"(mc[4]) :: "memory");
            float acc[8];
#pragma unroll
            for (int e = 0; e < 8; ++e) acc[e] = 0.f;
#pragma unroll
            for (int i = 0; i < 5; ++i) {
                const float cv = bflo(mc[i]);
                const float sv = cv * __builtin_amdgcn_rcpf(1.f + __builtin_amdgcn_exp2f(-cv * LOG2E));
                const u32 slo = pack2(sv, 0.f), shi = slo << 16;
#pragma unroll
                for (int d = 0; d < 4; ++d) { acc[2 * d] = dot2(mw[i][d], slo, acc[2 * d]); acc[2 * d + 1] = dot2(mw[i][d], shi, acc[2 * d + 1]); }
            }
#pragma unroll
            for (int e = 0; e < 8; ++e) { const float v = acc[e]; acc[e] = xsum4(v + dppf<0x128>(v)); }
            if (l < 8) {
                stsf4(L0 + L_MODP + w * 256 + l * 32, (f32x4){acc[0], acc[1], acc[2], acc[3]});
                stsf4(L0 + L_MODP + w * 256 + l * 32 + 16, (f32x4){acc[4], acc[5], acc[6], acc[7]});
            }
            asm volatile("s_waitcnt lgkmcnt(0)" ::: "memory");
            u32 old = 0;
            if (l == 0) old = lds_add(L0 + L_CNT, 1u);
            if (__builtin_amdgcn_readfirstlane(old) == 3) {
                float v = 0.f;
#pragma unroll
                for (int ww = 0; ww < 4; ++ww) v += ldsf(L0 + L_MODP + ww * 256 + l * 4);
                const float v1 = dppf<0x55>(v), v2 = dppf<0xAA>(v), v3 = dppf<0xFF>(v), v0 = dppf<0x00>(v);
                const u32x4 gr = {packh(v0, v1), packh(v2, v3), (u32)tag, (u32)(tag >> 32)};
                if (p4 == 0) ST16C(gr, WS_MODG + ((b & 3) * 960 + (b >> 2) * 16 + (l >> 2)) * 16);
            }
        }
        STAMP(1);
        worker_sync(L0 + L_CNT + 16, l);
        const int tok = l & 7;
        u32x4 xr[3];
#pragma unroll
        for (int i = 0; i < 3; ++i) xr[i] = lds128(L0 + L_XS + tok * 1280 + (20 * w + (i < 2 ? (l >> 3) + 8 * i : 16 + ((l >> 3) & 3))) * 16);
        {
            float s1 = 0.f, s2 = 0.f;
#pragma unroll
            for (int i = 0; i < 3; ++i) {
                float a1 = 0.f, a2 = 0.f;
#pragma unroll
                for (int d = 0; d < 4; ++d) { a1 = dot2(xr[i][d], 0x3F803F80u, a1); a2 = dot2(xr[i][d], xr[i][d], a2); }
                if (i < 2 || l < 32) { s1 += a1; s2 += a2; }
            }
            asm volatile("s_waitcnt lgkmcnt(0)" ::: "memory");
            if (l == 0) lds_add(L0 + L_CNT + 20, 1u);
            s1 += dppf<0x128>(s1); s2 += dppf<0x128>(s2);
            s1 = xsum4(s1); s2 = xsum4(s2);
            if (l < 8) stsf2(L0 + L_STP + (w * 8 + l) * 8, (f32x2){s1, s2});
        }
        gather_strip(ws, 40 * w, 160 + 40 * w, L0 + L_GS + w * 640, l, tag, ws, 1);
        STAMP(2);
        worker_sync(L0 + L_CNT + 4, l);
        {
            f32x2 st = {0.f, 0.f};
#pragma unroll
            for (int ww = 0; ww < 4; ++ww) st += *(LAS f32x2*)(size_t)(L0 + L_STP + (ww * 8 + tok) * 8);
            const float mu = st[0] * (1.f / 640.f), var = fmaxf(st[1] * (1.f / 640.f) - mu * mu, 0.f);
            const float rs = __builtin_amdgcn_rsqf(var + 1e-5f), nmr = -mu * rs;
#pragma unroll
            for (int i = 0; i < 3; ++i) {
                const int cl = i < 2 ? (l >> 3) + 8 * i : 16 + ((l >> 3) & 3);
                const u32x4 sa = lds128(L0 + L_GS + w * 640 + cl * 16), gg = lds128(L0 + L_GS + w * 640 + 320 + cl * 16);
                u32x4 h;
#pragma unroll
                for (int d = 0; d < 4; ++d)
                    h[d] = pack2((bflo(xr[i][d]) * rs + nmr) * bflo(gg[d]) + bflo(sa[d]), (bfhi(xr[i][d]) * rs + nmr) * bfhi(gg[d]) + bfhi(sa[d]));
                if (i < 2 || l < 32) ST16C(h, WS_H + (((rb * 8 + (j >> 1)) * 80 + 20 * w + cl) * 16 + 8 * (j & 1) + tok) * 16);
            }
        }
        VMCNT(0);
        worker_sync(L0 + L_CNT + 8, l);
        if (tid == 0) st_agent(ws + WS_FH + (rb * 16 + j) * FS, tag);
        if (w == 0) poll_flags(ws + WS_FH + rb * 16 * FS, 16, l, tag, ws, 11);
        __builtin_amdgcn_s_setprio(0);
    }
    BAR();
    asm volatile("s_waitcnt vmcnt(%3)" : "+v"(wu0), "+v"(wu1), "+v"(wu2) : "i"(0) : "memory");
    STAMP(3);

    u32x4 xa[20];
    const u32 lo16 = l * 16;
    const char* hsrc = ws + WS_H + (rb * 8 + w) * 80 * 256;
#define XL(s) xa[s] = gld16c(((s) & 3) * 1024, hsrc + ((s) >> 2) * 4096, lo16)
    XL(0); XL(1); XL(2); XL(3); XL(4); XL(5);
    const char* kvx = ws + WS_KV + (rb * 16 + j) * 22528;
    const char* wtq = ws + WS_WT + j * 153600 + (100 + w) * 1024;
    {
        f32x4 acc[5];
#pragma unroll
        for (int n = 0; n < 5; ++n) acc[n] = Z4;
        const u32 b0 = L0 + L_WKV + lo16;
        u32x4 bf[2][5];
#pragma unroll
        for (int n = 0; n < 5; ++n) bf[0][n] = lds128(b0 + n * 1024);
        static_for<0, 20>([&](auto S) {
            constexpr int s = S;
            vmw1<(s < 14 ? 5 : 6)>(xa[s]);
            if constexpr (s + 6 < 20) XL(s + 6);
            if constexpr (s >= 13) { if (s < 19 || w < 2) glds16c(wtq + (s - 13) * 8192, lo16, L0 + L_WKV + (100 + w) * 1024 + (s - 13) * 8192); }
            SB0();
#pragma unroll
            for (int n = 0; n < 5; ++n) {
                if (s < 19) bf[(s + 1) & 1][n] = lds128(b0 + (s + 1) * 5120 + n * 1024);
                acc[n] = mfma(bf[s & 1][n], xa[s], acc[n]);
            }
#pragma unroll
            for (int n = 0; n < 5; ++n) { SGB(M_DSR, 1); SGB(M_MFMA, 1); }
            SB0();
        });
        STAMP(18);
        BAR();
        const int row = 256 + 16 * w + i16;
#pragma unroll
        for (int n = 0; n < 5; ++n) {
            const int Q = 4 * n + g;
            const u32x2 v = {pack2(acc[n][0], acc[n][1]), pack2(acc[n][2], acc[n][3])};
            const int wi = Q - 10, hd = wi >= 5 ? 1 : 0;
            const u32 a = Q < 10 ? L_K + row * 80 + 8 * Q : L_V + row * 96 + 48 * hd + 8 * (wi - 5 * hd);
            sts64(L0 + a, v);
        }
        if (g < 2) sts64(L0 + L_V + row * 96 + 48 * g + 40, (u32x2){0x3F80u, 0u});
        if (rb < 2) {
            const int nrow = rb == 0 ? 256 : 128;
            for (int i = tid; i < nrow * 5; i += 512) sts128(L0 + L_K + i * 16, (u32x4){0, 0, 0, 0});
            for (int i = tid; i < nrow * 6; i += 512) sts128(L0 + L_V + i * 16, (u32x4){0, 0, 0, 0});
        }
        vmw<0>();
        BAR();
    }
#undef XL
    STAMP(4);
    {
        u32x4 kvd[3];
#pragma unroll
        for (int e = 0; e < 3; ++e) {
            const int n = min(w + 8 * e, 21);
            kvd[e] = lds128(L0 + (n < 10 ? L_K + 20480 + n * 1024 : L_V + 24576 + (n - 10) * 1024) + lo16);
        }
        const u32 kvo = WS_KV + (rb * 16 + j) * 22528 + w * 1024 + lo16;
        const u16* wroot = wpm ? p.wu : p.wg;
        const char* wsrc = (const char*)wroot + (160 * wpq + 8 * w) * 3456 + wpb * 128;
        const u32 wvo = (l >> 3) * 3456 + (l & 7) * 16;
        f32x4 acc[3];
#pragma unroll
        for (int n = 0; n < 3; ++n) acc[n] = Z4;
        const u32 bq = L0 + L_WQ + lo16, bq2 = L0 + L_WQ + 2048 + g * 128 + (i16 & 7) * 16;
#define QRD(s, n) lds128(((n) < 2 ? bq + (n) * 1024 : bq2) + (s) * 2560)
        u32x4 bf[3][3];
#pragma unroll
        for (int n = 0; n < 3; ++n) { bf[0][n] = QRD(0, n); bf[1][n] = QRD(1, n); }
        static_for<0, 20>([&](auto S) {
            constexpr int s = S;
            if constexpr (s == 0) { if (rb < 15) ST16C(kvd[0], kvo); }
            if constexpr (s == 1) { if (rb < 15) ST16C(kvd[1], kvo + 8192); }
            if constexpr (s == 2) { if (rb < 15 && w < 6) ST16C(kvd[2], kvo + 16384); }
            if constexpr (s == 14) {
                vmw<0>();
                u32 old = 0;
                if (l == 0) old = lds_add(L0 + L_CNT + 28, 1u);
                if (__builtin_amdgcn_readfirstlane(old) == 7 && l == 0) st_agent(ws + WS_FKV + (j * 16 + rb) * FS, tag);
            }
            if constexpr (s == 15) { if (wpwg) glds16(wroot, wsrc, wvo, L0 + L_WPRAW + w * 1024); }
            if constexpr (s == 16) { if (wpwg) glds16(wroot, wsrc + 64 * 3456, wvo, L0 + L_WPRAW + (w + 8) * 1024); }
            if constexpr (s == 17) { if (wpwg && w < 4) glds16(wroot, wsrc + 128 * 3456, wvo, L0 + L_WPRAW + (w + 16) * 1024); }
            SB0();
#pragma unroll
            for (int n = 0; n < 3; ++n) {
                if (s < 18) bf[(s + 2) % 3][n] = QRD(s + 2, n);
                acc[n] = mfma(bf[s % 3][n], xa[s], acc[n]);
            }
#pragma unroll
            for (int n = 0; n < 3; ++n) { SGB(M_DSR, 1); SGB(M_MFMA, 1); }
            SB0();
        });
#undef QRD
        STAMP(21);
#pragma unroll
        for (int n = 0; n < 3; ++n) {
            const int Q = 4 * n + g;
            const u32x2 v = {pack2(acc[n][0] * QSCALE, acc[n][1] * QSCALE), pack2(acc[n][2] * QSCALE, acc[n][3] * QSCALE)};
            if (Q < 10) sts64(L0 + L_Q + (16 * w + i16) * 80 + 8 * Q, v);
        }
    }
    if (w == 0 && rb >= 1) poll_flags(ws + WS_FKV + (j * 16 + (rb >= 2 ? rb - 2 : rb - 1)) * FS, rb >= 2 ? 2 : 1, l, tag, ws, 3);
    VMCNT(0);
    BAR();
    STAMP(5);
    if (rb >= 1) {
#pragma unroll
        for (int e = 0; e < 3; ++e) {
            const int n = w + 8 * e;
            if (n < 22) {
                const u32 d = n < 10 ? L_K + n * 1024 : L_V + (n - 10) * 1024, st = n < 10 ? 10240 : 12288;
                glds16c(kvx - 16 * 22528 + n * 1024, lo16, L0 + d + st);
                if (rb >= 2) glds16c(kvx - 32 * 22528 + n * 1024, lo16, L0 + d);
            }
        }
    }
    auto wp_slot = [&](int gam, int& jj, int& slot) {
        jj = gam < 84 ? gam / 7 : 12 + (gam - 84) / 6;
        const int ar = gam - (jj < 12 ? 7 * jj : 84 + 6 * (jj - 12)), a = (jj < 12 || ar < 3) ? ar : ar + 1;
        slot = a < 3 ? 2 * a + wpm : a == 3 ? (wpm ? 13 : 6) : 7 + 2 * (a - 4) + wpm;
    };
    if (wpwg) {
#pragma unroll
        for (int i = 0; i < 3; ++i) {
            if (i < 2 || w < 4) {
                const int f = w + 8 * i, ks = f >> 2, t16 = f & 3;
                int jj, slot; wp_slot(4 * wpb + t16, jj, slot);
                const u32 a = L0 + L_WPRAW + (32 * ks + 8 * g + q4) * 128 + 32 * t16 + 8 * p4;
                const u32x4 wr = ldtr2(a, a + 512);
                const u32x4 g2 = lds128(L0 + L_G2 + (32 * ks + 8 * g) * 2), sb = lds128(L0 + L_SB + (32 * ks + 8 * g) * 2);
                u32x4 o; float us = 0.f, vs = 0.f;
#pragma unroll
                for (int d = 0; d < 4; ++d) {
                    o[d] = pack2(dot2(wr[d], g2[d] & 0xffffu, 0.f), dot2(wr[d], g2[d] & 0xffff0000u, 0.f));
                    us = dot2(o[d], 0x3F803F80u, us);
                    vs = dot2(wr[d], sb[d], vs);
                }
                ST16C(o, WS_WP + ((jj * 20 + 5 * wpq + ks) * 14 + slot) * 1024 + lo16);
                us = xsum4(us); vs = xsum4(vs);
                if (l < 32) stsf(L0 + L_UVL + f * 128 + l * 4, l < 16 ? us : vs);
            }
        }
    }
    if (wpwg) { if (w < 4) vmw<3>(); else vmw<2>(); } else vmw<0>();
    BAR();
    STAMP(6);
    if (wpwg && w == 3) {
#pragma unroll
        for (int e = 0; e < 2; ++e) {
            const int t16 = 2 * e + (l >> 5);
            int jj, slot; wp_slot(4 * wpb + t16, jj, slot);
            float v = 0.f;
#pragma unroll
            for (int ks = 0; ks < 5; ++ks) v += ldsf(L0 + L_UVL + (4 * ks + t16) * 128 + (l & 31) * 4);
            st_agent32(ws + WS_UV + ((jj * 14 + slot) * 4 + wpq) * 128 + (l & 31) * 4, __float_as_uint(v));
        }
    }
    const int hf = j & 1, cb = j >> 1;
    u32 wvoff[5];
#pragma unroll
    for (int i = 0; i < 5; ++i) {
        const u32 sg = 64 * i + l, pos = (sg * 6554) >> 16, c = sg - pos * 10, k = pos ^ (((pos >> 3) & 1) << 2);
        wvoff[i] = k * 1280 + c * 16;
    }
    const char* wosrc = (const char*)p.wo + cb * 160;
#define WO_STEP(sx) do { _Pragma("unroll") for (int i = 0; i < 5; ++i) glds16(p.wo, wosrc + (sx) * 32 * 1280, wvoff[i], L0 + L_WO + (sx) * 5120 + i * 1024); } while (0)
    STAMP(7);

    {
        const int hd = w & 1, qi = w >> 1;
        const int kr0 = 32 * qi;
        u32x4 qf[2];
#pragma unroll
        for (int a = 0; a < 2; ++a) {
            const u32 qa = L0 + L_Q + (kr0 + 16 * a + i16) * 80 + hd * 40;
            const u32x2 q0 = lds64(qa + 8 * g);
            u32x2 q1 = lds64(qa + 32);
            if (g != 0) q1 = (u32x2){0, 0};
            qf[a] = (u32x4){q0[0], q0[1], q1[0], q1[1]};
        }
        const u32 kb0 = L0 + L_K + (kr0 + i16) * 80 + hd * 40 + 8 * g, kb1 = L0 + L_K + (kr0 + i16) * 80 + hd * 40 + 32;
        f32x4 mfirst, mlast;
#pragma unroll
        for (int r = 0; r < 4; ++r) { const bool lo = 4 * g + r <= i16; mfirst[r] = lo ? -INFINITY : 0.f; mlast[r] = lo ? 0.f : -INFINITY; }
        f32x4 s0[17], s1[17];
        {
            u32x4 kf[6];
#define KRD(t) do { const u32x2 k0 = lds64(kb0 + (t) * 1280), k1 = lds64(kb1 + (t) * 1280); kf[(t) % 6] = (u32x4){k0[0], k0[1], k1[0], k1[1]}; } while (0)
            KRD(0); KRD(1); KRD(2); KRD(3); KRD(4);
            static_for<0, 18>([&](auto T) {
                constexpr int t = T;
                SB0();
                if constexpr (t < 13) KRD(t + 5);
                if constexpr (t < 17) s0[t] = mfma(kf[t % 6], qf[0], t == 0 ? mfirst : t == 16 ? mlast : Z4);
                if constexpr (t > 0) s1[t - 1] = mfma(kf[t % 6], qf[1], t == 1 ? mfirst : t == 17 ? mlast : Z4);
                SGB(M_DSR, 2); SGB(M_MFMA, 2);
                SB0();
            });
#undef KRD
        }
        WO_STEP(4 + w);
        if (rb < 2) {
            const int fv = rb == 1 ? 128 : 256;
#pragma unroll
            for (int t = 0; t < 16; ++t) {
                const bool d0 = kr0 + 16 * t < fv, d1 = kr0 + 16 * t + 16 < fv;
#pragma unroll
                for (int r = 0; r < 4; ++r) { s0[t][r] = d0 ? -INFINITY : s0[t][r]; s1[t][r] = d1 ? -INFINITY : s1[t][r]; }
            }
        }
        STAMP(22);
        float m0 = fmaxf(s0[0][0], s0[0][1]), m1 = fmaxf(s1[0][0], s1[0][1]), n0 = fmaxf(s0[0][2], s0[0][3]), n1 = fmaxf(s1[0][2], s1[0][3]);
#pragma unroll
        for (int t = 1; t < 17; ++t) {
            m0 = fmaxf(fmaxf(m0, s0[t][0]), s0[t][1]); m1 = fmaxf(fmaxf(m1, s1[t][0]), s1[t][1]);
            n0 = fmaxf(fmaxf(n0, s0[t][2]), s0[t][3]); n1 = fmaxf(fmaxf(n1, s1[t][2]), s1[t][3]);
        }
        m0 = fmaxf(m0, n0); m1 = fmaxf(m1, n1);
        m0 = xmax4(m0); m1 = xmax4(m1);
        WO_STEP(12 + w);
        STAMP(23);
        f32x4 o00 = Z4, o01 = Z4, o10 = Z4, o11 = Z4;
        const u32 vb = L0 + L_V + (kr0 + 4 * g + q4) * 96 + hd * 48 + 8 * p4;
        auto pv = [&](auto SUB) {
            constexpr bool sub = SUB;
            u32x4 vf[2][2];
            vf[0][0] = ldtr2(vb, vb + 16 * 96); vf[0][1] = ldtr2(vb + 32, vb + 16 * 96 + 32);
#define EX(v, m) __builtin_amdgcn_exp2f(sub ? (v) - (m) : (v))
            static_for<0, 9>([&](auto U) {
                constexpr int u = U;
                if constexpr (u < 8) { const u32 a = vb + (u + 1) * 32 * 96; vf[(u + 1) & 1][0] = ldtr2(a, a + 16 * 96); vf[(u + 1) & 1][1] = ldtr2(a + 32, a + 16 * 96 + 32); }
                u32x4 pa, pb;
                pa[0] = pack2(EX(s0[2 * u][0], m0), EX(s0[2 * u][1], m0)); pa[1] = pack2(EX(s0[2 * u][2], m0), EX(s0[2 * u][3], m0));
                if constexpr (u < 8) { pa[2] = pack2(EX(s0[2 * u + 1][0], m0), EX(s0[2 * u + 1][1], m0)); pa[3] = pack2(EX(s0[2 * u + 1][2], m0), EX(s0[2 * u + 1][3], m0)); }
                else { pa[2] = 0; pa[3] = 0; }
                if constexpr (u > 0) { pb[0] = pack2(EX(s1[2 * u - 1][0], m1), EX(s1[2 * u - 1][1], m1)); pb[1] = pack2(EX(s1[2 * u - 1][2], m1), EX(s1[2 * u - 1][3], m1)); }
                else { pb[0] = 0; pb[1] = 0; }
                pb[2] = pack2(EX(s1[2 * u][0], m1), EX(s1[2 * u][1], m1)); pb[3] = pack2(EX(s1[2 * u][2], m1), EX(s1[2 * u][3], m1));
                o00 = mfma(vf[u & 1][0], pa, o00); o01 = mfma(vf[u & 1][1], pa, o01);
                o10 = mfma(vf[u & 1][0], pb, o10); o11 = mfma(vf[u & 1][1], pb, o11);
                SB0();
            });
#undef EX
        };
        if (__all(fabsf(m0) <= 64.f && fabsf(m1) <= 64.f)) pv(std::false_type{}); else pv(std::true_type{});
        STAMP(24);
        const float i0 = __builtin_amdgcn_rcpf(__shfl(o01[0], i16 + 16)), i1 = __builtin_amdgcn_rcpf(__shfl(o11[0], i16 + 16));
        const u32 oa = L0 + L_Q + (kr0 + i16) * 80 + hd * 40;
        sts64(oa + 8 * g, (u32x2){pack2(o00[0] * i0, o00[1] * i0), pack2(o00[2] * i0, o00[3] * i0)});
        sts64(oa + 1280 + 8 * g, (u32x2){pack2(o10[0] * i1, o10[1] * i1), pack2(o10[2] * i1, o10[3] * i1)});
        if (g == 0) {
            sts64(oa + 32, (u32x2){pack2(o01[0] * i0, o01[1] * i0), pack2(o01[2] * i0, o01[3] * i0)});
            sts64(oa + 1280 + 32, (u32x2){pack2(o11[0] * i1, o11[1] * i1), pack2(o11[2] * i1, o11[3] * i1)});
        }
    }
    BAR();
    {
        const int tok = 64 * (w & 1) + l;
        const u32 oo = WS_O + (((rb * 8 + (tok >> 4)) * 80 + 5 * j) * 16 + (tok & 15)) * 16;
        ST16C(lds128(L0 + L_Q + tok * 80 + (w >> 1) * 16), oo + (w >> 1) * 256);
        if (w < 2) ST16C(lds128(L0 + L_Q + tok * 80 + 64), oo + 4 * 256);
    }
    VMCNT(0);
    BAR();
    if (tid == 0) {
        st_agent(ws + WS_FO + (rb * 16 + j) * FS, tag);
        if (wpwg) st_agent(ws + WS_FWP + b * FS, tag);
    }
    STAMP(8);

    const int wm = w & 3, wk = w >> 2;
    const int mt3 = rb * 8 + 4 * hf + wm;
    const int nt0 = wk == 0 ? 0 : 3, ntn = wk == 0 ? 3 : 2;
    u32x2 x1k[3];
    {
        const int wdu = b + 256 * (w - 6);
        const bool wdw = w >= 6 && wdu < 432;
        if (w >= 1 && w < 5) {
#pragma unroll
            for (int i = 0; i < 5; ++i) glds16(p.wo, wosrc + (w - 1) * 32 * 1280, wvoff[i], L0 + L_WO + (w - 1) * 5120 + i * 1024);
        } else if (w >= 5) {
#pragma unroll
            for (int e = 0; e < 4; ++e) {
                const int pc = w - 5 + 3 * e;
                if (pc < 10) {
                    const u32 sg = 64 * pc + l, r = (sg * 6554) >> 16, c = sg - r * 10;
                    glds16(p.x, (const char*)p.x + (rb * 128 + 64 * hf) * 1280 + cb * 160, r * 1280 + c * 16, L0 + L_XR + pc * 1024);
                }
            }
            if (wdw) {
                const int ucb = wdu / 54, us = wdu - 54 * ucb;
#pragma unroll
                for (int i = 0; i < 5; ++i) glds16(p.wd, (const char*)p.wd + ucb * 160 + us * 32 * 1280, wvoff[i], L0 + L_WDRAW + (w - 6) * 5120 + i * 1024);
            }
        }
        if (w == 1 && l < 20) stsf4(L0 + L_GA + l * 16, gather1(ws, 320 + 20 * cb + l, tag, ws, 4));
        if (w == 0) {
            const int bl = min((Gj >> 2) + ((l >> 2) % 3), (Gj + nj - 1) >> 2);
            poll_at(ws, WS_FWP + (108 * (l >= 12 && l < 24 ? 1 : 0) + 4 * bl + (l & 3)) * FS, l, tag, ws, 6);
            poll_flags(ws + WS_FO + rb * 16 * FS, 16, l, tag, ws, 5);
        }
        if (wdw) vmw<5>(); else vmw<0>();
        BAR();
        STAMP(9);
        u32x4 a[10];
        const char* asrc = ws + WS_O + (mt3 * 80 + 40 * wk) * 256;
#define AL(s) a[s] = gld16c(((s) & 3) * 1024, asrc + ((s) >> 2) * 4096, lo16)
        AL(0); AL(1); AL(2); AL(3);
        f32x4 acc[5];
#pragma unroll
        for (int n = 0; n < 5; ++n) acc[n] = Z4;
        const u32 wb = L0 + L_WO + wk * 51200;
        const u32 b0 = wb + 160 * (8 * g + 4 * (g & 1) + q4) + 8 * p4, b1 = wb + 160 * (8 * g + 4 * ((g & 1) ^ 1) + q4) + 8 * p4;
        u32x4 bf[2][5];
#pragma unroll
        for (int n = 0; n < 5; ++n) bf[0][n] = ldtr2(b0 + n * 32, b1 + n * 32);
        static_for<0, 10>([&](auto S) {
            constexpr int s = S;
            vmw1<(s < 7 ? 3 : 9 - s)>(a[s]);
            if constexpr (s + 4 < 10) AL(s + 4);
            SB0();
#pragma unroll
            for (int n = 0; n < 5; ++n) {
                if (s < 9) bf[(s + 1) & 1][n] = ldtr2(b0 + (s + 1) * 5120 + n * 32, b1 + (s + 1) * 5120 + n * 32);
                acc[n] = mfma(bf[s & 1][n], a[s], acc[n]);
            }
#pragma unroll
            for (int n = 0; n < 5; ++n) { SGB(M_DSR, 2); SGB(M_MFMA, 1); }
            SB0();
        });
#undef AL
        STAMP(25);
        if (wk == 0) { stsf4(L0 + L_RED3 + (wm * 5 + 3) * 1024 + lo16, acc[3]); stsf4(L0 + L_RED3 + (wm * 5 + 4) * 1024 + lo16, acc[4]); }
        else { stsf4(L0 + L_RED3 + (wm * 5 + 0) * 1024 + lo16, acc[0]); stsf4(L0 + L_RED3 + (wm * 5 + 1) * 1024 + lo16, acc[1]); stsf4(L0 + L_RED3 + (wm * 5 + 2) * 1024 + lo16, acc[2]); }
        BAR();
        float s1 = 0.f, s2 = 0.f;
#pragma unroll
        for (int n = 0; n < 3; ++n) {
            if (n < ntn) {
                const int nt = nt0 + n;
                const f32x4 mine = wk == 0 ? acc[n] : acc[n < 2 ? 3 + n : 4];
                const f32x4 v = mine + ldsf4(L0 + L_RED3 + (wm * 5 + nt) * 1024 + lo16);
                const f32x4 ga = ldsf4(L0 + L_GA + (16 * nt + 4 * g) * 4);
                const u32x2 xr = lds64(L0 + L_XR + (16 * wm + i16) * 160 + 32 * nt + 8 * g);
                const u32x2 o = {pack2(bflo(xr[0]) + ga[0] * v[0], bfhi(xr[0]) + ga[1] * v[1]), pack2(bflo(xr[1]) + ga[2] * v[2], bfhi(xr[1]) + ga[3] * v[3])};
                x1k[n] = o;
                s1 = dot2(o[0], 0x3F803F80u, s1); s1 = dot2(o[1], 0x3F803F80u, s1);
                s2 = dot2(o[0], o[0], s2); s2 = dot2(o[1], o[1], s2);
                sts64(L0 + L_X1S + (16 * wm + i16) * 160 + 32 * nt + 8 * g, o);
            } else x1k[n] = (u32x2){0, 0};
        }
        s1 = xsum4(s1); s2 = xsum4(s2);
        if (l < 16) stsf2(L0 + L_ST3 + (wk * 64 + 16 * wm + l) * 8, (f32x2){s1, s2});
        BAR();
        {
            const u32 xo = WS_X1 + (((rb * 8 + 4 * hf + g) * 80 + 10 * cb) * 16 + i16) * 16;
            ST16C(lds128(L0 + L_X1S + l * 160 + w * 16), xo + w * 256);
            if (w < 2) ST16C(lds128(L0 + L_X1S + l * 160 + (w + 8) * 16), xo + (w + 8) * 256);
            if (w == 2) {
                const f32x2 u = *(LAS f32x2*)(size_t)(L0 + L_ST3 + l * 8), v = *(LAS f32x2*)(size_t)(L0 + L_ST3 + (64 + l) * 8);
                const f32x2 sum = u + v;
                ST8C(__builtin_bit_cast(u32x2, sum), WS_STAT + ((rb * 8 + cb) * 128 + 64 * hf + l) * 8);
            }
        }
        if (wdw) {
            const u32 wr = L0 + L_WDRAW + (w - 6) * 5120;
            const u32 r0 = wr + 160 * (8 * g + 4 * (g & 1) + q4) + 8 * p4, r1 = wr + 160 * (8 * g + 4 * ((g & 1) ^ 1) + q4) + 8 * p4;
#pragma unroll
            for (int n = 0; n < 5; ++n) ST16C(ldtr2(r0 + n * 32, r1 + n * 32), WS_WD + (wdu * 5 + n) * 1024 + lo16);
        }
        STAMP(26);
        if (wdw) vmw<5>(); else vmw<0>();
        BAR();
        if (tid == 0) st_agent(ws + WS_FX1 + (rb * 16 + j) * FS, tag);
    }
    STAMP(10);

    const char* wdsrc = ws + WS_WD + (cb * 54 + wk * 27) * 5120;
    const u32 r5w = L0 + wk * 20480 + wm * 5120;
#define P5_B1(t, i) do { if ((i) == 0 || (i) == 4) glds16c(wdsrc + min(4 * (t) + wm, 26) * 5120 + (i) * 1024, lo16, r5w + r5_slot(t) + (i) * 1024); \
        else glds16c_next(((i) & 3) * 1024, wdsrc + min(4 * (t) + wm, 26) * 5120, lo16); } while (0)
    {
        const int wn = w >> 2;
        const int bst = w < 4 ? 4 * w : 16 + 3 * (w - 4);
        const char* wpj = ws + WS_WP + (j * 20 * 14 + bst) * 1024;
        const char* x1b = ws + WS_X1 + (rb * 8 + w) * 80 * 256;
        const u32 r4a = L0 + L_R4 + w * 2048, r4b = L0 + L_R4 + 16384 + bst * 1024;
#define P4_B1(t, i) do { if ((i) == 0) glds16c(wpj + (t) * 28 * 1024, lo16, r4b + ((t) % 3) * R4_STAGE); else glds16c_next((i) * 1024, wpj + (t) * 28 * 1024, lo16); } while (0)
#define P4_A1(t, i) do { if ((i) == 0) glds16c(x1b + 8 * (t) * 256, lo16, r4a + ((t) % 3) * R4_STAGE); else glds16c_next(1024, x1b + 8 * (t) * 256, lo16); } while (0)
#define P4_B(t) do { P4_B1(t, 0); P4_B1(t, 1); P4_B1(t, 2); if (w < 4) P4_B1(t, 3); } while (0)
        P4_B(0); P4_B(1); P4_B(2);
        if (w == 0) poll_flags(ws + WS_FX1 + rb * 16 * FS, 16, l, tag, ws, 7);
        BAR();
        STAMP(11);
        glds16c(ws + WS_STAT + rb * 8192 + w * 1024, lo16, L0 + L_ST4 + w * 1024);
        if (w < 7) glds16c(ws + WS_UV + j * 7168 + w * 1024, lo16, L0 + L_UV + w * 1024);
        P4_A1(0, 0); P4_A1(0, 1); P4_A1(1, 0); P4_A1(1, 1); P4_A1(2, 0); P4_A1(2, 1);
        f32x4 acc[7][2];
#pragma unroll
        for (int n = 0; n < 7; ++n) { acc[n][0] = Z4; acc[n][1] = Z4; }
        const u32 ra = opaque(L0 + L_R4 + 2 * wm * 2048 + lo16), rbb = opaque(L0 + L_R4 + 16384 + 7 * wn * 1024 + lo16);
        u32x4 fa[2][2], fb[2][7];
#define P4_RA(buf, t, ks) do { const u32 so = ((t) % 3) * R4_STAGE + (ks) * 1024; fa[buf][0] = lds128(ra + so); fa[buf][1] = lds128(ra + so + 2048); } while (0)
#define P4_RB(buf, t, ks, n) fb[buf][n] = lds128(rbb + ((t) % 3) * R4_STAGE + (ks) * 14336 + (n) * 1024)
#define P4_MM(buf, n) do { acc[n][0] = mfma(fb[buf][n], fa[buf][0], acc[n][0]); acc[n][1] = mfma(fb[buf][n], fa[buf][1], acc[n][1]); } while (0)
#define P4_HALF(buf, rd, t, ks, V0, V1, V2) do { \
            SB0(); if (rd) { P4_RA((buf) ^ 1, t, ks); P4_RB((buf) ^ 1, t, ks, 0); } P4_MM(buf, 0); SB0(); V0; \
            SB0(); if (rd) P4_RB((buf) ^ 1, t, ks, 1); P4_MM(buf, 1); SB0(); \
            SB0(); if (rd) P4_RB((buf) ^ 1, t, ks, 2); P4_MM(buf, 2); SB0(); V1; \
            SB0(); if (rd) P4_RB((buf) ^ 1, t, ks, 3); P4_MM(buf, 3); SB0(); \
            SB0(); if (rd) P4_RB((buf) ^ 1, t, ks, 4); P4_MM(buf, 4); SB0(); V2; \
            SB0(); if (rd) P4_RB((buf) ^ 1, t, ks, 5); P4_MM(buf, 5); SB0(); \
            SB0(); if (rd) P4_RB((buf) ^ 1, t, ks, 6); P4_MM(buf, 6); SB0(); } while (0)
        VMCNT(4);
        BAR();
        if (tid == 0) st_agent(ws + WS_FW2 + b * FS, tag);
        STAMP(27);
        u32x2 wdf = {0u, 0u};
        const u32 wdo = WS_FW2 + ((54 * cb + (l < 54 ? l : 0)) & 255) * FS;
        P4_RA(0, 0, 0);
#pragma unroll
        for (int n = 0; n < 7; ++n) P4_RB(0, 0, 0, n);
        static_for<0, 10>([&](auto T) {
            constexpr int t = T;
            constexpr bool f = t >= 1 && t + 2 <= 9, sN = t + 3 <= 9;
            if constexpr (t < 9) P4_HALF(0, true, t, 1, if (f && w < 4) P4_B1(t + 2, 3), if (f) P4_A1(t + 2, 0), if (f) P4_A1(t + 2, 1));
            else P4_HALF(0, true, t, 1, P5_B1(0, 3), P5_B1(0, 4), P5_B1(1, 0));
            if constexpr (t == 5) {
                if (tid < 128) {
                    f32x2 st = {0.f, 0.f};
#pragma unroll
                    for (int c = 0; c < 8; ++c) st += *(LAS f32x2*)(size_t)(L0 + L_ST4 + (c * 128 + tid) * 8);
                    const float mu = st[0] * (1.f / 640.f), var = fmaxf(st[1] * (1.f / 640.f) - mu * mu, 0.f), r = __builtin_amdgcn_rsqf(var + 1e-5f);
                    stsf2(L0 + L_MR + tid * 8, (f32x2){r, -mu * r});
                } else {
                    const int e = tid - 128;
                    stsf(L0 + L_UVS + e * 4, (ldsf(L0 + L_UV + (e >> 5) * 512 + (e & 31) * 4) + ldsf(L0 + L_UV + (e >> 5) * 512 + 128 + (e & 31) * 4))
                                           + (ldsf(L0 + L_UV + (e >> 5) * 512 + 256 + (e & 31) * 4) + ldsf(L0 + L_UV + (e >> 5) * 512 + 384 + (e & 31) * 4)));
                    if (e < 64) { const int e2 = e + 384;
                        stsf(L0 + L_UVS + e2 * 4, (ldsf(L0 + L_UV + (e2 >> 5) * 512 + (e2 & 31) * 4) + ldsf(L0 + L_UV + (e2 >> 5) * 512 + 128 + (e2 & 31) * 4))
                                                + (ldsf(L0 + L_UV + (e2 >> 5) * 512 + 256 + (e2 & 31) * 4) + ldsf(L0 + L_UV + (e2 >> 5) * 512 + 384 + (e2 & 31) * 4))); }
                }
            }
            if constexpr (t < 9) {
                if constexpr (t == 0) vmw<2>();
                else if constexpr (t == 8) {
                    asm volatile("s_waitcnt vmcnt(0)" : "+v"(wdf) :: "memory");
                    if (w == 7) { if (!__all(wdf[0] == (u32)tag && wdf[1] == (u32)(tag >> 32))) poll_at(ws, wdo, l, tag, ws, 12); }
                }
                else { if (w < 4) vmw<6>(); else vmw<5>(); }
                BAR();
                if constexpr (t == 7) { if (w == 7) asm volatile("s_nop 4\n\tglobal_load_dwordx2 %0, %1, %2 sc1" : "=v"(wdf) : "v"(wdo), "s"(ws) : "memory"); }
                if constexpr (t < 8) P4_HALF(1, true, t + 1, 0, if (sN) P4_B1(t + 3, 0), if (sN) P4_B1(t + 3, 1), if (sN) P4_B1(t + 3, 2));
                else P4_HALF(1, true, t + 1, 0, P5_B1(0, 0), P5_B1(0, 1), P5_B1(0, 2));
            } else {
                P4_HALF(1, false, 0, 0, P5_B1(1, 1), P5_B1(1, 2), P5_B1(1, 3));
                P5_B1(1, 4);
            }
        });
#undef P4_B
#undef P4_B1
#undef P4_A1
#undef P4_RA
#undef P4_RB
#undef P4_MM
#undef P4_HALF
        STAMP(12);
        f32x2 mr[2];
#pragma unroll
        for (int m = 0; m < 2; ++m) mr[m] = *(LAS f32x2*)(size_t)(L0 + L_MR + (32 * wm + 16 * m + i16) * 8);
#pragma unroll
        for (int n = 0; n < 7; ++n) {
            const f32x4 u = ldsf4(L0 + L_UVS + (7 * wn + n) * 128 + 16 * g), v = ldsf4(L0 + L_UVS + (7 * wn + n) * 128 + 64 + 16 * g);
#pragma unroll
            for (int m = 0; m < 2; ++m) acc[n][m] = acc[n][m] * mr[m][0] + (u * mr[m][1] + v);
        }
        if (nj == 7) stsf4(L0 + L_XCH + w * 1024 + lo16, wn == 0 ? acc[6][1] : acc[6][0]);
        auto silu_mul = [&](const f32x4 gt, const f32x4 up) -> u32x2 {
            const f32x4 e = gt * (-LOG2E), gu = gt * up;
            f32x4 d;
#pragma unroll
            for (int r = 0; r < 4; ++r) d[r] = __builtin_amdgcn_exp2f(e[r]);
            d = d + 1.f;
#pragma unroll
            for (int r = 0; r < 4; ++r) d[r] = __builtin_amdgcn_rcpf(d[r]);
            const f32x4 o = gu * d;
            return (u32x2){pack2(o[0], o[1]), pack2(o[2], o[3])};
        };
#pragma unroll
        for (int a = 0; a < 3; ++a) {
            const int la = a + 4 * wn, gam = Gj + (la <= 3 || nj == 7 ? la : la - 1);
            const u32x2 o0 = silu_mul(acc[2 * a][0], acc[2 * a + 1][0]), o1 = silu_mul(acc[2 * a][1], acc[2 * a + 1][1]);
            const auto s0 = __builtin_amdgcn_permlane16_swap(o0[0], o1[0], false, false);
            const auto s1 = __builtin_amdgcn_permlane16_swap(o0[1], o1[1], false, false);
            const u32x4 v = {s0[0], s1[0], s0[1], s1[1]};
            ST16C(v, WS_ACT + (((rb * 8 + 2 * wm + (g & 1)) * 216 + 2 * gam + (g >> 1)) * 16 + i16) * 16);
        }
        STAMP(28);
        BAR();
        if (nj == 7) {
            const f32x4 other = ldsf4(L0 + L_XCH + (w ^ 4) * 1024 + lo16);
            const u32x2 o = wn == 0 ? silu_mul(acc[6][0], other) : silu_mul(other, acc[6][1]);
            ST8C(o, WS_ACT + (((rb * 8 + 2 * wm + wn) * 216 + 2 * (Gj + 3) + (g >> 1)) * 16 + i16) * 16 + 8 * (g & 1));
        }
        P5_B1(2, 0); P5_B1(2, 1); P5_B1(2, 2); P5_B1(2, 3); P5_B1(2, 4);
        vmw<5>();
        BAR();
        if (tid == 0) st_agent(ws + WS_FACT + (rb * 16 + j) * FS, tag);
    }
    STAMP(13);

    {
        if (w == 1 && l < 20) stsf4(L0 + L_GB + l * 16, gather1(ws, 800 + 20 * cb + l, tag, ws, 8));
        if (w == 0) poll_flags(ws + WS_FACT + rb * 16 * FS, 16, l, tag, ws, 9);
        VMCNT(0);
        BAR();
        STAMP(14);
        const char* asrc = ws + WS_ACT + (mt3 * 216 + wk * 108) * 256;
        u32x4 a[3][4];
#define P5_A1(t, i) a[(t) % 3][i] = gld16c((i) * 1024, asrc + (t) * 4096, lo16)
        P5_A1(0, 0); P5_A1(0, 1); P5_A1(0, 2); P5_A1(0, 3); P5_A1(1, 0); P5_A1(1, 1); P5_A1(1, 2); P5_A1(1, 3); P5_A1(2, 0); P5_A1(2, 1); P5_A1(2, 2); P5_A1(2, 3);
        f32x4 acc[5];
#pragma unroll
        for (int n = 0; n < 5; ++n) acc[n] = Z4;
        const u32 rb5 = opaque(L0 + wk * 20480 + lo16);
        u32x4 fb[2][5];
#define P5_RD(buf, t, kk) do { _Pragma("unroll") for (int n = 0; n < 5; ++n) fb[buf][n] = lds128(rb5 + r5_slot(t) + (kk) * 5120 + n * 1024); } while (0)
#define P5_MM(buf, av) do { _Pragma("unroll") for (int n = 0; n < 5; ++n) acc[n] = mfma(fb[buf][n], (av), acc[n]); } while (0)
#define P5_MIX() do { _Pragma("unroll") for (int n = 0; n < 5; ++n) { SGB(M_DSR, 1); SGB(M_MFMA, 1); } } while (0)
        P5_RD(0, 0, 0);
        static_for<0, 7>([&](auto T) {
            constexpr int t = T, q = t % 3;
            constexpr bool nx = t + 3 < 7;
            constexpr int NA = t == 0 ? 8 : t == 1 ? 13 : t == 2 ? 18 : t == 3 ? 21 : t == 4 ? 20 : t == 5 ? 11 : 5;
            constexpr int NB = t == 2 ? 12 : t == 3 ? 12 : t == 4 ? 8 : t == 5 ? 0 : 63;
            vmw4<NA>(a[q][0], a[q][1], a[q][2], a[q][3]);
            SB0(); P5_RD(1, t, 1); P5_MM(0, a[q][0]); P5_MIX(); SB0();
            if constexpr (nx) P5_A1(t + 3, 0);
            SB0(); P5_RD(0, t, 2); P5_MM(1, a[q][1]); P5_MIX(); SB0();
            if constexpr (nx) P5_A1(t + 3, 1);
            if constexpr (t < 6) {
                SB0(); P5_RD(1, t, 3); P5_MM(0, a[q][2]); P5_MIX(); SB0();
                if constexpr (nx) P5_A1(t + 3, 2);
                vmw<NB>(); BAR();
                if constexpr (nx) { P5_B1(t + 3, 0); P5_B1(t + 3, 1); }
                SB0(); P5_RD(0, t + 1, 0); P5_MM(1, a[q][3]); P5_MIX(); SB0();
                if constexpr (nx) { if constexpr (t + 3 < 6) P5_A1(t + 3, 3); P5_B1(t + 3, 2); P5_B1(t + 3, 3); P5_B1(t + 3, 4); }
            } else {
                SB0(); P5_MM(0, a[q][2]); SB0();
            }
        });
#undef P5_B1
#undef P5_A1
#undef P5_RD
#undef P5_MM
#undef P5_MIX
        STAMP(15);
        if (wk == 0) { stsf4(L0 + L_RED5 + (wm * 5 + 3) * 1024 + lo16, acc[3]); stsf4(L0 + L_RED5 + (wm * 5 + 4) * 1024 + lo16, acc[4]); }
        else { stsf4(L0 + L_RED5 + (wm * 5 + 0) * 1024 + lo16, acc[0]); stsf4(L0 + L_RED5 + (wm * 5 + 1) * 1024 + lo16, acc[1]); stsf4(L0 + L_RED5 + (wm * 5 + 2) * 1024 + lo16, acc[2]); }
        BAR();
#pragma unroll
        for (int n = 0; n < 3; ++n) {
            if (n < ntn) {
                const int nt = nt0 + n;
                const f32x4 mine = wk == 0 ? acc[n] : acc[n < 2 ? 3 + n : 4];
                const f32x4 v = mine + ldsf4(L0 + L_RED5 + (wm * 5 + nt) * 1024 + lo16);
                const f32x4 gb = ldsf4(L0 + L_GB + (16 * nt + 4 * g) * 4);
                const u32x2 o = {pack2(bflo(x1k[n][0]) + gb[0] * v[0], bfhi(x1k[n][0]) + gb[1] * v[1]), pack2(bflo(x1k[n][1]) + gb[2] * v[2], bfhi(x1k[n][1]) + gb[3] * v[3])};
                sts64(L0 + L_OS + (16 * wm + i16) * 160 + 32 * nt + 8 * g, o);
            }
        }
        BAR();
#pragma unroll
        for (int i = 0; i < 2; ++i) {
            const int id = tid + 512 * i;
            if (id < 640) {
                const int r = (id * 6554) >> 16, c = id - 10 * r;
                *(GAS u32x4*)((char*)p.out + (rb * 128 + 64 * hf + r) * 1280 + cb * 160 + c * 16) = lds128(L0 + L_OS + id * 16);
            }
        }
    }
    STAMP(16); STAMP_RT(63); STAMP_OUT();
    if (it < NIT - 1) {
        VMCNT(0); BAR();
        if (tid == 0) st_agent(ws + WS_DONE + b * FS, tag);
        if (w < 4) poll_at(ws, WS_DONE + tid * FS, l, tag, ws, 20);
        BAR();
    } else if (rb == 15 && j == 0) {
        if (w < 4) poll_at(ws, WS_FO + tid * FS, l, tag, ws, 10);
        BAR();
        if (tid == 0) st_agent(ws + WS_EPOCH, tag);
    }
}
__global__ __launch_bounds__(512, 2) void mega_kernel(Params p) {
    __shared__ __attribute__((aligned(16))) char ldsmem[163840];
    const u32 L0 = (u32)(uintptr_t)(LAS char*)ldsmem;
    body<0, NITER>(p, L0);
    if constexpr (NITER > 1) body<1, NITER>(p, L0);
}

extern "C" void kernel_launch(void* const* d_in, const int* in_sizes, int n_in, void* d_out, int out_size, void* d_ws, size_t ws_size, hipStream_t stream) {
    Params p;
    p.x = (const u16*)d_in[0]; p.wqkv = (const u16*)d_in[1]; p.wo = (const u16*)d_in[2];
    p.wg = (const u16*)d_in[4]; p.wu = (const u16*)d_in[5]; p.wd = (const u16*)d_in[6];
    p.c = (const u16*)d_in[7]; p.wmod = (const u16*)d_in[8];
    p.out = (u16*)d_out; p.ws = (char*)d_ws;
    mega_kernel<<<256, 512, 0, stream>>>(p);
}
```

```cpp
#include <hip/hip_runtime.h>
#include <hip/hip_bf16.h>
#include <stdint.h>
#include <type_traits>

typedef unsigned short u16;
typedef unsigned u32;
typedef unsigned long long u64;
typedef __attribute__((ext_vector_type(2))) unsigned u32x2;
typedef __attribute__((ext_vector_type(4))) unsigned u32x4;
typedef __attribute__((ext_vector_type(2))) float f32x2;
typedef __attribute__((ext_vector_type(4))) float f32x4;
typedef __attribute__((ext_vector_type(2))) __bf16 bf16x2;
typedef __attribute__((ext_vector_type(8))) __bf16 bf16x8;
typedef __attribute__((ext_vector_type(4))) short s16x4;
#define LAS __attribute__((address_space(3)))
#define GAS __attribute__((address_space(1)))
#define DI __device__ __forceinline__

constexpr int SEQ = 2048, DM = 640, FF = 1728;
constexpr float QSCALE = 0.22360679774997896f * 1.4426950408889634f;
constexpr float LOG2E = 1.4426950408889634f;

constexpr u32 WS_EPOCH = 0;
constexpr u32 WS_ERR   = 256;
constexpr u32 FS = 128;
constexpr u32 WS_FKV   = 4096;
constexpr u32 WS_FO    = WS_FKV + 256 * FS;
constexpr u32 WS_FX1   = WS_FO + 256 * FS;
constexpr u32 WS_FACT  = WS_FX1 + 256 * FS;
constexpr u32 WS_FH    = WS_FACT + 256 * FS;
constexpr u32 WS_FWT   = WS_FH + 256 * FS;
constexpr u32 WS_FWP   = WS_FWT + 256 * FS;
constexpr u32 WS_FW2   = WS_FWP + 256 * FS;
constexpr u32 WS_MODG  = WS_FW2 + 256 * FS;
constexpr u32 WS_UV    = WS_MODG + 61440;
constexpr u32 WS_STAT  = WS_UV + 114688;
constexpr u32 WS_KV    = 589824;
constexpr u32 WS_O     = WS_KV + 256 * 22528;
constexpr u32 WS_X1    = WS_O + SEQ * DM * 2;
constexpr u32 WS_ACT   = WS_X1 + SEQ * DM * 2;
constexpr u32 WS_WP    = WS_ACT + SEQ * FF * 2;
constexpr u32 WS_H     = WS_WP + 16 * 20 * 14 * 1024;
constexpr u32 WS_WD    = WS_H + SEQ * DM * 2;
constexpr u32 WS_WO    = WS_WD + 8 * 54 * 5 * 1024;
constexpr u32 WS_WT    = WS_WO + 8 * 20 * 5 * 1024;
constexpr u32 WS_STAMP = WS_WT + 16 * 153600;
constexpr u32 WS_END   = WS_STAMP + 256 * 8 * 64 * 4;

constexpr u32 L_WKV = 0, L_WQ = 102400, L_GS = 153600, L_STP = 156160, L_MODP = 156416, L_CNT = 157440, L_SB = 157696, L_G2 = 158016;
constexpr u32 L_ODRAW = 101376, L_WTRAW = 116736, L_XS = 133120;
constexpr u32 L_K = 0, L_V = 30720, L_Q = 67584, L_WPRAW = 77824, L_UVL = 98304;
constexpr u32 L_WO = 61440, L_RED3 = 0, L_X1S = 20480, L_XR = 30720, L_GA = 40960, L_ST3 = 41472, L_WDRAW = 43008;
constexpr u32 L_R4 = 0, R4_STAGE = 45056, L_ST4 = 135168, L_UV = 143360, L_MR = 150528, L_XCH = 151552, L_UVS = 159744;
constexpr u32 L_RED5 = 126976, L_GB = 147456, L_OS = 148480;
constexpr u32 r5_slot(int t) { return t % 3 == 0 ? 45056 : t % 3 == 1 ? 86016 : 0; }

struct Params {
    const u16* x; const u16* wqkv; const u16* wo; const u16* wg; const u16* wu; const u16* wd;
    const u16* c; const u16* wmod; u16* out; char* ws;
};

DI float bflo(u32 d) { return __uint_as_float(d << 16); }
DI float bfhi(u32 d) { return __uint_as_float(d & 0xffff0000u); }
DI u32 pack2(float a, float b) { f32x2 v = {a, b}; return __builtin_bit_cast(u32, __builtin_convertvector(v, bf16x2)); }
DI float rbf(float a) { return bflo(pack2(a, a)); }
DI f32x4 mfma(u32x4 a, u32x4 b, f32x4 c) {
    return __builtin_amdgcn_mfma_f32_16x16x32_bf16(__builtin_bit_cast(bf16x8, a), __builtin_bit_cast(bf16x8, b), c, 0, 0, 0);
}
DI u32x2 ldtr(u32 a) { return __builtin_bit_cast(u32x2, __builtin_amdgcn_ds_read_tr16_b64_v4i16((LAS s16x4*)(size_t)a)); }
DI u32x4 ldtr2(u32 a0, u32 a1) { u32x2 x = ldtr(a0), y = ldtr(a1); return (u32x4){x[0], x[1], y[0], y[1]}; }
DI u32x4 lds128(u32 a) { return *(LAS u32x4*)(size_t)a; }
DI u32x2 lds64(u32 a) { return *(LAS u32x2*)(size_t)a; }
DI f32x4 ldsf4(u32 a) { return *(LAS f32x4*)(size_t)a; }
DI float ldsf(u32 a) { return *(LAS float*)(size_t)a; }
DI void sts128(u32 a, u32x4 v) { *(LAS u32x4*)(size_t)a = v; }
DI void sts64(u32 a, u32x2 v) { *(LAS u32x2*)(size_t)a = v; }
DI void stsf(u32 a, float v) { *(LAS float*)(size_t)a = v; }
DI void stsf4(u32 a, f32x4 v) { *(LAS f32x4*)(size_t)a = v; }
DI void stsf2(u32 a, f32x2 v) { *(LAS f32x2*)(size_t)a = v; }
DI void sts16(u32 a, u16 v) { *(LAS u16*)(size_t)a = v; }

DI void glds16_(const void* root, u32 voff, u32 ldsdst) {
    asm volatile("s_mov_b32 m0, %2\n\ts_nop 0\n\tglobal_load_lds_dwordx4 %0, %1" :: "v"(voff), "s"(root), "s"(__builtin_amdgcn_readfirstlane(ldsdst)) : "memory");
}
DI void glds16c_(const void* root, u32 voff, u32 ldsdst) {
    asm volatile("s_mov_b32 m0, %2\n\ts_nop 0\n\tglobal_load_lds_dwordx4 %0, %1 sc1" :: "v"(voff), "s"(root), "s"(__builtin_amdgcn_readfirstlane(ldsdst)) : "memory");
}
template <int OFF> DI void glds16c_next_(const void* root, u32 voff) {
    asm volatile("global_load_lds_dwordx4 %0, %1 offset:%2 sc1" :: "v"(voff), "s"(root), "i"(OFF) : "memory");
}
template <int OFF> DI u32x4 gld16(const void* root, u32 voff) {
    u32x4 r; asm volatile("global_load_dwordx4 %0, %1, %2 offset:%3" : "=v"(r) : "v"(voff), "s"(root), "i"(OFF) : "memory"); return r;
}
template <int OFF> DI u32x4 gld16c_(const void* root, u32 voff) {
    u32x4 r; asm volatile("global_load_dwordx4 %0, %1, %2 offset:%3 sc1" : "=v"(r) : "v"(voff), "s"(root), "i"(OFF) : "memory"); return r;
}
#define ROFF(root, ptr) ((u32)((const char*)(ptr) - (const char*)(root)))
#define glds16(root, ptr, voff, dst) glds16_((root), ROFF(root, ptr) + (voff), (dst))
#define glds16c(ptr, voff, dst) glds16c_(ws, ROFF(ws, ptr) + (voff), (dst))
#define glds16c_next(OFF, ptr, voff) glds16c_next_<(OFF)>(ws, ROFF(ws, ptr) + (voff))
#define gld16c(OFF, ptr, voff) gld16c_<(OFF)>(ws, ROFF(ws, ptr) + (voff))
DI u32 gldu16(const void* sbase, u32 voff) {
    u32 r; asm volatile("global_load_ushort %0, %1, %2" : "=v"(r) : "v"(voff), "s"(sbase) : "memory"); return r;
}
#define VMCNT(n) asm volatile("s_waitcnt vmcnt(" #n ")" ::: "memory")
#define BAR() asm volatile("s_waitcnt lgkmcnt(0)\n\ts_barrier" ::: "memory")
#define RLX_AGENT __ATOMIC_RELAXED, __HIP_MEMORY_SCOPE_AGENT
DI u64 ld_agent(const char* p) { return __hip_atomic_load((GAS u64*)p, RLX_AGENT); }
DI void st_agent(char* p, u64 v) { __hip_atomic_store((GAS u64*)p, v, RLX_AGENT); }
DI void st_agent32(char* p, u32 v) { __hip_atomic_store((GAS u32*)p, v, RLX_AGENT); }
DI float ld_agentf(const char* p) { return __uint_as_float(__hip_atomic_load((GAS u32*)p, RLX_AGENT)); }
DI float xsum4(float x) {
    const auto s = __builtin_amdgcn_permlane16_swap(__float_as_uint(x), __float_as_uint(x), false, false);
    x = __uint_as_float(s[0]) + __uint_as_float(s[1]);
    const auto t = __builtin_amdgcn_permlane32_swap(__float_as_uint(x), __float_as_uint(x), false, false);
    return __uint_as_float(t[0]) + __uint_as_float(t[1]);
}
DI float xmax4(float x) {
    const auto s = __builtin_amdgcn_permlane16_swap(__float_as_uint(x), __float_as_uint(x), false, false);
    x = fmaxf(__uint_as_float(s[0]), __uint_as_float(s[1]));
    const auto t = __builtin_amdgcn_permlane32_swap(__float_as_uint(x), __float_as_uint(x), false, false);
    return fmaxf(__uint_as_float(t[0]), __uint_as_float(t[1]));
}
template <int CTRL> DI float dppf(float v) { return __builtin_bit_cast(float, __builtin_amdgcn_update_dpp(0, __builtin_bit_cast(int, v), CTRL, 0xf, 0xf, true)); }

constexpr u32 SPIN_CAP = 1u << 17;
DI void poll_flags(const char* base, int n, int lane, u64 tag, char* ws, u32 code) {
    const char* a = base + (lane < n ? lane : 0) * FS;
    u32 spins = 0;
    for (;;) {
        const u64 v = ld_agent(a);
        if (__all(v == tag)) break;
        if (++spins > SPIN_CAP) { if (lane == 0) st_agent32(ws + WS_ERR, code); break; }
        __builtin_amdgcn_s_sleep(1);
    }
}
typedef __attribute__((ext_vector_type(2))) _Float16 f16x2;
DI float hlo(u32 d) { return (float)__builtin_bit_cast(f16x2, d)[0]; }
DI float hhi(u32 d) { return (float)__builtin_bit_cast(f16x2, d)[1]; }
DI u32 packh(float a, float b) { const f16x2 v = {(_Float16)a, (_Float16)b}; return __builtin_bit_cast(u32, v); }
DI bool gran_ok(const u32x4& v, u64 tag) { return v[2] == (u32)tag && v[3] == (u32)(tag >> 32); }
DI f32x4 gran_sum(const u32x4& a, const u32x4& b, const u32x4& c, const u32x4& d) {
    const f32x4 s = {(hlo(a[0]) + hlo(b[0])) + (hlo(c[0]) + hlo(d[0])), (hhi(a[0]) + hhi(b[0])) + (hhi(c[0]) + hhi(d[0])),
                     (hlo(a[1]) + hlo(b[1])) + (hlo(c[1]) + hlo(d[1])), (hhi(a[1]) + hhi(b[1])) + (hhi(c[1]) + hhi(d[1]))};
    return (f32x4){rbf(s[0]), rbf(s[1]), rbf(s[2]), rbf(s[3])};
}
DI f32x4 gather1(const char* ws, u32 g0, u64 tag, char* wsw, u32 code) {
    u32 spins = 0; u32x4 v0, v1, v2, v3;
    const u32 o = WS_MODG + g0 * 16;
    for (;;) {
        asm volatile("s_nop 4\n\tglobal_load_dwordx4 %0, %4, %5 sc1\n\tglobal_load_dwordx4 %1, %6, %5 sc1\n\tglobal_load_dwordx4 %2, %7, %5 sc1\n\tglobal_load_dwordx4 %3, %8, %5 sc1\n\ts_waitcnt vmcnt(0)"
                     : "=&v"(v0), "=&v"(v1), "=&v"(v2), "=&v"(v3) : "v"(o), "s"(ws), "v"(o + 15360), "v"(o + 30720), "v"(o + 46080) : "memory");
        if (__all(gran_ok(v0, tag) && gran_ok(v1, tag) && gran_ok(v2, tag) && gran_ok(v3, tag))) break;
        if (++spins > SPIN_CAP) { st_agent32(wsw + WS_ERR, code); break; }
        __builtin_amdgcn_s_sleep(1);
    }
    return gran_sum(v0, v1, v2, v3);
}
DI void gather2(const char* ws, u32 g0, u32 g1, u64 tag, char* wsw, u32 code, f32x4& ra, f32x4& rb) {
    u32 spins = 0; u32x4 v0, v1, v2, v3, u0, u1, u2, u3;
    const u32 o = WS_MODG + g0 * 16, q = WS_MODG + g1 * 16;
    for (;;) {
        asm volatile("s_nop 4\n\tglobal_load_dwordx4 %0, %8, %9 sc1\n\tglobal_load_dwordx4 %1, %10, %9 sc1\n\tglobal_load_dwordx4 %2, %11, %9 sc1\n\tglobal_load_dwordx4 %3, %12, %9 sc1\n\t"
                     "global_load_dwordx4 %4, %13, %9 sc1\n\tglobal_load_dwordx4 %5, %14, %9 sc1\n\tglobal_load_dwordx4 %6, %15, %9 sc1\n\tglobal_load_dwordx4 %7, %16, %9 sc1\n\ts_waitcnt vmcnt(0)"
                     : "=&v"(v0), "=&v"(v1), "=&v"(v2), "=&v"(v3), "=&v"(u0), "=&v"(u1), "=&v"(u2), "=&v"(u3)
                     : "v"(o), "s"(ws), "v"(o + 15360), "v"(o + 30720), "v"(o + 46080), "v"(q), "v"(q + 15360), "v"(q + 30720), "v"(q + 46080) : "memory");
        if (__all(gran_ok(v0, tag) && gran_ok(v1, tag) && gran_ok(v2, tag) && gran_ok(v3, tag) && gran_ok(u0, tag) && gran_ok(u1, tag) && gran_ok(u2, tag) && gran_ok(u3, tag))) break;
        if (++spins > SPIN_CAP) { st_agent32(wsw + WS_ERR, code); break; }
        __builtin_amdgcn_s_sleep(1);
    }
    ra = gran_sum(v0, v1, v2, v3); rb = gran_sum(u0, u1, u2, u3);
}
DI void gather_strip(const char* ws, u32 gs, u32 gc, u32 strip, int l, u64 tag, char* wsw, u32 code) {
    f32x4 a, c;
    gather2(ws, l < 40 ? gs + l : gc + (l - 40), gc + 24 + (l & 15), tag, wsw, code, a, c);
    if (l >= 40) a = a + 1.f;
    c = c + 1.f;
    *(LAS u32x2*)(size_t)(strip + (l < 40 ? l * 8 : 320 + (l - 40) * 8)) = (u32x2){pack2(a[0], a[1]), pack2(a[2], a[3])};
    if (l < 16) *(LAS u32x2*)(size_t)(strip + 320 + (24 + l) * 8) = (u32x2){pack2(c[0], c[1]), pack2(c[2], c[3])};
}
DI void poll_at(const char* ws, u32 off, int lane, u64 tag, char* wsw, u32 code) {
    u32 spins = 0;
    for (;;) {
        const u64 v = ld_agent(ws + off);
        if (__all(v == tag)) break;
        if (++spins > SPIN_CAP) { if (lane == 0) st_agent32(wsw + WS_ERR, code); break; }
        __builtin_amdgcn_s_sleep(1);
    }
}

#define STAMP(i) do {} while (0)
#define STAMPL(i) do {} while (0)
#define STAMP_RT(i) do {} while (0)
#define STAMP_OUT() do {} while (0)

template <int N> using IC = std::integral_constant<int, N>;
template <int I, int E, class F> DI void static_for(F&& f) { if constexpr (I < E) { f(IC<I>{}); static_for<I + 1, E>(f); } }
template <int N> DI void vmw() { asm volatile("s_waitcnt vmcnt(%0)" :: "i"(N) : "memory"); }
template <int N> DI void vmw1(u32x4& a) { asm volatile("s_waitcnt vmcnt(%1)" : "+v"(a) : "i"(N) : "memory"); }
template <int N> DI void vmw4(u32x4& a, u32x4& b, u32x4& c, u32x4& d) { asm volatile("s_waitcnt vmcnt(%4)" : "+v"(a), "+v"(b), "+v"(c), "+v"(d) : "i"(N) : "memory"); }
DI u32 opaque(u32 a) { asm("" : "+v"(a)); return a; }
DI float dot2(u32 a, u32 b, float c) { return __builtin_amdgcn_fdot2_f32_bf16(__builtin_bit_cast(bf16x2, a), __builtin_bit_cast(bf16x2, b), c, false); }
#define SB0() __builtin_amdgcn_sched_barrier(0)
#define SGB(m, n) __builtin_amdgcn_sched_group_barrier((m), (n), 0)
constexpr int M_MFMA = 0x8, M_DSR = 0x100;
#define Z4 ((f32x4){0.f, 0.f, 0.f, 0.f})
DI u32 lds_add(u32 a, u32 v) { return __hip_atomic_fetch_add((LAS u32*)(size_t)a, v, __ATOMIC_RELAXED, __HIP_MEMORY_SCOPE_WORKGROUP); }
DI u32 lds_ld(u32 a) { return __hip_atomic_load((LAS u32*)(size_t)a, __ATOMIC_RELAXED, __HIP_MEMORY_SCOPE_WORKGROUP); }
DI void worker_sync(u32 cnt, int lane) {
    asm volatile("s_waitcnt lgkmcnt(0)" ::: "memory");
    if (lane == 0) lds_add(cnt, 1u);
    for (u32 spins = 0; lds_ld(cnt) < 4u && spins < (1u << 22); ++spins) {}
    asm volatile("" ::: "memory");
}

__global__ __launch_bounds__(512, 2) void mega_kernel(Params p) {
    __shared__ __attribute__((aligned(16))) char ldsmem[163840];
    const u32 L0 = (u32)(uintptr_t)(LAS char*)ldsmem;
    const int tid = threadIdx.x, l = tid & 63;
    const int w = __builtin_amdgcn_readfirstlane(tid >> 6);
    const int b = blockIdx.x, xg = b & 7, sidx = b >> 3;
    const int rb = 4 * (xg >> 1) + (sidx >> 3), j = 8 * (xg & 1) + (sidx & 7);
    const int g = l >> 4, i16 = l & 15, q4 = i16 >> 2, p4 = l & 3;
    char* const ws = p.ws;
    asm volatile("" :: "s"(p.x), "s"(p.wqkv), "s"(p.wo), "s"(p.wg), "s"(p.wu), "s"(p.wd), "s"(p.c), "s"(p.wmod), "s"(p.out), "s"(p.ws));
    u64 tag;
#define ASK_TAG(ep) u32x2 ep; asm volatile("global_load_dwordx2 %0, %1, %2" : "=v"(ep) : "v"(WS_EPOCH), "s"(ws) : "memory")
#define TAKE_TAG(ep) do { asm volatile("s_waitcnt vmcnt(0)" : "+v"(ep) :: "memory"); \
        tag = (((u64)(u32)__builtin_amdgcn_readfirstlane(ep[1]) << 32) | (u32)__builtin_amdgcn_readfirstlane(ep[0])) + 1; } while (0)
    const __amdgpu_buffer_rsrc_t wsr = __builtin_amdgcn_make_buffer_rsrc(ws, 0, WS_END, 0x00020000);
#define ST16C(v, off) __builtin_amdgcn_raw_buffer_store_b128((v), wsr, (off), 0, 16)
#define ST8C(v, off)  __builtin_amdgcn_raw_buffer_store_b64((v), wsr, (off), 0, 16)
    STAMP_RT(62); STAMP(0);

    u32 wu0 = 0, wu1 = 0, wu2 = 0;
    if (w == 7) {
        const char* a0 = ws + (size_t)min(l, 31) * (1u << 20);
        const char* a1 = l < 3 ? (const char*)p.x + l * (1u << 20) : l < 6 ? (const char*)p.wqkv + (l - 3) * (1u << 20) : l < 7 ? (const char*)p.wo
                       : l < 10 ? (const char*)p.wg + (l - 7) * (1u << 20) : l < 13 ? (const char*)p.wu + (l - 10) * (1u << 20) : l < 16 ? (const char*)p.wd + (l - 13) * (1u << 20)
                       : l < 21 ? (const char*)p.wmod + (l - 16) * (1u << 20) : l < 24 ? (const char*)p.out + (l - 21) * (1u << 20) : (const char*)p.c;
        const char* pc; asm volatile("s_getpc_b64 %0" : "=s"(pc));
        const char* a2 = pc + (size_t)(sidx * 11 + min(l, 10)) * 128;
        asm volatile("global_load_dword %0, %1, off" : "=&v"(wu2) : "v"(a2) : "memory"); (void)a0; (void)a1;
    }
    const int nj = j < 12 ? 7 : 6, Gj = j < 12 ? 7 * j : 84 + 6 * (j - 12);
    const bool wpwg = b < 216;
    const int wpm = b >= 108 ? 1 : 0, wpb = (b - 108 * wpm) >> 2, wpq = b & 3;
    if (w >= 4) {
        __builtin_amdgcn_s_setprio(3);
        const int u = w - 4;
        const bool tw = b >= 106;
        const int tb = (b - 106) / 5, tk = (b - 106) - 5 * tb, ts = 4 * tk + u;
        if (tw) {
            const char* src = (const char*)p.wqkv + ts * 122880 + tb * 128;
            const u32 voff = (l >> 3) * 3840 + (l & 7) * 16;
#pragma unroll
            for (int i = 0; i < 4; ++i) glds16(p.wqkv, src + i * 8 * 3840, voff, L0 + L_WTRAW + u * 4096 + i * 1024);
        }
        ASK_TAG(epl);
        BAR();
        __builtin_amdgcn_s_setprio(0);
        TAKE_TAG(epl);
        if (tw) {
            const u32 a = L0 + L_WTRAW + u * 4096 + (8 * g + q4) * 128 + 8 * p4;
#pragma unroll
            for (int t = 0; t < 4; ++t) {
                const u32 cc = 8 * tb + 2 * t + (i16 >> 3);
                const u32 seg = cc >= 160 ? 2 : cc >= 80 ? 1 : 0, wi = cc - 80 * seg, jj = (wi * 13108) >> 16, c5 = wi - 5 * jj;
                const u32 c = seg == 0 ? 10 + c5 : seg == 1 ? c5 : 5 + c5, nt = c >> 1;
                const u32 dst = WS_WT + jj * 153600 + (nt < 5 ? ts * 5120 + nt * 1024 : 102400 + ts * 2560 + (nt - 5) * 1024)
                              + (nt < 7 ? (g * 16 + 8 * (c & 1) + (i16 & 7)) * 16 : g * 128 + (i16 & 7) * 16);
                ST16C(ldtr2(a + 32 * t, a + 32 * t + 512), dst);
            }
        }
        vmw<0>();
        {
            asm volatile("s_waitcnt lgkmcnt(0)" ::: "memory");
            u32 old = 0;
            if (l == 0) old = lds_add(L0 + L_CNT + 12, 1u);
            if (tw && __builtin_amdgcn_readfirstlane(old) == 3 && l == 0) st_agent(ws + WS_FWT + (b - 106) * FS, tag);
        }
        STAMPL(19);
        if (u == 0) {
            const int wh = l >= 30 ? 0 : l / 5, kr = l >= 30 ? 0 : l - 5 * wh;
            const int bl = (1280 * (wh >> 1) + 80 * j + 79 * (wh & 1)) >> 7;
            poll_at(ws, WS_FWT + (bl * 5 + kr) * FS, l, tag, ws, 13);
            for (u32 spins = 0; lds_ld(L0 + L_CNT + 12) < 4u && spins < (1u << 22); ++spins) {}
            if (l == 0) lds_add(L0 + L_CNT + 24, 1u);
        } else {
            for (u32 spins = 0; lds_ld(L0 + L_CNT + 24) < 1u && spins < (1u << 22); ++spins) {}
        }
        asm volatile("" ::: "memory");
        STAMPL(20);
        {
            const char* src = ws + WS_WT + j * 153600 + u * 1024;
            const u32 lo = l * 16;
#pragma unroll
            for (int i = 0; i < 25; ++i) glds16c(src + i * 4096, lo, L0 + L_WKV + u * 1024 + i * 4096);
        }
        if (wpwg && u == 0) gather_strip(ws, 480 + 40 * wpq, 640 + 40 * wpq, L0 + L_SB, l, tag, ws, 2);
        VMCNT(0);
        STAMPL(17);
    } else {
        __builtin_amdgcn_s_setprio(2);
        const bool modwg = b < 240;
        u32x4 mw[5]; u32 mc[5];
        if (modwg) {
#pragma unroll
            for (int i = 0; i < 5; ++i) {
                const int r = 160 * (b & 3) + (tid >> 3) + 32 * i;
                mw[i] = gld16<0>(p.wmod, r * 7680 + (b >> 2) * 128 + (tid & 7) * 16);
                mc[i] = gldu16(p.c, r * 2);
            }
        }
        {
            const char* src = (const char*)p.x + (rb * 128 + 8 * j) * 1280 + w * 1024;
            glds16(p.x, src, l * 16, L0 + L_XS + w * 1024);
            glds16(p.x, src + 4096, l * 16, L0 + L_XS + (w + 4) * 1024);
            if (w < 2) glds16(p.x, src + 8192, l * 16, L0 + L_XS + (w + 8) * 1024);
        }
        ASK_TAG(epw);
        if (tid < 16) *(LAS u32*)(size_t)(L0 + L_CNT + tid * 4) = 0u;
        BAR();
        STAMP(29);
        TAKE_TAG(epw);
        STAMP(30);
        if (modwg) {
            asm volatile("" : "+v"(mw[0]), "+v"(mw[1]), "+v"(mw[2]), "+v"(mw[3]), "+v"(mw[4]), "+v"(mc[0]), "+v"(mc[1]), "+v"(mc[2]), "+v"(mc[3]), "+v"(mc[4]) :: "memory");
            float acc[8];
#pragma unroll
            for (int e = 0; e < 8; ++e) acc[e] = 0.f;
#pragma unroll
            for (int i = 0; i < 5; ++i) {
                const float cv = bflo(mc[i]);
                const float sv = cv * __builtin_amdgcn_rcpf(1.f + __builtin_amdgcn_exp2f(-cv * LOG2E));
                const u32 slo = pack2(sv, 0.f), shi = slo << 16;
#pragma unroll
                for (int d = 0; d < 4; ++d) { acc[2 * d] = dot2(mw[i][d], slo, acc[2 * d]); acc[2 * d + 1] = dot2(mw[i][d], shi, acc[2 * d + 1]); }
            }
#pragma unroll
            for (int e = 0; e < 8; ++e) { const float v = acc[e]; acc[e] = xsum4(v + dppf<0x128>(v)); }
            if (l < 8) {
                stsf4(L0 + L_MODP + w * 256 + l * 32, (f32x4){acc[0], acc[1], acc[2], acc[3]});
                stsf4(L0 + L_MODP + w * 256 + l * 32 + 16, (f32x4){acc[4], acc[5], acc[6], acc[7]});
            }
            asm volatile("s_waitcnt lgkmcnt(0)" ::: "memory");
            u32 old = 0;
            if (l == 0) old = lds_add(L0 + L_CNT, 1u);
            if (__builtin_amdgcn_readfirstlane(old) == 3) {
                float v = 0.f;
#pragma unroll
                for (int ww = 0; ww < 4; ++ww) v += ldsf(L0 + L_MODP + ww * 256 + l * 4);
                const float v1 = dppf<0x55>(v), v2 = dppf<0xAA>(v), v3 = dppf<0xFF>(v), v0 = dppf<0x00>(v);
                const u32x4 gr = {packh(v0, v1), packh(v2, v3), (u32)tag, (u32)(tag >> 32)};
                if (p4 == 0) ST16C(gr, WS_MODG + ((b & 3) * 960 + (b >> 2) * 16 + (l >> 2)) * 16);
            }
        }
        STAMP(1);
        worker_sync(L0 + L_CNT + 16, l);
        const int tok = l & 7;
        u32x4 xr[3];
#pragma unroll
        for (int i = 0; i < 3; ++i) xr[i] = lds128(L0 + L_XS + tok * 1280 + (20 * w + (i < 2 ? (l >> 3) + 8 * i : 16 + ((l >> 3) & 3))) * 16);
        {
            float s1 = 0.f, s2 = 0.f;
#pragma unroll
            for (int i = 0; i < 3; ++i) {
                float a1 = 0.f, a2 = 0.f;
#pragma unroll
                for (int d = 0; d < 4; ++d) { a1 = dot2(xr[i][d], 0x3F803F80u, a1); a2 = dot2(xr[i][d], xr[i][d], a2); }
                if (i < 2 || l < 32) { s1 += a1; s2 += a2; }
            }
            asm volatile("s_waitcnt lgkmcnt(0)" ::: "memory");
            if (l == 0) lds_add(L0 + L_CNT + 20, 1u);
            s1 += dppf<0x128>(s1); s2 += dppf<0x128>(s2);
            s1 = xsum4(s1); s2 = xsum4(s2);
            if (l < 8) stsf2(L0 + L_STP + (w * 8 + l) * 8, (f32x2){s1, s2});
        }
        gather_strip(ws, 40 * w, 160 + 40 * w, L0 + L_GS + w * 640, l, tag, ws, 1);
        STAMP(2);
        worker_sync(L0 + L_CNT + 4, l);
        {
            f32x2 st = {0.f, 0.f};
#pragma unroll
            for (int ww = 0; ww < 4; ++ww) st += *(LAS f32x2*)(size_t)(L0 + L_STP + (ww * 8 + tok) * 8);
            const float mu = st[0] * (1.f / 640.f), var = fmaxf(st[1] * (1.f / 640.f) - mu * mu, 0.f);
            const float rs = __builtin_amdgcn_rsqf(var + 1e-5f), nmr = -mu * rs;
#pragma unroll
            for (int i = 0; i < 3; ++i) {
                const int cl = i < 2 ? (l >> 3) + 8 * i : 16 + ((l >> 3) & 3);
                const u32x4 sa = lds128(L0 + L_GS + w * 640 + cl * 16), gg = lds128(L0 + L_GS + w * 640 + 320 + cl * 16);
                u32x4 h;
#pragma unroll
                for (int d = 0; d < 4; ++d)
                    h[d] = pack2((bflo(xr[i][d]) * rs + nmr) * bflo(gg[d]) + bflo(sa[d]), (bfhi(xr[i][d]) * rs + nmr) * bfhi(gg[d]) + bfhi(sa[d]));
                if (i < 2 || l < 32) ST16C(h, WS_H + (((rb * 8 + (j >> 1)) * 80 + 20 * w + cl) * 16 + 8 * (j & 1) + tok) * 16);
            }
        }
        VMCNT(0);
        worker_sync(L0 + L_CNT + 8, l);
        if (tid == 0) st_agent(ws + WS_FH + (rb * 16 + j) * FS, tag);
        if (w == 0) poll_flags(ws + WS_FH + rb * 16 * FS, 16, l, tag, ws, 11);
        __builtin_amdgcn_s_setprio(0);
    }
    BAR();
    asm volatile("s_waitcnt vmcnt(%3)" : "+v"(wu0), "+v"(wu1), "+v"(wu2) : "i"(0) : "memory");
    STAMP(3);

    u32x4 xa[20];
    const u32 lo16 = l * 16;
    const char* hsrc = ws + WS_H + (rb * 8 + w) * 80 * 256;
#define XL(s) xa[s] = gld16c(((s) & 3) * 1024, hsrc + ((s) >> 2) * 4096, lo16)
    XL(0); XL(1); XL(2); XL(3); XL(4); XL(5);
    const char* kvx = ws + WS_KV + (rb * 16 + j) * 22528;
    const char* wtq = ws + WS_WT + j * 153600 + (100 + w) * 1024;
    {
        f32x4 acc[5];
#pragma unroll
        for (int n = 0; n < 5; ++n) acc[n] = Z4;
        const u32 b0 = L0 + L_WKV + lo16;
        u32x4 bf[2][5];
#pragma unroll
        for (int n = 0; n < 5; ++n) bf[0][n] = lds128(b0 + n * 1024);
        static_for<0, 20>([&](auto S) {
            constexpr int s = S;
            vmw1<(s < 14 ? 5 : 6)>(xa[s]);
            if constexpr (s + 6 < 20) XL(s + 6);
            if constexpr (s >= 13) { if (s < 19 || w < 2) glds16c(wtq + (s - 13) * 8192, lo16, L0 + L_WKV + (100 + w) * 1024 + (s - 13) * 8192); }
            SB0();
#pragma unroll
            for (int n = 0; n < 5; ++n) {
                if (s < 19) bf[(s + 1) & 1][n] = lds128(b0 + (s + 1) * 5120 + n * 1024);
                acc[n] = mfma(bf[s & 1][n], xa[s], acc[n]);
            }
#pragma unroll
            for (int n = 0; n < 5; ++n) { SGB(M_DSR, 1); SGB(M_MFMA, 1); }
            SB0();
        });
        STAMP(18);
        BAR();
        const int row = 256 + 16 * w + i16;
#pragma unroll
        for (int n = 0; n < 5; ++n) {
            const int Q = 4 * n + g;
            const u32x2 v = {pack2(acc[n][0], acc[n][1]), pack2(acc[n][2], acc[n][3])};
            const int wi = Q - 10, hd = wi >= 5 ? 1 : 0;
            const u32 a = Q < 10 ? L_K + row * 80 + 8 * Q : L_V + row * 96 + 48 * hd + 8 * (wi - 5 * hd);
            sts64(L0 + a, v);
        }
        if (g < 2) sts64(L0 + L_V + row * 96 + 48 * g + 40, (u32x2){0x3F80u, 0u});
        if (rb < 2) {
            const int nrow = rb == 0 ? 256 : 128;
            for (int i = tid; i < nrow * 5; i += 512) sts128(L0 + L_K + i * 16, (u32x4){0, 0, 0, 0});
            for (int i = tid; i < nrow * 6; i += 512) sts128(L0 + L_V + i * 16, (u32x4){0, 0, 0, 0});
        }
        vmw<0>();
        BAR();
    }
#undef XL
    STAMP(4);
    {
        u32x4 kvd[3];
#pragma unroll
        for (int e = 0; e < 3; ++e) {
            const int n = min(w + 8 * e, 21);
            kvd[e] = lds128(L0 + (n < 10 ? L_K + 20480 + n * 1024 : L_V + 24576 + (n - 10) * 1024) + lo16);
        }
        const u32 kvo = WS_KV + (rb * 16 + j) * 22528 + w * 1024 + lo16;
        const u16* wroot = wpm ? p.wu : p.wg;
        const char* wsrc = (const char*)wroot + (160 * wpq + 8 * w) * 3456 + wpb * 128;
        const u32 wvo = (l >> 3) * 3456 + (l & 7) * 16;
        f32x4 acc[3];
#pragma unroll
        for (int n = 0; n < 3; ++n) acc[n] = Z4;
        const u32 bq = L0 + L_WQ + lo16, bq2 = L0 + L_WQ + 2048 + g * 128 + (i16 & 7) * 16;
#define QRD(s, n) lds128(((n) < 2 ? bq + (n) * 1024 : bq2) + (s) * 2560)
        u32x4 bf[3][3];
#pragma unroll
        for (int n = 0; n < 3; ++n) { bf[0][n] = QRD(0, n); bf[1][n] = QRD(1, n); }
        static_for<0, 20>([&](auto S) {
            constexpr int s = S;
            if constexpr (s == 0) { if (rb < 15) ST16C(kvd[0], kvo); }
            if constexpr (s == 1) { if (rb < 15) ST16C(kvd[1], kvo + 8192); }
            if constexpr (s == 2) { if (rb < 15 && w < 6) ST16C(kvd[2], kvo + 16384); }
            if constexpr (s == 14) {
                vmw<0>();
                u32 old = 0;
                if (l == 0) old = lds_add(L0 + L_CNT + 28, 1u);
                if (__builtin_amdgcn_readfirstlane(old) == 7 && l == 0) st_agent(ws + WS_FKV + (j * 16 + rb) * FS, tag);
            }
            if constexpr (s == 15) { if (wpwg) glds16(wroot, wsrc, wvo, L0 + L_WPRAW + w * 1024); }
            if constexpr (s == 16) { if (wpwg) glds16(wroot, wsrc + 64 * 3456, wvo, L0 + L_WPRAW + (w + 8) * 1024); }
            if constexpr (s == 17) { if (wpwg && w < 4) glds16(wroot, wsrc + 128 * 3456, wvo, L0 + L_WPRAW + (w + 16) * 1024); }
            SB0();
#pragma unroll
            for (int n = 0; n < 3; ++n) {
                if (s < 18) bf[(s + 2) % 3][n] = QRD(s + 2, n);
                acc[n] = mfma(bf[s % 3][n], xa[s], acc[n]);
            }
#pragma unroll
            for (int n = 0; n < 3; ++n) { SGB(M_DSR, 1); SGB(M_MFMA, 1); }
            SB0();
        });
#undef QRD
        STAMP(21);
#pragma unroll
        for (int n = 0; n < 3; ++n) {
            const int Q = 4 * n + g;
            const u32x2 v = {pack2(acc[n][0] * QSCALE, acc[n][1] * QSCALE), pack2(acc[n][2] * QSCALE, acc[n][3] * QSCALE)};
            if (Q < 10) sts64(L0 + L_Q + (16 * w + i16) * 80 + 8 * Q, v);
        }
    }
    if (w == 0 && rb >= 1) poll_flags(ws + WS_FKV + (j * 16 + (rb >= 2 ? rb - 2 : rb - 1)) * FS, rb >= 2 ? 2 : 1, l, tag, ws, 3);
    VMCNT(0);
    BAR();
    STAMP(5);
    if (rb >= 1) {
#pragma unroll
        for (int e = 0; e < 3; ++e) {
            const int n = w + 8 * e;
            if (n < 22) {
                const u32 d = n < 10 ? L_K + n * 1024 : L_V + (n - 10) * 1024, st = n < 10 ? 10240 : 12288;
                glds16c(kvx - 16 * 22528 + n * 1024, lo16, L0 + d + st);
                if (rb >= 2) glds16c(kvx - 32 * 22528 + n * 1024, lo16, L0 + d);
            }
        }
    }
    auto wp_slot = [&](int gam, int& jj, int& slot) {
        jj = gam < 84 ? gam / 7 : 12 + (gam - 84) / 6;
        const int ar = gam - (jj < 12 ? 7 * jj : 84 + 6 * (jj - 12)), a = (jj < 12 || ar < 3) ? ar : ar + 1;
        slot = a < 3 ? 2 * a + wpm : a == 3 ? (wpm ? 13 : 6) : 7 + 2 * (a - 4) + wpm;
    };
    if (wpwg) {
#pragma unroll
        for (int i = 0; i < 3; ++i) {
            if (i < 2 || w < 4) {
                const int f = w + 8 * i, ks = f >> 2, t16 = f & 3;
                int jj, slot; wp_slot(4 * wpb + t16, jj, slot);
                const u32 a = L0 + L_WPRAW + (32 * ks + 8 * g + q4) * 128 + 32 * t16 + 8 * p4;
                const u32x4 wr = ldtr2(a, a + 512);
                const u32x4 g2 = lds128(L0 + L_G2 + (32 * ks + 8 * g) * 2), sb = lds128(L0 + L_SB + (32 * ks + 8 * g) * 2);
                u32x4 o; float us = 0.f, vs = 0.f;
#pragma unroll
                for (int d = 0; d < 4; ++d) {
                    o[d] = pack2(dot2(wr[d], g2[d] & 0xffffu, 0.f), dot2(wr[d], g2[d] & 0xffff0000u, 0.f));
                    us = dot2(o[d], 0x3F803F80u, us);
                    vs = dot2(wr[d], sb[d], vs);
                }
                ST16C(o, WS_WP + ((jj * 20 + 5 * wpq + ks) * 14 + slot) * 1024 + lo16);
                us = xsum4(us); vs = xsum4(vs);
                if (l < 32) stsf(L0 + L_UVL + f * 128 + l * 4, l < 16 ? us : vs);
            }
        }
    }
    if (wpwg) { if (w < 4) vmw<3>(); else vmw<2>(); } else vmw<0>();
    BAR();
    STAMP(6);
    if (wpwg && w == 3) {
#pragma unroll
        for (int e = 0; e < 2; ++e) {
            const int t16 = 2 * e + (l >> 5);
            int jj, slot; wp_slot(4 * wpb + t16, jj, slot);
            float v = 0.f;
#pragma unroll
            for (int ks = 0; ks < 5; ++ks) v += ldsf(L0 + L_UVL + (4 * ks + t16) * 128 + (l & 31) * 4);
            st_agent32(ws + WS_UV + ((jj * 14 + slot) * 4 + wpq) * 128 + (l & 31) * 4, __float_as_uint(v));
        }
    }
    const int hf = j & 1, cb = j >> 1;
    u32 wvoff[5];
#pragma unroll
    for (int i = 0; i < 5; ++i) {
        const u32 sg = 64 * i + l, pos = (sg * 6554) >> 16, c = sg - pos * 10, k = pos ^ (((pos >> 3) & 1) << 2);
        wvoff[i] = k * 1280 + c * 16;
    }
    const char* wosrc = (const char*)p.wo + cb * 160;
#define WO_STEP(sx) do { _Pragma("unroll") for (int i = 0; i < 5; ++i) glds16(p.wo, wosrc + (sx) * 32 * 1280, wvoff[i], L0 + L_WO + (sx) * 5120 + i * 1024); } while (0)
    STAMP(7);

    {
        const int hd = w & 1, qi = w >> 1;
        const int kr0 = 32 * qi;
        u32x4 qf[2];
#pragma unroll
        for (int a = 0; a < 2; ++a) {
            const u32 qa = L0 + L_Q + (kr0 + 16 * a + i16) * 80 + hd * 40;
            const u32x2 q0 = lds64(qa + 8 * g);
            u32x2 q1 = lds64(qa + 32);
            if (g != 0) q1 = (u32x2){0, 0};
            qf[a] = (u32x4){q0[0], q0[1], q1[0], q1[1]};
        }
        const u32 kb0 = L0 + L_K + (kr0 + i16) * 80 + hd * 40 + 8 * g, kb1 = L0 + L_K + (kr0 + i16) * 80 + hd * 40 + 32;
        f32x4 mfirst, mlast;
#pragma unroll
        for (int r = 0; r < 4; ++r) { const bool lo = 4 * g + r <= i16; mfirst[r] = lo ? -INFINITY : 0.f; mlast[r] = lo ? 0.f : -INFINITY; }
        f32x4 s0[17], s1[17];
        {
            u32x4 kf[6];
#define KRD(t) do { const u32x2 k0 = lds64(kb0 + (t) * 1280), k1 = lds64(kb1 + (t) * 1280); kf[(t) % 6] = (u32x4){k0[0], k0[1], k1[0], k1[1]}; } while (0)
            KRD(0); KRD(1); KRD(2); KRD(3); KRD(4);
            static_for<0, 18>([&](auto T) {
                constexpr int t = T;
                SB0();
                if constexpr (t < 13) KRD(t + 5);
                if constexpr (t < 17) s0[t] = mfma(kf[t % 6], qf[0], t == 0 ? mfirst : t == 16 ? mlast : Z4);
                if constexpr (t > 0) s1[t - 1] = mfma(kf[t % 6], qf[1], t == 1 ? mfirst : t == 17 ? mlast : Z4);
                SGB(M_DSR, 2); SGB(M_MFMA, 2);
                SB0();
            });
#undef KRD
        }
        WO_STEP(4 + w);
        if (rb < 2) {
            const int fv = rb == 1 ? 128 : 256;
#pragma unroll
            for (int t = 0; t < 16; ++t) {
                const bool d0 = kr0 + 16 * t < fv, d1 = kr0 + 16 * t + 16 < fv;
#pragma unroll
                for (int r = 0; r < 4; ++r) { s0[t][r] = d0 ? -INFINITY : s0[t][r]; s1[t][r] = d1 ? -INFINITY : s1[t][r]; }
            }
        }
        STAMP(22);
        float m0 = fmaxf(s0[0][0], s0[0][1]), m1 = fmaxf(s1[0][0], s1[0][1]), n0 = fmaxf(s0[0][2], s0[0][3]), n1 = fmaxf(s1[0][2], s1[0][3]);
#pragma unroll
        for (int t = 1; t < 17; ++t) {
            m0 = fmaxf(fmaxf(m0, s0[t][0]), s0[t][1]); m1 = fmaxf(fmaxf(m1, s1[t][0]), s1[t][1]);
            n0 = fmaxf(fmaxf(n0, s0[t][2]), s0[t][3]); n1 = fmaxf(fmaxf(n1, s1[t][2]), s1[t][3]);
        }
        m0 = fmaxf(m0, n0); m1 = fmaxf(m1, n1);
        m0 = xmax4(m0); m1 = xmax4(m1);
        WO_STEP(12 + w);
        STAMP(23);
        f32x4 o00 = Z4, o01 = Z4, o10 = Z4, o11 = Z4;
        const u32 vb = L0 + L_V + (kr0 + 4 * g + q4) * 96 + hd * 48 + 8 * p4;
        auto pv = [&](auto SUB) {
            constexpr bool sub = SUB;
            u32x4 vf[2][2];
            vf[0][0] = ldtr2(vb, vb + 16 * 96); vf[0][1] = ldtr2(vb + 32, vb + 16 * 96 + 32);
#define EX(v, m) __builtin_amdgcn_exp2f(sub ? (v) - (m) : (v))
            static_for<0, 9>([&](auto U) {
                constexpr int u = U;
                if constexpr (u < 8) { const u32 a = vb + (u + 1) * 32 * 96; vf[(u + 1) & 1][0] = ldtr2(a, a + 16 * 96); vf[(u + 1) & 1][1] = ldtr2(a + 32, a + 16 * 96 + 32); }
                u32x4 pa, pb;
                pa[0] = pack2(EX(s0[2 * u][0], m0), EX(s0[2 * u][1], m0)); pa[1] = pack2(EX(s0[2 * u][2], m0), EX(s0[2 * u][3], m0));
                if constexpr (u < 8) { pa[2] = pack2(EX(s0[2 * u + 1][0], m0), EX(s0[2 * u + 1][1], m0)); pa[3] = pack2(EX(s0[2 * u + 1][2], m0), EX(s0[2 * u + 1][3], m0)); }
                else { pa[2] = 0; pa[3] = 0; }
                if constexpr (u > 0) { pb[0] = pack2(EX(s1[2 * u - 1][0], m1), EX(s1[2 * u - 1][1], m1)); pb[1] = pack2(EX(s1[2 * u - 1][2], m1), EX(s1[2 * u - 1][3], m1)); }
                else { pb[0] = 0; pb[1] = 0; }
                pb[2] = pack2(EX(s1[2 * u][0], m1), EX(s1[2 * u][1], m1)); pb[3] = pack2(EX(s1[2 * u][2], m1), EX(s1[2 * u][3], m1));
                o00 = mfma(vf[u & 1][0], pa, o00); o01 = mfma(vf[u & 1][1], pa, o01);
                o10 = mfma(vf[u & 1][0], pb, o10); o11 = mfma(vf[u & 1][1], pb, o11);
                SB0();
            });
#undef EX
        };
        if (__all(fabsf(m0) <= 64.f && fabsf(m1) <= 64.f)) pv(std::false_type{}); else pv(std::true_type{});
        STAMP(24);
        const float i0 = __builtin_amdgcn_rcpf(__shfl(o01[0], i16 + 16)), i1 = __builtin_amdgcn_rcpf(__shfl(o11[0], i16 + 16));
        const u32 oa = L0 + L_Q + (kr0 + i16) * 80 + hd * 40;
        sts64(oa + 8 * g, (u32x2){pack2(o00[0] * i0, o00[1] * i0), pack2(o00[2] * i0, o00[3] * i0)});
        sts64(oa + 1280 + 8 * g, (u32x2){pack2(o10[0] * i1, o10[1] * i1), pack2(o10[2] * i1, o10[3] * i1)});
        if (g == 0) {
            sts64(oa + 32, (u32x2){pack2(o01[0] * i0, o01[1] * i0), pack2(o01[2] * i0, o01[3] * i0)});
            sts64(oa + 1280 + 32, (u32x2){pack2(o11[0] * i1, o11[1] * i1), pack2(o11[2] * i1, o11[3] * i1)});
        }
    }
    BAR();
    {
        const int tok = 64 * (w & 1) + l;
        const u32 oo = WS_O + (((rb * 8 + (tok >> 4)) * 80 + 5 * j) * 16 + (tok & 15)) * 16;
        ST16C(lds128(L0 + L_Q + tok * 80 + (w >> 1) * 16), oo + (w >> 1) * 256);
        if (w < 2) ST16C(lds128(L0 + L_Q + tok * 80 + 64), oo + 4 * 256);
    }
    VMCNT(0);
    BAR();
    if (tid == 0) {
        st_agent(ws + WS_FO + (rb * 16 + j) * FS, tag);
        if (wpwg) st_agent(ws + WS_FWP + b * FS, tag);
    }
    STAMP(8);

    const int wm = w & 3, wk = w >> 2;
    const int mt3 = rb * 8 + 4 * hf + wm;
    const int nt0 = wk == 0 ? 0 : 3, ntn = wk == 0 ? 3 : 2;
    u32x2 x1k[3];
    {
        const int wdu = b + 256 * (w - 6);
        const bool wdw = w >= 6 && wdu < 432;
        if (w >= 1 && w < 5) {
#pragma unroll
            for (int i = 0; i < 5; ++i) glds16(p.wo, wosrc + (w - 1) * 32 * 1280, wvoff[i], L0 + L_WO + (w - 1) * 5120 + i * 1024);
        } else if (w >= 5) {
#pragma unroll
            for (int e = 0; e < 4; ++e) {
                const int pc = w - 5 + 3 * e;
                if (pc < 10) {
                    const u32 sg = 64 * pc + l, r = (sg * 6554) >> 16, c = sg - r * 10;
                    glds16(p.x, (const char*)p.x + (rb * 128 + 64 * hf) * 1280 + cb * 160, r * 1280 + c * 16, L0 + L_XR + pc * 1024);
                }
            }
            if (wdw) {
                const int ucb = wdu / 54, us = wdu - 54 * ucb;
#pragma unroll
                for (int i = 0; i < 5; ++i) glds16(p.wd, (const char*)p.wd + ucb * 160 + us * 32 * 1280, wvoff[i], L0 + L_WDRAW + (w - 6) * 5120 + i * 1024);
            }
        }
        if (w == 1 && l < 20) stsf4(L0 + L_GA + l * 16, gather1(ws, 320 + 20 * cb + l, tag, ws, 4));
        if (w == 0) {
            const int bl = min((Gj >> 2) + ((l >> 2) % 3), (Gj + nj - 1) >> 2);
            poll_at(ws, WS_FWP + (108 * (l >= 12 && l < 24 ? 1 : 0) + 4 * bl + (l & 3)) * FS, l, tag, ws, 6);
            poll_flags(ws + WS_FO + rb * 16 * FS, 16, l, tag, ws, 5);
        }
        if (wdw) vmw<5>(); else vmw<0>();
        BAR();
        STAMP(9);
        u32x4 a[10];
        const char* asrc = ws + WS_O + (mt3 * 80 + 40 * wk) * 256;
#define AL(s) a[s] = gld16c(((s) & 3) * 1024, asrc + ((s) >> 2) * 4096, lo16)
        AL(0); AL(1); AL(2); AL(3);
        f32x4 acc[5];
#pragma unroll
        for (int n = 0; n < 5; ++n) acc[n] = Z4;
        const u32 wb = L0 + L_WO + wk * 51200;
        const u32 b0 = wb + 160 * (8 * g + 4 * (g & 1) + q4) + 8 * p4, b1 = wb + 160 * (8 * g + 4 * ((g & 1) ^ 1) + q4) + 8 * p4;
        u32x4 bf[2][5];
#pragma unroll
        for (int n = 0; n < 5; ++n) bf[0][n] = ldtr2(b0 + n * 32, b1 + n * 32);
        static_for<0, 10>([&](auto S) {
            constexpr int s = S;
            vmw1<(s < 7 ? 3 : 9 - s)>(a[s]);
            if constexpr (s + 4 < 10) AL(s + 4);
            SB0();
#pragma unroll
            for (int n = 0; n < 5; ++n) {
                if (s < 9) bf[(s + 1) & 1][n] = ldtr2(b0 + (s + 1) * 5120 + n * 32, b1 + (s + 1) * 5120 + n * 32);
                acc[n] = mfma(bf[s & 1][n], a[s], acc[n]);
            }
#pragma unroll
            for (int n = 0; n < 5; ++n) { SGB(M_DSR, 2); SGB(M_MFMA, 1); }
            SB0();
        });
#undef AL
        STAMP(25);
        if (wk == 0) { stsf4(L0 + L_RED3 + (wm * 5 + 3) * 1024 + lo16, acc[3]); stsf4(L0 + L_RED3 + (wm * 5 + 4) * 1024 + lo16, acc[4]); }
        else { stsf4(L0 + L_RED3 + (wm * 5 + 0) * 1024 + lo16, acc[0]); stsf4(L0 + L_RED3 + (wm * 5 + 1) * 1024 + lo16, acc[1]); stsf4(L0 + L_RED3 + (wm * 5 + 2) * 1024 + lo16, acc[2]); }
        BAR();
        float s1 = 0.f, s2 = 0.f;
#pragma unroll
        for (int n = 0; n < 3; ++n) {
            if (n < ntn) {
                const int nt = nt0 + n;
                const f32x4 mine = wk == 0 ? acc[n] : acc[n < 2 ? 3 + n : 4];
                const f32x4 v = mine + ldsf4(L0 + L_RED3 + (wm * 5 + nt) * 1024 + lo16);
                const f32x4 ga = ldsf4(L0 + L_GA + (16 * nt + 4 * g) * 4);
                const u32x2 xr = lds64(L0 + L_XR + (16 * wm + i16) * 160 + 32 * nt + 8 * g);
                const u32x2 o = {pack2(bflo(xr[0]) + ga[0] * v[0], bfhi(xr[0]) + ga[1] * v[1]), pack2(bflo(xr[1]) + ga[2] * v[2], bfhi(xr[1]) + ga[3] * v[3])};
                x1k[n] = o;
                s1 = dot2(o[0], 0x3F803F80u, s1); s1 = dot2(o[1], 0x3F803F80u, s1);
                s2 = dot2(o[0], o[0], s2); s2 = dot2(o[1], o[1], s2);
                sts64(L0 + L_X1S + (16 * wm + i16) * 160 + 32 * nt + 8 * g, o);
            } else x1k[n] = (u32x2){0, 0};
        }
        s1 = xsum4(s1); s2 = xsum4(s2);
        if (l < 16) stsf2(L0 + L_ST3 + (wk * 64 + 16 * wm + l) * 8, (f32x2){s1, s2});
        BAR();
        {
            const u32 xo = WS_X1 + (((rb * 8 + 4 * hf + g) * 80 + 10 * cb) * 16 + i16) * 16;
            ST16C(lds128(L0 + L_X1S + l * 160 + w * 16), xo + w * 256);
            if (w < 2) ST16C(lds128(L0 + L_X1S + l * 160 + (w + 8) * 16), xo + (w + 8) * 256);
            if (w == 2) {
                const f32x2 u = *(LAS f32x2*)(size_t)(L0 + L_ST3 + l * 8), v = *(LAS f32x2*)(size_t)(L0 + L_ST3 + (64 + l) * 8);
                const f32x2 sum = u + v;
                ST8C(__builtin_bit_cast(u32x2, sum), WS_STAT + ((rb * 8 + cb) * 128 + 64 * hf + l) * 8);
            }
        }
        if (wdw) {
            const u32 wr = L0 + L_WDRAW + (w - 6) * 5120;
            const u32 r0 = wr + 160 * (8 * g + 4 * (g & 1) + q4) + 8 * p4, r1 = wr + 160 * (8 * g + 4 * ((g & 1) ^ 1) + q4) + 8 * p4;
#pragma unroll
            for (int n = 0; n < 5; ++n) ST16C(ldtr2(r0 + n * 32, r1 + n * 32), WS_WD + (wdu * 5 + n) * 1024 + lo16);
        }
        STAMP(26);
        if (wdw) vmw<5>(); else vmw<0>();
        BAR();
        if (tid == 0) st_agent(ws + WS_FX1 + (rb * 16 + j) * FS, tag);
    }
    STAMP(10);

    const char* wdsrc = ws + WS_WD + (cb * 54 + wk * 27) * 5120;
    const u32 r5w = L0 + wk * 20480 + wm * 5120;
#define P5_B1(t, i) do { if ((i) == 0 || (i) == 4) glds16c(wdsrc + min(4 * (t) + wm, 26) * 5120 + (i) * 1024, lo16, r5w + r5_slot(t) + (i) * 1024); \
        else glds16c_next(((i) & 3) * 1024, wdsrc + min(4 * (t) + wm, 26) * 5120, lo16); } while (0)
    {
        const int wn = w >> 2;
        const int bst = w < 4 ? 4 * w : 16 + 3 * (w - 4);
        const char* wpj = ws + WS_WP + (j * 20 * 14 + bst) * 1024;
        const char* x1b = ws + WS_X1 + (rb * 8 + w) * 80 * 256;
        const u32 r4a = L0 + L_R4 + w * 2048, r4b = L0 + L_R4 + 16384 + bst * 1024;
#define P4_B1(t, i) do { if ((i) == 0) glds16c(wpj + (t) * 28 * 1024, lo16, r4b + ((t) % 3) * R4_STAGE); else glds16c_next((i) * 1024, wpj + (t) * 28 * 1024, lo16); } while (0)
#define P4_A1(t, i) do { if ((i) == 0) glds16c(x1b + 8 * (t) * 256, lo16, r4a + ((t) % 3) * R4_STAGE); else glds16c_next(1024, x1b + 8 * (t) * 256, lo16); } while (0)
#define P4_B(t) do { P4_B1(t, 0); P4_B1(t, 1); P4_B1(t, 2); if (w < 4) P4_B1(t, 3); } while (0)
        P4_B(0); P4_B(1); P4_B(2);
        if (w == 0) poll_flags(ws + WS_FX1 + rb * 16 * FS, 16, l, tag, ws, 7);
        BAR();
        STAMP(11);
        glds16c(ws + WS_STAT + rb * 8192 + w * 1024, lo16, L0 + L_ST4 + w * 1024);
        if (w < 7) glds16c(ws + WS_UV + j * 7168 + w * 1024, lo16, L0 + L_UV + w * 1024);
        P4_A1(0, 0); P4_A1(0, 1); P4_A1(1, 0); P4_A1(1, 1); P4_A1(2, 0); P4_A1(2, 1);
        f32x4 acc[7][2];
#pragma unroll
        for (int n = 0; n < 7; ++n) { acc[n][0] = Z4; acc[n][1] = Z4; }
        const u32 ra = opaque(L0 + L_R4 + 2 * wm * 2048 + lo16), rbb = opaque(L0 + L_R4 + 16384 + 7 * wn * 1024 + lo16);
        u32x4 fa[2][2], fb[2][7];
#define P4_RA(buf, t, ks) do { const u32 so = ((t) % 3) * R4_STAGE + (ks) * 1024; fa[buf][0] = lds128(ra + so); fa[buf][1] = lds128(ra + so + 2048); } while (0)
#define P4_RB(buf, t, ks, n) fb[buf][n] = lds128(rbb + ((t) % 3) * R4_STAGE + (ks) * 14336 + (n) * 1024)
#define P4_MM(buf, n) do { acc[n][0] = mfma(fb[buf][n], fa[buf][0], acc[n][0]); acc[n][1] = mfma(fb[buf][n], fa[buf][1], acc[n][1]); } while (0)
#define P4_HALF(buf, rd, t, ks, V0, V1, V2) do { \
            SB0(); if (rd) { P4_RA((buf) ^ 1, t, ks); P4_RB((buf) ^ 1, t, ks, 0); } P4_MM(buf, 0); SB0(); V0; \
            SB0(); if (rd) P4_RB((buf) ^ 1, t, ks, 1); P4_MM(buf, 1); SB0(); \
            SB0(); if (rd) P4_RB((buf) ^ 1, t, ks, 2); P4_MM(buf, 2); SB0(); V1; \
            SB0(); if (rd) P4_RB((buf) ^ 1, t, ks, 3); P4_MM(buf, 3); SB0(); \
            SB0(); if (rd) P4_RB((buf) ^ 1, t, ks, 4); P4_MM(buf, 4); SB0(); V2; \
            SB0(); if (rd) P4_RB((buf) ^ 1, t, ks, 5); P4_MM(buf, 5); SB0(); \
            SB0(); if (rd) P4_RB((buf) ^ 1, t, ks, 6); P4_MM(buf, 6); SB0(); } while (0)
        VMCNT(4);
        BAR();
        if (tid == 0) st_agent(ws + WS_FW2 + b * FS, tag);
        STAMP(27);
        u32x2 wdf = {0u, 0u};
        const u32 wdo = WS_FW2 + ((54 * cb + (l < 54 ? l : 0)) & 255) * FS;
        P4_RA(0, 0, 0);
#pragma unroll
        for (int n = 0; n < 7; ++n) P4_RB(0, 0, 0, n);
        static_for<0, 10>([&](auto T) {
            constexpr int t = T;
            constexpr bool f = t >= 1 && t + 2 <= 9, sN = t + 3 <= 9;
            if constexpr (t < 9) P4_HALF(0, true, t, 1, if (f && w < 4) P4_B1(t + 2, 3), if (f) P4_A1(t + 2, 0), if (f) P4_A1(t + 2, 1));
            else P4_HALF(0, true, t, 1, P5_B1(0, 3), P5_B1(0, 4), P5_B1(1, 0));
            if constexpr (t == 5) {
                if (tid < 128) {
                    f32x2 st = {0.f, 0.f};
#pragma unroll
                    for (int c = 0; c < 8; ++c) st += *(LAS f32x2*)(size_t)(L0 + L_ST4 + (c * 128 + tid) * 8);
                    const float mu = st[0] * (1.f / 640.f), var = fmaxf(st[1] * (1.f / 640.f) - mu * mu, 0.f), r = __builtin_amdgcn_rsqf(var + 1e-5f);
                    stsf2(L0 + L_MR + tid * 8, (f32x2){r, -mu * r});
                } else {
                    const int e = tid - 128;
                    stsf(L0 + L_UVS + e * 4, (ldsf(L0 + L_UV + (e >> 5) * 512 + (e & 31) * 4) + ldsf(L0 + L_UV + (e >> 5) * 512 + 128 + (e & 31) * 4))
                                           + (ldsf(L0 + L_UV + (e >> 5) * 512 + 256 + (e & 31) * 4) + ldsf(L0 + L_UV + (e >> 5) * 512 + 384 + (e & 31) * 4)));
                    if (e < 64) { const int e2 = e + 384;
                        stsf(L0 + L_UVS + e2 * 4, (ldsf(L0 + L_UV + (e2 >> 5) * 512 + (e2 & 31) * 4) + ldsf(L0 + L_UV + (e2 >> 5) * 512 + 128 + (e2 & 31) * 4))
                                                + (ldsf(L0 + L_UV + (e2 >> 5) * 512 + 256 + (e2 & 31) * 4) + ldsf(L0 + L_UV + (e2 >> 5) * 512 + 384 + (e2 & 31) * 4))); }
                }
            }
            if constexpr (t < 9) {
                if constexpr (t == 0) vmw<2>();
                else if constexpr (t == 8) {
                    asm volatile("s_waitcnt vmcnt(0)" : "+v"(wdf) :: "memory");
                    if (w == 7) { if (!__all(wdf[0] == (u32)tag && wdf[1] == (u32)(tag >> 32))) poll_at(ws, wdo, l, tag, ws, 12); }
                }
                else { if (w < 4) vmw<6>(); else vmw<5>(); }
                BAR();
                if constexpr (t == 7) { if (w == 7) asm volatile("s_nop 4\n\tglobal_load_dwordx2 %0, %1, %2 sc1" : "=v"(wdf) : "v"(wdo), "s"(ws) : "memory"); }
                if constexpr (t < 8) P4_HALF(1, true, t + 1, 0, if (sN) P4_B1(t + 3, 0), if (sN) P4_B1(t + 3, 1), if (sN) P4_B1(t + 3, 2));
                else P4_HALF(1, true, t + 1, 0, P5_B1(0, 0), P5_B1(0, 1), P5_B1(0, 2));
            } else {
                P4_HALF(1, false, 0, 0, P5_B1(1, 1), P5_B1(1, 2), P5_B1(1, 3));
                P5_B1(1, 4);
            }
        });
#undef P4_B
#undef P4_B1
#undef P4_A1
#undef P4_RA
#undef P4_RB
#undef P4_MM
#undef P4_HALF
        STAMP(12);
        f32x2 mr[2];
#pragma unroll
        for (int m = 0; m < 2; ++m) mr[m] = *(LAS f32x2*)(size_t)(L0 + L_MR + (32 * wm + 16 * m + i16) * 8);
#pragma unroll
        for (int n = 0; n < 7; ++n) {
            const f32x4 u = ldsf4(L0 + L_UVS + (7 * wn + n) * 128 + 16 * g), v = ldsf4(L0 + L_UVS + (7 * wn + n) * 128 + 64 + 16 * g);
#pragma unroll
            for (int m = 0; m < 2; ++m) acc[n][m] = acc[n][m] * mr[m][0] + (u * mr[m][1] + v);
        }
        if (nj == 7) stsf4(L0 + L_XCH + w * 1024 + lo16, wn == 0 ? acc[6][1] : acc[6][0]);
        auto silu_mul = [&](const f32x4 gt, const f32x4 up) -> u32x2 {
            const f32x4 e = gt * (-LOG2E), gu = gt * up;
            f32x4 d;
#pragma unroll
            for (int r = 0; r < 4; ++r) d[r] = __builtin_amdgcn_exp2f(e[r]);
            d = d + 1.f;
#pragma unroll
            for (int r = 0; r < 4; ++r) d[r] = __builtin_amdgcn_rcpf(d[r]);
            const f32x4 o = gu * d;
            return (u32x2){pack2(o[0], o[1]), pack2(o[2], o[3])};
        };
#pragma unroll
        for (int a = 0; a < 3; ++a) {
            const int la = a + 4 * wn, gam = Gj + (la <= 3 || nj == 7 ? la : la - 1);
            const u32x2 o0 = silu_mul(acc[2 * a][0], acc[2 * a + 1][0]), o1 = silu_mul(acc[2 * a][1], acc[2 * a + 1][1]);
            const auto s0 = __builtin_amdgcn_permlane16_swap(o0[0], o1[0], false, false);
            const auto s1 = __builtin_amdgcn_permlane16_swap(o0[1], o1[1], false, false);
            const u32x4 v = {s0[0], s1[0], s0[1], s1[1]};
            ST16C(v, WS_ACT + (((rb * 8 + 2 * wm + (g & 1)) * 216 + 2 * gam + (g >> 1)) * 16 + i16) * 16);
        }
        STAMP(28);
        BAR();
        if (nj == 7) {
            const f32x4 other = ldsf4(L0 + L_XCH + (w ^ 4) * 1024 + lo16);
            const u32x2 o = wn == 0 ? silu_mul(acc[6][0], other) : silu_mul(other, acc[6][1]);
            ST8C(o, WS_ACT + (((rb * 8 + 2 * wm + wn) * 216 + 2 * (Gj + 3) + (g >> 1)) * 16 + i16) * 16 + 8 * (g & 1));
        }
        P5_B1(2, 0); P5_B1(2, 1); P5_B1(2, 2); P5_B1(2, 3); P5_B1(2, 4);
        vmw<5>();
        BAR();
        if (tid == 0) st_agent(ws + WS_FACT + (rb * 16 + j) * FS, tag);
    }
    STAMP(13);

    {
        if (w == 1 && l < 20) stsf4(L0 + L_GB + l * 16, gather1(ws, 800 + 20 * cb + l, tag, ws, 8));
        if (w == 0) poll_flags(ws + WS_FACT + rb * 16 * FS, 16, l, tag, ws, 9);
        VMCNT(0);
        BAR();
        STAMP(14);
        const char* asrc = ws + WS_ACT + (mt3 * 216 + wk * 108) * 256;
        u32x4 a[3][4];
#define P5_A1(t, i) a[(t) % 3][i] = gld16c((i) * 1024, asrc + (t) * 4096, lo16)
        P5_A1(0, 0); P5_A1(0, 1); P5_A1(0, 2); P5_A1(0, 3); P5_A1(1, 0); P5_A1(1, 1); P5_A1(1, 2); P5_A1(1, 3); P5_A1(2, 0); P5_A1(2, 1); P5_A1(2, 2); P5_A1(2, 3);
        f32x4 acc[5];
#pragma unroll
        for (int n = 0; n < 5; ++n) acc[n] = Z4;
        const u32 rb5 = opaque(L0 + wk * 20480 + lo16);
        u32x4 fb[2][5];
#define P5_RD(buf, t, kk) do { _Pragma("unroll") for (int n = 0; n < 5; ++n) fb[buf][n] = lds128(rb5 + r5_slot(t) + (kk) * 5120 + n * 1024); } while (0)
#define P5_MM(buf, av) do { _Pragma("unroll") for (int n = 0; n < 5; ++n) acc[n] = mfma(fb[buf][n], (av), acc[n]); } while (0)
#define P5_MIX() do { _Pragma("unroll") for (int n = 0; n < 5; ++n) { SGB(M_DSR, 1); SGB(M_MFMA, 1); } } while (0)
        P5_RD(0, 0, 0);
        static_for<0, 7>([&](auto T) {
            constexpr int t = T, q = t % 3;
            constexpr bool nx = t + 3 < 7;
            constexpr int NA = t == 0 ? 8 : t == 1 ? 13 : t == 2 ? 18 : t == 3 ? 21 : t == 4 ? 20 : t == 5 ? 11 : 5;
            constexpr int NB = t == 2 ? 12 : t == 3 ? 12 : t == 4 ? 8 : t == 5 ? 0 : 63;
            vmw4<NA>(a[q][0], a[q][1], a[q][2], a[q][3]);
            SB0(); P5_RD(1, t, 1); P5_MM(0, a[q][0]); P5_MIX(); SB0();
            if constexpr (nx) P5_A1(t + 3, 0);
            SB0(); P5_RD(0, t, 2); P5_MM(1, a[q][1]); P5_MIX(); SB0();
            if constexpr (nx) P5_A1(t + 3, 1);
            if constexpr (t < 6) {
                SB0(); P5_RD(1, t, 3); P5_MM(0, a[q][2]); P5_MIX(); SB0();
                if constexpr (nx) P5_A1(t + 3, 2);
                vmw<NB>(); BAR();
                if constexpr (nx) { P5_B1(t + 3, 0); P5_B1(t + 3, 1); }
                SB0(); P5_RD(0, t + 1, 0); P5_MM(1, a[q][3]); P5_MIX(); SB0();
                if constexpr (nx) { if constexpr (t + 3 < 6) P5_A1(t + 3, 3); P5_B1(t + 3, 2); P5_B1(t + 3, 3); P5_B1(t + 3, 4); }
            } else {
                SB0(); P5_MM(0, a[q][2]); SB0();
            }
        });
#undef P5_B1
#undef P5_A1
#undef P5_RD
#undef P5_MM
#undef P5_MIX
        STAMP(15);
        if (wk == 0) { stsf4(L0 + L_RED5 + (wm * 5 + 3) * 1024 + lo16, acc[3]); stsf4(L0 + L_RED5 + (wm * 5 + 4) * 1024 + lo16, acc[4]); }
        else { stsf4(L0 + L_RED5 + (wm * 5 + 0) * 1024 + lo16, acc[0]); stsf4(L0 + L_RED5 + (wm * 5 + 1) * 1024 + lo16, acc[1]); stsf4(L0 + L_RED5 + (wm * 5 + 2) * 1024 + lo16, acc[2]); }
        BAR();
#pragma unroll
        for (int n = 0; n < 3; ++n) {
            if (n < ntn) {
                const int nt = nt0 + n;
                const f32x4 mine = wk == 0 ? acc[n] : acc[n < 2 ? 3 + n : 4];
                const f32x4 v = mine + ldsf4(L0 + L_RED5 + (wm * 5 + nt) * 1024 + lo16);
                const f32x4 gb = ldsf4(L0 + L_GB + (16 * nt + 4 * g) * 4);
                const u32x2 o = {pack2(bflo(x1k[n][0]) + gb[0] * v[0], bfhi(x1k[n][0]) + gb[1] * v[1]), pack2(bflo(x1k[n][1]) + gb[2] * v[2], bfhi(x1k[n][1]) + gb[3] * v[3])};
                sts64(L0 + L_OS + (16 * wm + i16) * 160 + 32 * nt + 8 * g, o);
            }
        }
        BAR();
#pragma unroll
        for (int i = 0; i < 2; ++i) {
            const int id = tid + 512 * i;
            if (id < 640) {
                const int r = (id * 6554) >> 16, c = id - 10 * r;
                *(GAS u32x4*)((char*)p.out + (rb * 128 + 64 * hf + r) * 1280 + cb * 160 + c * 16) = lds128(L0 + L_OS + id * 16);
            }
        }
    }
    STAMP(16); STAMP_RT(63); STAMP_OUT();
    if (rb == 15 && j == 0 && w < 4) {
        u32 spins = 0;
        for (;;) {
            const u64 v = ld_agent(ws + WS_FO + tid * FS);
            if (__all(v == tag)) break;
            if (++spins > SPIN_CAP) { st_agent32(ws + WS_ERR, 10); break; }
            __builtin_amdgcn_s_sleep(1);
        }
    }
    if (rb == 15 && j == 0) {
        BAR();
        if (tid == 0) st_agent(ws + WS_EPOCH, tag);
    }
}

extern "C" void kernel_launch(void* const* d_in, const int* in_sizes, int n_in, void* d_out, int out_size, void* d_ws, size_t ws_size, hipStream_t stream) {
    Params p;
    p.x = (const u16*)d_in[0]; p.wqkv = (const u16*)d_in[1]; p.wo = (const u16*)d_in[2];
    p.wg = (const u16*)d_in[4]; p.wu = (const u16*)d_in[5]; p.wd = (const u16*)d_in[6];
    p.c = (const u16*)d_in[7]; p.wmod = (const u16*)d_in[8];
    p.out = (u16*)d_out; p.ws = (char*)d_ws;
    mega_kernel<<<256, 512, 0, stream>>>(p);
}
```

```cpp
#include <hip/hip_runtime.h>
#include <hip/hip_bf16.h>
#include <stdint.h>
#include <type_traits>

typedef unsigned short u16;
typedef unsigned u32;
typedef unsigned long long u64;
typedef __attribute__((ext_vector_type(2))) unsigned u32x2;
typedef __attribute__((ext_vector_type(4))) unsigned u32x4;
typedef __attribute__((ext_vector_type(2))) float f32x2;
typedef __attribute__((ext_vector_type(4))) float f32x4;
typedef __attribute__((ext_vector_type(2))) __bf16 bf16x2;
typedef __attribute__((ext_vector_type(8))) __bf16 bf16x8;
typedef __attribute__((ext_vector_type(4))) short s16x4;
#define LAS __attribute__((address_space(3)))
#define GAS __attribute__((address_space(1)))
#define DI __device__ __forceinline__

constexpr int SEQ = 2048, DM = 640, FF = 1728;
constexpr float QSCALE = 0.22360679774997896f * 1.4426950408889634f;
constexpr float LOG2E = 1.4426950408889634f;

constexpr u32 WS_EPOCH = 0;
constexpr u32 WS_ERR   = 256;
constexpr u32 FS = 128;
constexpr u32 WS_FKV   = 4096;
constexpr u32 WS_FO    = WS_FKV + 256 * FS;
constexpr u32 WS_FX1   = WS_FO + 256 * FS;
constexpr u32 WS_FACT  = WS_FX1 + 256 * FS;
constexpr u32 WS_FH    = WS_FACT + 256 * FS;
constexpr u32 WS_FWT   = WS_FH + 256 * FS;
constexpr u32 WS_FWP   = WS_FWT + 256 * FS;
constexpr u32 WS_FW2   = WS_FWP + 256 * FS;
constexpr u32 WS_MODG  = WS_FW2 + 256 * FS;
constexpr u32 WS_UV    = WS_MODG + 61440;
constexpr u32 WS_STAT  = WS_UV + 114688;
constexpr u32 WS_KV    = 589824;
constexpr u32 WS_O     = WS_KV + 256 * 22528;
constexpr u32 WS_X1    = WS_O + SEQ * DM * 2;
constexpr u32 WS_ACT   = WS_X1 + SEQ * DM * 2;
constexpr u32 WS_WP    = WS_ACT + SEQ * FF * 2;
constexpr u32 WS_H     = WS_WP + 16 * 20 * 14 * 1024;
constexpr u32 WS_WD    = WS_H + SEQ * DM * 2;
constexpr u32 WS_WO    = WS_WD + 8 * 54 * 5 * 1024;
constexpr u32 WS_WT    = WS_WO + 8 * 20 * 5 * 1024;
constexpr u32 WS_STAMP = WS_WT + 16 * 153600;
constexpr u32 WS_END   = WS_STAMP + 256 * 8 * 64 * 4;

constexpr u32 L_WKV = 0, L_WQ = 102400, L_GS = 153600, L_STP = 156160, L_MODP = 156416, L_CNT = 157440, L_SB = 157696, L_G2 = 158016, L_Z16 = 158656;
constexpr u32 L_ODRAW = 101376, L_WTRAW = 116736, L_XS = 133120;
constexpr u32 L_K = 0, L_V = 30720, L_Q = 67584, L_WPRAW = 77824, L_UVL = 98304;
constexpr u32 L_WO = 61440, L_RED3 = 0, L_X1S = 20480, L_XR = 30720, L_GA = 40960, L_ST3 = 41472, L_WDRAW = 43008;
constexpr u32 L_R4 = 0, R4_STAGE = 45056, L_ST4 = 135168, L_UV = 143360, L_MR = 150528, L_XCH = 151552, L_UVS = 159744;
constexpr u32 L_RED5 = 126976, L_GB = 147456, L_OS = 148480;
constexpr u32 r5_slot(int t) { return t % 3 == 0 ? 45056 : t % 3 == 1 ? 86016 : 0; }

struct Params {
    const u16* x; const u16* wqkv; const u16* wo; const u16* wg; const u16* wu; const u16* wd;
    const u16* c; const u16* wmod; u16* out; char* ws;
};

DI float bflo(u32 d) { return __uint_as_float(d << 16); }
DI float bfhi(u32 d) { return __uint_as_float(d & 0xffff0000u); }
DI u32 pack2(float a, float b) { f32x2 v = {a, b}; return __builtin_bit_cast(u32, __builtin_convertvector(v, bf16x2)); }
DI float rbf(float a) { return bflo(pack2(a, a)); }
DI f32x4 mfma(u32x4 a, u32x4 b, f32x4 c) {
    return __builtin_amdgcn_mfma_f32_16x16x32_bf16(__builtin_bit_cast(bf16x8, a), __builtin_bit_cast(bf16x8, b), c, 0, 0, 0);
}
DI u32x2 ldtr(u32 a) { return __builtin_bit_cast(u32x2, __builtin_amdgcn_ds_read_tr16_b64_v4i16((LAS s16x4*)(size_t)a)); }
DI u32x4 ldtr2(u32 a0, u32 a1) { u32x2 x = ldtr(a0), y = ldtr(a1); return (u32x4){x[0], x[1], y[0], y[1]}; }
DI u32x4 lds128(u32 a) { return *(LAS u32x4*)(size_t)a; }
DI u32x2 lds64(u32 a) { return *(LAS u32x2*)(size_t)a; }
DI f32x4 ldsf4(u32 a) { return *(LAS f32x4*)(size_t)a; }
DI float ldsf(u32 a) { return *(LAS float*)(size_t)a; }
DI void sts128(u32 a, u32x4 v) { *(LAS u32x4*)(size_t)a = v; }
DI void sts64(u32 a, u32x2 v) { *(LAS u32x2*)(size_t)a = v; }
DI void stsf(u32 a, float v) { *(LAS float*)(size_t)a = v; }
DI void stsf4(u32 a, f32x4 v) { *(LAS f32x4*)(size_t)a = v; }
DI void stsf2(u32 a, f32x2 v) { *(LAS f32x2*)(size_t)a = v; }
DI void sts16(u32 a, u16 v) { *(LAS u16*)(size_t)a = v; }

DI void glds16_(const void* root, u32 voff, u32 ldsdst) {
    asm volatile("s_mov_b32 m0, %2\n\ts_nop 0\n\tglobal_load_lds_dwordx4 %0, %1" :: "v"(voff), "s"(root), "s"(__builtin_amdgcn_readfirstlane(ldsdst)) : "memory");
}
DI void glds16c_(const void* root, u32 voff, u32 ldsdst) {
    asm volatile("s_mov_b32 m0, %2\n\ts_nop 0\n\tglobal_load_lds_dwordx4 %0, %1 sc1" :: "v"(voff), "s"(root), "s"(__builtin_amdgcn_readfirstlane(ldsdst)) : "memory");
}
template <int OFF> DI void glds16c_next_(const void* root, u32 voff) {
    asm volatile("global_load_lds_dwordx4 %0, %1 offset:%2 sc1" :: "v"(voff), "s"(root), "i"(OFF) : "memory");
}
template <int OFF> DI u32x4 gld16(const void* root, u32 voff) {
    u32x4 r; asm volatile("global_load_dwordx4 %0, %1, %2 offset:%3" : "=v"(r) : "v"(voff), "s"(root), "i"(OFF) : "memory"); return r;
}
template <int OFF> DI u32x4 gld16c_(const void* root, u32 voff) {
    u32x4 r; asm volatile("global_load_dwordx4 %0, %1, %2 offset:%3 sc1" : "=v"(r) : "v"(voff), "s"(root), "i"(OFF) : "memory"); return r;
}
#define ROFF(root, ptr) ((u32)((const char*)(ptr) - (const char*)(root)))
#define glds16(root, ptr, voff, dst) glds16_((root), ROFF(root, ptr) + (voff), (dst))
#define glds16c(ptr, voff, dst) glds16c_(ws, ROFF(ws, ptr) + (voff), (dst))
#define glds16c_next(OFF, ptr, voff) glds16c_next_<(OFF)>(ws, ROFF(ws, ptr) + (voff))
#define gld16c(OFF, ptr, voff) gld16c_<(OFF)>(ws, ROFF(ws, ptr) + (voff))
DI u32 gldu16(const void* sbase, u32 voff) {
    u32 r; asm volatile("global_load_ushort %0, %1, %2" : "=v"(r) : "v"(voff), "s"(sbase) : "memory"); return r;
}
#define VMCNT(n) asm volatile("s_waitcnt vmcnt(" #n ")" ::: "memory")
#define BAR() asm volatile("s_waitcnt lgkmcnt(0)\n\ts_barrier" ::: "memory")
#define RLX_AGENT __ATOMIC_RELAXED, __HIP_MEMORY_SCOPE_AGENT
DI u64 ld_agent(const char* p) { return __hip_atomic_load((GAS u64*)p, RLX_AGENT); }
DI void st_agent(char* p, u64 v) { __hip_atomic_store((GAS u64*)p, v, RLX_AGENT); }
DI void st_agent32(char* p, u32 v) { __hip_atomic_store((GAS u32*)p, v, RLX_AGENT); }
DI float ld_agentf(const char* p) { return __uint_as_float(__hip_atomic_load((GAS u32*)p, RLX_AGENT)); }
DI float xsum4(float x) {
    const auto s = __builtin_amdgcn_permlane16_swap(__float_as_uint(x), __float_as_uint(x), false, false);
    x = __uint_as_float(s[0]) + __uint_as_float(s[1]);
    const auto t = __builtin_amdgcn_permlane32_swap(__float_as_uint(x), __float_as_uint(x), false, false);
    return __uint_as_float(t[0]) + __uint_as_float(t[1]);
}
DI float xmax4(float x) {
    const auto s = __builtin_amdgcn_permlane16_swap(__float_as_uint(x), __float_as_uint(x), false, false);
    x = fmaxf(__uint_as_float(s[0]), __uint_as_float(s[1]));
    const auto t = __builtin_amdgcn_permlane32_swap(__float_as_uint(x), __float_as_uint(x), false, false);
    return fmaxf(__uint_as_float(t[0]), __uint_as_float(t[1]));
}
template <int CTRL> DI float dppf(float v) { return __builtin_bit_cast(float, __builtin_amdgcn_update_dpp(0, __builtin_bit_cast(int, v), CTRL, 0xf, 0xf, true)); }

constexpr u32 SPIN_CAP = 1u << 17;
DI void poll_flags(const char* base, int n, int lane, u64 tag, char* ws, u32 code) {
    const char* a = base + (lane < n ? lane : 0) * FS;
    u32 spins = 0;
    for (;;) {
        const u64 v = ld_agent(a);
        if (__all(v == tag)) break;
        if (++spins > SPIN_CAP) { if (lane == 0) st_agent32(ws + WS_ERR, code); break; }
        __builtin_amdgcn_s_sleep(1);
    }
}
typedef __attribute__((ext_vector_type(2))) _Float16 f16x2;
DI float hlo(u32 d) { return (float)__builtin_bit_cast(f16x2, d)[0]; }
DI float hhi(u32 d) { return (float)__builtin_bit_cast(f16x2, d)[1]; }
DI u32 packh(float a, float b) { const f16x2 v = {(_Float16)a, (_Float16)b}; return __builtin_bit_cast(u32, v); }
DI bool gran_ok(const u32x4& v, u64 tag) { return v[2] == (u32)tag && v[3] == (u32)(tag >> 32); }
DI f32x4 gran_sum(const u32x4& a, const u32x4& b, const u32x4& c, const u32x4& d) {
    const f32x4 s = {(hlo(a[0]) + hlo(b[0])) + (hlo(c[0]) + hlo(d[0])), (hhi(a[0]) + hhi(b[0])) + (hhi(c[0]) + hhi(d[0])),
                     (hlo(a[1]) + hlo(b[1])) + (hlo(c[1]) + hlo(d[1])), (hhi(a[1]) + hhi(b[1])) + (hhi(c[1]) + hhi(d[1]))};
    return (f32x4){rbf(s[0]), rbf(s[1]), rbf(s[2]), rbf(s[3])};
}
DI f32x4 gather1(const char* ws, u32 g0, u64 tag, char* wsw, u32 code) {
    u32 spins = 0; u32x4 v0, v1, v2, v3;
    const u32 o = WS_MODG + g0 * 16;
    for (;;) {
        asm volatile("s_nop 4\n\tglobal_load_dwordx4 %0, %4, %5 sc1\n\tglobal_load_dwordx4 %1, %6, %5 sc1\n\tglobal_load_dwordx4 %2, %7, %5 sc1\n\tglobal_load_dwordx4 %3, %8, %5 sc1\n\ts_waitcnt vmcnt(0)"
                     : "=&v"(v0), "=&v"(v1), "=&v"(v2), "=&v"(v3) : "v"(o), "s"(ws), "v"(o + 15360), "v"(o + 30720), "v"(o + 46080) : "memory");
        if (__all(gran_ok(v0, tag) && gran_ok(v1, tag) && gran_ok(v2, tag) && gran_ok(v3, tag))) break;
        if (++spins > SPIN_CAP) { st_agent32(wsw + WS_ERR, code); break; }
        __builtin_amdgcn_s_sleep(1);
    }
    return gran_sum(v0, v1, v2, v3);
}
DI void gather2(const char* ws, u32 g0, u32 g1, u64 tag, char* wsw, u32 code, f32x4& ra, f32x4& rb) {
    u32 spins = 0; u32x4 v0, v1, v2, v3, u0, u1, u2, u3;
    const u32 o = WS_MODG + g0 * 16, q = WS_MODG + g1 * 16;
    for (;;) {
        asm volatile("s_nop 4\n\tglobal_load_dwordx4 %0, %8, %9 sc1\n\tglobal_load_dwordx4 %1, %10, %9 sc1\n\tglobal_load_dwordx4 %2, %11, %9 sc1\n\tglobal_load_dwordx4 %3, %12, %9 sc1\n\t"
                     "global_load_dwordx4 %4, %13, %9 sc1\n\tglobal_load_dwordx4 %5, %14, %9 sc1\n\tglobal_load_dwordx4 %6, %15, %9 sc1\n\tglobal_load_dwordx4 %7, %16, %9 sc1\n\ts_waitcnt vmcnt(0)"
                     : "=&v"(v0), "=&v"(v1), "=&v"(v2), "=&v"(v3), "=&v"(u0), "=&v"(u1), "=&v"(u2), "=&v"(u3)
                     : "v"(o), "s"(ws), "v"(o + 15360), "v"(o + 30720), "v"(o + 46080), "v"(q), "v"(q + 15360), "v"(q + 30720), "v"(q + 46080) : "memory");
        if (__all(gran_ok(v0, tag) && gran_ok(v1, tag) && gran_ok(v2, tag) && gran_ok(v3, tag) && gran_ok(u0, tag) && gran_ok(u1, tag) && gran_ok(u2, tag) && gran_ok(u3, tag))) break;
        if (++spins > SPIN_CAP) { st_agent32(wsw + WS_ERR, code); break; }
        __builtin_amdgcn_s_sleep(1);
    }
    ra = gran_sum(v0, v1, v2, v3); rb = gran_sum(u0, u1, u2, u3);
}
DI void gather_strip(const char* ws, u32 gs, u32 gc, u32 strip, int l, u64 tag, char* wsw, u32 code) {
    f32x4 a, c;
    gather2(ws, l < 40 ? gs + l : gc + (l - 40), gc + 24 + (l & 15), tag, wsw, code, a, c);
    if (l >= 40) a = a + 1.f;
    c = c + 1.f;
    *(LAS u32x2*)(size_t)(strip + (l < 40 ? l * 8 : 320 + (l - 40) * 8)) = (u32x2){pack2(a[0], a[1]), pack2(a[2], a[3])};
    if (l < 16) *(LAS u32x2*)(size_t)(strip + 320 + (24 + l) * 8) = (u32x2){pack2(c[0], c[1]), pack2(c[2], c[3])};
}
DI void gather_strip_spread(const char* ws, u32 gs, u32 gc, u32 strip, int l, u64 tag, char* wsw, u32 code) {
    f32x4 a, c;
    gather2(ws, l < 40 ? gs + l : gc + (l - 40), gc + 24 + (l & 15), tag, wsw, code, a, c);
    if (l < 40) *(LAS u32x2*)(size_t)(strip + l * 8) = (u32x2){pack2(a[0], a[1]), pack2(a[2], a[3])};
    else {
        const u32 p0 = pack2(a[0] + 1.f, a[1] + 1.f), p1 = pack2(a[2] + 1.f, a[3] + 1.f);
        *(LAS u32x4*)(size_t)(strip + 320 + (l - 40) * 16) = (u32x4){p0 & 0xffffu, p0 & 0xffff0000u, p1 & 0xffffu, p1 & 0xffff0000u};
    }
    if (l < 16) {
        const u32 p0 = pack2(c[0] + 1.f, c[1] + 1.f), p1 = pack2(c[2] + 1.f, c[3] + 1.f);
        *(LAS u32x4*)(size_t)(strip + 320 + (24 + l) * 16) = (u32x4){p0 & 0xffffu, p0 & 0xffff0000u, p1 & 0xffffu, p1 & 0xffff0000u};
    }
    if (l == 63) *(LAS u32x4*)(size_t)(strip + 960) = (u32x4){0, 0, 0, 0};
}
DI void poll_at(const char* ws, u32 off, int lane, u64 tag, char* wsw, u32 code) {
    u32 spins = 0;
    for (;;) {
        const u64 v = ld_agent(ws + off);
        if (__all(v == tag)) break;
        if (++spins > SPIN_CAP) { if (lane == 0) st_agent32(wsw + WS_ERR, code); break; }
        __builtin_amdgcn_s_sleep(1);
    }
}

#define STAMP(i) do {} while (0)
#define STAMPL(i) do {} while (0)
#define STAMP_RT(i) do {} while (0)
#define STAMP_OUT() do {} while (0)

template <int N> using IC = std::integral_constant<int, N>;
template <int I, int E, class F> DI void static_for(F&& f) { if constexpr (I < E) { f(IC<I>{}); static_for<I + 1, E>(f); } }
template <int N> DI void vmw() { asm volatile("s_waitcnt vmcnt(%0)" :: "i"(N) : "memory"); }
template <int N> DI void vmw1(u32x4& a) { asm volatile("s_waitcnt vmcnt(%1)" : "+v"(a) : "i"(N) : "memory"); }
template <int N> DI void vmw4(u32x4& a, u32x4& b, u32x4& c, u32x4& d) { asm volatile("s_waitcnt vmcnt(%4)" : "+v"(a), "+v"(b), "+v"(c), "+v"(d) : "i"(N) : "memory"); }
DI u32 opaque(u32 a) { asm("" : "+v"(a)); return a; }
DI float dot2(u32 a, u32 b, float c) { return __builtin_amdgcn_fdot2_f32_bf16(__builtin_bit_cast(bf16x2, a), __builtin_bit_cast(bf16x2, b), c, false); }
#define SB0() __builtin_amdgcn_sched_barrier(0)
#define SGB(m, n) __builtin_amdgcn_sched_group_barrier((m), (n), 0)
constexpr int M_MFMA = 0x8, M_DSR = 0x100;
#define Z4 ((f32x4){0.f, 0.f, 0.f, 0.f})
DI u32 lds_add(u32 a, u32 v) { return __hip_atomic_fetch_add((LAS u32*)(size_t)a, v, __ATOMIC_RELAXED, __HIP_MEMORY_SCOPE_WORKGROUP); }
DI u32 lds_ld(u32 a) { return __hip_atomic_load((LAS u32*)(size_t)a, __ATOMIC_RELAXED, __HIP_MEMORY_SCOPE_WORKGROUP); }
DI void worker_sync(u32 cnt, int lane) {
    asm volatile("s_waitcnt lgkmcnt(0)" ::: "memory");
    if (lane == 0) lds_add(cnt, 1u);
    for (u32 spins = 0; lds_ld(cnt) < 4u && spins < (1u << 22); ++spins) {}
    asm volatile("" ::: "memory");
}

__global__ __launch_bounds__(512, 2) void mega_kernel(Params p) {
    __shared__ __attribute__((aligned(16))) char ldsmem[163840];
    const u32 L0 = (u32)(uintptr_t)(LAS char*)ldsmem;
    const int tid = threadIdx.x, l = tid & 63;
    const int w = __builtin_amdgcn_readfirstlane(tid >> 6);
    const int b = blockIdx.x, xg = b & 7, sidx = b >> 3;
    const int rb = 4 * (xg >> 1) + (sidx >> 3), j = 8 * (xg & 1) + (sidx & 7);
    const int g = l >> 4, i16 = l & 15, q4 = i16 >> 2, p4 = l & 3;
    u32 wu0 = 0;
    if (w == 7) {
        const char* pc; u32 span;
        asm volatile("s_getpc_b64 %0\n.Lmk_code_from:\n\ts_mov_b32 %1, .Lmk_code_to-.Lmk_code_from" : "=s"(pc), "=s"(span));
        const u32 off = min((u32)(sidx + 32 * min(l, 15)) * 128u, span - 4u);
        asm volatile("s_nop 0\n\tglobal_load_dword %0, %1, %2" : "=v"(wu0) : "v"(off), "s"(pc) : "memory");
    }
    char* const ws = p.ws;
    asm volatile("" :: "s"(p.x), "s"(p.wqkv), "s"(p.wo), "s"(p.wg), "s"(p.wu), "s"(p.wd), "s"(p.c), "s"(p.wmod), "s"(p.out), "s"(p.ws));
    u64 tag;
#define ASK_TAG(ep) u32x2 ep; asm volatile("global_load_dwordx2 %0, %1, %2" : "=v"(ep) : "v"(WS_EPOCH), "s"(ws) : "memory")
#define TAKE_TAG(ep) do { asm volatile("s_waitcnt vmcnt(0)" : "+v"(ep) :: "memory"); \
        tag = (((u64)(u32)__builtin_amdgcn_readfirstlane(ep[1]) << 32) | (u32)__builtin_amdgcn_readfirstlane(ep[0])) + 1; } while (0)
    const __amdgpu_buffer_rsrc_t wsr = __builtin_amdgcn_make_buffer_rsrc(ws, 0, WS_END, 0x00020000);
#define ST16C(v, off) __builtin_amdgcn_raw_buffer_store_b128((v), wsr, (off), 0, 16)
#define ST8C(v, off)  __builtin_amdgcn_raw_buffer_store_b64((v), wsr, (off), 0, 16)
    STAMP_RT(62); STAMP(0);

    const int nj = j < 12 ? 7 : 6, Gj = j < 12 ? 7 * j : 84 + 6 * (j - 12);
    const bool wpwg = b < 216;
    const int wpm = b >= 108 ? 1 : 0, wpb = (b - 108 * wpm) >> 2, wpq = b & 3;
    if (w >= 4) {
        __builtin_amdgcn_s_setprio(3);
        const int u = w - 4;
        const bool tw = b >= 106;
        const int tb = (b - 106) / 5, tk = (b - 106) - 5 * tb, ts = 4 * tk + u;
        if (tw) {
            const char* src = (const char*)p.wqkv + ts * 122880 + tb * 128;
            const u32 voff = (l >> 3) * 3840 + (l & 7) * 16;
#pragma unroll
            for (int i = 0; i < 4; ++i) glds16(p.wqkv, src + i * 8 * 3840, voff, L0 + L_WTRAW + u * 4096 + i * 1024);
        }
        ASK_TAG(epl);
        BAR();
        __builtin_amdgcn_s_setprio(0);
        TAKE_TAG(epl);
        if (tw) {
            const u32 a = L0 + L_WTRAW + u * 4096 + (8 * g + q4) * 128 + 8 * p4;
#pragma unroll
            for (int t = 0; t < 4; ++t) {
                const u32 cc = 8 * tb + 2 * t + (i16 >> 3);
                const u32 seg = cc >= 160 ? 2 : cc >= 80 ? 1 : 0, wi = cc - 80 * seg, jj = (wi * 13108) >> 16, c5 = wi - 5 * jj;
                const u32 c = seg == 0 ? 10 + c5 : seg == 1 ? c5 : 5 + c5, nt = c >> 1;
                const u32 dst = WS_WT + jj * 153600 + (nt < 5 ? ts * 5120 + nt * 1024 : 102400 + ts * 2560 + (nt - 5) * 1024)
                              + (nt < 7 ? (g * 16 + 8 * (c & 1) + (i16 & 7)) * 16 : g * 128 + (i16 & 7) * 16);
                ST16C(ldtr2(a + 32 * t, a + 32 * t + 512), dst);
            }
        }
        vmw<0>();
        {
            asm volatile("s_waitcnt lgkmcnt(0)" ::: "memory");
            u32 old = 0;
            if (l == 0) old = lds_add(L0 + L_CNT + 12, 1u);
            if (tw && __builtin_amdgcn_readfirstlane(old) == 3 && l == 0) st_agent(ws + WS_FWT + (b - 106) * FS, tag);
        }
        STAMPL(19);
        if (u == 0) {
            const int wh = l >= 30 ? 0 : l / 5, kr = l >= 30 ? 0 : l - 5 * wh;
            const int bl = (1280 * (wh >> 1) + 80 * j + 79 * (wh & 1)) >> 7;
            poll_at(ws, WS_FWT + (bl * 5 + kr) * FS, l, tag, ws, 13);
            for (u32 spins = 0; lds_ld(L0 + L_CNT + 12) < 4u && spins < (1u << 22); ++spins) {}
            if (l == 0) lds_add(L0 + L_CNT + 24, 1u);
        } else {
            for (u32 spins = 0; lds_ld(L0 + L_CNT + 24) < 1u && spins < (1u << 22); ++spins) {}
        }
        asm volatile("" ::: "memory");
        STAMPL(20);
        {
            const char* src = ws + WS_WT + j * 153600 + u * 1024;
            const u32 lo = l * 16;
#pragma unroll
            for (int i = 0; i < 25; ++i) glds16c(src + i * 4096, lo, L0 + L_WKV + u * 1024 + i * 4096);
        }
        VMCNT(0);
        STAMPL(17);
    } else {
        __builtin_amdgcn_s_setprio(2);
        u32x4 mw[5]; u32 mc[5];
        auto mod_ask = [&]() {
#pragma unroll
            for (int i = 0; i < 5; ++i) {
                const int r = 160 * (b & 3) + (tid >> 3) + 32 * i;
                mw[i] = gld16<0>(p.wmod, r * 7680 + (b >> 2) * 128 + (tid & 7) * 16);
                mc[i] = gldu16(p.c, r * 2);
            }
        };
        auto mod_finish = [&]() {
            float acc[8];
#pragma unroll
            for (int e = 0; e < 8; ++e) acc[e] = 0.f;
#pragma unroll
            for (int i = 0; i < 5; ++i) {
                const float cv = bflo(mc[i]);
                const float sv = cv * __builtin_amdgcn_rcpf(1.f + __builtin_amdgcn_exp2f(-cv * LOG2E));
                const u32 slo = pack2(sv, 0.f), shi = slo << 16;
#pragma unroll
                for (int d = 0; d < 4; ++d) { acc[2 * d] = dot2(mw[i][d], slo, acc[2 * d]); acc[2 * d + 1] = dot2(mw[i][d], shi, acc[2 * d + 1]); }
            }
#pragma unroll
            for (int e = 0; e < 8; ++e) { const float v = acc[e]; acc[e] = xsum4(v + dppf<0x128>(v)); }
            if (l < 8) {
                stsf4(L0 + L_MODP + w * 256 + l * 32, (f32x4){acc[0], acc[1], acc[2], acc[3]});
                stsf4(L0 + L_MODP + w * 256 + l * 32 + 16, (f32x4){acc[4], acc[5], acc[6], acc[7]});
            }
            asm volatile("s_waitcnt lgkmcnt(0)" ::: "memory");
            u32 old = 0;
            if (l == 0) old = lds_add(L0 + L_CNT, 1u);
            if (__builtin_amdgcn_readfirstlane(old) == 3) {
                float v = 0.f;
#pragma unroll
                for (int ww = 0; ww < 4; ++ww) v += ldsf(L0 + L_MODP + ww * 256 + l * 4);
                const float v1 = dppf<0x55>(v), v2 = dppf<0xAA>(v), v3 = dppf<0xFF>(v), v0 = dppf<0x00>(v);
                const u32x4 gr = {packh(v0, v1), packh(v2, v3), (u32)tag, (u32)(tag >> 32)};
                if (p4 == 0) ST16C(gr, WS_MODG + ((b & 3) * 960 + (b >> 2) * 16 + (l >> 2)) * 16);
            }
        };
#define MOD_LANDED() asm volatile("s_waitcnt vmcnt(0)" : "+v"(mw[0]), "+v"(mw[1]), "+v"(mw[2]), "+v"(mw[3]), "+v"(mw[4]), "+v"(mc[0]), "+v"(mc[1]), "+v"(mc[2]), "+v"(mc[3]), "+v"(mc[4]) :: "memory")
        if (b < 240) mod_ask();
        {
            const char* src = (const char*)p.x + (rb * 128 + 8 * j) * 1280 + w * 1024;
            glds16(p.x, src, l * 16, L0 + L_XS + w * 1024);
            glds16(p.x, src + 4096, l * 16, L0 + L_XS + (w + 4) * 1024);
            if (w < 2) glds16(p.x, src + 8192, l * 16, L0 + L_XS + (w + 8) * 1024);
        }
        ASK_TAG(epw);
        if (tid < 16) *(LAS u32*)(size_t)(L0 + L_CNT + tid * 4) = 0u;
        BAR();
        STAMP(29);
        TAKE_TAG(epw);
        STAMP(30);
        if (b < 240) { MOD_LANDED(); mod_finish(); }
        STAMP(1);
        worker_sync(L0 + L_CNT + 16, l);
        const int tok = l & 7;
        u32x4 xr[3];
#pragma unroll
        for (int i = 0; i < 3; ++i) xr[i] = lds128(L0 + L_XS + tok * 1280 + (20 * w + (i < 2 ? (l >> 3) + 8 * i : 16 + ((l >> 3) & 3))) * 16);
        {
            float s1 = 0.f, s2 = 0.f;
#pragma unroll
            for (int i = 0; i < 3; ++i) {
                float a1 = 0.f, a2 = 0.f;
#pragma unroll
                for (int d = 0; d < 4; ++d) { a1 = dot2(xr[i][d], 0x3F803F80u, a1); a2 = dot2(xr[i][d], xr[i][d], a2); }
                if (i < 2 || l < 32) { s1 += a1; s2 += a2; }
            }
            asm volatile("s_waitcnt lgkmcnt(0)" ::: "memory");
            if (l == 0) lds_add(L0 + L_CNT + 20, 1u);
            s1 += dppf<0x128>(s1); s2 += dppf<0x128>(s2);
            s1 = xsum4(s1); s2 = xsum4(s2);
            if (l < 8) stsf2(L0 + L_STP + (w * 8 + l) * 8, (f32x2){s1, s2});
        }
        gather_strip(ws, 40 * w, 160 + 40 * w, L0 + L_GS + w * 640, l, tag, ws, 1);
        STAMP(2);
        worker_sync(L0 + L_CNT + 4, l);
        {
            f32x2 st = {0.f, 0.f};
#pragma unroll
            for (int ww = 0; ww < 4; ++ww) st += *(LAS f32x2*)(size_t)(L0 + L_STP + (ww * 8 + tok) * 8);
            const float mu = st[0] * (1.f / 640.f), var = fmaxf(st[1] * (1.f / 640.f) - mu * mu, 0.f);
            const float rs = __builtin_amdgcn_rsqf(var + 1e-5f), nmr = -mu * rs;
#pragma unroll
            for (int i = 0; i < 3; ++i) {
                const int cl = i < 2 ? (l >> 3) + 8 * i : 16 + ((l >> 3) & 3);
                const u32x4 sa = lds128(L0 + L_GS + w * 640 + cl * 16), gg = lds128(L0 + L_GS + w * 640 + 320 + cl * 16);
                u32x4 h;
#pragma unroll
                for (int d = 0; d < 4; ++d)
                    h[d] = pack2((bflo(xr[i][d]) * rs + nmr) * bflo(gg[d]) + bflo(sa[d]), (bfhi(xr[i][d]) * rs + nmr) * bfhi(gg[d]) + bfhi(sa[d]));
                if (i < 2 || l < 32) ST16C(h, WS_H + (((rb * 8 + (j >> 1)) * 80 + 20 * w + cl) * 16 + 8 * (j & 1) + tok) * 16);
            }
        }
        VMCNT(0);
        worker_sync(L0 + L_CNT + 8, l);
        if (tid == 0) st_agent(ws + WS_FH + (rb * 16 + j) * FS, tag);
        if (w == 0) poll_flags(ws + WS_FH + rb * 16 * FS, 16, l, tag, ws, 11);
#undef MOD_LANDED
        __builtin_amdgcn_s_setprio(0);
    }
    BAR();
    asm volatile("s_waitcnt vmcnt(0)" : "+v"(wu0) :: "memory");
    STAMP(3);

    u32x4 xa[20];
    const u32 lo16 = l * 16;
    const char* hsrc = ws + WS_H + (rb * 8 + w) * 80 * 256;
#define XL(s) xa[s] = gld16c(((s) & 3) * 1024, hsrc + ((s) >> 2) * 4096, lo16)
    XL(0); XL(1); XL(2); XL(3); XL(4); XL(5);
    const char* kvx = ws + WS_KV + (rb * 16 + j) * 22528;
    const char* wtq = ws + WS_WT + j * 153600 + (100 + w) * 1024;
    {
        f32x4 acc[5];
#pragma unroll
        for (int n = 0; n < 5; ++n) acc[n] = Z4;
        const u32 b0 = L0 + L_WKV + lo16;
        u32x4 bf[2][5];
#pragma unroll
        for (int n = 0; n < 5; ++n) bf[0][n] = lds128(b0 + n * 1024);
        static_for<0, 20>([&](auto S) {
            constexpr int s = S;
            vmw1<(s < 14 ? 5 : 6)>(xa[s]);
            if constexpr (s + 6 < 20) XL(s + 6);
            if constexpr (s >= 13) { if (s < 19 || w < 2) glds16c(wtq + (s - 13) * 8192, lo16, L0 + L_WKV + (100 + w) * 1024 + (s - 13) * 8192); }
            SB0();
#pragma unroll
            for (int n = 0; n < 5; ++n) {
                if (s < 19) bf[(s + 1) & 1][n] = lds128(b0 + (s + 1) * 5120 + n * 1024);
                acc[n] = mfma(bf[s & 1][n], xa[s], acc[n]);
            }
#pragma unroll
            for (int n = 0; n < 5; ++n) { SGB(M_DSR, 1); SGB(M_MFMA, 1); }
            SB0();
        });
        STAMP(18);
        BAR();
        const int row = 256 + 16 * w + i16;
#pragma unroll
        for (int n = 0; n < 5; ++n) {
            const int Q = 4 * n + g;
            const u32x2 v = {pack2(acc[n][0], acc[n][1]), pack2(acc[n][2], acc[n][3])};
            const int wi = Q - 10, hd = wi >= 5 ? 1 : 0;
            const u32 a = Q < 10 ? L_K + row * 80 + 8 * Q : L_V + row * 96 + 48 * hd + 8 * (wi - 5 * hd);
            sts64(L0 + a, v);
        }
        if (g < 2) sts64(L0 + L_V + row * 96 + 48 * g + 40, (u32x2){0x3F80u, 0u});
        if (rb < 2) {
            const int nrow = rb == 0 ? 256 : 128;
            for (int i = tid; i < nrow * 5; i += 512) sts128(L0 + L_K + i * 16, (u32x4){0, 0, 0, 0});
            for (int i = tid; i < nrow * 6; i += 512) sts128(L0 + L_V + i * 16, (u32x4){0, 0, 0, 0});
        }
        vmw<0>();
        BAR();
    }
#undef XL
    STAMP(4);
    {
        u32x4 kvd[3];
#pragma unroll
        for (int e = 0; e < 3; ++e) {
            const int n = min(w + 8 * e, 21);
            kvd[e] = lds128(L0 + (n < 10 ? L_K + 20480 + n * 1024 : L_V + 24576 + (n - 10) * 1024) + lo16);
        }
        const u32 kvo = WS_KV + (rb * 16 + j) * 22528 + w * 1024 + lo16;
        const u16* wroot = wpm ? p.wu : p.wg;
        const char* wsrc = (const char*)wroot + (160 * wpq + 8 * w) * 3456 + wpb * 128;
        const u32 wvo = (l >> 3) * 3456 + (l & 7) * 16;
        f32x4 acc[3];
#pragma unroll
        for (int n = 0; n < 3; ++n) acc[n] = Z4;
        const u32 bq = L0 + L_WQ + lo16, bq2 = L0 + L_WQ + 2048 + g * 128 + (i16 & 7) * 16;
#define QRD(s, n) lds128(((n) < 2 ? bq + (n) * 1024 : bq2) + (s) * 2560)
        u32x4 bf[3][3];
#pragma unroll
        for (int n = 0; n < 3; ++n) { bf[0][n] = QRD(0, n); bf[1][n] = QRD(1, n); }
        static_for<0, 20>([&](auto S) {
            constexpr int s = S;
            if constexpr (s == 0) { if (rb < 15) ST16C(kvd[0], kvo); }
            if constexpr (s == 1) { if (rb < 15) ST16C(kvd[1], kvo + 8192); }
            if constexpr (s == 2) { if (rb < 15 && w < 6) ST16C(kvd[2], kvo + 16384); }
            if constexpr (s == 14) {
                vmw<0>();
                u32 old = 0;
                if (l == 0) old = lds_add(L0 + L_CNT + 28, 1u);
                if (__builtin_amdgcn_readfirstlane(old) == 7 && l == 0) st_agent(ws + WS_FKV + (j * 16 + rb) * FS, tag);
            }
            if constexpr (s == 15) { if (wpwg) glds16(wroot, wsrc, wvo, L0 + L_WPRAW + w * 1024); }
            if constexpr (s == 16) { if (wpwg) glds16(wroot, wsrc + 64 * 3456, wvo, L0 + L_WPRAW + (w + 8) * 1024); }
            if constexpr (s == 17) { if (wpwg && w < 4) glds16(wroot, wsrc + 128 * 3456, wvo, L0 + L_WPRAW + (w + 16) * 1024); }
            SB0();
#pragma unroll
            for (int n = 0; n < 3; ++n) {
                if (s < 18) bf[(s + 2) % 3][n] = QRD(s + 2, n);
                acc[n] = mfma(bf[s % 3][n], xa[s], acc[n]);
            }
#pragma unroll
            for (int n = 0; n < 3; ++n) { SGB(M_DSR, 1); SGB(M_MFMA, 1); }
            SB0();
        });
#undef QRD
        STAMP(21);
#pragma unroll
        for (int n = 0; n < 3; ++n) {
            const int Q = 4 * n + g;
            const u32x2 v = {pack2(acc[n][0] * QSCALE, acc[n][1] * QSCALE), pack2(acc[n][2] * QSCALE, acc[n][3] * QSCALE)};
            if (Q < 10) sts64(L0 + L_Q + (16 * w + i16) * 80 + 8 * Q, v);
        }
    }
    if (w == 0 && rb >= 1) poll_flags(ws + WS_FKV + (j * 16 + (rb >= 2 ? rb - 2 : rb - 1)) * FS, rb >= 2 ? 2 : 1, l, tag, ws, 3);
    if (w == 7 && wpwg) gather_strip_spread(ws, 480 + 40 * wpq, 640 + 40 * wpq, L0 + L_SB, l, tag, ws, 2);
    VMCNT(0);
    BAR();
    STAMP(5);
    if (rb >= 1) {
#pragma unroll
        for (int e = 0; e < 3; ++e) {
            const int n = w + 8 * e;
            if (n < 22) {
                const u32 d = n < 10 ? L_K + n * 1024 : L_V + (n - 10) * 1024, st = n < 10 ? 10240 : 12288;
                glds16c(kvx - 16 * 22528 + n * 1024, lo16, L0 + d + st);
                if (rb >= 2) glds16c(kvx - 32 * 22528 + n * 1024, lo16, L0 + d);
            }
        }
    }
    auto wp_slot = [&](int gam, int& jj, int& slot) {
        jj = gam < 84 ? gam / 7 : 12 + (gam - 84) / 6;
        const int ar = gam - (jj < 12 ? 7 * jj : 84 + 6 * (jj - 12)), a = (jj < 12 || ar < 3) ? ar : ar + 1;
        slot = a < 3 ? 2 * a + wpm : a == 3 ? (wpm ? 13 : 6) : 7 + 2 * (a - 4) + wpm;
    };
    if (wpwg) {
        const u32 one = i16 == 0 ? 0x3F803F80u : 0u;
        const u32x4 ones = {one, one, one, one};
#pragma unroll
        for (int i = 0; i < 3; ++i) {
            if (i < 2 || w < 4) {
                const int f = w + 8 * i, ks = f >> 2, t16 = f & 3;
                int jj, slot; wp_slot(4 * wpb + t16, jj, slot);
                const u32 a = L0 + L_WPRAW + (32 * ks + 8 * g + q4) * 128 + 32 * t16 + 8 * p4;
                u32x4 wr = ldtr2(a, a + 512);
                const u32x4 ga = lds128(L0 + L_G2 + (32 * ks + 8 * g) * 4), gc = lds128(L0 + L_G2 + (32 * ks + 8 * g) * 4 + 16);
                const u32x4 sbz = lds128(L0 + (i16 == 0 ? L_SB + (32 * ks + 8 * g) * 2 : L_Z16));
                const f32x4 vs = mfma(wr, sbz, Z4);
                u32x4 o; float t0, t1, t2, t3, t4, t5, t6, t7;
                asm volatile("v_dot2_f32_bf16 %4, %12, %16, 0\n\tv_dot2_f32_bf16 %5, %12, %17, 0\n\tv_dot2_f32_bf16 %6, %13, %18, 0\n\tv_dot2_f32_bf16 %7, %13, %19, 0\n\t"
                             "v_dot2_f32_bf16 %8, %14, %20, 0\n\tv_dot2_f32_bf16 %9, %14, %21, 0\n\tv_dot2_f32_bf16 %10, %15, %22, 0\n\tv_dot2_f32_bf16 %11, %15, %23, 0\n\t"
                             "v_cvt_pk_bf16_f32 %0, %4, %5\n\tv_cvt_pk_bf16_f32 %1, %6, %7\n\tv_cvt_pk_bf16_f32 %2, %8, %9\n\ts_nop 0\n\tv_cvt_pk_bf16_f32 %3, %10, %11\n\ts_nop 1"
                             : "=&v"(o[0]), "=&v"(o[1]), "=&v"(o[2]), "=&v"(o[3]),
                               "=&v"(t0), "=&v"(t1), "=&v"(t2), "=&v"(t3), "=&v"(t4), "=&v"(t5), "=&v"(t6), "=&v"(t7)
                             : "v"(wr[0]), "v"(wr[1]), "v"(wr[2]), "v"(wr[3]), "v"(ga[0]), "v"(ga[1]), "v"(ga[2]), "v"(ga[3]), "v"(gc[0]), "v"(gc[1]), "v"(gc[2]), "v"(gc[3]));
                const f32x4 us = mfma(o, ones, Z4);
                ST16C(o, WS_WP + ((jj * 20 + 5 * wpq + ks) * 14 + slot) * 1024 + lo16);
                if (i16 == 0) { stsf4(L0 + L_UVL + f * 128 + g * 16, us); stsf4(L0 + L_UVL + f * 128 + 64 + g * 16, vs); }
            }
        }
    }
    if (wpwg) { if (w < 4) vmw<3>(); else vmw<2>(); } else vmw<0>();
    BAR();
    STAMP(6);
    if (wpwg && w == 3) {
#pragma unroll
        for (int e = 0; e < 2; ++e) {
            const int t16 = 2 * e + (l >> 5);
            int jj, slot; wp_slot(4 * wpb + t16, jj, slot);
            float v = 0.f;
#pragma unroll
            for (int ks = 0; ks < 5; ++ks) v += ldsf(L0 + L_UVL + (4 * ks + t16) * 128 + (l & 31) * 4);
            st_agent32(ws + WS_UV + ((jj * 14 + slot) * 4 + wpq) * 128 + (l & 31) * 4, __float_as_uint(v));
        }
    }
    const int hf = j & 1, cb = j >> 1;
    u32 wvoff[5];
#pragma unroll
    for (int i = 0; i < 5; ++i) {
        const u32 sg = 64 * i + l, pos = (sg * 6554) >> 16, c = sg - pos * 10, k = pos ^ (((pos >> 3) & 1) << 2);
        wvoff[i] = k * 1280 + c * 16;
    }
    const char* wosrc = (const char*)p.wo + cb * 160;
#define WO_STEP(sx) do { _Pragma("unroll") for (int i = 0; i < 5; ++i) glds16(p.wo, wosrc + (sx) * 32 * 1280, wvoff[i], L0 + L_WO + (sx) * 5120 + i * 1024); } while (0)
    STAMP(7);

    {
        const int hd = w & 1, qi = w >> 1;
        const int kr0 = 32 * qi;
        u32x4 qf[2];
#pragma unroll
        for (int a = 0; a < 2; ++a) {
            const u32 qa = L0 + L_Q + (kr0 + 16 * a + i16) * 80 + hd * 40;
            const u32x2 q0 = lds64(qa + 8 * g);
            u32x2 q1 = lds64(qa + 32);
            if (g != 0) q1 = (u32x2){0, 0};
            qf[a] = (u32x4){q0[0], q0[1], q1[0], q1[1]};
        }
        const u32 kb0 = L0 + L_K + (kr0 + i16) * 80 + hd * 40 + 8 * g, kb1 = L0 + L_K + (kr0 + i16) * 80 + hd * 40 + 32;
        f32x4 mfirst, mlast;
#pragma unroll
        for (int r = 0; r < 4; ++r) { const bool lo = 4 * g + r <= i16; mfirst[r] = lo ? -INFINITY : 0.f; mlast[r] = lo ? 0.f : -INFINITY; }
        f32x4 s0[17], s1[17];
        {
            u32x4 kf[6];
#define KRD(t) do { const u32x2 k0 = lds64(kb0 + (t) * 1280), k1 = lds64(kb1 + (t) * 1280); kf[(t) % 6] = (u32x4){k0[0], k0[1], k1[0], k1[1]}; } while (0)
            KRD(0); KRD(1); KRD(2); KRD(3); KRD(4);
            static_for<0, 18>([&](auto T) {
                constexpr int t = T;
                SB0();
                if constexpr (t < 13) KRD(t + 5);
                if constexpr (t < 17) s0[t] = mfma(kf[t % 6], qf[0], t == 0 ? mfirst : t == 16 ? mlast : Z4);
                if constexpr (t > 0) s1[t - 1] = mfma(kf[t % 6], qf[1], t == 1 ? mfirst : t == 17 ? mlast : Z4);
                SGB(M_DSR, 2); SGB(M_MFMA, 2);
                SB0();
            });
#undef KRD
        }
        WO_STEP(4 + w);
        if (rb < 2) {
            const int fv = rb == 1 ? 128 : 256;
#pragma unroll
            for (int t = 0; t < 16; ++t) {
                const bool d0 = kr0 + 16 * t < fv, d1 = kr0 + 16 * t + 16 < fv;
#pragma unroll
                for (int r = 0; r < 4; ++r) { s0[t][r] = d0 ? -INFINITY : s0[t][r]; s1[t][r] = d1 ? -INFINITY : s1[t][r]; }
            }
        }
        STAMP(22);
        float m0 = fmaxf(s0[0][0], s0[0][1]), m1 = fmaxf(s1[0][0], s1[0][1]), n0 = fmaxf(s0[0][2], s0[0][3]), n1 = fmaxf(s1[0][2], s1[0][3]);
#pragma unroll
        for (int t = 1; t < 17; ++t) {
            m0 = fmaxf(fmaxf(m0, s0[t][0]), s0[t][1]); m1 = fmaxf(fmaxf(m1, s1[t][0]), s1[t][1]);
            n0 = fmaxf(fmaxf(n0, s0[t][2]), s0[t][3]); n1 = fmaxf(fmaxf(n1, s1[t][2]), s1[t][3]);
        }
        m0 = fmaxf(m0, n0); m1 = fmaxf(m1, n1);
        m0 = xmax4(m0); m1 = xmax4(m1);
        WO_STEP(12 + w);
        STAMP(23);
        f32x4 o00 = Z4, o01 = Z4, o10 = Z4, o11 = Z4;
        const u32 vb = L0 + L_V + (kr0 + 4 * g + q4) * 96 + hd * 48 + 8 * p4;
        auto pv = [&](auto SUB) {
            constexpr bool sub = SUB;
            u32x4 vf[2][2];
            vf[0][0] = ldtr2(vb, vb + 16 * 96); vf[0][1] = ldtr2(vb + 32, vb + 16 * 96 + 32);
#define EX(v, m) __builtin_amdgcn_exp2f(sub ? (v) - (m) : (v))
            static_for<0, 9>([&](auto U) {
                constexpr int u = U;
                if constexpr (u < 8) { const u32 a = vb + (u + 1) * 32 * 96; vf[(u + 1) & 1][0] = ldtr2(a, a + 16 * 96); vf[(u + 1) & 1][1] = ldtr2(a + 32, a + 16 * 96 + 32); }
                u32x4 pa, pb;
                pa[0] = pack2(EX(s0[2 * u][0], m0), EX(s0[2 * u][1], m0)); pa[1] = pack2(EX(s0[2 * u][2], m0), EX(s0[2 * u][3], m0));
                if constexpr (u < 8) { pa[2] = pack2(EX(s0[2 * u + 1][0], m0), EX(s0[2 * u + 1][1], m0)); pa[3] = pack2(EX(s0[2 * u + 1][2], m0), EX(s0[2 * u + 1][3], m0)); }
                else { pa[2] = 0; pa[3] = 0; }
                if constexpr (u > 0) { pb[0] = pack2(EX(s1[2 * u - 1][0], m1), EX(s1[2 * u - 1][1], m1)); pb[1] = pack2(EX(s1[2 * u - 1][2], m1), EX(s1[2 * u - 1][3], m1)); }
                else { pb[0] = 0; pb[1] = 0; }
                pb[2] = pack2(EX(s1[2 * u][0], m1), EX(s1[2 * u][1], m1)); pb[3] = pack2(EX(s1[2 * u][2], m1), EX(s1[2 * u][3], m1));
                o00 = mfma(vf[u & 1][0], pa, o00); o01 = mfma(vf[u & 1][1], pa, o01);
                o10 = mfma(vf[u & 1][0], pb, o10); o11 = mfma(vf[u & 1][1], pb, o11);
                SB0();
            });
#undef EX
        };
        if (__all(fabsf(m0) <= 64.f && fabsf(m1) <= 64.f)) pv(std::false_type{}); else pv(std::true_type{});
        STAMP(24);
        const float i0 = __builtin_amdgcn_rcpf(__shfl(o01[0], i16 + 16)), i1 = __builtin_amdgcn_rcpf(__shfl(o11[0], i16 + 16));
        const u32 oa = L0 + L_Q + (kr0 + i16) * 80 + hd * 40;
        sts64(oa + 8 * g, (u32x2){pack2(o00[0] * i0, o00[1] * i0), pack2(o00[2] * i0, o00[3] * i0)});
        sts64(oa + 1280 + 8 * g, (u32x2){pack2(o10[0] * i1, o10[1] * i1), pack2(o10[2] * i1, o10[3] * i1)});
        if (g == 0) {
            sts64(oa + 32, (u32x2){pack2(o01[0] * i0, o01[1] * i0), pack2(o01[2] * i0, o01[3] * i0)});
            sts64(oa + 1280 + 32, (u32x2){pack2(o11[0] * i1, o11[1] * i1), pack2(o11[2] * i1, o11[3] * i1)});
        }
    }
    BAR();
    {
        const int tok = 64 * (w & 1) + l;
        const u32 oo = WS_O + (((rb * 8 + (tok >> 4)) * 80 + 5 * j) * 16 + (tok & 15)) * 16;
        ST16C(lds128(L0 + L_Q + tok * 80 + (w >> 1) * 16), oo + (w >> 1) * 256);
        if (w < 2) ST16C(lds128(L0 + L_Q + tok * 80 + 64), oo + 4 * 256);
    }
    VMCNT(0);
    BAR();
    if (tid == 0) {
        st_agent(ws + WS_FO + (rb * 16 + j) * FS, tag);
        if (wpwg) st_agent(ws + WS_FWP + b * FS, tag);
    }
    STAMP(8);

    const int wm = w & 3, wk = w >> 2;
    const int mt3 = rb * 8 + 4 * hf + wm;
    u32x2 x1k[5];
    {
        const int wdu = b + 256 * (w - 6);
        const bool wdw = w >= 6 && wdu < 432;
        if (w >= 1 && w < 5) {
#pragma unroll
            for (int i = 0; i < 5; ++i) glds16(p.wo, wosrc + (w - 1) * 32 * 1280, wvoff[i], L0 + L_WO + (w - 1) * 5120 + i * 1024);
        } else if (w >= 5) {
#pragma unroll
            for (int e = 0; e < 4; ++e) {
                const int pc = w - 5 + 3 * e;
                if (pc < 10) {
                    const u32 sg = 64 * pc + l, r = (sg * 6554) >> 16, c = sg - r * 10;
                    glds16(p.x, (const char*)p.x + (rb * 128 + 64 * hf) * 1280 + cb * 160, r * 1280 + c * 16, L0 + L_XR + pc * 1024);
                }
            }
            if (wdw) {
                const int ucb = wdu / 54, us = wdu - 54 * ucb;
#pragma unroll
                for (int i = 0; i < 5; ++i) glds16(p.wd, (const char*)p.wd + ucb * 160 + us * 32 * 1280, wvoff[i], L0 + L_WDRAW + (w - 6) * 5120 + i * 1024);
            }
        }
        if (w == 1 && l < 20) stsf4(L0 + L_GA + l * 16, gather1(ws, 320 + 20 * cb + l, tag, ws, 4));
        if (w == 0) {
            const int bl = min((Gj >> 2) + ((l >> 2) % 3), (Gj + nj - 1) >> 2);
            poll_at(ws, WS_FWP + (108 * (l >= 12 && l < 24 ? 1 : 0) + 4 * bl + (l & 3)) * FS, l, tag, ws, 6);
            poll_flags(ws + WS_FO + rb * 16 * FS, 16, l, tag, ws, 5);
        }
        if (wdw) vmw<5>(); else vmw<0>();
        BAR();
        STAMP(9);
        u32x4 a[10];
        const char* asrc = ws + WS_O + (mt3 * 80 + 40 * wk) * 256;
#define AL(s) a[s] = gld16c(((s) & 3) * 1024, asrc + ((s) >> 2) * 4096, lo16)
        AL(0); AL(1); AL(2); AL(3);
        f32x4 acc[5];
#pragma unroll
        for (int n = 0; n < 5; ++n) acc[n] = Z4;
        const u32 wb = L0 + L_WO + wk * 51200;
        const u32 b0 = wb + 160 * (8 * g + 4 * (g & 1) + q4) + 8 * p4, b1 = wb + 160 * (8 * g + 4 * ((g & 1) ^ 1) + q4) + 8 * p4;
        u32x4 bf[2][5];
#pragma unroll
        for (int n = 0; n < 5; ++n) bf[0][n] = ldtr2(b0 + n * 32, b1 + n * 32);
        static_for<0, 10>([&](auto S) {
            constexpr int s = S;
            vmw1<(s < 7 ? 3 : 9 - s)>(a[s]);
            if constexpr (s + 4 < 10) AL(s + 4);
            SB0();
#pragma unroll
            for (int n = 0; n < 5; ++n) {
                if (s < 9) bf[(s + 1) & 1][n] = ldtr2(b0 + (s + 1) * 5120 + n * 32, b1 + (s + 1) * 5120 + n * 32);
                acc[n] = mfma(bf[s & 1][n], a[s], acc[n]);
            }
#pragma unroll
            for (int n = 0; n < 5; ++n) { SGB(M_DSR, 2); SGB(M_MFMA, 1); }
            SB0();
        });
#undef AL
        STAMP(25);
        if (wk == 1) {
#pragma unroll
            for (int n = 0; n < 5; ++n) stsf4(L0 + L_RED3 + (wm * 5 + n) * 1024 + lo16, acc[n]);
        }
        BAR();
        if (wk == 0) {
            float s1 = 0.f, s2 = 0.f;
#pragma unroll
            for (int n = 0; n < 5; ++n) {
                const f32x4 v = acc[n] + ldsf4(L0 + L_RED3 + (wm * 5 + n) * 1024 + lo16);
                const f32x4 ga = ldsf4(L0 + L_GA + (16 * n + 4 * g) * 4);
                const u32x2 xr = lds64(L0 + L_XR + (16 * wm + i16) * 160 + 32 * n + 8 * g);
                const u32x2 o = {pack2(bflo(xr[0]) + ga[0] * v[0], bfhi(xr[0]) + ga[1] * v[1]), pack2(bflo(xr[1]) + ga[2] * v[2], bfhi(xr[1]) + ga[3] * v[3])};
                x1k[n] = o;
                s1 = dot2(o[0], 0x3F803F80u, s1); s1 = dot2(o[1], 0x3F803F80u, s1);
                s2 = dot2(o[0], o[0], s2); s2 = dot2(o[1], o[1], s2);
            }
            const u32 xo = WS_X1 + ((mt3 * 80 + 10 * cb) * 16 + i16) * 16;
#pragma unroll
            for (int n = 0; n < 4; n += 2) {
                const auto a0 = __builtin_amdgcn_permlane16_swap(x1k[n][0], x1k[n + 1][0], false, false);
                const auto a1 = __builtin_amdgcn_permlane16_swap(x1k[n][1], x1k[n + 1][1], false, false);
                ST16C(((u32x4){a0[0], a1[0], a0[1], a1[1]}), xo + (2 * (n + (g & 1)) + (g >> 1)) * 256);
            }
            ST8C(x1k[4], xo + (8 + (g >> 1)) * 256 + 8 * (g & 1));
            s1 = xsum4(s1); s2 = xsum4(s2);
            if (l < 16) ST8C(((u32x2){__float_as_uint(s1), __float_as_uint(s2)}), WS_STAT + ((rb * 8 + cb) * 128 + 64 * hf + 16 * wm + l) * 8);
            STAMP(26);
            vmw<0>();
        } else {
#pragma unroll
            for (int n = 0; n < 5; ++n) x1k[n] = (u32x2){0, 0};
            if (wdw) {
                const u32 wr = L0 + L_WDRAW + (w - 6) * 5120;
                const u32 r0 = wr + 160 * (8 * g + 4 * (g & 1) + q4) + 8 * p4, r1 = wr + 160 * (8 * g + 4 * ((g & 1) ^ 1) + q4) + 8 * p4;
#pragma unroll
                for (int n = 0; n < 5; ++n) ST16C(ldtr2(r0 + n * 32, r1 + n * 32), WS_WD + (wdu * 5 + n) * 1024 + lo16);
            }
        }
        BAR();
        if (tid == 0) st_agent(ws + WS_FX1 + (rb * 16 + j) * FS, tag);
    }
    STAMP(10);

    const char* wdsrc = ws + WS_WD + (cb * 54 + wk * 27) * 5120;
    const u32 r5w = L0 + wk * 20480 + wm * 5120;
#define P5_B1(t, i) do { if ((i) == 0 || (i) == 4) glds16c(wdsrc + min(4 * (t) + wm, 26) * 5120 + (i) * 1024, lo16, r5w + r5_slot(t) + (i) * 1024); \
        else glds16c_next(((i) & 3) * 1024, wdsrc + min(4 * (t) + wm, 26) * 5120, lo16); } while (0)
    {
        const int wn = w >> 2;
        const int bst = w < 4 ? 4 * w : 16 + 3 * (w - 4);
        const char* wpj = ws + WS_WP + (j * 20 * 14 + bst) * 1024;
        const char* x1b = ws + WS_X1 + (rb * 8 + w) * 80 * 256;
        const u32 r4a = L0 + L_R4 + w * 2048, r4b = L0 + L_R4 + 16384 + bst * 1024;
#define P4_B1(t, i) do { if ((i) == 0) glds16c(wpj + (t) * 28 * 1024, lo16, r4b + ((t) % 3) * R4_STAGE); else glds16c_next((i) * 1024, wpj + (t) * 28 * 1024, lo16); } while (0)
#define P4_A1(t, i) do { if ((i) == 0) glds16c(x1b + 8 * (t) * 256, lo16, r4a + ((t) % 3) * R4_STAGE); else glds16c_next(1024, x1b + 8 * (t) * 256, lo16); } while (0)
#define P4_B(t) do { P4_B1(t, 0); P4_B1(t, 1); P4_B1(t, 2); if (w < 4) P4_B1(t, 3); } while (0)
        P4_B(0); P4_B(1); P4_B(2);
        if (w == 0) poll_flags(ws + WS_FX1 + rb * 16 * FS, 16, l, tag, ws, 7);
        BAR();
        STAMP(11);
        glds16c(ws + WS_STAT + rb * 8192 + w * 1024, lo16, L0 + L_ST4 + w * 1024);
        if (w < 7) glds16c(ws + WS_UV + j * 7168 + w * 1024, lo16, L0 + L_UV + w * 1024);
        P4_A1(0, 0); P4_A1(0, 1); P4_A1(1, 0); P4_A1(1, 1); P4_A1(2, 0); P4_A1(2, 1);
        f32x4 acc[7][2];
#pragma unroll
        for (int n = 0; n < 7; ++n) { acc[n][0] = Z4; acc[n][1] = Z4; }
        const u32 ra = opaque(L0 + L_R4 + 2 * wm * 2048 + lo16), rbb = opaque(L0 + L_R4 + 16384 + 7 * wn * 1024 + lo16);
        u32x4 fa[2][2], fb[2][7];
#define P4_RA(buf, t, ks) do { const u32 so = ((t) % 3) * R4_STAGE + (ks) * 1024; fa[buf][0] = lds128(ra + so); fa[buf][1] = lds128(ra + so + 2048); } while (0)
#define P4_RB(buf, t, ks, n) fb[buf][n] = lds128(rbb + ((t) % 3) * R4_STAGE + (ks) * 14336 + (n) * 1024)
#define P4_MM(buf, n) do { acc[n][0] = mfma(fb[buf][n], fa[buf][0], acc[n][0]); acc[n][1] = mfma(fb[buf][n], fa[buf][1], acc[n][1]); } while (0)
#define P4_HALF(buf, rd, t, ks, V0, V1, V2) do { \
            SB0(); if (rd) { P4_RA((buf) ^ 1, t, ks); P4_RB((buf) ^ 1, t, ks, 0); } P4_MM(buf, 0); SB0(); V0; \
            SB0(); if (rd) P4_RB((buf) ^ 1, t, ks, 1); P4_MM(buf, 1); SB0(); \
            SB0(); if (rd) P4_RB((buf) ^ 1, t, ks, 2); P4_MM(buf, 2); SB0(); V1; \
            SB0(); if (rd) P4_RB((buf) ^ 1, t, ks, 3); P4_MM(buf, 3); SB0(); \
            SB0(); if (rd) P4_RB((buf) ^ 1, t, ks, 4); P4_MM(buf, 4); SB0(); V2; \
            SB0(); if (rd) P4_RB((buf) ^ 1, t, ks, 5); P4_MM(buf, 5); SB0(); \
            SB0(); if (rd) P4_RB((buf) ^ 1, t, ks, 6); P4_MM(buf, 6); SB0(); } while (0)
        VMCNT(4);
        BAR();
        if (tid == 0) st_agent(ws + WS_FW2 + b * FS, tag);
        STAMP(27);
        u32x2 wdf = {0u, 0u};
        const u32 wdo = WS_FW2 + ((54 * cb + (l < 54 ? l : 0)) & 255) * FS;
        P4_RA(0, 0, 0);
#pragma unroll
        for (int n = 0; n < 7; ++n) P4_RB(0, 0, 0, n);
        static_for<0, 10>([&](auto T) {
            constexpr int t = T;
            constexpr bool f = t >= 1 && t + 2 <= 9, sN = t + 3 <= 9;
            if constexpr (t < 9) P4_HALF(0, true, t, 1, if (f && w < 4) P4_B1(t + 2, 3), if (f) P4_A1(t + 2, 0), if (f) P4_A1(t + 2, 1));
            else P4_HALF(0, true, t, 1, P5_B1(0, 3), P5_B1(0, 4), P5_B1(1, 0));
            if constexpr (t == 5) {
                if (tid < 128) {
                    f32x2 st = {0.f, 0.f};
#pragma unroll
                    for (int c = 0; c < 8; ++c) st += *(LAS f32x2*)(size_t)(L0 + L_ST4 + (c * 128 + tid) * 8);
                    const float mu = st[0] * (1.f / 640.f), var = fmaxf(st[1] * (1.f / 640.f) - mu * mu, 0.f), r = __builtin_amdgcn_rsqf(var + 1e-5f);
                    stsf2(L0 + L_MR + tid * 8, (f32x2){r, -mu * r});
                } else {
                    const int e = tid - 128;
                    stsf(L0 + L_UVS + e * 4, (ldsf(L0 + L_UV + (e >> 5) * 512 + (e & 31) * 4) + ldsf(L0 + L_UV + (e >> 5) * 512 + 128 + (e & 31) * 4))
                                           + (ldsf(L0 + L_UV + (e >> 5) * 512 + 256 + (e & 31) * 4) + ldsf(L0 + L_UV + (e >> 5) * 512 + 384 + (e & 31) * 4)));
                    if (e < 64) { const int e2 = e + 384;
                        stsf(L0 + L_UVS + e2 * 4, (ldsf(L0 + L_UV + (e2 >> 5) * 512 + (e2 & 31) * 4) + ldsf(L0 + L_UV + (e2 >> 5) * 512 + 128 + (e2 & 31) * 4))
                                                + (ldsf(L0 + L_UV + (e2 >> 5) * 512 + 256 + (e2 & 31) * 4) + ldsf(L0 + L_UV + (e2 >> 5) * 512 + 384 + (e2 & 31) * 4))); }
                }
            }
            if constexpr (t < 9) {
                if constexpr (t == 0) vmw<2>();
                else if constexpr (t == 8) {
                    asm volatile("s_waitcnt vmcnt(0)" : "+v"(wdf) :: "memory");
                    if (w == 7) { if (!__all(wdf[0] == (u32)tag && wdf[1] == (u32)(tag >> 32))) poll_at(ws, wdo, l, tag, ws, 12); }
                }
                else { if (w < 4) vmw<6>(); else vmw<5>(); }
                BAR();
                if constexpr (t == 7) { if (w == 7) asm volatile("s_nop 4\n\tglobal_load_dwordx2 %0, %1, %2 sc1" : "=v"(wdf) : "v"(wdo), "s"(ws) : "memory"); }
                if constexpr (t < 8) P4_HALF(1, true, t + 1, 0, if (sN) P4_B1(t + 3, 0), if (sN) P4_B1(t + 3, 1), if (sN) P4_B1(t + 3, 2));
                else P4_HALF(1, true, t + 1, 0, P5_B1(0, 0), P5_B1(0, 1), P5_B1(0, 2));
            } else {
                P4_HALF(1, false, 0, 0, P5_B1(1, 1), P5_B1(1, 2), P5_B1(1, 3));
                P5_B1(1, 4);
            }
        });
#undef P4_B
#undef P4_B1
#undef P4_A1
#undef P4_RA
#undef P4_RB
#undef P4_MM
#undef P4_HALF
        STAMP(12);
        f32x2 mr[2];
#pragma unroll
        for (int m = 0; m < 2; ++m) mr[m] = *(LAS f32x2*)(size_t)(L0 + L_MR + (32 * wm + 16 * m + i16) * 8);
#pragma unroll
        for (int n = 0; n < 7; ++n) {
            const f32x4 u = ldsf4(L0 + L_UVS + (7 * wn + n) * 128 + 16 * g), v = ldsf4(L0 + L_UVS + (7 * wn + n) * 128 + 64 + 16 * g);
#pragma unroll
            for (int m = 0; m < 2; ++m) acc[n][m] = acc[n][m] * mr[m][0] + (u * mr[m][1] + v);
        }
        if (nj == 7) stsf4(L0 + L_XCH + w * 1024 + lo16, wn == 0 ? acc[6][1] : acc[6][0]);
        auto silu_mul = [&](const f32x4 gt, const f32x4 up) -> u32x2 {
            const f32x4 e = gt * (-LOG2E), gu = gt * up;
            f32x4 d;
#pragma unroll
            for (int r = 0; r < 4; ++r) d[r] = __builtin_amdgcn_exp2f(e[r]);
            d = d + 1.f;
#pragma unroll
            for (int r = 0; r < 4; ++r) d[r] = __builtin_amdgcn_rcpf(d[r]);
            const f32x4 o = gu * d;
            return (u32x2){pack2(o[0], o[1]), pack2(o[2], o[3])};
        };
#pragma unroll
        for (int a = 0; a < 3; ++a) {
            const int la = a + 4 * wn, gam = Gj + (la <= 3 || nj == 7 ? la : la - 1);
            const u32x2 o0 = silu_mul(acc[2 * a][0], acc[2 * a + 1][0]), o1 = silu_mul(acc[2 * a][1], acc[2 * a + 1][1]);
            const auto s0 = __builtin_amdgcn_permlane16_swap(o0[0], o1[0], false, false);
            const auto s1 = __builtin_amdgcn_permlane16_swap(o0[1], o1[1], false, false);
            const u32x4 v = {s0[0], s1[0], s0[1], s1[1]};
            ST16C(v, WS_ACT + (((rb * 8 + 2 * wm + (g & 1)) * 216 + 2 * gam + (g >> 1)) * 16 + i16) * 16);
        }
        STAMP(28);
        BAR();
        if (nj == 7) {
            const f32x4 other = ldsf4(L0 + L_XCH + (w ^ 4) * 1024 + lo16);
            const u32x2 o = wn == 0 ? silu_mul(acc[6][0], other) : silu_mul(other, acc[6][1]);
            ST8C(o, WS_ACT + (((rb * 8 + 2 * wm + wn) * 216 + 2 * (Gj + 3) + (g >> 1)) * 16 + i16) * 16 + 8 * (g & 1));
        }
        P5_B1(2, 0); P5_B1(2, 1); P5_B1(2, 2); P5_B1(2, 3); P5_B1(2, 4);
        vmw<5>();
        BAR();
        if (tid == 0) st_agent(ws + WS_FACT + (rb * 16 + j) * FS, tag);
    }
    STAMP(13);

    {
        if (w == 1 && l < 20) stsf4(L0 + L_GB + l * 16, gather1(ws, 800 + 20 * cb + l, tag, ws, 8));
        if (w == 0) poll_flags(ws + WS_FACT + rb * 16 * FS, 16, l, tag, ws, 9);
        const bool bump = rb == 15 && j == 0;
        if (bump && w >= 4) poll_at(ws, WS_FO + (tid - 256) * FS, l, tag, ws, 10);
        VMCNT(0);
        BAR();
        if (bump && tid == 0) st_agent(ws + WS_EPOCH, tag);
        STAMP(14);
        const char* asrc = ws + WS_ACT + (mt3 * 216 + wk * 108) * 256;
        u32x4 a[3][4];
#define P5_A1(t, i) a[(t) % 3][i] = gld16c((i) * 1024, asrc + (t) * 4096, lo16)
        P5_A1(0, 0); P5_A1(0, 1); P5_A1(0, 2); P5_A1(0, 3); P5_A1(1, 0); P5_A1(1, 1); P5_A1(1, 2); P5_A1(1, 3); P5_A1(2, 0); P5_A1(2, 1); P5_A1(2, 2); P5_A1(2, 3);
        f32x4 acc[5];
#pragma unroll
        for (int n = 0; n < 5; ++n) acc[n] = Z4;
        const u32 rb5 = opaque(L0 + wk * 20480 + lo16);
        u32x4 fb[2][5];
#define P5_RD(buf, t, kk) do { _Pragma("unroll") for (int n = 0; n < 5; ++n) fb[buf][n] = lds128(rb5 + r5_slot(t) + (kk) * 5120 + n * 1024); } while (0)
#define P5_MM(buf, av) do { _Pragma("unroll") for (int n = 0; n < 5; ++n) acc[n] = mfma(fb[buf][n], (av), acc[n]); } while (0)
#define P5_MIX() do { _Pragma("unroll") for (int n = 0; n < 5; ++n) { SGB(M_DSR, 1); SGB(M_MFMA, 1); } } while (0)
        P5_RD(0, 0, 0);
        static_for<0, 7>([&](auto T) {
            constexpr int t = T, q = t % 3;
            constexpr bool nx = t + 3 < 7;
            constexpr int NA = t == 0 ? 8 : t == 1 ? 13 : t == 2 ? 18 : t == 3 ? 21 : t == 4 ? 20 : t == 5 ? 11 : 5;
            constexpr int NB = t == 2 ? 12 : t == 3 ? 12 : t == 4 ? 8 : t == 5 ? 0 : 63;
            vmw4<NA>(a[q][0], a[q][1], a[q][2], a[q][3]);
            SB0(); P5_RD(1, t, 1); P5_MM(0, a[q][0]); P5_MIX(); SB0();
            if constexpr (nx) P5_A1(t + 3, 0);
            SB0(); P5_RD(0, t, 2); P5_MM(1, a[q][1]); P5_MIX(); SB0();
            if constexpr (nx) P5_A1(t + 3, 1);
            if constexpr (t < 6) {
                SB0(); P5_RD(1, t, 3); P5_MM(0, a[q][2]); P5_MIX(); SB0();
                if constexpr (nx) P5_A1(t + 3, 2);
                vmw<NB>(); BAR();
                if constexpr (nx) { P5_B1(t + 3, 0); P5_B1(t + 3, 1); }
                SB0(); P5_RD(0, t + 1, 0); P5_MM(1, a[q][3]); P5_MIX(); SB0();
                if constexpr (nx) { if constexpr (t + 3 < 6) P5_A1(t + 3, 3); P5_B1(t + 3, 2); P5_B1(t + 3, 3); P5_B1(t + 3, 4); }
            } else {
                SB0(); P5_MM(0, a[q][2]); SB0();
            }
        });
#undef P5_B1
#undef P5_A1
#undef P5_RD
#undef P5_MM
#undef P5_MIX
        STAMP(15);
        if (wk == 1) {
#pragma unroll
            for (int n = 0; n < 5; ++n) stsf4(L0 + L_RED5 + (wm * 5 + n) * 1024 + lo16, acc[n]);
        }
        BAR();
        if (wk == 0) {
#pragma unroll
            for (int n = 0; n < 5; ++n) {
                const f32x4 v = acc[n] + ldsf4(L0 + L_RED5 + (wm * 5 + n) * 1024 + lo16);
                const f32x4 gb = ldsf4(L0 + L_GB + (16 * n + 4 * g) * 4);
                const u32x2 o = {pack2(bflo(x1k[n][0]) + gb[0] * v[0], bfhi(x1k[n][0]) + gb[1] * v[1]), pack2(bflo(x1k[n][1]) + gb[2] * v[2], bfhi(x1k[n][1]) + gb[3] * v[3])};
                sts64(L0 + L_OS + (16 * wm + i16) * 160 + 32 * n + 8 * g, o);
            }
        }
        BAR();
#pragma unroll
        for (int i = 0; i < 2; ++i) {
            const int id = tid + 512 * i;
            if (id < 640) {
                const int r = (id * 6554) >> 16, c = id - 10 * r;
                *(GAS u32x4*)((char*)p.out + (rb * 128 + 64 * hf + r) * 1280 + cb * 160 + c * 16) = lds128(L0 + L_OS + id * 16);
            }
        }
    }
    STAMP(16); STAMP_RT(63); STAMP_OUT();
    asm volatile(".Lmk_code_to:");
}

extern "C" void kernel_launch(void* const* d_in, const int* in_sizes, int n_in, void* d_out, int out_size, void* d_ws, size_t ws_size, hipStream_t stream) {
    Params p;
    p.x = (const u16*)d_in[0]; p.wqkv = (const u16*)d_in[1]; p.wo = (const u16*)d_in[2];
    p.wg = (const u16*)d_in[4]; p.wu = (const u16*)d_in[5]; p.wd = (const u16*)d_in[6];
    p.c = (const u16*)d_in[7]; p.wmod = (const u16*)d_in[8];
    p.out = (u16*)d_out; p.ws = (char*)d_ws;
    mega_kernel<<<256, 512, 0, stream>>>(p);
}
```

```cpp
#include <hip/hip_runtime.h>
#include <hip/hip_bf16.h>
#include <stdint.h>
#include <type_traits>

typedef unsigned short u16;
typedef unsigned u32;
typedef unsigned long long u64;
typedef __attribute__((ext_vector_type(2))) unsigned u32x2;
typedef __attribute__((ext_vector_type(4))) unsigned u32x4;
typedef __attribute__((ext_vector_type(2))) float f32x2;
typedef __attribute__((ext_vector_type(4))) float f32x4;
typedef __attribute__((ext_vector_type(2))) __bf16 bf16x2;
typedef __attribute__((ext_vector_type(8))) __bf16 bf16x8;
typedef __attribute__((ext_vector_type(4))) short s16x4;
#define LAS __attribute__((address_space(3)))
#define GAS __attribute__((address_space(1)))
#define DI __device__ __forceinline__

constexpr int SEQ = 2048, DM = 640, FF = 1728;
constexpr float QSCALE = 0.22360679774997896f * 1.4426950408889634f;
constexpr float LOG2E = 1.4426950408889634f;

constexpr u32 WS_EPOCH = 0;
constexpr u32 WS_ERR   = 256;
constexpr u32 FS = 128;
constexpr u32 WS_FKV   = 4096;
constexpr u32 WS_FO    = WS_FKV + 256 * FS;
constexpr u32 WS_FX1   = WS_FO + 256 * FS;
constexpr u32 WS_FACT  = WS_FX1 + 256 * FS;
constexpr u32 WS_FH    = WS_FACT + 256 * FS;
constexpr u32 WS_FWT   = WS_FH + 256 * FS;
constexpr u32 WS_FWP   = WS_FWT + 256 * FS;
constexpr u32 WS_FW2   = WS_FWP + 256 * FS;
constexpr u32 WS_MODG  = WS_FW2 + 256 * FS;
constexpr u32 WS_UV    = WS_MODG + 61440;
constexpr u32 WS_STAT  = WS_UV + 114688;
constexpr u32 WS_KV    = 589824;
constexpr u32 WS_O     = WS_KV + 256 * 22528;
constexpr u32 WS_X1    = WS_O + SEQ * DM * 2;
constexpr u32 WS_ACT   = WS_X1 + SEQ * DM * 2;
constexpr u32 WS_WP    = WS_ACT + SEQ * FF * 2;
constexpr u32 WS_H     = WS_WP + 16 * 20 * 14 * 1024;
constexpr u32 WS_WD    = WS_H + SEQ * DM * 2;
constexpr u32 WS_WO    = WS_WD + 8 * 54 * 5 * 1024;
constexpr u32 WS_WT    = WS_WO + 8 * 20 * 5 * 1024;
constexpr u32 WS_STAMP = WS_WT + 16 * 153600;
constexpr u32 WS_END   = WS_STAMP + 256 * 8 * 64 * 4;

constexpr u32 L_WKV = 0, L_WQ = 102400, L_GS = 153600, L_STP = 156160, L_MODP = 156416, L_CNT = 157440, L_SB = 157696, L_G2 = 158016, L_Z16 = 158656;
constexpr u32 L_ODRAW = 101376, L_WTRAW = 116736, L_XS = 133120;
constexpr u32 L_K = 0, L_V = 30720, L_Q = 67584, L_WPRAW = 77824, L_UVL = 98304;
constexpr u32 L_WO = 61440, L_RED3 = 0, L_X1S = 20480, L_XR = 30720, L_GA = 40960, L_ST3 = 41472, L_WDRAW = 43008;
constexpr u32 L_R4 = 0, R4_STAGE = 45056, L_ST4 = 135168, L_UV = 143360, L_MR = 150528, L_XCH = 151552, L_UVS = 159744;
constexpr u32 L_RED5 = 126976, L_GB = 147456, L_OS = 148480;
constexpr u32 r5_slot(int t) { return t % 3 == 0 ? 45056 : t % 3 == 1 ? 86016 : 0; }

struct Params {
    const u16* x; const u16* wqkv; const u16* wo; const u16* wg; const u16* wu; const u16* wd;
    const u16* c; const u16* wmod; u16* out; char* ws;
};

DI float bflo(u32 d) { return __uint_as_float(d << 16); }
DI float bfhi(u32 d) { return __uint_as_float(d & 0xffff0000u); }
DI u32 pack2(float a, float b) { f32x2 v = {a, b}; return __builtin_bit_cast(u32, __builtin_convertvector(v, bf16x2)); }
DI float rbf(float a) { return bflo(pack2(a, a)); }
DI f32x4 mfma(u32x4 a, u32x4 b, f32x4 c) {
    return __builtin_amdgcn_mfma_f32_16x16x32_bf16(__builtin_bit_cast(bf16x8, a), __builtin_bit_cast(bf16x8, b), c, 0, 0, 0);
}
DI u32x2 ldtr(u32 a) { return __builtin_bit_cast(u32x2, __builtin_amdgcn_ds_read_tr16_b64_v4i16((LAS s16x4*)(size_t)a)); }
DI u32x4 ldtr2(u32 a0, u32 a1) { u32x2 x = ldtr(a0), y = ldtr(a1); return (u32x4){x[0], x[1], y[0], y[1]}; }
DI u32x4 lds128(u32 a) { return *(LAS u32x4*)(size_t)a; }
DI u32x2 lds64(u32 a) { return *(LAS u32x2*)(size_t)a; }
DI f32x4 ldsf4(u32 a) { return *(LAS f32x4*)(size_t)a; }
DI float ldsf(u32 a) { return *(LAS float*)(size_t)a; }
DI void sts128(u32 a, u32x4 v) { *(LAS u32x4*)(size_t)a = v; }
DI void sts64(u32 a, u32x2 v) { *(LAS u32x2*)(size_t)a = v; }
DI void stsf(u32 a, float v) { *(LAS float*)(size_t)a = v; }
DI void stsf4(u32 a, f32x4 v) { *(LAS f32x4*)(size_t)a = v; }
DI void stsf2(u32 a, f32x2 v) { *(LAS f32x2*)(size_t)a = v; }
DI void sts16(u32 a, u16 v) { *(LAS u16*)(size_t)a = v; }

DI void glds16_(const void* root, u32 voff, u32 ldsdst) {
    asm volatile("s_mov_b32 m0, %2\n\ts_nop 0\n\tglobal_load_lds_dwordx4 %0, %1" :: "v"(voff), "s"(root), "s"(__builtin_amdgcn_readfirstlane(ldsdst)) : "memory");
}
DI void glds16c_(const void* root, u32 voff, u32 ldsdst) {
    asm volatile("s_mov_b32 m0, %2\n\ts_nop 0\n\tglobal_load_lds_dwordx4 %0, %1 sc1" :: "v"(voff), "s"(root), "s"(__builtin_amdgcn_readfirstlane(ldsdst)) : "memory");
}
template <int OFF> DI void glds16c_next_(const void* root, u32 voff) {
    asm volatile("global_load_lds_dwordx4 %0, %1 offset:%2 sc1" :: "v"(voff), "s"(root), "i"(OFF) : "memory");
}
template <int OFF> DI u32x4 gld16(const void* root, u32 voff) {
    u32x4 r; asm volatile("global_load_dwordx4 %0, %1, %2 offset:%3" : "=v"(r) : "v"(voff), "s"(root), "i"(OFF) : "memory"); return r;
}
template <int OFF> DI u32x4 gld16c_(const void* root, u32 voff) {
    u32x4 r; asm volatile("global_load_dwordx4 %0, %1, %2 offset:%3 sc1" : "=v"(r) : "v"(voff), "s"(root), "i"(OFF) : "memory"); return r;
}
#define ROFF(root, ptr) ((u32)((const char*)(ptr) - (const char*)(root)))
#define glds16(root, ptr, voff, dst) glds16_((root), ROFF(root, ptr) + (voff), (dst))
#define glds16c(ptr, voff, dst) glds16c_(ws, ROFF(ws, ptr) + (voff), (dst))
#define glds16c_next(OFF, ptr, voff) glds16c_next_<(OFF)>(ws, ROFF(ws, ptr) + (voff))
#define gld16c(OFF, ptr, voff) gld16c_<(OFF)>(ws, ROFF(ws, ptr) + (voff))
DI u32 gldu16(const void* sbase, u32 voff) {
    u32 r; asm volatile("global_load_ushort %0, %1, %2" : "=v"(r) : "v"(voff), "s"(sbase) : "memory"); return r;
}
#define VMCNT(n) asm volatile("s_waitcnt vmcnt(" #n ")" ::: "memory")
#define BAR() asm volatile("s_waitcnt lgkmcnt(0)\n\ts_barrier" ::: "memory")
#define RLX_AGENT __ATOMIC_RELAXED, __HIP_MEMORY_SCOPE_AGENT
DI u64 ld_agent(const char* p) { return __hip_atomic_load((GAS u64*)p, RLX_AGENT); }
DI void st_agent(char* p, u64 v) { __hip_atomic_store((GAS u64*)p, v, RLX_AGENT); }
DI void st_agent32(char* p, u32 v) { __hip_atomic_store((GAS u32*)p, v, RLX_AGENT); }
DI float ld_agentf(const char* p) { return __uint_as_float(__hip_atomic_load((GAS u32*)p, RLX_AGENT)); }
DI float xsum4(float x) {
    const auto s = __builtin_amdgcn_permlane16_swap(__float_as_uint(x), __float_as_uint(x), false, false);
    x = __uint_as_float(s[0]) + __uint_as_float(s[1]);
    const auto t = __builtin_amdgcn_permlane32_swap(__float_as_uint(x), __float_as_uint(x), false, false);
    return __uint_as_float(t[0]) + __uint_as_float(t[1]);
}
DI float xmax4(float x) {
    const auto s = __builtin_amdgcn_permlane16_swap(__float_as_uint(x), __float_as_uint(x), false, false);
    x = fmaxf(__uint_as_float(s[0]), __uint_as_float(s[1]));
    const auto t = __builtin_amdgcn_permlane32_swap(__float_as_uint(x), __float_as_uint(x), false, false);
    return fmaxf(__uint_as_float(t[0]), __uint_as_float(t[1]));
}
template <int CTRL> DI float dppf(float v) { return __builtin_bit_cast(float, __builtin_amdgcn_update_dpp(0, __builtin_bit_cast(int, v), CTRL, 0xf, 0xf, true)); }

constexpr u32 SPIN_CAP = 1u << 17;
DI void poll_flags(const char* base, int n, int lane, u64 tag, char* ws, u32 code) {
    const char* a = base + (lane < n ? lane : 0) * FS;
    u32 spins = 0;
    for (;;) {
        const u64 v = ld_agent(a);
        if (__all(v == tag)) break;
        if (++spins > SPIN_CAP) { if (lane == 0) st_agent32(ws + WS_ERR, code); break; }
        __builtin_amdgcn_s_sleep(1);
    }
}
typedef __attribute__((ext_vector_type(2))) _Float16 f16x2;
DI float hlo(u32 d) { return (float)__builtin_bit_cast(f16x2, d)[0]; }
DI float hhi(u32 d) { return (float)__builtin_bit_cast(f16x2, d)[1]; }
DI u32 packh(float a, float b) { const f16x2 v = {(_Float16)a, (_Float16)b}; return __builtin_bit_cast(u32, v); }
DI bool gran_ok(const u32x4& v, u64 tag) { return v[2] == (u32)tag && v[3] == (u32)(tag >> 32); }
DI f32x4 gran_sum(const u32x4& a, const u32x4& b, const u32x4& c, const u32x4& d) {
    const f32x4 s = {(hlo(a[0]) + hlo(b[0])) + (hlo(c[0]) + hlo(d[0])), (hhi(a[0]) + hhi(b[0])) + (hhi(c[0]) + hhi(d[0])),
                     (hlo(a[1]) + hlo(b[1])) + (hlo(c[1]) + hlo(d[1])), (hhi(a[1]) + hhi(b[1])) + (hhi(c[1]) + hhi(d[1]))};
    return (f32x4){rbf(s[0]), rbf(s[1]), rbf(s[2]), rbf(s[3])};
}
DI f32x4 gather1(const char* ws, u32 g0, u64 tag, char* wsw, u32 code) {
    u32 spins = 0; u32x4 v0, v1, v2, v3;
    const u32 o = WS_MODG + g0 * 16;
    for (;;) {
        asm volatile("s_nop 4\n\tglobal_load_dwordx4 %0, %4, %5 sc1\n\tglobal_load_dwordx4 %1, %6, %5 sc1\n\tglobal_load_dwordx4 %2, %7, %5 sc1\n\tglobal_load_dwordx4 %3, %8, %5 sc1\n\ts_waitcnt vmcnt(0)"
                     : "=&v"(v0), "=&v"(v1), "=&v"(v2), "=&v"(v3) : "v"(o), "s"(ws), "v"(o + 15360), "v"(o + 30720), "v"(o + 46080) : "memory");
        if (__all(gran_ok(v0, tag) && gran_ok(v1, tag) && gran_ok(v2, tag) && gran_ok(v3, tag))) break;
        if (++spins > SPIN_CAP) { st_agent32(wsw + WS_ERR, code); break; }
        __builtin_amdgcn_s_sleep(1);
    }
    return gran_sum(v0, v1, v2, v3);
}
DI void gather2(const char* ws, u32 g0, u32 g1, u64 tag, char* wsw, u32 code, f32x4& ra, f32x4& rb) {
    u32 spins = 0; u32x4 v0, v1, v2, v3, u0, u1, u2, u3;
    const u32 o = WS_MODG + g0 * 16, q = WS_MODG + g1 * 16;
    for (;;) {
        asm volatile("s_nop 4\n\tglobal_load_dwordx4 %0, %8, %9 sc1\n\tglobal_load_dwordx4 %1, %10, %9 sc1\n\tglobal_load_dwordx4 %2, %11, %9 sc1\n\tglobal_load_dwordx4 %3, %12, %9 sc1\n\t"
                     "global_load_dwordx4 %4, %13, %9 sc1\n\tglobal_load_dwordx4 %5, %14, %9 sc1\n\tglobal_load_dwordx4 %6, %15, %9 sc1\n\tglobal_load_dwordx4 %7, %16, %9 sc1\n\ts_waitcnt vmcnt(0)"
                     : "=&v"(v0), "=&v"(v1), "=&v"(v2), "=&v"(v3), "=&v"(u0), "=&v"(u1), "=&v"(u2), "=&v"(u3)
                     : "v"(o), "s"(ws), "v"(o + 15360), "v"(o + 30720), "v"(o + 46080), "v"(q), "v"(q + 15360), "v"(q + 30720), "v"(q + 46080) : "memory");
        if (__all(gran_ok(v0, tag) && gran_ok(v1, tag) && gran_ok(v2, tag) && gran_ok(v3, tag) && gran_ok(u0, tag) && gran_ok(u1, tag) && gran_ok(u2, tag) && gran_ok(u3, tag))) break;
        if (++spins > SPIN_CAP) { st_agent32(wsw + WS_ERR, code); break; }
        __builtin_amdgcn_s_sleep(1);
    }
    ra = gran_sum(v0, v1, v2, v3); rb = gran_sum(u0, u1, u2, u3);
}
struct Gather { u32x4 v0, v1, v2, v3, u0, u1, u2, u3; };
DI void gather_ask(const char* ws, u32 g0, u32 g1, Gather& G) {
    const u32 o = WS_MODG + g0 * 16, q = WS_MODG + g1 * 16;
    asm volatile("global_load_dwordx4 %0, %8, %9 sc1\n\tglobal_load_dwordx4 %1, %10, %9 sc1\n\tglobal_load_dwordx4 %2, %11, %9 sc1\n\tglobal_load_dwordx4 %3, %12, %9 sc1\n\t"
                 "global_load_dwordx4 %4, %13, %9 sc1\n\tglobal_load_dwordx4 %5, %14, %9 sc1\n\tglobal_load_dwordx4 %6, %15, %9 sc1\n\tglobal_load_dwordx4 %7, %16, %9 sc1"
                 : "=&v"(G.v0), "=&v"(G.v1), "=&v"(G.v2), "=&v"(G.v3), "=&v"(G.u0), "=&v"(G.u1), "=&v"(G.u2), "=&v"(G.u3)
                 : "v"(o), "s"(ws), "v"(o + 15360), "v"(o + 30720), "v"(o + 46080), "v"(q), "v"(q + 15360), "v"(q + 30720), "v"(q + 46080) : "memory");
}
DI void gather_finish(const char* ws, u32 g0, u32 g1, Gather& G, u64 tag, char* wsw, u32 code, f32x4& ra, f32x4& rb) {
    asm volatile("" : "+v"(G.v0), "+v"(G.v1), "+v"(G.v2), "+v"(G.v3), "+v"(G.u0), "+v"(G.u1), "+v"(G.u2), "+v"(G.u3) :: "memory");
    if (__all(gran_ok(G.v0, tag) && gran_ok(G.v1, tag) && gran_ok(G.v2, tag) && gran_ok(G.v3, tag) && gran_ok(G.u0, tag) && gran_ok(G.u1, tag) && gran_ok(G.u2, tag) && gran_ok(G.u3, tag))) {
        ra = gran_sum(G.v0, G.v1, G.v2, G.v3); rb = gran_sum(G.u0, G.u1, G.u2, G.u3);
    } else gather2(ws, g0, g1, tag, wsw, code, ra, rb);
}
DI void gather_strip(const char* ws, u32 gs, u32 gc, u32 strip, int l, u64 tag, char* wsw, u32 code) {
    f32x4 a, c;
    gather2(ws, l < 40 ? gs + l : gc + (l - 40), gc + 24 + (l & 15), tag, wsw, code, a, c);
    if (l >= 40) a = a + 1.f;
    c = c + 1.f;
    *(LAS u32x2*)(size_t)(strip + (l < 40 ? l * 8 : 320 + (l - 40) * 8)) = (u32x2){pack2(a[0], a[1]), pack2(a[2], a[3])};
    if (l < 16) *(LAS u32x2*)(size_t)(strip + 320 + (24 + l) * 8) = (u32x2){pack2(c[0], c[1]), pack2(c[2], c[3])};
}
DI void put_strip_spread(const f32x4& a, const f32x4& c, u32 strip, int l) {
    if (l < 40) *(LAS u32x2*)(size_t)(strip + l * 8) = (u32x2){pack2(a[0], a[1]), pack2(a[2], a[3])};
    else {
        const u32 p0 = pack2(a[0] + 1.f, a[1] + 1.f), p1 = pack2(a[2] + 1.f, a[3] + 1.f);
        *(LAS u32x4*)(size_t)(strip + 320 + (l - 40) * 16) = (u32x4){p0 & 0xffffu, p0 & 0xffff0000u, p1 & 0xffffu, p1 & 0xffff0000u};
    }
    if (l < 16) {
        const u32 p0 = pack2(c[0] + 1.f, c[1] + 1.f), p1 = pack2(c[2] + 1.f, c[3] + 1.f);
        *(LAS u32x4*)(size_t)(strip + 320 + (24 + l) * 16) = (u32x4){p0 & 0xffffu, p0 & 0xffff0000u, p1 & 0xffffu, p1 & 0xffff0000u};
    }
    if (l == 63) *(LAS u32x4*)(size_t)(strip + 960) = (u32x4){0, 0, 0, 0};
}
DI void poll_at(const char* ws, u32 off, int lane, u64 tag, char* wsw, u32 code) {
    u32 spins = 0;
    for (;;) {
        const u64 v = ld_agent(ws + off);
        if (__all(v == tag)) break;
        if (++spins > SPIN_CAP) { if (lane == 0) st_agent32(wsw + WS_ERR, code); break; }
        __builtin_amdgcn_s_sleep(1);
    }
}

#define STAMP(i) do {} while (0)
#define STAMPL(i) do {} while (0)
#define STAMP_RT(i) do {} while (0)
#define STAMP_OUT() do {} while (0)

template <int N> using IC = std::integral_constant<int, N>;
template <int I, int E, class F> DI void static_for(F&& f) { if constexpr (I < E) { f(IC<I>{}); static_for<I + 1, E>(f); } }
template <int N> DI void vmw() { asm volatile("s_waitcnt vmcnt(%0)" :: "i"(N) : "memory"); }
template <int N> DI void vmw1(u32x4& a) { asm volatile("s_waitcnt vmcnt(%1)" : "+v"(a) : "i"(N) : "memory"); }
template <int N> DI void vmw4(u32x4& a, u32x4& b, u32x4& c, u32x4& d) { asm volatile("s_waitcnt vmcnt(%4)" : "+v"(a), "+v"(b), "+v"(c), "+v"(d) : "i"(N) : "memory"); }
DI u32 opaque(u32 a) { asm("" : "+v"(a)); return a; }
DI float dot2(u32 a, u32 b, float c) { return __builtin_amdgcn_fdot2_f32_bf16(__builtin_bit_cast(bf16x2, a), __builtin_bit_cast(bf16x2, b), c, false); }
#define SB0() __builtin_amdgcn_sched_barrier(0)
#define SGB(m, n) __builtin_amdgcn_sched_group_barrier((m), (n), 0)
constexpr int M_MFMA = 0x8, M_DSR = 0x100;
#define Z4 ((f32x4){0.f, 0.f, 0.f, 0.f})
DI u32 lds_add(u32 a, u32 v) { return __hip_atomic_fetch_add((LAS u32*)(size_t)a, v, __ATOMIC_RELAXED, __HIP_MEMORY_SCOPE_WORKGROUP); }
DI u32 lds_ld(u32 a) { return __hip_atomic_load((LAS u32*)(size_t)a, __ATOMIC_RELAXED, __HIP_MEMORY_SCOPE_WORKGROUP); }
DI void worker_sync(u32 cnt, int lane) {
    asm volatile("s_waitcnt lgkmcnt(0)" ::: "memory");
    if (lane == 0) lds_add(cnt, 1u);
    for (u32 spins = 0; lds_ld(cnt) < 4u && spins < (1u << 22); ++spins) {}
    asm volatile("" ::: "memory");
}

__global__ __launch_bounds__(512, 2) void mega_kernel(Params p) {
    __shared__ __attribute__((aligned(16))) char ldsmem[163840];
    const u32 L0 = (u32)(uintptr_t)(LAS char*)ldsmem;
    const int tid = threadIdx.x, l = tid & 63;
    const int w = __builtin_amdgcn_readfirstlane(tid >> 6);
    const int b = blockIdx.x, xg = b & 7, sidx = b >> 3;
    const int rb = 4 * (xg >> 1) + (sidx >> 3), j = 8 * (xg & 1) + (sidx & 7);
    const int g = l >> 4, i16 = l & 15, q4 = i16 >> 2, p4 = l & 3;
    u32 wu0 = 0;
    if (w == 7) {
        const char* pc; u32 span;
        asm volatile("s_getpc_b64 %0\n.Lmk_code_from:\n\ts_mov_b32 %1, .Lmk_code_to-.Lmk_code_from" : "=s"(pc), "=s"(span));
        const u32 off = min((u32)(sidx + 32 * min(l, 15)) * 128u, span - 4u);
        asm volatile("s_nop 0\n\tglobal_load_dword %0, %1, %2" : "=v"(wu0) : "v"(off), "s"(pc) : "memory");
    }
    char* const ws = p.ws;
    asm volatile("" :: "s"(p.x), "s"(p.wqkv), "s"(p.wo), "s"(p.wg), "s"(p.wu), "s"(p.wd), "s"(p.c), "s"(p.wmod), "s"(p.out), "s"(p.ws));
    u64 tag;
#define ASK_TAG(ep) u32x2 ep; asm volatile("global_load_dwordx2 %0, %1, %2" : "=v"(ep) : "v"(WS_EPOCH), "s"(ws) : "memory")
#define TAKE_TAG(ep) do { asm volatile("s_waitcnt vmcnt(0)" : "+v"(ep) :: "memory"); \
        tag = (((u64)(u32)__builtin_amdgcn_readfirstlane(ep[1]) << 32) | (u32)__builtin_amdgcn_readfirstlane(ep[0])) + 1; } while (0)
    const __amdgpu_buffer_rsrc_t wsr = __builtin_amdgcn_make_buffer_rsrc(ws, 0, WS_END, 0x00020000);
#define ST16C(v, off) __builtin_amdgcn_raw_buffer_store_b128((v), wsr, (off), 0, 16)
#define ST8C(v, off)  __builtin_amdgcn_raw_buffer_store_b64((v), wsr, (off), 0, 16)
    STAMP_RT(62); STAMP(0);

    const int nj = j < 12 ? 7 : 6, Gj = j < 12 ? 7 * j : 84 + 6 * (j - 12);
    const bool wpwg = b < 216;
    const int wpm = b >= 108 ? 1 : 0, wpb = (b - 108 * wpm) >> 2, wpq = b & 3;
    u32x4 mw[5]; u32 mc[5];
    auto mod_ask = [&]() {
#pragma unroll
        for (int i = 0; i < 5; ++i) {
            const int r = 160 * (b & 3) + ((tid & 255) >> 3) + 32 * i;
            mw[i] = gld16<0>(p.wmod, r * 7680 + (b >> 2) * 128 + (tid & 7) * 16);
            mc[i] = gldu16(p.c, r * 2);
        }
    };
    auto mod_finish = [&]() {
        float acc[8];
#pragma unroll
        for (int e = 0; e < 8; ++e) acc[e] = 0.f;
#pragma unroll
        for (int i = 0; i < 5; ++i) {
            const float cv = bflo(mc[i]);
            const float sv = cv * __builtin_amdgcn_rcpf(1.f + __builtin_amdgcn_exp2f(-cv * LOG2E));
            const u32 slo = pack2(sv, 0.f), shi = slo << 16;
#pragma unroll
            for (int d = 0; d < 4; ++d) { acc[2 * d] = dot2(mw[i][d], slo, acc[2 * d]); acc[2 * d + 1] = dot2(mw[i][d], shi, acc[2 * d + 1]); }
        }
#pragma unroll
        for (int e = 0; e < 8; ++e) { const float v = acc[e]; acc[e] = xsum4(v + dppf<0x128>(v)); }
        if (l < 8) {
            stsf4(L0 + L_MODP + (w & 3) * 256 + l * 32, (f32x4){acc[0], acc[1], acc[2], acc[3]});
            stsf4(L0 + L_MODP + (w & 3) * 256 + l * 32 + 16, (f32x4){acc[4], acc[5], acc[6], acc[7]});
        }
        asm volatile("s_waitcnt lgkmcnt(0)" ::: "memory");
        u32 old = 0;
        if (l == 0) old = lds_add(L0 + L_CNT, 1u);
        if (__builtin_amdgcn_readfirstlane(old) == 3) {
            float v = 0.f;
#pragma unroll
            for (int ww = 0; ww < 4; ++ww) v += ldsf(L0 + L_MODP + ww * 256 + l * 4);
            const float v1 = dppf<0x55>(v), v2 = dppf<0xAA>(v), v3 = dppf<0xFF>(v), v0 = dppf<0x00>(v);
            const u32x4 gr = {packh(v0, v1), packh(v2, v3), (u32)tag, (u32)(tag >> 32)};
            if (p4 == 0) ST16C(gr, WS_MODG + ((b & 3) * 960 + (b >> 2) * 16 + (l >> 2)) * 16);
        }
    };
#define MOD_LANDED() asm volatile("s_waitcnt vmcnt(0)" : "+v"(mw[0]), "+v"(mw[1]), "+v"(mw[2]), "+v"(mw[3]), "+v"(mw[4]), "+v"(mc[0]), "+v"(mc[1]), "+v"(mc[2]), "+v"(mc[3]), "+v"(mc[4]) :: "memory")
    if (w >= 4) {
        __builtin_amdgcn_s_setprio(3);
        const int u = w - 4;
        const bool tw = b >= 106;
        const int tb = (b - 106) / 5, tk = (b - 106) - 5 * tb, ts = 4 * tk + u;
        if (tw) {
            const char* src = (const char*)p.wqkv + ts * 122880 + tb * 128;
            const u32 voff = (l >> 3) * 3840 + (l & 7) * 16;
#pragma unroll
            for (int i = 0; i < 4; ++i) glds16(p.wqkv, src + i * 8 * 3840, voff, L0 + L_WTRAW + u * 4096 + i * 1024);
        }
        ASK_TAG(epl);
        BAR();
        __builtin_amdgcn_s_setprio(0);
        TAKE_TAG(epl);
        if (tw) {
            const u32 a = L0 + L_WTRAW + u * 4096 + (8 * g + q4) * 128 + 8 * p4;
#pragma unroll
            for (int t = 0; t < 4; ++t) {
                const u32 cc = 8 * tb + 2 * t + (i16 >> 3);
                const u32 seg = cc >= 160 ? 2 : cc >= 80 ? 1 : 0, wi = cc - 80 * seg, jj = (wi * 13108) >> 16, c5 = wi - 5 * jj;
                const u32 c = seg == 0 ? 10 + c5 : seg == 1 ? c5 : 5 + c5, nt = c >> 1;
                const u32 dst = WS_WT + jj * 153600 + (nt < 5 ? ts * 5120 + nt * 1024 : 102400 + ts * 2560 + (nt - 5) * 1024)
                              + (nt < 7 ? (g * 16 + 8 * (c & 1) + (i16 & 7)) * 16 : g * 128 + (i16 & 7) * 16);
                ST16C(ldtr2(a + 32 * t, a + 32 * t + 512), dst);
            }
        }
        vmw<0>();
        {
            asm volatile("s_waitcnt lgkmcnt(0)" ::: "memory");
            u32 old = 0;
            if (l == 0) old = lds_add(L0 + L_CNT + 12, 1u);
            if (tw && __builtin_amdgcn_readfirstlane(old) == 3 && l == 0) st_agent(ws + WS_FWT + (b - 106) * FS, tag);
        }
        STAMPL(19);
        if (b >= 80 && b < 240) {
            if (!tw) __builtin_amdgcn_s_sleep(32);
            mod_ask(); MOD_LANDED(); mod_finish();
        }
        if (u == 0) {
            const int wh = l >= 30 ? 0 : l / 5, kr = l >= 30 ? 0 : l - 5 * wh;
            const int bl = (1280 * (wh >> 1) + 80 * j + 79 * (wh & 1)) >> 7;
            poll_at(ws, WS_FWT + (bl * 5 + kr) * FS, l, tag, ws, 13);
            for (u32 spins = 0; lds_ld(L0 + L_CNT + 12) < 4u && spins < (1u << 22); ++spins) {}
            if (l == 0) lds_add(L0 + L_CNT + 24, 1u);
        } else {
            for (u32 spins = 0; lds_ld(L0 + L_CNT + 24) < 1u && spins < (1u << 22); ++spins) {}
        }
        asm volatile("" ::: "memory");
        STAMPL(20);
        {
            const char* src = ws + WS_WT + j * 153600 + u * 1024;
            const u32 lo = l * 16;
#pragma unroll
            for (int i = 0; i < 25; ++i) glds16c(src + i * 4096, lo, L0 + L_WKV + u * 1024 + i * 4096);
        }
        VMCNT(0);
        STAMPL(17);
    } else {
        __builtin_amdgcn_s_setprio(2);
        if (b < 80) mod_ask();
        ASK_TAG(epw);
        if (tid < 16) *(LAS u32*)(size_t)(L0 + L_CNT + tid * 4) = 0u;
        BAR();
        STAMP(29);
        TAKE_TAG(epw);
        STAMP(30);
        if (b < 80) { MOD_LANDED(); mod_finish(); }
        {
            const char* src = (const char*)p.x + (rb * 128 + 8 * j) * 1280 + w * 1024;
            glds16(p.x, src, l * 16, L0 + L_XS + w * 1024);
            glds16(p.x, src + 4096, l * 16, L0 + L_XS + (w + 4) * 1024);
            if (w < 2) glds16(p.x, src + 8192, l * 16, L0 + L_XS + (w + 8) * 1024);
        }
        VMCNT(0);
        STAMP(1);
        worker_sync(L0 + L_CNT + 16, l);
        const int tok = l & 7;
        u32x4 xr[3];
#pragma unroll
        for (int i = 0; i < 3; ++i) xr[i] = lds128(L0 + L_XS + tok * 1280 + (20 * w + (i < 2 ? (l >> 3) + 8 * i : 16 + ((l >> 3) & 3))) * 16);
        {
            float s1 = 0.f, s2 = 0.f;
#pragma unroll
            for (int i = 0; i < 3; ++i) {
                float a1 = 0.f, a2 = 0.f;
#pragma unroll
                for (int d = 0; d < 4; ++d) { a1 = dot2(xr[i][d], 0x3F803F80u, a1); a2 = dot2(xr[i][d], xr[i][d], a2); }
                if (i < 2 || l < 32) { s1 += a1; s2 += a2; }
            }
            asm volatile("s_waitcnt lgkmcnt(0)" ::: "memory");
            if (l == 0) lds_add(L0 + L_CNT + 20, 1u);
            s1 += dppf<0x128>(s1); s2 += dppf<0x128>(s2);
            s1 = xsum4(s1); s2 = xsum4(s2);
            if (l < 8) stsf2(L0 + L_STP + (w * 8 + l) * 8, (f32x2){s1, s2});
        }
        gather_strip(ws, 40 * w, 160 + 40 * w, L0 + L_GS + w * 640, l, tag, ws, 1);
        STAMP(2);
        worker_sync(L0 + L_CNT + 4, l);
        {
            f32x2 st = {0.f, 0.f};
#pragma unroll
            for (int ww = 0; ww < 4; ++ww) st += *(LAS f32x2*)(size_t)(L0 + L_STP + (ww * 8 + tok) * 8);
            const float mu = st[0] * (1.f / 640.f), var = fmaxf(st[1] * (1.f / 640.f) - mu * mu, 0.f);
            const float rs = __builtin_amdgcn_rsqf(var + 1e-5f), nmr = -mu * rs;
#pragma unroll
            for (int i = 0; i < 3; ++i) {
                const int cl = i < 2 ? (l >> 3) + 8 * i : 16 + ((l >> 3) & 3);
                const u32x4 sa = lds128(L0 + L_GS + w * 640 + cl * 16), gg = lds128(L0 + L_GS + w * 640 + 320 + cl * 16);
                u32x4 h;
#pragma unroll
                for (int d = 0; d < 4; ++d)
                    h[d] = pack2((bflo(xr[i][d]) * rs + nmr) * bflo(gg[d]) + bflo(sa[d]), (bfhi(xr[i][d]) * rs + nmr) * bfhi(gg[d]) + bfhi(sa[d]));
                if (i < 2 || l < 32) ST16C(h, WS_H + (((rb * 8 + (j >> 1)) * 80 + 20 * w + cl) * 16 + 8 * (j & 1) + tok) * 16);
            }
        }
        VMCNT(0);
        worker_sync(L0 + L_CNT + 8, l);
        if (tid == 0) st_agent(ws + WS_FH + (rb * 16 + j) * FS, tag);
        if (w == 0) poll_flags(ws + WS_FH + rb * 16 * FS, 16, l, tag, ws, 11);
        __builtin_amdgcn_s_setprio(0);
    }
    BAR();
    asm volatile("s_waitcnt vmcnt(0)" : "+v"(wu0) :: "memory");
    STAMP(3);

    u32x4 xa[20];
    const u32 lo16 = l * 16;
    const char* hsrc = ws + WS_H + (rb * 8 + w) * 80 * 256;
#define XL(s) xa[s] = gld16c(((s) & 3) * 1024, hsrc + ((s) >> 2) * 4096, lo16)
    XL(0); XL(1); XL(2); XL(3); XL(4); XL(5);
    const char* kvx = ws + WS_KV + (rb * 16 + j) * 22528;
    const char* wtq = ws + WS_WT + j * 153600 + (100 + w) * 1024;
    {
        f32x4 acc[5];
#pragma unroll
        for (int n = 0; n < 5; ++n) acc[n] = Z4;
        const u32 b0 = L0 + L_WKV + lo16;
        u32x4 bf[2][5];
#pragma unroll
        for (int n = 0; n < 5; ++n) bf[0][n] = lds128(b0 + n * 1024);
        static_for<0, 20>([&](auto S) {
            constexpr int s = S;
            vmw1<(s < 14 ? 5 : 6)>(xa[s]);
            if constexpr (s + 6 < 20) XL(s + 6);
            if constexpr (s >= 13) { if (s < 19 || w < 2) glds16c(wtq + (s - 13) * 8192, lo16, L0 + L_WKV + (100 + w) * 1024 + (s - 13) * 8192); }
            SB0();
#pragma unroll
            for (int n = 0; n < 5; ++n) {
                if (s < 19) bf[(s + 1) & 1][n] = lds128(b0 + (s + 1) * 5120 + n * 1024);
                acc[n] = mfma(bf[s & 1][n], xa[s], acc[n]);
            }
#pragma unroll
            for (int n = 0; n < 5; ++n) { SGB(M_DSR, 1); SGB(M_MFMA, 1); }
            SB0();
        });
        STAMP(18);
        BAR();
        const int row = 256 + 16 * w + i16;
#pragma unroll
        for (int n = 0; n < 5; ++n) {
            const int Q = 4 * n + g;
            const u32x2 v = {pack2(acc[n][0], acc[n][1]), pack2(acc[n][2], acc[n][3])};
            const int wi = Q - 10, hd = wi >= 5 ? 1 : 0;
            const u32 a = Q < 10 ? L_K + row * 80 + 8 * Q : L_V + row * 96 + 48 * hd + 8 * (wi - 5 * hd);
            sts64(L0 + a, v);
        }
        if (g < 2) sts64(L0 + L_V + row * 96 + 48 * g + 40, (u32x2){0x3F80u, 0u});
        if (rb < 2) {
            const int nrow = rb == 0 ? 256 : 128;
            for (int i = tid; i < nrow * 5; i += 512) sts128(L0 + L_K + i * 16, (u32x4){0, 0, 0, 0});
            for (int i = tid; i < nrow * 6; i += 512) sts128(L0 + L_V + i * 16, (u32x4){0, 0, 0, 0});
        }
        vmw<0>();
        BAR();
    }
#undef XL
    STAMP(4);
    {
        u32x4 kvd[3];
#pragma unroll
        for (int e = 0; e < 3; ++e) {
            const int n = min(w + 8 * e, 21);
            kvd[e] = lds128(L0 + (n < 10 ? L_K + 20480 + n * 1024 : L_V + 24576 + (n - 10) * 1024) + lo16);
        }
        const u32 kvo = WS_KV + (rb * 16 + j) * 22528 + w * 1024 + lo16;
        const u16* wroot = wpm ? p.wu : p.wg;
        const char* wsrc = (const char*)wroot + (160 * wpq + 8 * w) * 3456 + wpb * 128;
        const u32 wvo = (l >> 3) * 3456 + (l & 7) * 16;
        f32x4 acc[3];
#pragma unroll
        for (int n = 0; n < 3; ++n) acc[n] = Z4;
        const u32 bq = L0 + L_WQ + lo16, bq2 = L0 + L_WQ + 2048 + g * 128 + (i16 & 7) * 16;
#define QRD(s, n) lds128(((n) < 2 ? bq + (n) * 1024 : bq2) + (s) * 2560)
        u32x4 bf[3][3];
#pragma unroll
        for (int n = 0; n < 3; ++n) { bf[0][n] = QRD(0, n); bf[1][n] = QRD(1, n); }
        Gather G;
        const u32 gg0 = l < 40 ? 480 + 40 * wpq + l : 640 + 40 * wpq + (l - 40), gg1 = 640 + 40 * wpq + 24 + (l & 15);
        static_for<0, 20>([&](auto S) {
            constexpr int s = S;
            if constexpr (s == 6) { if (w == 7 && wpwg) gather_ask(ws, gg0, gg1, G); }
            if constexpr (s == 0) { if (rb < 15) ST16C(kvd[0], kvo); }
            if constexpr (s == 1) { if (rb < 15) ST16C(kvd[1], kvo + 8192); }
            if constexpr (s == 2) { if (rb < 15 && w < 6) ST16C(kvd[2], kvo + 16384); }
            if constexpr (s == 14) {
                vmw<0>();
                u32 old = 0;
                if (l == 0) old = lds_add(L0 + L_CNT + 28, 1u);
                if (__builtin_amdgcn_readfirstlane(old) == 7 && l == 0) st_agent(ws + WS_FKV + (j * 16 + rb) * FS, tag);
                if (w == 7 && wpwg) { f32x4 ra, rc; gather_finish(ws, gg0, gg1, G, tag, ws, 2, ra, rc); put_strip_spread(ra, rc, L0 + L_SB, l); }
            }
            if constexpr (s == 15) { if (wpwg) glds16(wroot, wsrc, wvo, L0 + L_WPRAW + w * 1024); }
            if constexpr (s == 16) { if (wpwg) glds16(wroot, wsrc + 64 * 3456, wvo, L0 + L_WPRAW + (w + 8) * 1024); }
            if constexpr (s == 17) { if (wpwg && w < 4) glds16(wroot, wsrc + 128 * 3456, wvo, L0 + L_WPRAW + (w + 16) * 1024); }
            SB0();
#pragma unroll
            for (int n = 0; n < 3; ++n) {
                if (s < 18) bf[(s + 2) % 3][n] = QRD(s + 2, n);
                acc[n] = mfma(bf[s % 3][n], xa[s], acc[n]);
            }
#pragma unroll
            for (int n = 0; n < 3; ++n) { SGB(M_DSR, 1); SGB(M_MFMA, 1); }
            SB0();
        });
#undef QRD
        STAMP(21);
#pragma unroll
        for (int n = 0; n < 3; ++n) {
            const int Q = 4 * n + g;
            const u32x2 v = {pack2(acc[n][0] * QSCALE, acc[n][1] * QSCALE), pack2(acc[n][2] * QSCALE, acc[n][3] * QSCALE)};
            if (Q < 10) sts64(L0 + L_Q + (16 * w + i16) * 80 + 8 * Q, v);
        }
    }
    if (w == 0 && rb >= 1) poll_flags(ws + WS_FKV + (j * 16 + (rb >= 2 ? rb - 2 : rb - 1)) * FS, rb >= 2 ? 2 : 1, l, tag, ws, 3);
    VMCNT(0);
    BAR();
    STAMP(5);
    if (rb >= 1) {
#pragma unroll
        for (int e = 0; e < 3; ++e) {
            const int n = w + 8 * e;
            if (n < 22) {
                const u32 d = n < 10 ? L_K + n * 1024 : L_V + (n - 10) * 1024, st = n < 10 ? 10240 : 12288;
                glds16c(kvx - 16 * 22528 + n * 1024, lo16, L0 + d + st);
                if (rb >= 2) glds16c(kvx - 32 * 22528 + n * 1024, lo16, L0 + d);
            }
        }
    }
    auto wp_slot = [&](int gam, int& jj, int& slot) {
        jj = gam < 84 ? gam / 7 : 12 + (gam - 84) / 6;
        const int ar = gam - (jj < 12 ? 7 * jj : 84 + 6 * (jj - 12)), a = (jj < 12 || ar < 3) ? ar : ar + 1;
        slot = a < 3 ? 2 * a + wpm : a == 3 ? (wpm ? 13 : 6) : 7 + 2 * (a - 4) + wpm;
    };
    if (wpwg) {
        const u32 one = i16 == 0 ? 0x3F803F80u : 0u;
        const u32x4 ones = {one, one, one, one};
#pragma unroll
        for (int i = 0; i < 3; ++i) {
            if (i < 2 || w < 4) {
                const int f = w + 8 * i, ks = f >> 2, t16 = f & 3;
                int jj, slot; wp_slot(4 * wpb + t16, jj, slot);
                const u32 a = L0 + L_WPRAW + (32 * ks + 8 * g + q4) * 128 + 32 * t16 + 8 * p4;
                u32x4 wr = ldtr2(a, a + 512);
                const u32x4 ga = lds128(L0 + L_G2 + (32 * ks + 8 * g) * 4), gc = lds128(L0 + L_G2 + (32 * ks + 8 * g) * 4 + 16);
                const u32x4 sbz = lds128(L0 + (i16 == 0 ? L_SB + (32 * ks + 8 * g) * 2 : L_Z16));
                const f32x4 vs = mfma(wr, sbz, Z4);
                u32x4 o; float t0, t1, t2, t3, t4, t5, t6, t7;
                asm volatile("v_dot2_f32_bf16 %4, %12, %16, 0\n\tv_dot2_f32_bf16 %5, %12, %17, 0\n\tv_dot2_f32_bf16 %6, %13, %18, 0\n\tv_dot2_f32_bf16 %7, %13, %19, 0\n\t"
                             "v_dot2_f32_bf16 %8, %14, %20, 0\n\tv_dot2_f32_bf16 %9, %14, %21, 0\n\tv_dot2_f32_bf16 %10, %15, %22, 0\n\tv_dot2_f32_bf16 %11, %15, %23, 0\n\t"
                             "v_cvt_pk_bf16_f32 %0, %4, %5\n\tv_cvt_pk_bf16_f32 %1, %6, %7\n\tv_cvt_pk_bf16_f32 %2, %8, %9\n\ts_nop 0\n\tv_cvt_pk_bf16_f32 %3, %10, %11\n\ts_nop 1"
                             : "=&v"(o[0]), "=&v"(o[1]), "=&v"(o[2]), "=&v"(o[3]),
                               "=&v"(t0), "=&v"(t1), "=&v"(t2), "=&v"(t3), "=&v"(t4), "=&v"(t5), "=&v"(t6), "=&v"(t7)
                             : "v"(wr[0]), "v"(wr[1]), "v"(wr[2]), "v"(wr[3]), "v"(ga[0]), "v"(ga[1]), "v"(ga[2]), "v"(ga[3]), "v"(gc[0]), "v"(gc[1]), "v"(gc[2]), "v"(gc[3]));
                const f32x4 us = mfma(o, ones, Z4);
                ST16C(o, WS_WP + ((jj * 20 + 5 * wpq + ks) * 14 + slot) * 1024 + lo16);
                if (i16 == 0) { stsf4(L0 + L_UVL + f * 128 + g * 16, us); stsf4(L0 + L_UVL + f * 128 + 64 + g * 16, vs); }
            }
        }
    }
    if (wpwg) { if (w < 4) vmw<3>(); else vmw<2>(); } else vmw<0>();
    BAR();
    STAMP(6);
    if (wpwg && w == 3) {
#pragma unroll
        for (int e = 0; e < 2; ++e) {
            const int t16 = 2 * e + (l >> 5);
            int jj, slot; wp_slot(4 * wpb + t16, jj, slot);
            float v = 0.f;
#pragma unroll
            for (int ks = 0; ks < 5; ++ks) v += ldsf(L0 + L_UVL + (4 * ks + t16) * 128 + (l & 31) * 4);
            st_agent32(ws + WS_UV + ((jj * 14 + slot) * 4 + wpq) * 128 + (l & 31) * 4, __float_as_uint(v));
        }
    }
    const int hf = j & 1, cb = j >> 1;
    u32 wvoff[5];
#pragma unroll
    for (int i = 0; i < 5; ++i) {
        const u32 sg = 64 * i + l, pos = (sg * 6554) >> 16, c = sg - pos * 10, k = pos ^ (((pos >> 3) & 1) << 2);
        wvoff[i] = k * 1280 + c * 16;
    }
    const char* wosrc = (const char*)p.wo + cb * 160;
#define WO_STEP(sx) do { _Pragma("unroll") for (int i = 0; i < 5; ++i) glds16(p.wo, wosrc + (sx) * 32 * 1280, wvoff[i], L0 + L_WO + (sx) * 5120 + i * 1024); } while (0)
    STAMP(7);

    {
        const int hd = w & 1, qi = w >> 1;
        const int kr0 = 32 * qi;
        u32x4 qf[2];
#pragma unroll
        for (int a = 0; a < 2; ++a) {
            const u32 qa = L0 + L_Q + (kr0 + 16 * a + i16) * 80 + hd * 40;
            const u32x2 q0 = lds64(qa + 8 * g);
            u32x2 q1 = lds64(qa + 32);
            if (g != 0) q1 = (u32x2){0, 0};
            qf[a] = (u32x4){q0[0], q0[1], q1[0], q1[1]};
        }
        const u32 kb0 = L0 + L_K + (kr0 + i16) * 80 + hd * 40 + 8 * g, kb1 = L0 + L_K + (kr0 + i16) * 80 + hd * 40 + 32;
        f32x4 mfirst, mlast;
#pragma unroll
        for (int r = 0; r < 4; ++r) { const bool lo = 4 * g + r <= i16; mfirst[r] = lo ? -INFINITY : 0.f; mlast[r] = lo ? 0.f : -INFINITY; }
        f32x4 s0[17], s1[17];
        {
            u32x4 kf[6];
#define KRD(t) do { const u32x2 k0 = lds64(kb0 + (t) * 1280), k1 = lds64(kb1 + (t) * 1280); kf[(t) % 6] = (u32x4){k0[0], k0[1], k1[0], k1[1]}; } while (0)
            KRD(0); KRD(1); KRD(2); KRD(3); KRD(4);
            static_for<0, 18>([&](auto T) {
                constexpr int t = T;
                SB0();
                if constexpr (t < 13) KRD(t + 5);
                if constexpr (t < 17) s0[t] = mfma(kf[t % 6], qf[0], t == 0 ? mfirst : t == 16 ? mlast : Z4);
                if constexpr (t > 0) s1[t - 1] = mfma(kf[t % 6], qf[1], t == 1 ? mfirst : t == 17 ? mlast : Z4);
                SGB(M_DSR, 2); SGB(M_MFMA, 2);
                SB0();
            });
#undef KRD
        }
        WO_STEP(4 + w);
        if (rb < 2) {
            const int fv = rb == 1 ? 128 : 256;
#pragma unroll
            for (int t = 0; t < 16; ++t) {
                const bool d0 = kr0 + 16 * t < fv, d1 = kr0 + 16 * t + 16 < fv;
#pragma unroll
                for (int r = 0; r < 4; ++r) { s0[t][r] = d0 ? -INFINITY : s0[t][r]; s1[t][r] = d1 ? -INFINITY : s1[t][r]; }
            }
        }
        STAMP(22);
        float m0 = fmaxf(s0[0][0], s0[0][1]), m1 = fmaxf(s1[0][0], s1[0][1]), n0 = fmaxf(s0[0][2], s0[0][3]), n1 = fmaxf(s1[0][2], s1[0][3]);
#pragma unroll
        for (int t = 1; t < 17; ++t) {
            m0 = fmaxf(fmaxf(m0, s0[t][0]), s0[t][1]); m1 = fmaxf(fmaxf(m1, s1[t][0]), s1[t][1]);
            n0 = fmaxf(fmaxf(n0, s0[t][2]), s0[t][3]); n1 = fmaxf(fmaxf(n1, s1[t][2]), s1[t][3]);
        }
        m0 = fmaxf(m0, n0); m1 = fmaxf(m1, n1);
        m0 = xmax4(m0); m1 = xmax4(m1);
        WO_STEP(12 + w);
        STAMP(23);
        f32x4 o00 = Z4, o01 = Z4, o10 = Z4, o11 = Z4;
        const u32 vb = L0 + L_V + (kr0 + 4 * g + q4) * 96 + hd * 48 + 8 * p4;
        auto pv = [&](auto SUB) {
            constexpr bool sub = SUB;
            u32x4 vf[2][2];
            vf[0][0] = ldtr2(vb, vb + 16 * 96); vf[0][1] = ldtr2(vb + 32, vb + 16 * 96 + 32);
#define EX(v, m) __builtin_amdgcn_exp2f(sub ? (v) - (m) : (v))
            static_for<0, 9>([&](auto U) {
                constexpr int u = U;
                if constexpr (u < 8) { const u32 a = vb + (u + 1) * 32 * 96; vf[(u + 1) & 1][0] = ldtr2(a, a + 16 * 96); vf[(u + 1) & 1][1] = ldtr2(a + 32, a + 16 * 96 + 32); }
                u32x4 pa, pb;
                pa[0] = pack2(EX(s0[2 * u][0], m0), EX(s0[2 * u][1], m0)); pa[1] = pack2(EX(s0[2 * u][2], m0), EX(s0[2 * u][3], m0));
                if constexpr (u < 8) { pa[2] = pack2(EX(s0[2 * u + 1][0], m0), EX(s0[2 * u + 1][1], m0)); pa[3] = pack2(EX(s0[2 * u + 1][2], m0), EX(s0[2 * u + 1][3], m0)); }
                else { pa[2] = 0; pa[3] = 0; }
                if constexpr (u > 0) { pb[0] = pack2(EX(s1[2 * u - 1][0], m1), EX(s1[2 * u - 1][1], m1)); pb[1] = pack2(EX(s1[2 * u - 1][2], m1), EX(s1[2 * u - 1][3], m1)); }
                else { pb[0] = 0; pb[1] = 0; }
                pb[2] = pack2(EX(s1[2 * u][0], m1), EX(s1[2 * u][1], m1)); pb[3] = pack2(EX(s1[2 * u][2], m1), EX(s1[2 * u][3], m1));
                o00 = mfma(vf[u & 1][0], pa, o00); o01 = mfma(vf[u & 1][1], pa, o01);
                o10 = mfma(vf[u & 1][0], pb, o10); o11 = mfma(vf[u & 1][1], pb, o11);
                SB0();
            });
#undef EX
        };
        if (__all(fabsf(m0) <= 64.f && fabsf(m1) <= 64.f)) pv(std::false_type{}); else pv(std::true_type{});
        STAMP(24);
        const float i0 = __builtin_amdgcn_rcpf(__shfl(o01[0], i16 + 16)), i1 = __builtin_amdgcn_rcpf(__shfl(o11[0], i16 + 16));
        const u32 oa = L0 + L_Q + (kr0 + i16) * 80 + hd * 40;
        sts64(oa + 8 * g, (u32x2){pack2(o00[0] * i0, o00[1] * i0), pack2(o00[2] * i0, o00[3] * i0)});
        sts64(oa + 1280 + 8 * g, (u32x2){pack2(o10[0] * i1, o10[1] * i1), pack2(o10[2] * i1, o10[3] * i1)});
        if (g == 0) {
            sts64(oa + 32, (u32x2){pack2(o01[0] * i0, o01[1] * i0), pack2(o01[2] * i0, o01[3] * i0)});
            sts64(oa + 1280 + 32, (u32x2){pack2(o11[0] * i1, o11[1] * i1), pack2(o11[2] * i1, o11[3] * i1)});
        }
    }
    BAR();
    {
        const int tok = 64 * (w & 1) + l;
        const u32 oo = WS_O + (((rb * 8 + (tok >> 4)) * 80 + 5 * j) * 16 + (tok & 15)) * 16;
        ST16C(lds128(L0 + L_Q + tok * 80 + (w >> 1) * 16), oo + (w >> 1) * 256);
        if (w < 2) ST16C(lds128(L0 + L_Q + tok * 80 + 64), oo + 4 * 256);
    }
    VMCNT(0);
    BAR();
    if (tid == 0) {
        st_agent(ws + WS_FO + (rb * 16 + j) * FS, tag);
        if (wpwg) st_agent(ws + WS_FWP + b * FS, tag);
    }
    STAMP(8);

    const int wm = w & 3, wk = w >> 2;
    const int mt3 = rb * 8 + 4 * hf + wm;
    u32x2 x1k[5];
    {
        const int wdu = b + 256 * (w - 6);
        const bool wdw = w >= 6 && wdu < 432;
        if (w >= 1 && w < 5) {
#pragma unroll
            for (int i = 0; i < 5; ++i) glds16(p.wo, wosrc + (w - 1) * 32 * 1280, wvoff[i], L0 + L_WO + (w - 1) * 5120 + i * 1024);
        } else if (w >= 5) {
#pragma unroll
            for (int e = 0; e < 4; ++e) {
                const int pc = w - 5 + 3 * e;
                if (pc < 10) {
                    const u32 sg = 64 * pc + l, r = (sg * 6554) >> 16, c = sg - r * 10;
                    glds16(p.x, (const char*)p.x + (rb * 128 + 64 * hf) * 1280 + cb * 160, r * 1280 + c * 16, L0 + L_XR + pc * 1024);
                }
            }
            if (wdw) {
                const int ucb = wdu / 54, us = wdu - 54 * ucb;
#pragma unroll
                for (int i = 0; i < 5; ++i) glds16(p.wd, (const char*)p.wd + ucb * 160 + us * 32 * 1280, wvoff[i], L0 + L_WDRAW + (w - 6) * 5120 + i * 1024);
            }
        }
        if (w == 1 && l < 20) stsf4(L0 + L_GA + l * 16, gather1(ws, 320 + 20 * cb + l, tag, ws, 4));
        if (w == 0) {
            const int bl = min((Gj >> 2) + ((l >> 2) % 3), (Gj + nj - 1) >> 2);
            poll_at(ws, WS_FWP + (108 * (l >= 12 && l < 24 ? 1 : 0) + 4 * bl + (l & 3)) * FS, l, tag, ws, 6);
            poll_flags(ws + WS_FO + rb * 16 * FS, 16, l, tag, ws, 5);
        }
        if (wdw) vmw<5>(); else vmw<0>();
        BAR();
        STAMP(9);
        u32x4 a[10];
        const char* asrc = ws + WS_O + (mt3 * 80 + 40 * wk) * 256;
#define AL(s) a[s] = gld16c(((s) & 3) * 1024, asrc + ((s) >> 2) * 4096, lo16)
        AL(0); AL(1); AL(2); AL(3);
        f32x4 acc[5];
#pragma unroll
        for (int n = 0; n < 5; ++n) acc[n] = Z4;
        const u32 wb = L0 + L_WO + wk * 51200;
        const u32 b0 = wb + 160 * (8 * g + 4 * (g & 1) + q4) + 8 * p4, b1 = wb + 160 * (8 * g + 4 * ((g & 1) ^ 1) + q4) + 8 * p4;
        u32x4 bf[2][5];
#pragma unroll
        for (int n = 0; n < 5; ++n) bf[0][n] = ldtr2(b0 + n * 32, b1 + n * 32);
        static_for<0, 10>([&](auto S) {
            constexpr int s = S;
            vmw1<(s < 7 ? 3 : 9 - s)>(a[s]);
            if constexpr (s + 4 < 10) AL(s + 4);
            SB0();
#pragma unroll
            for (int n = 0; n < 5; ++n) {
                if (s < 9) bf[(s + 1) & 1][n] = ldtr2(b0 + (s + 1) * 5120 + n * 32, b1 + (s + 1) * 5120 + n * 32);
                acc[n] = mfma(bf[s & 1][n], a[s], acc[n]);
            }
#pragma unroll
            for (int n = 0; n < 5; ++n) { SGB(M_DSR, 2); SGB(M_MFMA, 1); }
            SB0();
        });
#undef AL
        STAMP(25);
        if (wk == 1) {
#pragma unroll
            for (int n = 0; n < 5; ++n) stsf4(L0 + L_RED3 + (wm * 5 + n) * 1024 + lo16, acc[n]);
        }
        BAR();
        if (wk == 0) {
            float s1 = 0.f, s2 = 0.f;
#pragma unroll
            for (int n = 0; n < 5; ++n) {
                const f32x4 v = acc[n] + ldsf4(L0 + L_RED3 + (wm * 5 + n) * 1024 + lo16);
                const f32x4 ga = ldsf4(L0 + L_GA + (16 * n + 4 * g) * 4);
                const u32x2 xr = lds64(L0 + L_XR + (16 * wm + i16) * 160 + 32 * n + 8 * g);
                const u32x2 o = {pack2(bflo(xr[0]) + ga[0] * v[0], bfhi(xr[0]) + ga[1] * v[1]), pack2(bflo(xr[1]) + ga[2] * v[2], bfhi(xr[1]) + ga[3] * v[3])};
                x1k[n] = o;
                s1 = dot2(o[0], 0x3F803F80u, s1); s1 = dot2(o[1], 0x3F803F80u, s1);
                s2 = dot2(o[0], o[0], s2); s2 = dot2(o[1], o[1], s2);
            }
            const u32 xo = WS_X1 + ((mt3 * 80 + 10 * cb) * 16 + i16) * 16;
#pragma unroll
            for (int n = 0; n < 4; n += 2) {
                const auto a0 = __builtin_amdgcn_permlane16_swap(x1k[n][0], x1k[n + 1][0], false, false);
                const auto a1 = __builtin_amdgcn_permlane16_swap(x1k[n][1], x1k[n + 1][1], false, false);
                ST16C(((u32x4){a0[0], a1[0], a0[1], a1[1]}), xo + (2 * (n + (g & 1)) + (g >> 1)) * 256);
            }
            ST8C(x1k[4], xo + (8 + (g >> 1)) * 256 + 8 * (g & 1));
            s1 = xsum4(s1); s2 = xsum4(s2);
            if (l < 16) ST8C(((u32x2){__float_as_uint(s1), __float_as_uint(s2)}), WS_STAT + ((rb * 8 + cb) * 128 + 64 * hf + 16 * wm + l) * 8);
            STAMP(26);
            vmw<0>();
        } else {
#pragma unroll
            for (int n = 0; n < 5; ++n) x1k[n] = (u32x2){0, 0};
            if (wdw) {
                const u32 wr = L0 + L_WDRAW + (w - 6) * 5120;
                const u32 r0 = wr + 160 * (8 * g + 4 * (g & 1) + q4) + 8 * p4, r1 = wr + 160 * (8 * g + 4 * ((g & 1) ^ 1) + q4) + 8 * p4;
#pragma unroll
                for (int n = 0; n < 5; ++n) ST16C(ldtr2(r0 + n * 32, r1 + n * 32), WS_WD + (wdu * 5 + n) * 1024 + lo16);
            }
        }
        BAR();
        if (tid == 0) st_agent(ws + WS_FX1 + (rb * 16 + j) * FS, tag);
    }
    STAMP(10);

    const char* wdsrc = ws + WS_WD + (cb * 54 + wk * 27) * 5120;
    const u32 r5w = L0 + wk * 20480 + wm * 5120;
#define P5_B1(t, i) do { if ((i) == 0 || (i) == 4) glds16c(wdsrc + min(4 * (t) + wm, 26) * 5120 + (i) * 1024, lo16, r5w + r5_slot(t) + (i) * 1024); \
        else glds16c_next(((i) & 3) * 1024, wdsrc + min(4 * (t) + wm, 26) * 5120, lo16); } while (0)
    {
        const int wn = w >> 2;
        const int bst = w < 4 ? 4 * w : 16 + 3 * (w - 4);
        const char* wpj = ws + WS_WP + (j * 20 * 14 + bst) * 1024;
        const char* x1b = ws + WS_X1 + (rb * 8 + w) * 80 * 256;
        const u32 r4a = L0 + L_R4 + w * 2048, r4b = L0 + L_R4 + 16384 + bst * 1024;
#define P4_B1(t, i) do { if ((i) == 0) glds16c(wpj + (t) * 28 * 1024, lo16, r4b + ((t) % 3) * R4_STAGE); else glds16c_next((i) * 1024, wpj + (t) * 28 * 1024, lo16); } while (0)
#define P4_A1(t, i) do { if ((i) == 0) glds16c(x1b + 8 * (t) * 256, lo16, r4a + ((t) % 3) * R4_STAGE); else glds16c_next(1024, x1b + 8 * (t) * 256, lo16); } while (0)
#define P4_B(t) do { P4_B1(t, 0); P4_B1(t, 1); P4_B1(t, 2); if (w < 4) P4_B1(t, 3); } while (0)
        P4_B(0); P4_B(1); P4_B(2);
        if (w == 0) poll_flags(ws + WS_FX1 + rb * 16 * FS, 16, l, tag, ws, 7);
        BAR();
        STAMP(11);
        glds16c(ws + WS_STAT + rb * 8192 + w * 1024, lo16, L0 + L_ST4 + w * 1024);
        if (w < 7) glds16c(ws + WS_UV + j * 7168 + w * 1024, lo16, L0 + L_UV + w * 1024);
        P4_A1(0, 0); P4_A1(0, 1); P4_A1(1, 0); P4_A1(1, 1); P4_A1(2, 0); P4_A1(2, 1);
        f32x4 acc[7][2];
#pragma unroll
        for (int n = 0; n < 7; ++n) { acc[n][0] = Z4; acc[n][1] = Z4; }
        const u32 ra = opaque(L0 + L_R4 + 2 * wm * 2048 + lo16), rbb = opaque(L0 + L_R4 + 16384 + 7 * wn * 1024 + lo16);
        u32x4 fa[2][2], fb[2][7];
#define P4_RA(buf, t, ks) do { const u32 so = ((t) % 3) * R4_STAGE + (ks) * 1024; fa[buf][0] = lds128(ra + so); fa[buf][1] = lds128(ra + so + 2048); } while (0)
#define P4_RB(buf, t, ks, n) fb[buf][n] = lds128(rbb + ((t) % 3) * R4_STAGE + (ks) * 14336 + (n) * 1024)
#define P4_MM(buf, n) do { acc[n][0] = mfma(fb[buf][n], fa[buf][0], acc[n][0]); acc[n][1] = mfma(fb[buf][n], fa[buf][1], acc[n][1]); } while (0)
#define P4_HALF(buf, rd, t, ks, V0, V1, V2) do { \
            SB0(); if (rd) { P4_RA((buf) ^ 1, t, ks); P4_RB((buf) ^ 1, t, ks, 0); } P4_MM(buf, 0); SB0(); V0; \
            SB0(); if (rd) P4_RB((buf) ^ 1, t, ks, 1); P4_MM(buf, 1); SB0(); \
            SB0(); if (rd) P4_RB((buf) ^ 1, t, ks, 2); P4_MM(buf, 2); SB0(); V1; \
            SB0(); if (rd) P4_RB((buf) ^ 1, t, ks, 3); P4_MM(buf, 3); SB0(); \
            SB0(); if (rd) P4_RB((buf) ^ 1, t, ks, 4); P4_MM(buf, 4); SB0(); V2; \
            SB0(); if (rd) P4_RB((buf) ^ 1, t, ks, 5); P4_MM(buf, 5); SB0(); \
            SB0(); if (rd) P4_RB((buf) ^ 1, t, ks, 6); P4_MM(buf, 6); SB0(); } while (0)
        VMCNT(4);
        BAR();
        if (tid == 0) st_agent(ws + WS_FW2 + b * FS, tag);
        STAMP(27);
        u32x2 wdf = {0u, 0u};
        const u32 wdo = WS_FW2 + ((54 * cb + (l < 54 ? l : 0)) & 255) * FS;
        P4_RA(0, 0, 0);
#pragma unroll
        for (int n = 0; n < 7; ++n) P4_RB(0, 0, 0, n);
        static_for<0, 10>([&](auto T) {
            constexpr int t = T;
            constexpr bool f = t >= 1 && t + 2 <= 9, sN = t + 3 <= 9;
            if constexpr (t < 9) P4_HALF(0, true, t, 1, if (f && w < 4) P4_B1(t + 2, 3), if (f) P4_A1(t + 2, 0), if (f) P4_A1(t + 2, 1));
            else P4_HALF(0, true, t, 1, P5_B1(0, 3), P5_B1(0, 4), P5_B1(1, 0));
            if constexpr (t == 5) {
                if (tid < 128) {
                    f32x2 st = {0.f, 0.f};
#pragma unroll
                    for (int c = 0; c < 8; ++c) st += *(LAS f32x2*)(size_t)(L0 + L_ST4 + (c * 128 + tid) * 8);
                    const float mu = st[0] * (1.f / 640.f), var = fmaxf(st[1] * (1.f / 640.f) - mu * mu, 0.f), r = __builtin_amdgcn_rsqf(var + 1e-5f);
                    stsf2(L0 + L_MR + tid * 8, (f32x2){r, -mu * r});
                } else {
                    const int e = tid - 128;
                    stsf(L0 + L_UVS + e * 4, (ldsf(L0 + L_UV + (e >> 5) * 512 + (e & 31) * 4) + ldsf(L0 + L_UV + (e >> 5) * 512 + 128 + (e & 31) * 4))
                                           + (ldsf(L0 + L_UV + (e >> 5) * 512 + 256 + (e & 31) * 4) + ldsf(L0 + L_UV + (e >> 5) * 512 + 384 + (e & 31) * 4)));
                    if (e < 64) { const int e2 = e + 384;
                        stsf(L0 + L_UVS + e2 * 4, (ldsf(L0 + L_UV + (e2 >> 5) * 512 + (e2 & 31) * 4) + ldsf(L0 + L_UV + (e2 >> 5) * 512 + 128 + (e2 & 31) * 4))
                                                + (ldsf(L0 + L_UV + (e2 >> 5) * 512 + 256 + (e2 & 31) * 4) + ldsf(L0 + L_UV + (e2 >> 5) * 512 + 384 + (e2 & 31) * 4))); }
                }
            }
            if constexpr (t < 9) {
                if constexpr (t == 0) vmw<2>();
                else if constexpr (t == 8) {
                    asm volatile("s_waitcnt vmcnt(0)" : "+v"(wdf) :: "memory");
                    if (w == 7) { if (!__all(wdf[0] == (u32)tag && wdf[1] == (u32)(tag >> 32))) poll_at(ws, wdo, l, tag, ws, 12); }
                }
                else { if (w < 4) vmw<6>(); else vmw<5>(); }
                BAR();
                if constexpr (t == 7) { if (w == 7) asm volatile("s_nop 4\n\tglobal_load_dwordx2 %0, %1, %2 sc1" : "=v"(wdf) : "v"(wdo), "s"(ws) : "memory"); }
                if constexpr (t < 8) P4_HALF(1, true, t + 1, 0, if (sN) P4_B1(t + 3, 0), if (sN) P4_B1(t + 3, 1), if (sN) P4_B1(t + 3, 2));
                else P4_HALF(1, true, t + 1, 0, P5_B1(0, 0), P5_B1(0, 1), P5_B1(0, 2));
            } else {
                P4_HALF(1, false, 0, 0, P5_B1(1, 1), P5_B1(1, 2), P5_B1(1, 3));
                P5_B1(1, 4);
            }
        });
#undef P4_B
#undef P4_B1
#undef P4_A1
#undef P4_RA
#undef P4_RB
#undef P4_MM
#undef P4_HALF
        STAMP(12);
        f32x2 mr[2];
#pragma unroll
        for (int m = 0; m < 2; ++m) mr[m] = *(LAS f32x2*)(size_t)(L0 + L_MR + (32 * wm + 16 * m + i16) * 8);
#pragma unroll
        for (int n = 0; n < 7; ++n) {
            const f32x4 u = ldsf4(L0 + L_UVS + (7 * wn + n) * 128 + 16 * g), v = ldsf4(L0 + L_UVS + (7 * wn + n) * 128 + 64 + 16 * g);
#pragma unroll
            for (int m = 0; m < 2; ++m) acc[n][m] = acc[n][m] * mr[m][0] + (u * mr[m][1] + v);
        }
        if (nj == 7) stsf4(L0 + L_XCH + w * 1024 + lo16, wn == 0 ? acc[6][1] : acc[6][0]);
        auto silu_mul = [&](const f32x4 gt, const f32x4 up) -> u32x2 {
            const f32x4 e = gt * (-LOG2E), gu = gt * up;
            f32x4 d;
#pragma unroll
            for (int r = 0; r < 4; ++r) d[r] = __builtin_amdgcn_exp2f(e[r]);
            d = d + 1.f;
#pragma unroll
            for (int r = 0; r < 4; ++r) d[r] = __builtin_amdgcn_rcpf(d[r]);
            const f32x4 o = gu * d;
            return (u32x2){pack2(o[0], o[1]), pack2(o[2], o[3])};
        };
#pragma unroll
        for (int a = 0; a < 3; ++a) {
            const int la = a + 4 * wn, gam = Gj + (la <= 3 || nj == 7 ? la : la - 1);
            const u32x2 o0 = silu_mul(acc[2 * a][0], acc[2 * a + 1][0]), o1 = silu_mul(acc[2 * a][1], acc[2 * a + 1][1]);
            const auto s0 = __builtin_amdgcn_permlane16_swap(o0[0], o1[0], false, false);
            const auto s1 = __builtin_amdgcn_permlane16_swap(o0[1], o1[1], false, false);
            const u32x4 v = {s0[0], s1[0], s0[1], s1[1]};
            ST16C(v, WS_ACT + (((rb * 8 + 2 * wm + (g & 1)) * 216 + 2 * gam + (g >> 1)) * 16 + i16) * 16);
        }
        STAMP(28);
        BAR();
        if (nj == 7) {
            const f32x4 other = ldsf4(L0 + L_XCH + (w ^ 4) * 1024 + lo16);
            const u32x2 o = wn == 0 ? silu_mul(acc[6][0], other) : silu_mul(other, acc[6][1]);
            ST8C(o, WS_ACT + (((rb * 8 + 2 * wm + wn) * 216 + 2 * (Gj + 3) + (g >> 1)) * 16 + i16) * 16 + 8 * (g & 1));
        }
        P5_B1(2, 0); P5_B1(2, 1); P5_B1(2, 2); P5_B1(2, 3); P5_B1(2, 4);
        vmw<5>();
        BAR();
        if (tid == 0) st_agent(ws + WS_FACT + (rb * 16 + j) * FS, tag);
    }
    STAMP(13);

    {
        if (w == 1 && l < 20) stsf4(L0 + L_GB + l * 16, gather1(ws, 800 + 20 * cb + l, tag, ws, 8));
        if (w == 0) poll_flags(ws + WS_FACT + rb * 16 * FS, 16, l, tag, ws, 9);
        const bool bump = rb == 15 && j == 0;
        if (bump && w >= 4) poll_at(ws, WS_FO + (tid - 256) * FS, l, tag, ws, 10);
        VMCNT(0);
        BAR();
        if (bump && tid == 0) st_agent(ws + WS_EPOCH, tag);
        STAMP(14);
        const char* asrc = ws + WS_ACT + (mt3 * 216 + wk * 108) * 256;
        u32x4 a[3][4];
#define P5_A1(t, i) a[(t) % 3][i] = gld16c((i) * 1024, asrc + (t) * 4096, lo16)
        P5_A1(0, 0); P5_A1(0, 1); P5_A1(0, 2); P5_A1(0, 3); P5_A1(1, 0); P5_A1(1, 1); P5_A1(1, 2); P5_A1(1, 3); P5_A1(2, 0); P5_A1(2, 1); P5_A1(2, 2); P5_A1(2, 3);
        f32x4 acc[5];
#pragma unroll
        for (int n = 0; n < 5; ++n) acc[n] = Z4;
        const u32 rb5 = opaque(L0 + wk * 20480 + lo16);
        u32x4 fb[2][5];
#define P5_RD(buf, t, kk) do { _Pragma("unroll") for (int n = 0; n < 5; ++n) fb[buf][n] = lds128(rb5 + r5_slot(t) + (kk) * 5120 + n * 1024); } while (0)
#define P5_MM(buf, av) do { _Pragma("unroll") for (int n = 0; n < 5; ++n) acc[n] = mfma(fb[buf][n], (av), acc[n]); } while (0)
#define P5_MIX() do { _Pragma("unroll") for (int n = 0; n < 5; ++n) { SGB(M_DSR, 1); SGB(M_MFMA, 1); } } while (0)
        P5_RD(0, 0, 0);
        static_for<0, 7>([&](auto T) {
            constexpr int t = T, q = t % 3;
            constexpr bool nx = t + 3 < 7;
            constexpr int NA = t == 0 ? 8 : t == 1 ? 13 : t == 2 ? 18 : t == 3 ? 21 : t == 4 ? 20 : t == 5 ? 11 : 5;
            constexpr int NB = t == 2 ? 12 : t == 3 ? 12 : t == 4 ? 8 : t == 5 ? 0 : 63;
            vmw4<NA>(a[q][0], a[q][1], a[q][2], a[q][3]);
            SB0(); P5_RD(1, t, 1); P5_MM(0, a[q][0]); P5_MIX(); SB0();
            if constexpr (nx) P5_A1(t + 3, 0);
            SB0(); P5_RD(0, t, 2); P5_MM(1, a[q][1]); P5_MIX(); SB0();
            if constexpr (nx) P5_A1(t + 3, 1);
            if constexpr (t < 6) {
                SB0(); P5_RD(1, t, 3); P5_MM(0, a[q][2]); P5_MIX(); SB0();
                if constexpr (nx) P5_A1(t + 3, 2);
                vmw<NB>(); BAR();
                if constexpr (nx) { P5_B1(t + 3, 0); P5_B1(t + 3, 1); }
                SB0(); P5_RD(0, t + 1, 0); P5_MM(1, a[q][3]); P5_MIX(); SB0();
                if constexpr (nx) { if constexpr (t + 3 < 6) P5_A1(t + 3, 3); P5_B1(t + 3, 2); P5_B1(t + 3, 3); P5_B1(t + 3, 4); }
            } else {
                SB0(); P5_MM(0, a[q][2]); SB0();
            }
        });
#undef P5_B1
#undef P5_A1
#undef P5_RD
#undef P5_MM
#undef P5_MIX
        STAMP(15);
        if (wk == 1) {
#pragma unroll
            for (int n = 0; n < 5; ++n) stsf4(L0 + L_RED5 + (wm * 5 + n) * 1024 + lo16, acc[n]);
        }
        BAR();
        if (wk == 0) {
#pragma unroll
            for (int n = 0; n < 5; ++n) {
                const f32x4 v = acc[n] + ldsf4(L0 + L_RED5 + (wm * 5 + n) * 1024 + lo16);
                const f32x4 gb = ldsf4(L0 + L_GB + (16 * n + 4 * g) * 4);
                const u32x2 o = {pack2(bflo(x1k[n][0]) + gb[0] * v[0], bfhi(x1k[n][0]) + gb[1] * v[1]), pack2(bflo(x1k[n][1]) + gb[2] * v[2], bfhi(x1k[n][1]) + gb[3] * v[3])};
                sts64(L0 + L_OS + (16 * wm + i16) * 160 + 32 * n + 8 * g, o);
            }
        }
        BAR();
#pragma unroll
        for (int i = 0; i < 2; ++i) {
            const int id = tid + 512 * i;
            if (id < 640) {
                const int r = (id * 6554) >> 16, c = id - 10 * r;
                *(GAS u32x4*)((char*)p.out + (rb * 128 + 64 * hf + r) * 1280 + cb * 160 + c * 16) = lds128(L0 + L_OS + id * 16);
            }
        }
    }
    STAMP(16); STAMP_RT(63); STAMP_OUT();
    asm volatile(".Lmk_code_to:");
}

extern "C" void kernel_launch(void* const* d_in, const int* in_sizes, int n_in, void* d_out, int out_size, void* d_ws, size_t ws_size, hipStream_t stream) {
    Params p;
    p.x = (const u16*)d_in[0]; p.wqkv = (const u16*)d_in[1]; p.wo = (const u16*)d_in[2];
    p.wg = (const u16*)d_in[4]; p.wu = (const u16*)d_in[5]; p.wd = (const u16*)d_in[6];
    p.c = (const u16*)d_in[7]; p.wmod = (const u16*)d_in[8];
    p.out = (u16*)d_out; p.ws = (char*)d_ws;
    mega_kernel<<<256, 512, 0, stream>>>(p);
}
```

```cpp
#define STAMPS 1
#include <hip/hip_runtime.h>
#include <hip/hip_bf16.h>
#include <stdint.h>
#include <stdio.h>
#include <type_traits>

typedef unsigned short u16;
typedef unsigned u32;
typedef unsigned long long u64;
typedef __attribute__((ext_vector_type(2))) unsigned u32x2;
typedef __attribute__((ext_vector_type(4))) unsigned u32x4;
typedef __attribute__((ext_vector_type(2))) float f32x2;
typedef __attribute__((ext_vector_type(4))) float f32x4;
typedef __attribute__((ext_vector_type(2))) __bf16 bf16x2;
typedef __attribute__((ext_vector_type(8))) __bf16 bf16x8;
typedef __attribute__((ext_vector_type(4))) short s16x4;
#define LAS __attribute__((address_space(3)))
#define GAS __attribute__((address_space(1)))
#define DI __device__ __forceinline__

constexpr int SEQ = 2048, DM = 640, FF = 1728;
constexpr float QSCALE = 0.22360679774997896f * 1.4426950408889634f;
constexpr float LOG2E = 1.4426950408889634f;

constexpr u32 WS_EPOCH = 0;
constexpr u32 WS_ERR   = 256;
constexpr u32 FS = 128;
constexpr u32 WS_FKV   = 4096;
constexpr u32 WS_FO    = WS_FKV + 256 * FS;
constexpr u32 WS_FX1   = WS_FO + 256 * FS;
constexpr u32 WS_FACT  = WS_FX1 + 256 * FS;
constexpr u32 WS_FH    = WS_FACT + 256 * FS;
constexpr u32 WS_FWT   = WS_FH + 256 * FS;
constexpr u32 WS_FWP   = WS_FWT + 256 * FS;
constexpr u32 WS_FW2   = WS_FWP + 256 * FS;
constexpr u32 WS_MODG  = WS_FW2 + 256 * FS;
constexpr u32 WS_UV    = WS_MODG + 61440;
constexpr u32 WS_STAT  = WS_UV + 114688;
constexpr u32 WS_KV    = 589824;
constexpr u32 WS_O     = WS_KV + 256 * 22528;
constexpr u32 WS_X1    = WS_O + SEQ * DM * 2;
constexpr u32 WS_ACT   = WS_X1 + SEQ * DM * 2;
constexpr u32 WS_WP    = WS_ACT + SEQ * FF * 2;
constexpr u32 WS_H     = WS_WP + 16 * 20 * 14 * 1024;
constexpr u32 WS_WD    = WS_H + SEQ * DM * 2;
constexpr u32 WS_WO    = WS_WD + 8 * 54 * 5 * 1024;
constexpr u32 WS_WT    = WS_WO + 8 * 20 * 5 * 1024;
constexpr u32 WS_STAMP = WS_WT + 16 * 153600;
constexpr u32 WS_END   = WS_STAMP + 256 * 8 * 64 * 4;

constexpr u32 L_WKV = 0, L_WQ = 102400, L_GS = 153600, L_STP = 156160, L_MODP = 156416, L_CNT = 157440, L_SB = 157696, L_G2 = 158016, L_Z16 = 158656;
constexpr u32 L_ODRAW = 101376, L_WTRAW = 116736, L_XS = 133120;
constexpr u32 L_K = 0, L_V = 30720, L_Q = 67584, L_WPRAW = 77824, L_UVL = 98304;
constexpr u32 L_WO = 61440, L_RED3 = 0, L_X1S = 20480, L_XR = 30720, L_GA = 40960, L_ST3 = 41472, L_WDRAW = 43008;
constexpr u32 L_R4 = 0, R4_STAGE = 45056, L_ST4 = 135168, L_UV = 143360, L_MR = 150528, L_XCH = 151552, L_UVS = 159744;
constexpr u32 L_RED5 = 126976, L_GB = 147456, L_OS = 148480;
constexpr u32 r5_slot(int t) { return t % 3 == 0 ? 45056 : t % 3 == 1 ? 86016 : 0; }

struct Params {
    const u16* x; const u16* wqkv; const u16* wo; const u16* wg; const u16* wu; const u16* wd;
    const u16* c; const u16* wmod; u16* out; char* ws;
};

DI float bflo(u32 d) { return __uint_as_float(d << 16); }
DI float bfhi(u32 d) { return __uint_as_float(d & 0xffff0000u); }
DI u32 pack2(float a, float b) { f32x2 v = {a, b}; return __builtin_bit_cast(u32, __builtin_convertvector(v, bf16x2)); }
DI float rbf(float a) { return bflo(pack2(a, a)); }
DI f32x4 mfma(u32x4 a, u32x4 b, f32x4 c) {
    return __builtin_amdgcn_mfma_f32_16x16x32_bf16(__builtin_bit_cast(bf16x8, a), __builtin_bit_cast(bf16x8, b), c, 0, 0, 0);
}
DI u32x2 ldtr(u32 a) { return __builtin_bit_cast(u32x2, __builtin_amdgcn_ds_read_tr16_b64_v4i16((LAS s16x4*)(size_t)a)); }
DI u32x4 ldtr2(u32 a0, u32 a1) { u32x2 x = ldtr(a0), y = ldtr(a1); return (u32x4){x[0], x[1], y[0], y[1]}; }
DI u32x4 lds128(u32 a) { return *(LAS u32x4*)(size_t)a; }
DI u32x2 lds64(u32 a) { return *(LAS u32x2*)(size_t)a; }
DI f32x4 ldsf4(u32 a) { return *(LAS f32x4*)(size_t)a; }
DI float ldsf(u32 a) { return *(LAS float*)(size_t)a; }
DI void sts128(u32 a, u32x4 v) { *(LAS u32x4*)(size_t)a = v; }
DI void sts64(u32 a, u32x2 v) { *(LAS u32x2*)(size_t)a = v; }
DI void stsf(u32 a, float v) { *(LAS float*)(size_t)a = v; }
DI void stsf4(u32 a, f32x4 v) { *(LAS f32x4*)(size_t)a = v; }
DI void stsf2(u32 a, f32x2 v) { *(LAS f32x2*)(size_t)a = v; }
DI void sts16(u32 a, u16 v) { *(LAS u16*)(size_t)a = v; }

DI void glds16_(const void* root, u32 voff, u32 ldsdst) {
    asm volatile("s_mov_b32 m0, %2\n\ts_nop 0\n\tglobal_load_lds_dwordx4 %0, %1" :: "v"(voff), "s"(root), "s"(__builtin_amdgcn_readfirstlane(ldsdst)) : "memory");
}
DI void glds16c_(const void* root, u32 voff, u32 ldsdst) {
    asm volatile("s_mov_b32 m0, %2\n\ts_nop 0\n\tglobal_load_lds_dwordx4 %0, %1 sc1" :: "v"(voff), "s"(root), "s"(__builtin_amdgcn_readfirstlane(ldsdst)) : "memory");
}
template <int OFF> DI void glds16c_next_(const void* root, u32 voff) {
    asm volatile("global_load_lds_dwordx4 %0, %1 offset:%2 sc1" :: "v"(voff), "s"(root), "i"(OFF) : "memory");
}
template <int OFF> DI u32x4 gld16(const void* root, u32 voff) {
    u32x4 r; asm volatile("global_load_dwordx4 %0, %1, %2 offset:%3" : "=v"(r) : "v"(voff), "s"(root), "i"(OFF) : "memory"); return r;
}
template <int OFF> DI u32x4 gld16c_(const void* root, u32 voff) {
    u32x4 r; asm volatile("global_load_dwordx4 %0, %1, %2 offset:%3 sc1" : "=v"(r) : "v"(voff), "s"(root), "i"(OFF) : "memory"); return r;
}
#define ROFF(root, ptr) ((u32)((const char*)(ptr) - (const char*)(root)))
#define glds16(root, ptr, voff, dst) glds16_((root), ROFF(root, ptr) + (voff), (dst))
#define glds16c(ptr, voff, dst) glds16c_(ws, ROFF(ws, ptr) + (voff), (dst))
#define glds16c_next(OFF, ptr, voff) glds16c_next_<(OFF)>(ws, ROFF(ws, ptr) + (voff))
#define gld16c(OFF, ptr, voff) gld16c_<(OFF)>(ws, ROFF(ws, ptr) + (voff))
DI u32 gldu16(const void* sbase, u32 voff) {
    u32 r; asm volatile("global_load_ushort %0, %1, %2" : "=v"(r) : "v"(voff), "s"(sbase) : "memory"); return r;
}
#define VMCNT(n) asm volatile("s_waitcnt vmcnt(" #n ")" ::: "memory")
#define BAR() asm volatile("s_waitcnt lgkmcnt(0)\n\ts_barrier" ::: "memory")
#define RLX_AGENT __ATOMIC_RELAXED, __HIP_MEMORY_SCOPE_AGENT
DI u64 ld_agent(const char* p) { return __hip_atomic_load((GAS u64*)p, RLX_AGENT); }
DI void st_agent(char* p, u64 v) { __hip_atomic_store((GAS u64*)p, v, RLX_AGENT); }
DI void st_agent32(char* p, u32 v) { __hip_atomic_store((GAS u32*)p, v, RLX_AGENT); }
DI float ld_agentf(const char* p) { return __uint_as_float(__hip_atomic_load((GAS u32*)p, RLX_AGENT)); }
DI float xsum4(float x) {
    const auto s = __builtin_amdgcn_permlane16_swap(__float_as_uint(x), __float_as_uint(x), false, false);
    x = __uint_as_float(s[0]) + __uint_as_float(s[1]);
    const auto t = __builtin_amdgcn_permlane32_swap(__float_as_uint(x), __float_as_uint(x), false, false);
    return __uint_as_float(t[0]) + __uint_as_float(t[1]);
}
DI float xmax4(float x) {
    const auto s = __builtin_amdgcn_permlane16_swap(__float_as_uint(x), __float_as_uint(x), false, false);
    x = fmaxf(__uint_as_float(s[0]), __uint_as_float(s[1]));
    const auto t = __builtin_amdgcn_permlane32_swap(__float_as_uint(x), __float_as_uint(x), false, false);
    return fmaxf(__uint_as_float(t[0]), __uint_as_float(t[1]));
}
template <int CTRL> DI float dppf(float v) { return __builtin_bit_cast(float, __builtin_amdgcn_update_dpp(0, __builtin_bit_cast(int, v), CTRL, 0xf, 0xf, true)); }

constexpr u32 SPIN_CAP = 1u << 17;
DI void poll_flags(const char* base, int n, int lane, u64 tag, char* ws, u32 code) {
    const char* a = base + (lane < n ? lane : 0) * FS;
    u32 spins = 0;
    for (;;) {
        const u64 v = ld_agent(a);
        if (__all(v == tag)) break;
        if (++spins > SPIN_CAP) { if (lane == 0) st_agent32(ws + WS_ERR, code); break; }
        __builtin_amdgcn_s_sleep(1);
    }
}
typedef __attribute__((ext_vector_type(2))) _Float16 f16x2;
DI float hlo(u32 d) { return (float)__builtin_bit_cast(f16x2, d)[0]; }
DI float hhi(u32 d) { return (float)__builtin_bit_cast(f16x2, d)[1]; }
DI u32 packh(float a, float b) { const f16x2 v = {(_Float16)a, (_Float16)b}; return __builtin_bit_cast(u32, v); }
DI bool gran_ok(const u32x4& v, u64 tag) { return v[2] == (u32)tag && v[3] == (u32)(tag >> 32); }
DI f32x4 gran_sum(const u32x4& a, const u32x4& b, const u32x4& c, const u32x4& d) {
    const f32x4 s = {(hlo(a[0]) + hlo(b[0])) + (hlo(c[0]) + hlo(d[0])), (hhi(a[0]) + hhi(b[0])) + (hhi(c[0]) + hhi(d[0])),
                     (hlo(a[1]) + hlo(b[1])) + (hlo(c[1]) + hlo(d[1])), (hhi(a[1]) + hhi(b[1])) + (hhi(c[1]) + hhi(d[1]))};
    return (f32x4){rbf(s[0]), rbf(s[1]), rbf(s[2]), rbf(s[3])};
}
DI f32x4 gather1(const char* ws, u32 g0, u64 tag, char* wsw, u32 code) {
    u32 spins = 0; u32x4 v0, v1, v2, v3;
    const u32 o = WS_MODG + g0 * 16;
    for (;;) {
        asm volatile("s_nop 4\n\tglobal_load_dwordx4 %0, %4, %5 sc1\n\tglobal_load_dwordx4 %1, %6, %5 sc1\n\tglobal_load_dwordx4 %2, %7, %5 sc1\n\tglobal_load_dwordx4 %3, %8, %5 sc1\n\ts_waitcnt vmcnt(0)"
                     : "=&v"(v0), "=&v"(v1), "=&v"(v2), "=&v"(v3) : "v"(o), "s"(ws), "v"(o + 15360), "v"(o + 30720), "v"(o + 46080) : "memory");
        if (__all(gran_ok(v0, tag) && gran_ok(v1, tag) && gran_ok(v2, tag) && gran_ok(v3, tag))) break;
        if (++spins > SPIN_CAP) { st_agent32(wsw + WS_ERR, code); break; }
        __builtin_amdgcn_s_sleep(1);
    }
    return gran_sum(v0, v1, v2, v3);
}
DI void gather2(const char* ws, u32 g0, u32 g1, u64 tag, char* wsw, u32 code, f32x4& ra, f32x4& rb) {
    u32 spins = 0; u32x4 v0, v1, v2, v3, u0, u1, u2, u3;
    const u32 o = WS_MODG + g0 * 16, q = WS_MODG + g1 * 16;
    for (;;) {
        asm volatile("s_nop 4\n\tglobal_load_dwordx4 %0, %8, %9 sc1\n\tglobal_load_dwordx4 %1, %10, %9 sc1\n\tglobal_load_dwordx4 %2, %11, %9 sc1\n\tglobal_load_dwordx4 %3, %12, %9 sc1\n\t"
                     "global_load_dwordx4 %4, %13, %9 sc1\n\tglobal_load_dwordx4 %5, %14, %9 sc1\n\tglobal_load_dwordx4 %6, %15, %9 sc1\n\tglobal_load_dwordx4 %7, %16, %9 sc1\n\ts_waitcnt vmcnt(0)"
                     : "=&v"(v0), "=&v"(v1), "=&v"(v2), "=&v"(v3), "=&v"(u0), "=&v"(u1), "=&v"(u2), "=&v"(u3)
                     : "v"(o), "s"(ws), "v"(o + 15360), "v"(o + 30720), "v"(o + 46080), "v"(q), "v"(q + 15360), "v"(q + 30720), "v"(q + 46080) : "memory");
        if (__all(gran_ok(v0, tag) && gran_ok(v1, tag) && gran_ok(v2, tag) && gran_ok(v3, tag) && gran_ok(u0, tag) && gran_ok(u1, tag) && gran_ok(u2, tag) && gran_ok(u3, tag))) break;
        if (++spins > SPIN_CAP) { st_agent32(wsw + WS_ERR, code); break; }
        __builtin_amdgcn_s_sleep(1);
    }
    ra = gran_sum(v0, v1, v2, v3); rb = gran_sum(u0, u1, u2, u3);
}
struct Gather { u32x4 v0, v1, v2, v3, u0, u1, u2, u3; };
DI void gather_ask(const char* ws, u32 g0, u32 g1, Gather& G) {
    const u32 o = WS_MODG + g0 * 16, q = WS_MODG + g1 * 16;
    asm volatile("global_load_dwordx4 %0, %8, %9 sc1\n\tglobal_load_dwordx4 %1, %10, %9 sc1\n\tglobal_load_dwordx4 %2, %11, %9 sc1\n\tglobal_load_dwordx4 %3, %12, %9 sc1\n\t"
                 "global_load_dwordx4 %4, %13, %9 sc1\n\tglobal_load_dwordx4 %5, %14, %9 sc1\n\tglobal_load_dwordx4 %6, %15, %9 sc1\n\tglobal_load_dwordx4 %7, %16, %9 sc1"
                 : "=&v"(G.v0), "=&v"(G.v1), "=&v"(G.v2), "=&v"(G.v3), "=&v"(G.u0), "=&v"(G.u1), "=&v"(G.u2), "=&v"(G.u3)
                 : "v"(o), "s"(ws), "v"(o + 15360), "v"(o + 30720), "v"(o + 46080), "v"(q), "v"(q + 15360), "v"(q + 30720), "v"(q + 46080) : "memory");
}
DI void gather_finish(const char* ws, u32 g0, u32 g1, Gather& G, u64 tag, char* wsw, u32 code, f32x4& ra, f32x4& rb) {
    asm volatile("" : "+v"(G.v0), "+v"(G.v1), "+v"(G.v2), "+v"(G.v3), "+v"(G.u0), "+v"(G.u1), "+v"(G.u2), "+v"(G.u3) :: "memory");
    if (__all(gran_ok(G.v0, tag) && gran_ok(G.v1, tag) && gran_ok(G.v2, tag) && gran_ok(G.v3, tag) && gran_ok(G.u0, tag) && gran_ok(G.u1, tag) && gran_ok(G.u2, tag) && gran_ok(G.u3, tag))) {
        ra = gran_sum(G.v0, G.v1, G.v2, G.v3); rb = gran_sum(G.u0, G.u1, G.u2, G.u3);
    } else gather2(ws, g0, g1, tag, wsw, code, ra, rb);
}
DI void gather_strip(const char* ws, u32 gs, u32 gc, u32 strip, int l, u64 tag, char* wsw, u32 code) {
    f32x4 a, c;
    gather2(ws, l < 40 ? gs + l : gc + (l - 40), gc + 24 + (l & 15), tag, wsw, code, a, c);
    if (l >= 40) a = a + 1.f;
    c = c + 1.f;
    *(LAS u32x2*)(size_t)(strip + (l < 40 ? l * 8 : 320 + (l - 40) * 8)) = (u32x2){pack2(a[0], a[1]), pack2(a[2], a[3])};
    if (l < 16) *(LAS u32x2*)(size_t)(strip + 320 + (24 + l) * 8) = (u32x2){pack2(c[0], c[1]), pack2(c[2], c[3])};
}
DI void put_strip_spread(const f32x4& a, const f32x4& c, u32 strip, int l) {
    if (l < 40) *(LAS u32x2*)(size_t)(strip + l * 8) = (u32x2){pack2(a[0], a[1]), pack2(a[2], a[3])};
    else {
        const u32 p0 = pack2(a[0] + 1.f, a[1] + 1.f), p1 = pack2(a[2] + 1.f, a[3] + 1.f);
        *(LAS u32x4*)(size_t)(strip + 320 + (l - 40) * 16) = (u32x4){p0 & 0xffffu, p0 & 0xffff0000u, p1 & 0xffffu, p1 & 0xffff0000u};
    }
    if (l < 16) {
        const u32 p0 = pack2(c[0] + 1.f, c[1] + 1.f), p1 = pack2(c[2] + 1.f, c[3] + 1.f);
        *(LAS u32x4*)(size_t)(strip + 320 + (24 + l) * 16) = (u32x4){p0 & 0xffffu, p0 & 0xffff0000u, p1 & 0xffffu, p1 & 0xffff0000u};
    }
    if (l == 63) *(LAS u32x4*)(size_t)(strip + 960) = (u32x4){0, 0, 0, 0};
}
DI void poll_at(const char* ws, u32 off, int lane, u64 tag, char* wsw, u32 code) {
    u32 spins = 0;
    for (;;) {
        const u64 v = ld_agent(ws + off);
        if (__all(v == tag)) break;
        if (++spins > SPIN_CAP) { if (lane == 0) st_agent32(wsw + WS_ERR, code); break; }
        __builtin_amdgcn_s_sleep(1);
    }
}

#define WRL(val, lane) asm volatile("s_nop 4\n\tv_writelane_b32 %0, %1, " #lane : "+v"(stv) : "s"(val))
#define STAMP(i) do { const u32 t_ = (u32)__builtin_amdgcn_s_memtime(); WRL(t_, i); } while (0)
#define STAMPL(i) STAMP(i)
#define STAMP_RT(i) do { const u32 t_ = (u32)__builtin_amdgcn_s_memrealtime(); WRL(t_, i); } while (0)
#define STAMP_OUT() do { if ((w == 0 || w == 4) && (b == 3 || b == 150)) { \
    const int t0_ = __builtin_amdgcn_readlane(stv, 0); \
    if (l < 37 || l >= 62) printf("[%d.%d %d %d]", b, w, l, l >= 62 ? stv : stv - t0_); } } while (0)

template <int N> using IC = std::integral_constant<int, N>;
template <int I, int E, class F> DI void static_for(F&& f) { if constexpr (I < E) { f(IC<I>{}); static_for<I + 1, E>(f); } }
template <int N> DI void vmw() { asm volatile("s_waitcnt vmcnt(%0)" :: "i"(N) : "memory"); }
template <int N> DI void vmw1(u32x4& a) { asm volatile("s_waitcnt vmcnt(%1)" : "+v"(a) : "i"(N) : "memory"); }
template <int N> DI void vmw4(u32x4& a, u32x4& b, u32x4& c, u32x4& d) { asm volatile("s_waitcnt vmcnt(%4)" : "+v"(a), "+v"(b), "+v"(c), "+v"(d) : "i"(N) : "memory"); }
DI u32 opaque(u32 a) { asm("" : "+v"(a)); return a; }
DI float dot2(u32 a, u32 b, float c) { return __builtin_amdgcn_fdot2_f32_bf16(__builtin_bit_cast(bf16x2, a), __builtin_bit_cast(bf16x2, b), c, false); }
#define SB0() __builtin_amdgcn_sched_barrier(0)
#define SGB(m, n) __builtin_amdgcn_sched_group_barrier((m), (n), 0)
constexpr int M_MFMA = 0x8, M_DSR = 0x100;
#define Z4 ((f32x4){0.f, 0.f, 0.f, 0.f})
DI u32 lds_add(u32 a, u32 v) { return __hip_atomic_fetch_add((LAS u32*)(size_t)a, v, __ATOMIC_RELAXED, __HIP_MEMORY_SCOPE_WORKGROUP); }
DI u32 lds_ld(u32 a) { return __hip_atomic_load((LAS u32*)(size_t)a, __ATOMIC_RELAXED, __HIP_MEMORY_SCOPE_WORKGROUP); }
DI void worker_sync(u32 cnt, int lane) {
    asm volatile("s_waitcnt lgkmcnt(0)" ::: "memory");
    if (lane == 0) lds_add(cnt, 1u);
    for (u32 spins = 0; lds_ld(cnt) < 4u && spins < (1u << 22); ++spins) {}
    asm volatile("" ::: "memory");
}

__global__ __launch_bounds__(512, 2) void mega_kernel(Params p) {
    __shared__ __attribute__((aligned(16))) char ldsmem[163840];
    const u32 L0 = (u32)(uintptr_t)(LAS char*)ldsmem;
    const int tid = threadIdx.x, l = tid & 63;
    const int w = __builtin_amdgcn_readfirstlane(tid >> 6);
    const int b = blockIdx.x, xg = b & 7, sidx = b >> 3;
    const int rb = 4 * (xg >> 1) + (sidx >> 3), j = 8 * (xg & 1) + (sidx & 7);
    const int g = l >> 4, i16 = l & 15, q4 = i16 >> 2, p4 = l & 3;
    u32 wu0 = 0;
    if (w == 7) {
        const char* pc; u32 span;
        asm volatile("s_getpc_b64 %0\n.Lmk_code_from:\n\ts_mov_b32 %1, .Lmk_code_to-.Lmk_code_from" : "=s"(pc), "=s"(span));
        const u32 off = min((u32)(sidx + 32 * min(l, 15)) * 128u, span - 4u);
        asm volatile("s_nop 0\n\tglobal_load_dword %0, %1, %2" : "=v"(wu0) : "v"(off), "s"(pc) : "memory");
    }
    char* const ws = p.ws;
    asm volatile("" :: "s"(p.x), "s"(p.wqkv), "s"(p.wo), "s"(p.wg), "s"(p.wu), "s"(p.wd), "s"(p.c), "s"(p.wmod), "s"(p.out), "s"(p.ws));
    u64 tag;
#define ASK_TAG(ep) u32x2 ep; asm volatile("global_load_dwordx2 %0, %1, %2" : "=v"(ep) : "v"(WS_EPOCH), "s"(ws) : "memory")
#define TAKE_TAG(ep) do { asm volatile("s_waitcnt vmcnt(0)" : "+v"(ep) :: "memory"); \
        tag = (((u64)(u32)__builtin_amdgcn_readfirstlane(ep[1]) << 32) | (u32)__builtin_amdgcn_readfirstlane(ep[0])) + 1; } while (0)
    const __amdgpu_buffer_rsrc_t wsr = __builtin_amdgcn_make_buffer_rsrc(ws, 0, WS_END, 0x00020000);
#define ST16C(v, off) __builtin_amdgcn_raw_buffer_store_b128((v), wsr, (off), 0, 16)
#define ST8C(v, off)  __builtin_amdgcn_raw_buffer_store_b64((v), wsr, (off), 0, 16)
    int stv = 0;
    STAMP_RT(62); STAMP(0);

    const int nj = j < 12 ? 7 : 6, Gj = j < 12 ? 7 * j : 84 + 6 * (j - 12);
    const bool wpwg = b < 216;
    const int wpm = b >= 108 ? 1 : 0, wpb = (b - 108 * wpm) >> 2, wpq = b & 3;
    u32x4 mw[5]; u32 mc[5];
    auto mod_ask = [&]() {
#pragma unroll
        for (int i = 0; i < 5; ++i) {
            const int r = 160 * (b & 3) + ((tid & 255) >> 3) + 32 * i;
            mw[i] = gld16<0>(p.wmod, r * 7680 + (b >> 2) * 128 + (tid & 7) * 16);
            mc[i] = gldu16(p.c, r * 2);
        }
    };
    auto mod_finish = [&]() {
        float acc[8];
#pragma unroll
        for (int e = 0; e < 8; ++e) acc[e] = 0.f;
#pragma unroll
        for (int i = 0; i < 5; ++i) {
            const float cv = bflo(mc[i]);
            const float sv = cv * __builtin_amdgcn_rcpf(1.f + __builtin_amdgcn_exp2f(-cv * LOG2E));
            const u32 slo = pack2(sv, 0.f), shi = slo << 16;
#pragma unroll
            for (int d = 0; d < 4; ++d) { acc[2 * d] = dot2(mw[i][d], slo, acc[2 * d]); acc[2 * d + 1] = dot2(mw[i][d], shi, acc[2 * d + 1]); }
        }
#pragma unroll
        for (int e = 0; e < 8; ++e) { const float v = acc[e]; acc[e] = xsum4(v + dppf<0x128>(v)); }
        if (l < 8) {
            stsf4(L0 + L_MODP + (w & 3) * 256 + l * 32, (f32x4){acc[0], acc[1], acc[2], acc[3]});
            stsf4(L0 + L_MODP + (w & 3) * 256 + l * 32 + 16, (f32x4){acc[4], acc[5], acc[6], acc[7]});
        }
        asm volatile("s_waitcnt lgkmcnt(0)" ::: "memory");
        u32 old = 0;
        if (l == 0) old = lds_add(L0 + L_CNT, 1u);
        if (__builtin_amdgcn_readfirstlane(old) == 3) {
            float v = 0.f;
#pragma unroll
            for (int ww = 0; ww < 4; ++ww) v += ldsf(L0 + L_MODP + ww * 256 + l * 4);
            const float v1 = dppf<0x55>(v), v2 = dppf<0xAA>(v), v3 = dppf<0xFF>(v), v0 = dppf<0x00>(v);
            const u32x4 gr = {packh(v0, v1), packh(v2, v3), (u32)tag, (u32)(tag >> 32)};
            if (p4 == 0) ST16C(gr, WS_MODG + ((b & 3) * 960 + (b >> 2) * 16 + (l >> 2)) * 16);
        }
    };
#define MOD_LANDED() asm volatile("s_waitcnt vmcnt(0)" : "+v"(mw[0]), "+v"(mw[1]), "+v"(mw[2]), "+v"(mw[3]), "+v"(mw[4]), "+v"(mc[0]), "+v"(mc[1]), "+v"(mc[2]), "+v"(mc[3]), "+v"(mc[4]) :: "memory")
    if (w >= 4) {
        __builtin_amdgcn_s_setprio(3);
        const int u = w - 4;
        const bool tw = b >= 106;
        const int tb = (b - 106) / 5, tk = (b - 106) - 5 * tb, ts = 4 * tk + u;
        if (tw) {
            const char* src = (const char*)p.wqkv + ts * 122880 + tb * 128;
            const u32 voff = (l >> 3) * 3840 + (l & 7) * 16;
#pragma unroll
            for (int i = 0; i < 4; ++i) glds16(p.wqkv, src + i * 8 * 3840, voff, L0 + L_WTRAW + u * 4096 + i * 1024);
        }
        ASK_TAG(epl);
        BAR();
        __builtin_amdgcn_s_setprio(0);
        TAKE_TAG(epl);
        if (tw) {
            const u32 a = L0 + L_WTRAW + u * 4096 + (8 * g + q4) * 128 + 8 * p4;
#pragma unroll
            for (int t = 0; t < 4; ++t) {
                const u32 cc = 8 * tb + 2 * t + (i16 >> 3);
                const u32 seg = cc >= 160 ? 2 : cc >= 80 ? 1 : 0, wi = cc - 80 * seg, jj = (wi * 13108) >> 16, c5 = wi - 5 * jj;
                const u32 c = seg == 0 ? 10 + c5 : seg == 1 ? c5 : 5 + c5, nt = c >> 1;
                const u32 dst = WS_WT + jj * 153600 + (nt < 5 ? ts * 5120 + nt * 1024 : 102400 + ts * 2560 + (nt - 5) * 1024)
                              + (nt < 7 ? (g * 16 + 8 * (c & 1) + (i16 & 7)) * 16 : g * 128 + (i16 & 7) * 16);
                ST16C(ldtr2(a + 32 * t, a + 32 * t + 512), dst);
            }
        }
        vmw<0>();
        {
            asm volatile("s_waitcnt lgkmcnt(0)" ::: "memory");
            u32 old = 0;
            if (l == 0) old = lds_add(L0 + L_CNT + 12, 1u);
            if (tw && __builtin_amdgcn_readfirstlane(old) == 3 && l == 0) st_agent(ws + WS_FWT + (b - 106) * FS, tag);
        }
        STAMPL(19);
        if (b >= 80 && b < 240) {
            if (!tw) __builtin_amdgcn_s_sleep(32);
            mod_ask(); MOD_LANDED(); mod_finish();
        }
        if (u == 0) {
            const int wh = l >= 30 ? 0 : l / 5, kr = l >= 30 ? 0 : l - 5 * wh;
            const int bl = (1280 * (wh >> 1) + 80 * j + 79 * (wh & 1)) >> 7;
            poll_at(ws, WS_FWT + (bl * 5 + kr) * FS, l, tag, ws, 13);
            for (u32 spins = 0; lds_ld(L0 + L_CNT + 12) < 4u && spins < (1u << 22); ++spins) {}
            if (l == 0) lds_add(L0 + L_CNT + 24, 1u);
        } else {
            for (u32 spins = 0; lds_ld(L0 + L_CNT + 24) < 1u && spins < (1u << 22); ++spins) {}
        }
        asm volatile("" ::: "memory");
        STAMPL(20);
        {
            const char* src = ws + WS_WT + j * 153600 + u * 1024;
            const u32 lo = l * 16;
#pragma unroll
            for (int i = 0; i < 25; ++i) glds16c(src + i * 4096, lo, L0 + L_WKV + u * 1024 + i * 4096);
        }
        VMCNT(0);
        STAMPL(17);
    } else {
        __builtin_amdgcn_s_setprio(2);
        if (b < 80) mod_ask();
        ASK_TAG(epw);
        if (tid < 16) *(LAS u32*)(size_t)(L0 + L_CNT + tid * 4) = 0u;
        BAR();
        STAMP(29);
        TAKE_TAG(epw);
        STAMP(30);
        if (b < 80) { MOD_LANDED(); mod_finish(); }
        {
            const char* src = (const char*)p.x + (rb * 128 + 8 * j) * 1280 + w * 1024;
            glds16(p.x, src, l * 16, L0 + L_XS + w * 1024);
            glds16(p.x, src + 4096, l * 16, L0 + L_XS + (w + 4) * 1024);
            if (w < 2) glds16(p.x, src + 8192, l * 16, L0 + L_XS + (w + 8) * 1024);
        }
        VMCNT(0);
        STAMP(1);
        worker_sync(L0 + L_CNT + 16, l);
        const int tok = l & 7;
        u32x4 xr[3];
#pragma unroll
        for (int i = 0; i < 3; ++i) xr[i] = lds128(L0 + L_XS + tok * 1280 + (20 * w + (i < 2 ? (l >> 3) + 8 * i : 16 + ((l >> 3) & 3))) * 16);
        {
            float s1 = 0.f, s2 = 0.f;
#pragma unroll
            for (int i = 0; i < 3; ++i) {
                float a1 = 0.f, a2 = 0.f;
#pragma unroll
                for (int d = 0; d < 4; ++d) { a1 = dot2(xr[i][d], 0x3F803F80u, a1); a2 = dot2(xr[i][d], xr[i][d], a2); }
                if (i < 2 || l < 32) { s1 += a1; s2 += a2; }
            }
            asm volatile("s_waitcnt lgkmcnt(0)" ::: "memory");
            if (l == 0) lds_add(L0 + L_CNT + 20, 1u);
            s1 += dppf<0x128>(s1); s2 += dppf<0x128>(s2);
            s1 = xsum4(s1); s2 = xsum4(s2);
            if (l < 8) stsf2(L0 + L_STP + (w * 8 + l) * 8, (f32x2){s1, s2});
        }
        gather_strip(ws, 40 * w, 160 + 40 * w, L0 + L_GS + w * 640, l, tag, ws, 1);
        STAMP(2);
        worker_sync(L0 + L_CNT + 4, l);
        {
            f32x2 st = {0.f, 0.f};
#pragma unroll
            for (int ww = 0; ww < 4; ++ww) st += *(LAS f32x2*)(size_t)(L0 + L_STP + (ww * 8 + tok) * 8);
            const float mu = st[0] * (1.f / 640.f), var = fmaxf(st[1] * (1.f / 640.f) - mu * mu, 0.f);
            const float rs = __builtin_amdgcn_rsqf(var + 1e-5f), nmr = -mu * rs;
#pragma unroll
            for (int i = 0; i < 3; ++i) {
                const int cl = i < 2 ? (l >> 3) + 8 * i : 16 + ((l >> 3) & 3);
                const u32x4 sa = lds128(L0 + L_GS + w * 640 + cl * 16), gg = lds128(L0 + L_GS + w * 640 + 320 + cl * 16);
                u32x4 h;
#pragma unroll
                for (int d = 0; d < 4; ++d)
                    h[d] = pack2((bflo(xr[i][d]) * rs + nmr) * bflo(gg[d]) + bflo(sa[d]), (bfhi(xr[i][d]) * rs + nmr) * bfhi(gg[d]) + bfhi(sa[d]));
                if (i < 2 || l < 32) ST16C(h, WS_H + (((rb * 8 + (j >> 1)) * 80 + 20 * w + cl) * 16 + 8 * (j & 1) + tok) * 16);
            }
        }
        VMCNT(0);
        worker_sync(L0 + L_CNT + 8, l);
        if (tid == 0) st_agent(ws + WS_FH + (rb * 16 + j) * FS, tag);
        if (w == 0) poll_flags(ws + WS_FH + rb * 16 * FS, 16, l, tag, ws, 11);
        __builtin_amdgcn_s_setprio(0);
    }
    BAR();
    asm volatile("s_waitcnt vmcnt(0)" : "+v"(wu0) :: "memory");
    STAMP(3);

    u32x4 xa[20];
    const u32 lo16 = l * 16;
    const char* hsrc = ws + WS_H + (rb * 8 + w) * 80 * 256;
#define XL(s) xa[s] = gld16c(((s) & 3) * 1024, hsrc + ((s) >> 2) * 4096, lo16)
    XL(0); XL(1); XL(2); XL(3); XL(4); XL(5);
    const char* kvx = ws + WS_KV + (rb * 16 + j) * 22528;
    const char* wtq = ws + WS_WT + j * 153600 + (100 + w) * 1024;
    {
        f32x4 acc[5];
#pragma unroll
        for (int n = 0; n < 5; ++n) acc[n] = Z4;
        const u32 b0 = L0 + L_WKV + lo16;
        u32x4 bf[2][5];
#pragma unroll
        for (int n = 0; n < 5; ++n) bf[0][n] = lds128(b0 + n * 1024);
        static_for<0, 20>([&](auto S) {
            constexpr int s = S;
            vmw1<(s < 14 ? 5 : 6)>(xa[s]);
            if constexpr (s + 6 < 20) XL(s + 6);
            if constexpr (s >= 13) { if (s < 19 || w < 2) glds16c(wtq + (s - 13) * 8192, lo16, L0 + L_WKV + (100 + w) * 1024 + (s - 13) * 8192); }
            SB0();
#pragma unroll
            for (int n = 0; n < 5; ++n) {
                if (s < 19) bf[(s + 1) & 1][n] = lds128(b0 + (s + 1) * 5120 + n * 1024);
                acc[n] = mfma(bf[s & 1][n], xa[s], acc[n]);
            }
#pragma unroll
            for (int n = 0; n < 5; ++n) { SGB(M_DSR, 1); SGB(M_MFMA, 1); }
            SB0();
        });
        STAMP(18);
        BAR();
        const int row = 256 + 16 * w + i16;
#pragma unroll
        for (int n = 0; n < 5; ++n) {
            const int Q = 4 * n + g;
            const u32x2 v = {pack2(acc[n][0], acc[n][1]), pack2(acc[n][2], acc[n][3])};
            const int wi = Q - 10, hd = wi >= 5 ? 1 : 0;
            const u32 a = Q < 10 ? L_K + row * 80 + 8 * Q : L_V + row * 96 + 48 * hd + 8 * (wi - 5 * hd);
            sts64(L0 + a, v);
        }
        if (g < 2) sts64(L0 + L_V + row * 96 + 48 * g + 40, (u32x2){0x3F80u, 0u});
        if (rb < 2) {
            const int nrow = rb == 0 ? 256 : 128;
            for (int i = tid; i < nrow * 5; i += 512) sts128(L0 + L_K + i * 16, (u32x4){0, 0, 0, 0});
            for (int i = tid; i < nrow * 6; i += 512) sts128(L0 + L_V + i * 16, (u32x4){0, 0, 0, 0});
        }
        vmw<0>();
        BAR();
    }
#undef XL
    STAMP(4);
    {
        u32x4 kvd[3];
#pragma unroll
        for (int e = 0; e < 3; ++e) {
            const int n = min(w + 8 * e, 21);
            kvd[e] = lds128(L0 + (n < 10 ? L_K + 20480 + n * 1024 : L_V + 24576 + (n - 10) * 1024) + lo16);
        }
        const u32 kvo = WS_KV + (rb * 16 + j) * 22528 + w * 1024 + lo16;
        const u16* wroot = wpm ? p.wu : p.wg;
        const char* wsrc = (const char*)wroot + (160 * wpq + 8 * w) * 3456 + wpb * 128;
        const u32 wvo = (l >> 3) * 3456 + (l & 7) * 16;
        f32x4 acc[3];
#pragma unroll
        for (int n = 0; n < 3; ++n) acc[n] = Z4;
        const u32 bq = L0 + L_WQ + lo16, bq2 = L0 + L_WQ + 2048 + g * 128 + (i16 & 7) * 16;
#define QRD(s, n) lds128(((n) < 2 ? bq + (n) * 1024 : bq2) + (s) * 2560)
        u32x4 bf[3][3];
#pragma unroll
        for (int n = 0; n < 3; ++n) { bf[0][n] = QRD(0, n); bf[1][n] = QRD(1, n); }
        Gather G;
        const u32 gg0 = l < 40 ? 480 + 40 * wpq + l : 640 + 40 * wpq + (l - 40), gg1 = 640 + 40 * wpq + 24 + (l & 15);
        static_for<0, 20>([&](auto S) {
            constexpr int s = S;
            if constexpr (s == 6) { if (w == 7 && wpwg) gather_ask(ws, gg0, gg1, G); }
            if constexpr (s == 0) { if (rb < 15) ST16C(kvd[0], kvo); }
            if constexpr (s == 1) { if (rb < 15) ST16C(kvd[1], kvo + 8192); }
            if constexpr (s == 2) { if (rb < 15 && w < 6) ST16C(kvd[2], kvo + 16384); }
            if constexpr (s == 14) {
                vmw<0>();
                u32 old = 0;
                if (l == 0) old = lds_add(L0 + L_CNT + 28, 1u);
                if (__builtin_amdgcn_readfirstlane(old) == 7 && l == 0) st_agent(ws + WS_FKV + (j * 16 + rb) * FS, tag);
                if (w == 7 && wpwg) { f32x4 ra, rc; gather_finish(ws, gg0, gg1, G, tag, ws, 2, ra, rc); put_strip_spread(ra, rc, L0 + L_SB, l); }
            }
            if constexpr (s == 15) { if (wpwg) glds16(wroot, wsrc, wvo, L0 + L_WPRAW + w * 1024); }
            if constexpr (s == 16) { if (wpwg) glds16(wroot, wsrc + 64 * 3456, wvo, L0 + L_WPRAW + (w + 8) * 1024); }
            if constexpr (s == 17) { if (wpwg && w < 4) glds16(wroot, wsrc + 128 * 3456, wvo, L0 + L_WPRAW + (w + 16) * 1024); }
            SB0();
#pragma unroll
            for (int n = 0; n < 3; ++n) {
                if (s < 18) bf[(s + 2) % 3][n] = QRD(s + 2, n);
                acc[n] = mfma(bf[s % 3][n], xa[s], acc[n]);
            }
#pragma unroll
            for (int n = 0; n < 3; ++n) { SGB(M_DSR, 1); SGB(M_MFMA, 1); }
            SB0();
        });
#undef QRD
        STAMP(21);
#pragma unroll
        for (int n = 0; n < 3; ++n) {
            const int Q = 4 * n + g;
            const u32x2 v = {pack2(acc[n][0] * QSCALE, acc[n][1] * QSCALE), pack2(acc[n][2] * QSCALE, acc[n][3] * QSCALE)};
            if (Q < 10) sts64(L0 + L_Q + (16 * w + i16) * 80 + 8 * Q, v);
        }
    }
    if (w == 0 && rb >= 1) poll_flags(ws + WS_FKV + (j * 16 + (rb >= 2 ? rb - 2 : rb - 1)) * FS, rb >= 2 ? 2 : 1, l, tag, ws, 3);
    VMCNT(0);
    BAR();
    STAMP(5);
    if (rb >= 1) {
#pragma unroll
        for (int e = 0; e < 3; ++e) {
            const int n = w + 8 * e;
            if (n < 22) {
                const u32 d = n < 10 ? L_K + n * 1024 : L_V + (n - 10) * 1024, st = n < 10 ? 10240 : 12288;
                glds16c(kvx - 16 * 22528 + n * 1024, lo16, L0 + d + st);
                if (rb >= 2) glds16c(kvx - 32 * 22528 + n * 1024, lo16, L0 + d);
            }
        }
    }
    auto wp_slot = [&](int gam, int& jj, int& slot) {
        jj = gam < 84 ? gam / 7 : 12 + (gam - 84) / 6;
        const int ar = gam - (jj < 12 ? 7 * jj : 84 + 6 * (jj - 12)), a = (jj < 12 || ar < 3) ? ar : ar + 1;
        slot = a < 3 ? 2 * a + wpm : a == 3 ? (wpm ? 13 : 6) : 7 + 2 * (a - 4) + wpm;
    };
    if (wpwg) {
        const u32 one = i16 == 0 ? 0x3F803F80u : 0u;
        const u32x4 ones = {one, one, one, one};
#pragma unroll
        for (int i = 0; i < 3; ++i) {
            if (i < 2 || w < 4) {
                const int f = w + 8 * i, ks = f >> 2, t16 = f & 3;
                int jj, slot; wp_slot(4 * wpb + t16, jj, slot);
                const u32 a = L0 + L_WPRAW + (32 * ks + 8 * g + q4) * 128 + 32 * t16 + 8 * p4;
                u32x4 wr = ldtr2(a, a + 512);
                const u32x4 ga = lds128(L0 + L_G2 + (32 * ks + 8 * g) * 4), gc = lds128(L0 + L_G2 + (32 * ks + 8 * g) * 4 + 16);
                const u32x4 sbz = lds128(L0 + (i16 == 0 ? L_SB + (32 * ks + 8 * g) * 2 : L_Z16));
                const f32x4 vs = mfma(wr, sbz, Z4);
                u32x4 o; float t0, t1, t2, t3, t4, t5, t6, t7;
                asm volatile("v_dot2_f32_bf16 %4, %12, %16, 0\n\tv_dot2_f32_bf16 %5, %12, %17, 0\n\tv_dot2_f32_bf16 %6, %13, %18, 0\n\tv_dot2_f32_bf16 %7, %13, %19, 0\n\t"
                             "v_dot2_f32_bf16 %8, %14, %20, 0\n\tv_dot2_f32_bf16 %9, %14, %21, 0\n\tv_dot2_f32_bf16 %10, %15, %22, 0\n\tv_dot2_f32_bf16 %11, %15, %23, 0\n\t"
                             "v_cvt_pk_bf16_f32 %0, %4, %5\n\tv_cvt_pk_bf16_f32 %1, %6, %7\n\tv_cvt_pk_bf16_f32 %2, %8, %9\n\ts_nop 0\n\tv_cvt_pk_bf16_f32 %3, %10, %11\n\ts_nop 1"
                             : "=&v"(o[0]), "=&v"(o[1]), "=&v"(o[2]), "=&v"(o[3]),
                               "=&v"(t0), "=&v"(t1), "=&v"(t2), "=&v"(t3), "=&v"(t4), "=&v"(t5), "=&v"(t6), "=&v"(t7)
                             : "v"(wr[0]), "v"(wr[1]), "v"(wr[2]), "v"(wr[3]), "v"(ga[0]), "v"(ga[1]), "v"(ga[2]), "v"(ga[3]), "v"(gc[0]), "v"(gc[1]), "v"(gc[2]), "v"(gc[3]));
                const f32x4 us = mfma(o, ones, Z4);
                ST16C(o, WS_WP + ((jj * 20 + 5 * wpq + ks) * 14 + slot) * 1024 + lo16);
                if (i16 == 0) { stsf4(L0 + L_UVL + f * 128 + g * 16, us); stsf4(L0 + L_UVL + f * 128 + 64 + g * 16, vs); }
            }
        }
    }
    if (wpwg) { if (w < 4) vmw<3>(); else vmw<2>(); } else vmw<0>();
    BAR();
    STAMP(6);
    if (wpwg && w == 3) {
#pragma unroll
        for (int e = 0; e < 2; ++e) {
            const int t16 = 2 * e + (l >> 5);
            int jj, slot; wp_slot(4 * wpb + t16, jj, slot);
            float v = 0.f;
#pragma unroll
            for (int ks = 0; ks < 5; ++ks) v += ldsf(L0 + L_UVL + (4 * ks + t16) * 128 + (l & 31) * 4);
            st_agent32(ws + WS_UV + ((jj * 14 + slot) * 4 + wpq) * 128 + (l & 31) * 4, __float_as_uint(v));
        }
    }
    const int hf = j & 1, cb = j >> 1;
    u32 wvoff[5];
#pragma unroll
    for (int i = 0; i < 5; ++i) {
        const u32 sg = 64 * i + l, pos = (sg * 6554) >> 16, c = sg - pos * 10, k = pos ^ (((pos >> 3) & 1) << 2);
        wvoff[i] = k * 1280 + c * 16;
    }
    const char* wosrc = (const char*)p.wo + cb * 160;
#define WO_STEP(sx) do { _Pragma("unroll") for (int i = 0; i < 5; ++i) glds16(p.wo, wosrc + (sx) * 32 * 1280, wvoff[i], L0 + L_WO + (sx) * 5120 + i * 1024); } while (0)
    STAMP(7);

    {
        const int hd = w & 1, qi = w >> 1;
        const int kr0 = 32 * qi;
        u32x4 qf[2];
#pragma unroll
        for (int a = 0; a < 2; ++a) {
            const u32 qa = L0 + L_Q + (kr0 + 16 * a + i16) * 80 + hd * 40;
            const u32x2 q0 = lds64(qa + 8 * g);
            u32x2 q1 = lds64(qa + 32);
            if (g != 0) q1 = (u32x2){0, 0};
            qf[a] = (u32x4){q0[0], q0[1], q1[0], q1[1]};
        }
        const u32 kb0 = L0 + L_K + (kr0 + i16) * 80 + hd * 40 + 8 * g, kb1 = L0 + L_K + (kr0 + i16) * 80 + hd * 40 + 32;
        f32x4 mfirst, mlast;
#pragma unroll
        for (int r = 0; r < 4; ++r) { const bool lo = 4 * g + r <= i16; mfirst[r] = lo ? -INFINITY : 0.f; mlast[r] = lo ? 0.f : -INFINITY; }
        f32x4 s0[17], s1[17];
        {
            u32x4 kf[6];
#define KRD(t) do { const u32x2 k0 = lds64(kb0 + (t) * 1280), k1 = lds64(kb1 + (t) * 1280); kf[(t) % 6] = (u32x4){k0[0], k0[1], k1[0], k1[1]}; } while (0)
            KRD(0); KRD(1); KRD(2); KRD(3); KRD(4);
            static_for<0, 18>([&](auto T) {
                constexpr int t = T;
                SB0();
                if constexpr (t < 13) KRD(t + 5);
                if constexpr (t < 17) s0[t] = mfma(kf[t % 6], qf[0], t == 0 ? mfirst : t == 16 ? mlast : Z4);
                if constexpr (t > 0) s1[t - 1] = mfma(kf[t % 6], qf[1], t == 1 ? mfirst : t == 17 ? mlast : Z4);
                SGB(M_DSR, 2); SGB(M_MFMA, 2);
                SB0();
            });
#undef KRD
        }
        WO_STEP(4 + w);
        if (rb < 2) {
            const int fv = rb == 1 ? 128 : 256;
#pragma unroll
            for (int t = 0; t < 16; ++t) {
                const bool d0 = kr0 + 16 * t < fv, d1 = kr0 + 16 * t + 16 < fv;
#pragma unroll
                for (int r = 0; r < 4; ++r) { s0[t][r] = d0 ? -INFINITY : s0[t][r]; s1[t][r] = d1 ? -INFINITY : s1[t][r]; }
            }
        }
        STAMP(22);
        float m0 = fmaxf(s0[0][0], s0[0][1]), m1 = fmaxf(s1[0][0], s1[0][1]), n0 = fmaxf(s0[0][2], s0[0][3]), n1 = fmaxf(s1[0][2], s1[0][3]);
#pragma unroll
        for (int t = 1; t < 17; ++t) {
            m0 = fmaxf(fmaxf(m0, s0[t][0]), s0[t][1]); m1 = fmaxf(fmaxf(m1, s1[t][0]), s1[t][1]);
            n0 = fmaxf(fmaxf(n0, s0[t][2]), s0[t][3]); n1 = fmaxf(fmaxf(n1, s1[t][2]), s1[t][3]);
        }
        m0 = fmaxf(m0, n0); m1 = fmaxf(m1, n1);
        m0 = xmax4(m0); m1 = xmax4(m1);
        WO_STEP(12 + w);
        STAMP(23);
        f32x4 o00 = Z4, o01 = Z4, o10 = Z4, o11 = Z4;
        const u32 vb = L0 + L_V + (kr0 + 4 * g + q4) * 96 + hd * 48 + 8 * p4;
        auto pv = [&](auto SUB) {
            constexpr bool sub = SUB;
            u32x4 vf[2][2];
            vf[0][0] = ldtr2(vb, vb + 16 * 96); vf[0][1] = ldtr2(vb + 32, vb + 16 * 96 + 32);
#define EX(v, m) __builtin_amdgcn_exp2f(sub ? (v) - (m) : (v))
            static_for<0, 9>([&](auto U) {
                constexpr int u = U;
                if constexpr (u < 8) { const u32 a = vb + (u + 1) * 32 * 96; vf[(u + 1) & 1][0] = ldtr2(a, a + 16 * 96); vf[(u + 1) & 1][1] = ldtr2(a + 32, a + 16 * 96 + 32); }
                u32x4 pa, pb;
                pa[0] = pack2(EX(s0[2 * u][0], m0), EX(s0[2 * u][1], m0)); pa[1] = pack2(EX(s0[2 * u][2], m0), EX(s0[2 * u][3], m0));
                if constexpr (u < 8) { pa[2] = pack2(EX(s0[2 * u + 1][0], m0), EX(s0[2 * u + 1][1], m0)); pa[3] = pack2(EX(s0[2 * u + 1][2], m0), EX(s0[2 * u + 1][3], m0)); }
                else { pa[2] = 0; pa[3] = 0; }
                if constexpr (u > 0) { pb[0] = pack2(EX(s1[2 * u - 1][0], m1), EX(s1[2 * u - 1][1], m1)); pb[1] = pack2(EX(s1[2 * u - 1][2], m1), EX(s1[2 * u - 1][3], m1)); }
                else { pb[0] = 0; pb[1] = 0; }
                pb[2] = pack2(EX(s1[2 * u][0], m1), EX(s1[2 * u][1], m1)); pb[3] = pack2(EX(s1[2 * u][2], m1), EX(s1[2 * u][3], m1));
                o00 = mfma(vf[u & 1][0], pa, o00); o01 = mfma(vf[u & 1][1], pa, o01);
                o10 = mfma(vf[u & 1][0], pb, o10); o11 = mfma(vf[u & 1][1], pb, o11);
                SB0();
            });
#undef EX
        };
        if (__all(fabsf(m0) <= 64.f && fabsf(m1) <= 64.f)) pv(std::false_type{}); else pv(std::true_type{});
        STAMP(24);
        const float i0 = __builtin_amdgcn_rcpf(__shfl(o01[0], i16 + 16)), i1 = __builtin_amdgcn_rcpf(__shfl(o11[0], i16 + 16));
        const u32 oa = L0 + L_Q + (kr0 + i16) * 80 + hd * 40;
        sts64(oa + 8 * g, (u32x2){pack2(o00[0] * i0, o00[1] * i0), pack2(o00[2] * i0, o00[3] * i0)});
        sts64(oa + 1280 + 8 * g, (u32x2){pack2(o10[0] * i1, o10[1] * i1), pack2(o10[2] * i1, o10[3] * i1)});
        if (g == 0) {
            sts64(oa + 32, (u32x2){pack2(o01[0] * i0, o01[1] * i0), pack2(o01[2] * i0, o01[3] * i0)});
            sts64(oa + 1280 + 32, (u32x2){pack2(o11[0] * i1, o11[1] * i1), pack2(o11[2] * i1, o11[3] * i1)});
        }
    }
    BAR();
    {
        const int tok = 64 * (w & 1) + l;
        const u32 oo = WS_O + (((rb * 8 + (tok >> 4)) * 80 + 5 * j) * 16 + (tok & 15)) * 16;
        ST16C(lds128(L0 + L_Q + tok * 80 + (w >> 1) * 16), oo + (w >> 1) * 256);
        if (w < 2) ST16C(lds128(L0 + L_Q + tok * 80 + 64), oo + 4 * 256);
    }
    VMCNT(0);
    BAR();
    if (tid == 0) {
        st_agent(ws + WS_FO + (rb * 16 + j) * FS, tag);
        if (wpwg) st_agent(ws + WS_FWP + b * FS, tag);
    }
    STAMP(8);

    const int wm = w & 3, wk = w >> 2;
    const int mt3 = rb * 8 + 4 * hf + wm;
    u32x2 x1k[5];
    {
        const int wdu = b + 256 * (w - 6);
        const bool wdw = w >= 6 && wdu < 432;
        if (w >= 1 && w < 5) {
#pragma unroll
            for (int i = 0; i < 5; ++i) glds16(p.wo, wosrc + (w - 1) * 32 * 1280, wvoff[i], L0 + L_WO + (w - 1) * 5120 + i * 1024);
        } else if (w >= 5) {
#pragma unroll
            for (int e = 0; e < 4; ++e) {
                const int pc = w - 5 + 3 * e;
                if (pc < 10) {
                    const u32 sg = 64 * pc + l, r = (sg * 6554) >> 16, c = sg - r * 10;
                    glds16(p.x, (const char*)p.x + (rb * 128 + 64 * hf) * 1280 + cb * 160, r * 1280 + c * 16, L0 + L_XR + pc * 1024);
                }
            }
            if (wdw) {
                const int ucb = wdu / 54, us = wdu - 54 * ucb;
#pragma unroll
                for (int i = 0; i < 5; ++i) glds16(p.wd, (const char*)p.wd + ucb * 160 + us * 32 * 1280, wvoff[i], L0 + L_WDRAW + (w - 6) * 5120 + i * 1024);
            }
        }
        if (w == 1 && l < 20) stsf4(L0 + L_GA + l * 16, gather1(ws, 320 + 20 * cb + l, tag, ws, 4));
        if (w == 0) {
            const int bl = min((Gj >> 2) + ((l >> 2) % 3), (Gj + nj - 1) >> 2);
            poll_at(ws, WS_FWP + (108 * (l >= 12 && l < 24 ? 1 : 0) + 4 * bl + (l & 3)) * FS, l, tag, ws, 6);
            poll_flags(ws + WS_FO + rb * 16 * FS, 16, l, tag, ws, 5);
        }
        if (wdw) vmw<5>(); else vmw<0>();
        BAR();
        STAMP(9);
        u32x4 a[10];
        const char* asrc = ws + WS_O + (mt3 * 80 + 40 * wk) * 256;
#define AL(s) a[s] = gld16c(((s) & 3) * 1024, asrc + ((s) >> 2) * 4096, lo16)
        AL(0); AL(1); AL(2); AL(3);
        f32x4 acc[5];
#pragma unroll
        for (int n = 0; n < 5; ++n) acc[n] = Z4;
        const u32 wb = L0 + L_WO + wk * 51200;
        const u32 b0 = wb + 160 * (8 * g + 4 * (g & 1) + q4) + 8 * p4, b1 = wb + 160 * (8 * g + 4 * ((g & 1) ^ 1) + q4) + 8 * p4;
        u32x4 bf[2][5];
#pragma unroll
        for (int n = 0; n < 5; ++n) bf[0][n] = ldtr2(b0 + n * 32, b1 + n * 32);
        static_for<0, 10>([&](auto S) {
            constexpr int s = S;
            vmw1<(s < 7 ? 3 : 9 - s)>(a[s]);
            if constexpr (s + 4 < 10) AL(s + 4);
            SB0();
#pragma unroll
            for (int n = 0; n < 5; ++n) {
                if (s < 9) bf[(s + 1) & 1][n] = ldtr2(b0 + (s + 1) * 5120 + n * 32, b1 + (s + 1) * 5120 + n * 32);
                acc[n] = mfma(bf[s & 1][n], a[s], acc[n]);
            }
#pragma unroll
            for (int n = 0; n < 5; ++n) { SGB(M_DSR, 2); SGB(M_MFMA, 1); }
            SB0();
        });
#undef AL
        STAMP(25);
        if (wk == 1) {
#pragma unroll
            for (int n = 0; n < 5; ++n) stsf4(L0 + L_RED3 + (wm * 5 + n) * 1024 + lo16, acc[n]);
        }
        BAR();
        if (wk == 0) {
            float s1 = 0.f, s2 = 0.f;
#pragma unroll
            for (int n = 0; n < 5; ++n) {
                const f32x4 v = acc[n] + ldsf4(L0 + L_RED3 + (wm * 5 + n) * 1024 + lo16);
                const f32x4 ga = ldsf4(L0 + L_GA + (16 * n + 4 * g) * 4);
                const u32x2 xr = lds64(L0 + L_XR + (16 * wm + i16) * 160 + 32 * n + 8 * g);
                const u32x2 o = {pack2(bflo(xr[0]) + ga[0] * v[0], bfhi(xr[0]) + ga[1] * v[1]), pack2(bflo(xr[1]) + ga[2] * v[2], bfhi(xr[1]) + ga[3] * v[3])};
                x1k[n] = o;
                s1 = dot2(o[0], 0x3F803F80u, s1); s1 = dot2(o[1], 0x3F803F80u, s1);
                s2 = dot2(o[0], o[0], s2); s2 = dot2(o[1], o[1], s2);
            }
            const u32 xo = WS_X1 + ((mt3 * 80 + 10 * cb) * 16 + i16) * 16;
#pragma unroll
            for (int n = 0; n < 4; n += 2) {
                const auto a0 = __builtin_amdgcn_permlane16_swap(x1k[n][0], x1k[n + 1][0], false, false);
                const auto a1 = __builtin_amdgcn_permlane16_swap(x1k[n][1], x1k[n + 1][1], false, false);
                ST16C(((u32x4){a0[0], a1[0], a0[1], a1[1]}), xo + (2 * (n + (g & 1)) + (g >> 1)) * 256);
            }
            ST8C(x1k[4], xo + (8 + (g >> 1)) * 256 + 8 * (g & 1));
            s1 = xsum4(s1); s2 = xsum4(s2);
            if (l < 16) ST8C(((u32x2){__float_as_uint(s1), __float_as_uint(s2)}), WS_STAT + ((rb * 8 + cb) * 128 + 64 * hf + 16 * wm + l) * 8);
            STAMP(26);
            vmw<0>();
        } else {
#pragma unroll
            for (int n = 0; n < 5; ++n) x1k[n] = (u32x2){0, 0};
            if (wdw) {
                const u32 wr = L0 + L_WDRAW + (w - 6) * 5120;
                const u32 r0 = wr + 160 * (8 * g + 4 * (g & 1) + q4) + 8 * p4, r1 = wr + 160 * (8 * g + 4 * ((g & 1) ^ 1) + q4) + 8 * p4;
#pragma unroll
                for (int n = 0; n < 5; ++n) ST16C(ldtr2(r0 + n * 32, r1 + n * 32), WS_WD + (wdu * 5 + n) * 1024 + lo16);
            }
        }
        BAR();
        if (tid == 0) st_agent(ws + WS_FX1 + (rb * 16 + j) * FS, tag);
    }
    STAMP(10);

    const char* wdsrc = ws + WS_WD + (cb * 54 + wk * 27) * 5120;
    const u32 r5w = L0 + wk * 20480 + wm * 5120;
#define P5_B1(t, i) do { if ((i) == 0 || (i) == 4) glds16c(wdsrc + min(4 * (t) + wm, 26) * 5120 + (i) * 1024, lo16, r5w + r5_slot(t) + (i) * 1024); \
        else glds16c_next(((i) & 3) * 1024, wdsrc + min(4 * (t) + wm, 26) * 5120, lo16); } while (0)
    {
        const int wn = w >> 2;
        const int bst = w < 4 ? 4 * w : 16 + 3 * (w - 4);
        const char* wpj = ws + WS_WP + (j * 20 * 14 + bst) * 1024;
        const char* x1b = ws + WS_X1 + (rb * 8 + w) * 80 * 256;
        const u32 r4a = L0 + L_R4 + w * 2048, r4b = L0 + L_R4 + 16384 + bst * 1024;
#define P4_B1(t, i) do { if ((i) == 0) glds16c(wpj + (t) * 28 * 1024, lo16, r4b + ((t) % 3) * R4_STAGE); else glds16c_next((i) * 1024, wpj + (t) * 28 * 1024, lo16); } while (0)
#define P4_A1(t, i) do { if ((i) == 0) glds16c(x1b + 8 * (t) * 256, lo16, r4a + ((t) % 3) * R4_STAGE); else glds16c_next(1024, x1b + 8 * (t) * 256, lo16); } while (0)
#define P4_B(t) do { P4_B1(t, 0); P4_B1(t, 1); P4_B1(t, 2); if (w < 4) P4_B1(t, 3); } while (0)
        P4_B(0); P4_B(1); P4_B(2);
        if (w == 0) poll_flags(ws + WS_FX1 + rb * 16 * FS, 16, l, tag, ws, 7);
        BAR();
        STAMP(11);
        glds16c(ws + WS_STAT + rb * 8192 + w * 1024, lo16, L0 + L_ST4 + w * 1024);
        if (w < 7) glds16c(ws + WS_UV + j * 7168 + w * 1024, lo16, L0 + L_UV + w * 1024);
        P4_A1(0, 0); P4_A1(0, 1); P4_A1(1, 0); P4_A1(1, 1); P4_A1(2, 0); P4_A1(2, 1);
        f32x4 acc[7][2];
#pragma unroll
        for (int n = 0; n < 7; ++n) { acc[n][0] = Z4; acc[n][1] = Z4; }
        const u32 ra = opaque(L0 + L_R4 + 2 * wm * 2048 + lo16), rbb = opaque(L0 + L_R4 + 16384 + 7 * wn * 1024 + lo16);
        u32x4 fa[2][2], fb[2][7];
#define P4_RA(buf, t, ks) do { const u32 so = ((t) % 3) * R4_STAGE + (ks) * 1024; fa[buf][0] = lds128(ra + so); fa[buf][1] = lds128(ra + so + 2048); } while (0)
#define P4_RB(buf, t, ks, n) fb[buf][n] = lds128(rbb + ((t) % 3) * R4_STAGE + (ks) * 14336 + (n) * 1024)
#define P4_MM(buf, n) do { acc[n][0] = mfma(fb[buf][n], fa[buf][0], acc[n][0]); acc[n][1] = mfma(fb[buf][n], fa[buf][1], acc[n][1]); } while (0)
#define P4_HALF(buf, rd, t, ks, V0, V1, V2) do { \
            SB0(); if (rd) { P4_RA((buf) ^ 1, t, ks); P4_RB((buf) ^ 1, t, ks, 0); } P4_MM(buf, 0); SB0(); V0; \
            SB0(); if (rd) P4_RB((buf) ^ 1, t, ks, 1); P4_MM(buf, 1); SB0(); \
            SB0(); if (rd) P4_RB((buf) ^ 1, t, ks, 2); P4_MM(buf, 2); SB0(); V1; \
            SB0(); if (rd) P4_RB((buf) ^ 1, t, ks, 3); P4_MM(buf, 3); SB0(); \
            SB0(); if (rd) P4_RB((buf) ^ 1, t, ks, 4); P4_MM(buf, 4); SB0(); V2; \
            SB0(); if (rd) P4_RB((buf) ^ 1, t, ks, 5); P4_MM(buf, 5); SB0(); \
            SB0(); if (rd) P4_RB((buf) ^ 1, t, ks, 6); P4_MM(buf, 6); SB0(); } while (0)
        VMCNT(4);
        BAR();
        if (tid == 0) st_agent(ws + WS_FW2 + b * FS, tag);
        STAMP(27);
        u32x2 wdf = {0u, 0u};
        const u32 wdo = WS_FW2 + ((54 * cb + (l < 54 ? l : 0)) & 255) * FS;
        P4_RA(0, 0, 0);
#pragma unroll
        for (int n = 0; n < 7; ++n) P4_RB(0, 0, 0, n);
        static_for<0, 10>([&](auto T) {
            constexpr int t = T;
            constexpr bool f = t >= 1 && t + 2 <= 9, sN = t + 3 <= 9;
            if constexpr (t < 9) P4_HALF(0, true, t, 1, if (f && w < 4) P4_B1(t + 2, 3), if (f) P4_A1(t + 2, 0), if (f) P4_A1(t + 2, 1));
            else P4_HALF(0, true, t, 1, P5_B1(0, 3), P5_B1(0, 4), P5_B1(1, 0));
            if constexpr (t == 5) {
                if (tid < 128) {
                    f32x2 st = {0.f, 0.f};
#pragma unroll
                    for (int c = 0; c < 8; ++c) st += *(LAS f32x2*)(size_t)(L0 + L_ST4 + (c * 128 + tid) * 8);
                    const float mu = st[0] * (1.f / 640.f), var = fmaxf(st[1] * (1.f / 640.f) - mu * mu, 0.f), r = __builtin_amdgcn_rsqf(var + 1e-5f);
                    stsf2(L0 + L_MR + tid * 8, (f32x2){r, -mu * r});
                } else {
                    const int e = tid - 128;
                    stsf(L0 + L_UVS + e * 4, (ldsf(L0 + L_UV + (e >> 5) * 512 + (e & 31) * 4) + ldsf(L0 + L_UV + (e >> 5) * 512 + 128 + (e & 31) * 4))
                                           + (ldsf(L0 + L_UV + (e >> 5) * 512 + 256 + (e & 31) * 4) + ldsf(L0 + L_UV + (e >> 5) * 512 + 384 + (e & 31) * 4)));
                    if (e < 64) { const int e2 = e + 384;
                        stsf(L0 + L_UVS + e2 * 4, (ldsf(L0 + L_UV + (e2 >> 5) * 512 + (e2 & 31) * 4) + ldsf(L0 + L_UV + (e2 >> 5) * 512 + 128 + (e2 & 31) * 4))
                                                + (ldsf(L0 + L_UV + (e2 >> 5) * 512 + 256 + (e2 & 31) * 4) + ldsf(L0 + L_UV + (e2 >> 5) * 512 + 384 + (e2 & 31) * 4))); }
                }
            }
            if constexpr (t < 9) {
                if constexpr (t == 0) vmw<2>();
                else if constexpr (t == 8) {
                    asm volatile("s_waitcnt vmcnt(0)" : "+v"(wdf) :: "memory");
                    if (w == 7) { if (!__all(wdf[0] == (u32)tag && wdf[1] == (u32)(tag >> 32))) poll_at(ws, wdo, l, tag, ws, 12); }
                }
                else { if (w < 4) vmw<6>(); else vmw<5>(); }
                BAR();
                if constexpr (t == 7) { if (w == 7) asm volatile("s_nop 4\n\tglobal_load_dwordx2 %0, %1, %2 sc1" : "=v"(wdf) : "v"(wdo), "s"(ws) : "memory"); }
                if constexpr (t < 8) P4_HALF(1, true, t + 1, 0, if (sN) P4_B1(t + 3, 0), if (sN) P4_B1(t + 3, 1), if (sN) P4_B1(t + 3, 2));
                else P4_HALF(1, true, t + 1, 0, P5_B1(0, 0), P5_B1(0, 1), P5_B1(0, 2));
            } else {
                P4_HALF(1, false, 0, 0, P5_B1(1, 1), P5_B1(1, 2), P5_B1(1, 3));
                P5_B1(1, 4);
            }
        });
#undef P4_B
#undef P4_B1
#undef P4_A1
#undef P4_RA
#undef P4_RB
#undef P4_MM
#undef P4_HALF
        STAMP(12);
        f32x2 mr[2];
#pragma unroll
        for (int m = 0; m < 2; ++m) mr[m] = *(LAS f32x2*)(size_t)(L0 + L_MR + (32 * wm + 16 * m + i16) * 8);
#pragma unroll
        for (int n = 0; n < 7; ++n) {
            const f32x4 u = ldsf4(L0 + L_UVS + (7 * wn + n) * 128 + 16 * g), v = ldsf4(L0 + L_UVS + (7 * wn + n) * 128 + 64 + 16 * g);
#pragma unroll
            for (int m = 0; m < 2; ++m) acc[n][m] = acc[n][m] * mr[m][0] + (u * mr[m][1] + v);
        }
        if (nj == 7) stsf4(L0 + L_XCH + w * 1024 + lo16, wn == 0 ? acc[6][1] : acc[6][0]);
        auto silu_mul = [&](const f32x4 gt, const f32x4 up) -> u32x2 {
            const f32x4 e = gt * (-LOG2E), gu = gt * up;
            f32x4 d;
#pragma unroll
            for (int r = 0; r < 4; ++r) d[r] = __builtin_amdgcn_exp2f(e[r]);
            d = d + 1.f;
#pragma unroll
            for (int r = 0; r < 4; ++r) d[r] = __builtin_amdgcn_rcpf(d[r]);
            const f32x4 o = gu * d;
            return (u32x2){pack2(o[0], o[1]), pack2(o[2], o[3])};
        };
#pragma unroll
        for (int a = 0; a < 3; ++a) {
            const int la = a + 4 * wn, gam = Gj + (la <= 3 || nj == 7 ? la : la - 1);
            const u32x2 o0 = silu_mul(acc[2 * a][0], acc[2 * a + 1][0]), o1 = silu_mul(acc[2 * a][1], acc[2 * a + 1][1]);
            const auto s0 = __builtin_amdgcn_permlane16_swap(o0[0], o1[0], false, false);
            const auto s1 = __builtin_amdgcn_permlane16_swap(o0[1], o1[1], false, false);
            const u32x4 v = {s0[0], s1[0], s0[1], s1[1]};
            ST16C(v, WS_ACT + (((rb * 8 + 2 * wm + (g & 1)) * 216 + 2 * gam + (g >> 1)) * 16 + i16) * 16);
        }
        STAMP(28);
        BAR();
        if (nj == 7) {
            const f32x4 other = ldsf4(L0 + L_XCH + (w ^ 4) * 1024 + lo16);
            const u32x2 o = wn == 0 ? silu_mul(acc[6][0], other) : silu_mul(other, acc[6][1]);
            ST8C(o, WS_ACT + (((rb * 8 + 2 * wm + wn) * 216 + 2 * (Gj + 3) + (g >> 1)) * 16 + i16) * 16 + 8 * (g & 1));
        }
        P5_B1(2, 0); P5_B1(2, 1); P5_B1(2, 2); P5_B1(2, 3); P5_B1(2, 4);
        vmw<5>();
        BAR();
        if (tid == 0) st_agent(ws + WS_FACT + (rb * 16 + j) * FS, tag);
    }
    STAMP(13);

    {
        if (w == 1 && l < 20) stsf4(L0 + L_GB + l * 16, gather1(ws, 800 + 20 * cb + l, tag, ws, 8));
        if (w == 0) poll_flags(ws + WS_FACT + rb * 16 * FS, 16, l, tag, ws, 9);
        const bool bump = rb == 15 && j == 0;
        if (bump && w >= 4) poll_at(ws, WS_FO + (tid - 256) * FS, l, tag, ws, 10);
        VMCNT(0);
        BAR();
        if (bump && tid == 0) st_agent(ws + WS_EPOCH, tag);
        STAMP(14);
        const char* asrc = ws + WS_ACT + (mt3 * 216 + wk * 108) * 256;
        u32x4 a[3][4];
#define P5_A1(t, i) a[(t) % 3][i] = gld16c((i) * 1024, asrc + (t) * 4096, lo16)
        P5_A1(0, 0); P5_A1(0, 1); P5_A1(0, 2); P5_A1(0, 3); P5_A1(1, 0); P5_A1(1, 1); P5_A1(1, 2); P5_A1(1, 3); P5_A1(2, 0); P5_A1(2, 1); P5_A1(2, 2); P5_A1(2, 3);
        f32x4 acc[5];
#pragma unroll
        for (int n = 0; n < 5; ++n) acc[n] = Z4;
        const u32 rb5 = opaque(L0 + wk * 20480 + lo16);
        u32x4 fb[2][5];
#define P5_RD(buf, t, kk) do { _Pragma("unroll") for (int n = 0; n < 5; ++n) fb[buf][n] = lds128(rb5 + r5_slot(t) + (kk) * 5120 + n * 1024); } while (0)
#define P5_MM(buf, av) do { _Pragma("unroll") for (int n = 0; n < 5; ++n) acc[n] = mfma(fb[buf][n], (av), acc[n]); } while (0)
#define P5_MIX() do { _Pragma("unroll") for (int n = 0; n < 5; ++n) { SGB(M_DSR, 1); SGB(M_MFMA, 1); } } while (0)
        P5_RD(0, 0, 0);
        static_for<0, 7>([&](auto T) {
            constexpr int t = T, q = t % 3;
            constexpr bool nx = t + 3 < 7;
            constexpr int NA = t == 0 ? 8 : t == 1 ? 13 : t == 2 ? 18 : t == 3 ? 21 : t == 4 ? 20 : t == 5 ? 11 : 5;
            constexpr int NB = t == 2 ? 12 : t == 3 ? 12 : t == 4 ? 8 : t == 5 ? 0 : 63;
            vmw4<NA>(a[q][0], a[q][1], a[q][2], a[q][3]);
            SB0(); P5_RD(1, t, 1); P5_MM(0, a[q][0]); P5_MIX(); SB0();
            if constexpr (nx) P5_A1(t + 3, 0);
            SB0(); P5_RD(0, t, 2); P5_MM(1, a[q][1]); P5_MIX(); SB0();
            if constexpr (nx) P5_A1(t + 3, 1);
            if constexpr (t < 6) {
                SB0(); P5_RD(1, t, 3); P5_MM(0, a[q][2]); P5_MIX(); SB0();
                if constexpr (nx) P5_A1(t + 3, 2);
                vmw<NB>(); BAR();
                if constexpr (nx) { P5_B1(t + 3, 0); P5_B1(t + 3, 1); }
                SB0(); P5_RD(0, t + 1, 0); P5_MM(1, a[q][3]); P5_MIX(); SB0();
                if constexpr (nx) { if constexpr (t + 3 < 6) P5_A1(t + 3, 3); P5_B1(t + 3, 2); P5_B1(t + 3, 3); P5_B1(t + 3, 4); }
            } else {
                SB0(); P5_MM(0, a[q][2]); SB0();
            }
        });
#undef P5_B1
#undef P5_A1
#undef P5_RD
#undef P5_MM
#undef P5_MIX
        STAMP(15);
        if (wk == 1) {
#pragma unroll
            for (int n = 0; n < 5; ++n) stsf4(L0 + L_RED5 + (wm * 5 + n) * 1024 + lo16, acc[n]);
        }
        BAR();
        if (wk == 0) {
#pragma unroll
            for (int n = 0; n < 5; ++n) {
                const f32x4 v = acc[n] + ldsf4(L0 + L_RED5 + (wm * 5 + n) * 1024 + lo16);
                const f32x4 gb = ldsf4(L0 + L_GB + (16 * n + 4 * g) * 4);
                const u32x2 o = {pack2(bflo(x1k[n][0]) + gb[0] * v[0], bfhi(x1k[n][0]) + gb[1] * v[1]), pack2(bflo(x1k[n][1]) + gb[2] * v[2], bfhi(x1k[n][1]) + gb[3] * v[3])};
                sts64(L0 + L_OS + (16 * wm + i16) * 160 + 32 * n + 8 * g, o);
            }
        }
        BAR();
#pragma unroll
        for (int i = 0; i < 2; ++i) {
            const int id = tid + 512 * i;
            if (id < 640) {
                const int r = (id * 6554) >> 16, c = id - 10 * r;
                *(GAS u32x4*)((char*)p.out + (rb * 128 + 64 * hf + r) * 1280 + cb * 160 + c * 16) = lds128(L0 + L_OS + id * 16);
            }
        }
    }
    STAMP(16); STAMP_RT(63); STAMP_OUT();
    asm volatile(".Lmk_code_to:");
}

extern "C" void kernel_launch(void* const* d_in, const int* in_sizes, int n_in, void* d_out, int out_size, void* d_ws, size_t ws_size, hipStream_t stream) {
    Params p;
    p.x = (const u16*)d_in[0]; p.wqkv = (const u16*)d_in[1]; p.wo = (const u16*)d_in[2];
    p.wg = (const u16*)d_in[4]; p.wu = (const u16*)d_in[5]; p.wd = (const u16*)d_in[6];
    p.c = (const u16*)d_in[7]; p.wmod = (const u16*)d_in[8];
    p.out = (u16*)d_out; p.ws = (char*)d_ws;
    mega_kernel<<<256, 512, 0, stream>>>(p);
}
```

```cpp
#include <hip/hip_runtime.h>
#include <hip/hip_bf16.h>
#include <stdint.h>
#include <type_traits>

typedef unsigned short u16;
typedef unsigned u32;
typedef unsigned long long u64;
typedef __attribute__((ext_vector_type(2))) unsigned u32x2;
typedef __attribute__((ext_vector_type(4))) unsigned u32x4;
typedef __attribute__((ext_vector_type(2))) float f32x2;
typedef __attribute__((ext_vector_type(4))) float f32x4;
typedef __attribute__((ext_vector_type(2))) __bf16 bf16x2;
typedef __attribute__((ext_vector_type(8))) __bf16 bf16x8;
typedef __attribute__((ext_vector_type(4))) short s16x4;
#define LAS __attribute__((address_space(3)))
#define GAS __attribute__((address_space(1)))
#define DI __device__ __forceinline__

constexpr int SEQ = 2048, DM = 640, FF = 1728;
constexpr float QSCALE = 0.22360679774997896f * 1.4426950408889634f;
constexpr float LOG2E = 1.4426950408889634f;

constexpr u32 WS_EPOCH = 0;
constexpr u32 WS_ERR   = 256;
constexpr u32 FS = 128;
constexpr u32 WS_FKV   = 4096;
constexpr u32 WS_FO    = WS_FKV + 256 * FS;
constexpr u32 WS_FX1   = WS_FO + 256 * FS;
constexpr u32 WS_FACT  = WS_FX1 + 256 * FS;
constexpr u32 WS_FH    = WS_FACT + 256 * FS;
constexpr u32 WS_FWT   = WS_FH + 256 * FS;
constexpr u32 WS_FWP   = WS_FWT + 256 * FS;
constexpr u32 WS_FW2   = WS_FWP + 256 * FS;
constexpr u32 WS_MODG  = WS_FW2 + 256 * FS;
constexpr u32 WS_UV    = WS_MODG + 61440;
constexpr u32 WS_STAT  = WS_UV + 114688;
constexpr u32 WS_KV    = 589824;
constexpr u32 WS_O     = WS_KV + 256 * 22528;
constexpr u32 WS_X1    = WS_O + SEQ * DM * 2;
constexpr u32 WS_ACT   = WS_X1 + SEQ * DM * 2;
constexpr u32 WS_WP    = WS_ACT + SEQ * FF * 2;
constexpr u32 WS_H     = WS_WP + 16 * 20 * 14 * 1024;
constexpr u32 WS_WD    = WS_H + SEQ * DM * 2;
constexpr u32 WS_WO    = WS_WD + 8 * 54 * 5 * 1024;
constexpr u32 WS_WT    = WS_WO + 8 * 20 * 5 * 1024;
constexpr u32 WS_STAMP = WS_WT + 16 * 153600;
constexpr u32 WS_END   = WS_STAMP + 256 * 8 * 64 * 4;

constexpr u32 L_WKV = 0, L_WQ = 102400, L_GS = 153600, L_STP = 156160, L_MODP = 156416, L_CNT = 157440, L_SB = 157696, L_G2 = 158016, L_Z16 = 158656;
constexpr u32 L_ODRAW = 101376, L_WTRAW = 116736, L_XS = 133120;
constexpr u32 L_K = 0, L_V = 30720, L_Q = 67584, L_WPRAW = 77824, L_UVL = 98304;
constexpr u32 L_WO = 61440, L_RED3 = 0, L_X1S = 20480, L_XR = 30720, L_GA = 40960, L_ST3 = 41472, L_WDRAW = 43008;
constexpr u32 L_R4 = 0, R4_STAGE = 45056, L_ST4 = 135168, L_UV = 143360, L_MR = 150528, L_XCH = 151552, L_UVS = 159744;
constexpr u32 L_RED5 = 126976, L_GB = 147456, L_OS = 148480;
constexpr u32 r5_slot(int t) { return t % 3 == 0 ? 45056 : t % 3 == 1 ? 86016 : 0; }

struct Params {
    const u16* x; const u16* wqkv; const u16* wo; const u16* wg; const u16* wu; const u16* wd;
    const u16* c; const u16* wmod; u16* out; char* ws;
};

DI float bflo(u32 d) { return __uint_as_float(d << 16); }
DI float bfhi(u32 d) { return __uint_as_float(d & 0xffff0000u); }
DI u32 pack2(float a, float b) { f32x2 v = {a, b}; return __builtin_bit_cast(u32, __builtin_convertvector(v, bf16x2)); }
DI float rbf(float a) { return bflo(pack2(a, a)); }
DI f32x4 mfma(u32x4 a, u32x4 b, f32x4 c) {
    return __builtin_amdgcn_mfma_f32_16x16x32_bf16(__builtin_bit_cast(bf16x8, a), __builtin_bit_cast(bf16x8, b), c, 0, 0, 0);
}
DI u32x2 ldtr(u32 a) { return __builtin_bit_cast(u32x2, __builtin_amdgcn_ds_read_tr16_b64_v4i16((LAS s16x4*)(size_t)a)); }
DI u32x4 ldtr2(u32 a0, u32 a1) { u32x2 x = ldtr(a0), y = ldtr(a1); return (u32x4){x[0], x[1], y[0], y[1]}; }
DI u32x4 lds128(u32 a) { return *(LAS u32x4*)(size_t)a; }
DI u32x2 lds64(u32 a) { return *(LAS u32x2*)(size_t)a; }
DI f32x4 ldsf4(u32 a) { return *(LAS f32x4*)(size_t)a; }
DI float ldsf(u32 a) { return *(LAS float*)(size_t)a; }
DI void sts128(u32 a, u32x4 v) { *(LAS u32x4*)(size_t)a = v; }
DI void sts64(u32 a, u32x2 v) { *(LAS u32x2*)(size_t)a = v; }
DI void stsf(u32 a, float v) { *(LAS float*)(size_t)a = v; }
DI void stsf4(u32 a, f32x4 v) { *(LAS f32x4*)(size_t)a = v; }
DI void stsf2(u32 a, f32x2 v) { *(LAS f32x2*)(size_t)a = v; }
DI void sts16(u32 a, u16 v) { *(LAS u16*)(size_t)a = v; }

DI void glds16_(const void* root, u32 voff, u32 ldsdst) {
    asm volatile("s_mov_b32 m0, %2\n\ts_nop 0\n\tglobal_load_lds_dwordx4 %0, %1" :: "v"(voff), "s"(root), "s"(__builtin_amdgcn_readfirstlane(ldsdst)) : "memory");
}
DI void glds16c_(const void* root, u32 voff, u32 ldsdst) {
    asm volatile("s_mov_b32 m0, %2\n\ts_nop 0\n\tglobal_load_lds_dwordx4 %0, %1 sc1" :: "v"(voff), "s"(root), "s"(__builtin_amdgcn_readfirstlane(ldsdst)) : "memory");
}
template <int OFF> DI void glds16c_next_(const void* root, u32 voff) {
    asm volatile("global_load_lds_dwordx4 %0, %1 offset:%2 sc1" :: "v"(voff), "s"(root), "i"(OFF) : "memory");
}
template <int OFF> DI u32x4 gld16(const void* root, u32 voff) {
    u32x4 r; asm volatile("global_load_dwordx4 %0, %1, %2 offset:%3" : "=v"(r) : "v"(voff), "s"(root), "i"(OFF) : "memory"); return r;
}
template <int OFF> DI u32x4 gld16c_(const void* root, u32 voff) {
    u32x4 r; asm volatile("global_load_dwordx4 %0, %1, %2 offset:%3 sc1" : "=v"(r) : "v"(voff), "s"(root), "i"(OFF) : "memory"); return r;
}
#define ROFF(root, ptr) ((u32)((const char*)(ptr) - (const char*)(root)))
#define glds16(root, ptr, voff, dst) glds16_((root), ROFF(root, ptr) + (voff), (dst))
#define glds16c(ptr, voff, dst) glds16c_(ws, ROFF(ws, ptr) + (voff), (dst))
#define glds16c_next(OFF, ptr, voff) glds16c_next_<(OFF)>(ws, ROFF(ws, ptr) + (voff))
#define gld16c(OFF, ptr, voff) gld16c_<(OFF)>(ws, ROFF(ws, ptr) + (voff))
DI u32 gldu16(const void* sbase, u32 voff) {
    u32 r; asm volatile("global_load_ushort %0, %1, %2" : "=v"(r) : "v"(voff), "s"(sbase) : "memory"); return r;
}
#define VMCNT(n) asm volatile("s_waitcnt vmcnt(" #n ")" ::: "memory")
#define BAR() asm volatile("s_waitcnt lgkmcnt(0)\n\ts_barrier" ::: "memory")
#define RLX_AGENT __ATOMIC_RELAXED, __HIP_MEMORY_SCOPE_AGENT
DI u64 ld_agent(const char* p) { return __hip_atomic_load((GAS u64*)p, RLX_AGENT); }
DI void st_agent(char* p, u64 v) { __hip_atomic_store((GAS u64*)p, v, RLX_AGENT); }
DI void st_agent32(char* p, u32 v) { __hip_atomic_store((GAS u32*)p, v, RLX_AGENT); }
DI float ld_agentf(const char* p) { return __uint_as_float(__hip_atomic_load((GAS u32*)p, RLX_AGENT)); }
DI float xsum4(float x) {
    const auto s = __builtin_amdgcn_permlane16_swap(__float_as_uint(x), __float_as_uint(x), false, false);
    x = __uint_as_float(s[0]) + __uint_as_float(s[1]);
    const auto t = __builtin_amdgcn_permlane32_swap(__float_as_uint(x), __float_as_uint(x), false, false);
    return __uint_as_float(t[0]) + __uint_as_float(t[1]);
}
DI float xmax4(float x) {
    const auto s = __builtin_amdgcn_permlane16_swap(__float_as_uint(x), __float_as_uint(x), false, false);
    x = fmaxf(__uint_as_float(s[0]), __uint_as_float(s[1]));
    const auto t = __builtin_amdgcn_permlane32_swap(__float_as_uint(x), __float_as_uint(x), false, false);
    return fmaxf(__uint_as_float(t[0]), __uint_as_float(t[1]));
}
template <int CTRL> DI float dppf(float v) { return __builtin_bit_cast(float, __builtin_amdgcn_update_dpp(0, __builtin_bit_cast(int, v), CTRL, 0xf, 0xf, true)); }

constexpr u32 SPIN_CAP = 1u << 17;
DI void poll_flags(const char* base, int n, int lane, u64 tag, char* ws, u32 code) {
    const char* a = base + (lane < n ? lane : 0) * FS;
    u32 spins = 0;
    for (;;) {
        const u64 v = ld_agent(a);
        if (__all(v == tag)) break;
        if (++spins > SPIN_CAP) { if (lane == 0) st_agent32(ws + WS_ERR, code); break; }
        __builtin_amdgcn_s_sleep(1);
    }
}
typedef __attribute__((ext_vector_type(2))) _Float16 f16x2;
DI float hlo(u32 d) { return (float)__builtin_bit_cast(f16x2, d)[0]; }
DI float hhi(u32 d) { return (float)__builtin_bit_cast(f16x2, d)[1]; }
DI u32 packh(float a, float b) { const f16x2 v = {(_Float16)a, (_Float16)b}; return __builtin_bit_cast(u32, v); }
DI bool gran_ok(const u32x4& v, u64 tag) { return v[2] == (u32)tag && v[3] == (u32)(tag >> 32); }
DI f32x4 gran_sum(const u32x4& a, const u32x4& b, const u32x4& c, const u32x4& d) {
    const f32x4 s = {(hlo(a[0]) + hlo(b[0])) + (hlo(c[0]) + hlo(d[0])), (hhi(a[0]) + hhi(b[0])) + (hhi(c[0]) + hhi(d[0])),
                     (hlo(a[1]) + hlo(b[1])) + (hlo(c[1]) + hlo(d[1])), (hhi(a[1]) + hhi(b[1])) + (hhi(c[1]) + hhi(d[1]))};
    return (f32x4){rbf(s[0]), rbf(s[1]), rbf(s[2]), rbf(s[3])};
}
DI f32x4 gather1(const char* ws, u32 g0, u64 tag, char* wsw, u32 code) {
    u32 spins = 0; u32x4 v0, v1, v2, v3;
    const u32 o = WS_MODG + g0 * 16;
    for (;;) {
        asm volatile("s_nop 4\n\tglobal_load_dwordx4 %0, %4, %5 sc1\n\tglobal_load_dwordx4 %1, %6, %5 sc1\n\tglobal_load_dwordx4 %2, %7, %5 sc1\n\tglobal_load_dwordx4 %3, %8, %5 sc1\n\ts_waitcnt vmcnt(0)"
                     : "=&v"(v0), "=&v"(v1), "=&v"(v2), "=&v"(v3) : "v"(o), "s"(ws), "v"(o + 15360), "v"(o + 30720), "v"(o + 46080) : "memory");
        if (__all(gran_ok(v0, tag) && gran_ok(v1, tag) && gran_ok(v2, tag) && gran_ok(v3, tag))) break;
        if (++spins > SPIN_CAP) { st_agent32(wsw + WS_ERR, code); break; }
        __builtin_amdgcn_s_sleep(1);
    }
    return gran_sum(v0, v1, v2, v3);
}
DI void reask(u32x4& v, const char* ws, u32 off, u64 tag) {
    if (!gran_ok(v, tag)) asm volatile("global_load_dwordx4 %0, %1, %2 sc1" : "+v"(v) : "v"(off), "s"(ws) : "memory");
}
DI void gather2(const char* ws, u32 g0, u32 g1, u64 tag, char* wsw, u32 code, f32x4& ra, f32x4& rb) {
    u32 spins = 0; u32x4 v0, v1, v2, v3, u0, u1, u2, u3;
    const u32 o = WS_MODG + g0 * 16, q = WS_MODG + g1 * 16;
    asm volatile("global_load_dwordx4 %0, %8, %9 sc1\n\tglobal_load_dwordx4 %1, %10, %9 sc1\n\tglobal_load_dwordx4 %2, %11, %9 sc1\n\tglobal_load_dwordx4 %3, %12, %9 sc1\n\t"
                 "global_load_dwordx4 %4, %13, %9 sc1\n\tglobal_load_dwordx4 %5, %14, %9 sc1\n\tglobal_load_dwordx4 %6, %15, %9 sc1\n\tglobal_load_dwordx4 %7, %16, %9 sc1\n\ts_waitcnt vmcnt(0)"
                 : "=&v"(v0), "=&v"(v1), "=&v"(v2), "=&v"(v3), "=&v"(u0), "=&v"(u1), "=&v"(u2), "=&v"(u3)
                 : "v"(o), "s"(ws), "v"(o + 15360), "v"(o + 30720), "v"(o + 46080), "v"(q), "v"(q + 15360), "v"(q + 30720), "v"(q + 46080) : "memory");
    for (;;) {
        if (__all(gran_ok(v0, tag) && gran_ok(v1, tag) && gran_ok(v2, tag) && gran_ok(v3, tag) && gran_ok(u0, tag) && gran_ok(u1, tag) && gran_ok(u2, tag) && gran_ok(u3, tag))) break;
        if (++spins > SPIN_CAP) { st_agent32(wsw + WS_ERR, code); break; }
        reask(v0, ws, o, tag); reask(v1, ws, o + 15360, tag); reask(v2, ws, o + 30720, tag); reask(v3, ws, o + 46080, tag);
        reask(u0, ws, q, tag); reask(u1, ws, q + 15360, tag); reask(u2, ws, q + 30720, tag); reask(u3, ws, q + 46080, tag);
        asm volatile("s_waitcnt vmcnt(0)" : "+v"(v0), "+v"(v1), "+v"(v2), "+v"(v3), "+v"(u0), "+v"(u1), "+v"(u2), "+v"(u3) :: "memory");
    }
    ra = gran_sum(v0, v1, v2, v3); rb = gran_sum(u0, u1, u2, u3);
}
struct Gather { u32x4 v0, v1, v2, v3, u0, u1, u2, u3; };
DI void gather_ask(const char* ws, u32 g0, u32 g1, Gather& G) {
    const u32 o = WS_MODG + g0 * 16, q = WS_MODG + g1 * 16;
    asm volatile("global_load_dwordx4 %0, %8, %9 sc1\n\tglobal_load_dwordx4 %1, %10, %9 sc1\n\tglobal_load_dwordx4 %2, %11, %9 sc1\n\tglobal_load_dwordx4 %3, %12, %9 sc1\n\t"
                 "global_load_dwordx4 %4, %13, %9 sc1\n\tglobal_load_dwordx4 %5, %14, %9 sc1\n\tglobal_load_dwordx4 %6, %15, %9 sc1\n\tglobal_load_dwordx4 %7, %16, %9 sc1"
                 : "=&v"(G.v0), "=&v"(G.v1), "=&v"(G.v2), "=&v"(G.v3), "=&v"(G.u0), "=&v"(G.u1), "=&v"(G.u2), "=&v"(G.u3)
                 : "v"(o), "s"(ws), "v"(o + 15360), "v"(o + 30720), "v"(o + 46080), "v"(q), "v"(q + 15360), "v"(q + 30720), "v"(q + 46080) : "memory");
}
DI void gather_finish(const char* ws, u32 g0, u32 g1, Gather& G, u64 tag, char* wsw, u32 code, f32x4& ra, f32x4& rb) {
    asm volatile("" : "+v"(G.v0), "+v"(G.v1), "+v"(G.v2), "+v"(G.v3), "+v"(G.u0), "+v"(G.u1), "+v"(G.u2), "+v"(G.u3) :: "memory");
    if (__all(gran_ok(G.v0, tag) && gran_ok(G.v1, tag) && gran_ok(G.v2, tag) && gran_ok(G.v3, tag) && gran_ok(G.u0, tag) && gran_ok(G.u1, tag) && gran_ok(G.u2, tag) && gran_ok(G.u3, tag))) {
        ra = gran_sum(G.v0, G.v1, G.v2, G.v3); rb = gran_sum(G.u0, G.u1, G.u2, G.u3);
    } else gather2(ws, g0, g1, tag, wsw, code, ra, rb);
}
DI void gather_strip(const char* ws, u32 gs, u32 gc, u32 strip, int l, u64 tag, char* wsw, u32 code) {
    f32x4 a, c;
    gather2(ws, l < 40 ? gs + l : gc + (l - 40), gc + 24 + (l & 15), tag, wsw, code, a, c);
    if (l >= 40) a = a + 1.f;
    c = c + 1.f;
    *(LAS u32x2*)(size_t)(strip + (l < 40 ? l * 8 : 320 + (l - 40) * 8)) = (u32x2){pack2(a[0], a[1]), pack2(a[2], a[3])};
    if (l < 16) *(LAS u32x2*)(size_t)(strip + 320 + (24 + l) * 8) = (u32x2){pack2(c[0], c[1]), pack2(c[2], c[3])};
}
DI void put_strip_spread(const f32x4& a, const f32x4& c, u32 strip, int l) {
    if (l < 40) *(LAS u32x2*)(size_t)(strip + l * 8) = (u32x2){pack2(a[0], a[1]), pack2(a[2], a[3])};
    else {
        const u32 p0 = pack2(a[0] + 1.f, a[1] + 1.f), p1 = pack2(a[2] + 1.f, a[3] + 1.f);
        *(LAS u32x4*)(size_t)(strip + 320 + (l - 40) * 16) = (u32x4){p0 & 0xffffu, p0 & 0xffff0000u, p1 & 0xffffu, p1 & 0xffff0000u};
    }
    if (l < 16) {
        const u32 p0 = pack2(c[0] + 1.f, c[1] + 1.f), p1 = pack2(c[2] + 1.f, c[3] + 1.f);
        *(LAS u32x4*)(size_t)(strip + 320 + (24 + l) * 16) = (u32x4){p0 & 0xffffu, p0 & 0xffff0000u, p1 & 0xffffu, p1 & 0xffff0000u};
    }
    if (l == 63) *(LAS u32x4*)(size_t)(strip + 960) = (u32x4){0, 0, 0, 0};
}
DI void poll_at(const char* ws, u32 off, int lane, u64 tag, char* wsw, u32 code) {
    u32 spins = 0;
    for (;;) {
        const u64 v = ld_agent(ws + off);
        if (__all(v == tag)) break;
        if (++spins > SPIN_CAP) { if (lane == 0) st_agent32(wsw + WS_ERR, code); break; }
        __builtin_amdgcn_s_sleep(1);
    }
}

#define STAMP(i) do {} while (0)
#define STAMPL(i) do {} while (0)
#define STAMP_RT(i) do {} while (0)
#define STAMP_OUT() do {} while (0)

template <int N> using IC = std::integral_constant<int, N>;
template <int I, int E, class F> DI void static_for(F&& f) { if constexpr (I < E) { f(IC<I>{}); static_for<I + 1, E>(f); } }
template <int N> DI void vmw() { asm volatile("s_waitcnt vmcnt(%0)" :: "i"(N) : "memory"); }
template <int N> DI void vmw1(u32x4& a) { asm volatile("s_waitcnt vmcnt(%1)" : "+v"(a) : "i"(N) : "memory"); }
template <int N> DI void vmw4(u32x4& a, u32x4& b, u32x4& c, u32x4& d) { asm volatile("s_waitcnt vmcnt(%4)" : "+v"(a), "+v"(b), "+v"(c), "+v"(d) : "i"(N) : "memory"); }
DI u32 opaque(u32 a) { asm("" : "+v"(a)); return a; }
DI float dot2(u32 a, u32 b, float c) { return __builtin_amdgcn_fdot2_f32_bf16(__builtin_bit_cast(bf16x2, a), __builtin_bit_cast(bf16x2, b), c, false); }
#define SB0() __builtin_amdgcn_sched_barrier(0)
#define SGB(m, n) __builtin_amdgcn_sched_group_barrier((m), (n), 0)
constexpr int M_MFMA = 0x8, M_DSR = 0x100;
#define Z4 ((f32x4){0.f, 0.f, 0.f, 0.f})
DI u32 lds_add(u32 a, u32 v) { return __hip_atomic_fetch_add((LAS u32*)(size_t)a, v, __ATOMIC_RELAXED, __HIP_MEMORY_SCOPE_WORKGROUP); }
DI u32 lds_ld(u32 a) { return __hip_atomic_load((LAS u32*)(size_t)a, __ATOMIC_RELAXED, __HIP_MEMORY_SCOPE_WORKGROUP); }
DI void worker_sync(u32 cnt, int lane) {
    asm volatile("s_waitcnt lgkmcnt(0)" ::: "memory");
    if (lane == 0) lds_add(cnt, 1u);
    for (u32 spins = 0; lds_ld(cnt) < 4u && spins < (1u << 22); ++spins) {}
    asm volatile("" ::: "memory");
}

__global__ __launch_bounds__(512, 2) void mega_kernel(Params p) {
    __shared__ __attribute__((aligned(16))) char ldsmem[163840];
    const u32 L0 = (u32)(uintptr_t)(LAS char*)ldsmem;
    const int tid = threadIdx.x, l = tid & 63;
    const int w = __builtin_amdgcn_readfirstlane(tid >> 6);
    const int b = blockIdx.x, xg = b & 7, sidx = b >> 3;
    const int rb = 4 * (xg >> 1) + (sidx >> 3), j = 8 * (xg & 1) + (sidx & 7);
    const int g = l >> 4, i16 = l & 15, q4 = i16 >> 2, p4 = l & 3;
    u32 wu0 = 0;
    if (w == 7) {
        const char* pc; u32 span;
        asm volatile("s_getpc_b64 %0\n.Lmk_code_from:\n\ts_mov_b32 %1, .Lmk_code_to-.Lmk_code_from" : "=s"(pc), "=s"(span));
        const u32 off = min((u32)(sidx + 32 * min(l, 15)) * 128u, span - 4u);
        asm volatile("s_nop 0\n\tglobal_load_dword %0, %1, %2" : "=v"(wu0) : "v"(off), "s"(pc) : "memory");
    }
    char* const ws = p.ws;
    asm volatile("" :: "s"(p.x), "s"(p.wqkv), "s"(p.wo), "s"(p.wg), "s"(p.wu), "s"(p.wd), "s"(p.c), "s"(p.wmod), "s"(p.out), "s"(p.ws));
    u64 tag;
#define ASK_TAG(ep) u32x2 ep; asm volatile("global_load_dwordx2 %0, %1, %2" : "=v"(ep) : "v"(WS_EPOCH), "s"(ws) : "memory")
#define TAKE_TAG(ep) do { asm volatile("s_waitcnt vmcnt(0)" : "+v"(ep) :: "memory"); \
        tag = (((u64)(u32)__builtin_amdgcn_readfirstlane(ep[1]) << 32) | (u32)__builtin_amdgcn_readfirstlane(ep[0])) + 1; } while (0)
    const __amdgpu_buffer_rsrc_t wsr = __builtin_amdgcn_make_buffer_rsrc(ws, 0, WS_END, 0x00020000);
#define ST16C(v, off) __builtin_amdgcn_raw_buffer_store_b128((v), wsr, (off), 0, 16)
#define ST8C(v, off)  __builtin_amdgcn_raw_buffer_store_b64((v), wsr, (off), 0, 16)
    STAMP_RT(62); STAMP(0);

    const int nj = j < 12 ? 7 : 6, Gj = j < 12 ? 7 * j : 84 + 6 * (j - 12);
    const bool wpwg = b < 216;
    const int wpm = b >= 108 ? 1 : 0, wpb = (b - 108 * wpm) >> 2, wpq = b & 3;
    u32x4 mw[5]; u32 mc[5];
    auto mod_ask = [&]() {
#pragma unroll
        for (int i = 0; i < 5; ++i) {
            const int r = 160 * (b & 3) + ((tid & 255) >> 3) + 32 * i;
            mw[i] = gld16<0>(p.wmod, r * 7680 + (b >> 2) * 128 + (tid & 7) * 16);
            mc[i] = gldu16(p.c, r * 2);
        }
    };
    auto mod_finish = [&]() {
        float acc[8];
#pragma unroll
        for (int e = 0; e < 8; ++e) acc[e] = 0.f;
#pragma unroll
        for (int i = 0; i < 5; ++i) {
            const float cv = bflo(mc[i]);
            const float sv = cv * __builtin_amdgcn_rcpf(1.f + __builtin_amdgcn_exp2f(-cv * LOG2E));
            const u32 slo = pack2(sv, 0.f), shi = slo << 16;
#pragma unroll
            for (int d = 0; d < 4; ++d) { acc[2 * d] = dot2(mw[i][d], slo, acc[2 * d]); acc[2 * d + 1] = dot2(mw[i][d], shi, acc[2 * d + 1]); }
        }
#pragma unroll
        for (int e = 0; e < 8; ++e) { const float v = acc[e]; acc[e] = xsum4(v + dppf<0x128>(v)); }
        if (l < 8) {
            stsf4(L0 + L_MODP + (w & 3) * 256 + l * 32, (f32x4){acc[0], acc[1], acc[2], acc[3]});
            stsf4(L0 + L_MODP + (w & 3) * 256 + l * 32 + 16, (f32x4){acc[4], acc[5], acc[6], acc[7]});
        }
        asm volatile("s_waitcnt lgkmcnt(0)" ::: "memory");
        u32 old = 0;
        if (l == 0) old = lds_add(L0 + L_CNT, 1u);
        if (__builtin_amdgcn_readfirstlane(old) == 3) {
            float v = 0.f;
#pragma unroll
            for (int ww = 0; ww < 4; ++ww) v += ldsf(L0 + L_MODP + ww * 256 + l * 4);
            const float v1 = dppf<0x55>(v), v2 = dppf<0xAA>(v), v3 = dppf<0xFF>(v), v0 = dppf<0x00>(v);
            const u32x4 gr = {packh(v0, v1), packh(v2, v3), (u32)tag, (u32)(tag >> 32)};
            if (p4 == 0) ST16C(gr, WS_MODG + ((b & 3) * 960 + (b >> 2) * 16 + (l >> 2)) * 16);
        }
    };
#define MOD_LANDED() asm volatile("s_waitcnt vmcnt(0)" : "+v"(mw[0]), "+v"(mw[1]), "+v"(mw[2]), "+v"(mw[3]), "+v"(mw[4]), "+v"(mc[0]), "+v"(mc[1]), "+v"(mc[2]), "+v"(mc[3]), "+v"(mc[4]) :: "memory")
    if (w >= 4) {
        __builtin_amdgcn_s_setprio(3);
        const int u = w - 4;
        const bool tw = b < 150;
        const int tb = b / 5, tk = b - 5 * tb, ts = 4 * tk + u;
        if (tw) {
            const char* src = (const char*)p.wqkv + ts * 122880 + tb * 128;
            const u32 voff = (l >> 3) * 3840 + (l & 7) * 16;
#pragma unroll
            for (int i = 0; i < 4; ++i) glds16(p.wqkv, src + i * 8 * 3840, voff, L0 + L_WTRAW + u * 4096 + i * 1024);
        }
        ASK_TAG(epl);
        BAR();
        __builtin_amdgcn_s_setprio(0);
        TAKE_TAG(epl);
        if (tw) {
            const u32 a = L0 + L_WTRAW + u * 4096 + (8 * g + q4) * 128 + 8 * p4;
#pragma unroll
            for (int t = 0; t < 4; ++t) {
                const u32 cc = 8 * tb + 2 * t + (i16 >> 3);
                const u32 seg = cc >= 160 ? 2 : cc >= 80 ? 1 : 0, wi = cc - 80 * seg, jj = (wi * 13108) >> 16, c5 = wi - 5 * jj;
                const u32 c = seg == 0 ? 10 + c5 : seg == 1 ? c5 : 5 + c5, nt = c >> 1;
                const u32 dst = WS_WT + jj * 153600 + (nt < 5 ? ts * 5120 + nt * 1024 : 102400 + ts * 2560 + (nt - 5) * 1024)
                              + (nt < 7 ? (g * 16 + 8 * (c & 1) + (i16 & 7)) * 16 : g * 128 + (i16 & 7) * 16);
                ST16C(ldtr2(a + 32 * t, a + 32 * t + 512), dst);
            }
        }
        vmw<0>();
        {
            asm volatile("s_waitcnt lgkmcnt(0)" ::: "memory");
            u32 old = 0;
            if (l == 0) old = lds_add(L0 + L_CNT + 12, 1u);
            if (tw && __builtin_amdgcn_readfirstlane(old) == 3 && l == 0) st_agent(ws + WS_FWT + b * FS, tag);
        }
        STAMPL(19);
        if (b >= 80 && b < 240) {
            if (!tw) __builtin_amdgcn_s_sleep(32);
            mod_ask(); MOD_LANDED(); mod_finish();
        }
        if (u == 0) {
            const int wh = l >= 30 ? 0 : l / 5, kr = l >= 30 ? 0 : l - 5 * wh;
            const int bl = (1280 * (wh >> 1) + 80 * j + 79 * (wh & 1)) >> 7;
            poll_at(ws, WS_FWT + (bl * 5 + kr) * FS, l, tag, ws, 13);
            for (u32 spins = 0; lds_ld(L0 + L_CNT + 12) < 4u && spins < (1u << 22); ++spins) {}
            if (l == 0) lds_add(L0 + L_CNT + 24, 1u);
        } else {
            for (u32 spins = 0; lds_ld(L0 + L_CNT + 24) < 1u && spins < (1u << 22); ++spins) {}
        }
        asm volatile("" ::: "memory");
        STAMPL(20);
        {
            const char* src = ws + WS_WT + j * 153600 + u * 1024;
            const u32 lo = l * 16;
#pragma unroll
            for (int i = 0; i < 25; ++i) glds16c(src + i * 4096, lo, L0 + L_WKV + u * 1024 + i * 4096);
        }
        VMCNT(0);
        STAMPL(17);
    } else {
        __builtin_amdgcn_s_setprio(2);
        if (b < 80) mod_ask();
        ASK_TAG(epw);
        if (tid < 16) *(LAS u32*)(size_t)(L0 + L_CNT + tid * 4) = 0u;
        BAR();
        STAMP(29);
        TAKE_TAG(epw);
        STAMP(30);
        if (b < 80) { MOD_LANDED(); mod_finish(); }
        {
            const char* src = (const char*)p.x + (rb * 128 + 8 * j) * 1280 + w * 1024;
            glds16(p.x, src, l * 16, L0 + L_XS + w * 1024);
            glds16(p.x, src + 4096, l * 16, L0 + L_XS + (w + 4) * 1024);
            if (w < 2) glds16(p.x, src + 8192, l * 16, L0 + L_XS + (w + 8) * 1024);
        }
        VMCNT(0);
        STAMP(1);
        worker_sync(L0 + L_CNT + 16, l);
        const int tok = l & 7;
        u32x4 xr[3];
#pragma unroll
        for (int i = 0; i < 3; ++i) xr[i] = lds128(L0 + L_XS + tok * 1280 + (20 * w + (i < 2 ? (l >> 3) + 8 * i : 16 + ((l >> 3) & 3))) * 16);
        {
            float s1 = 0.f, s2 = 0.f;
#pragma unroll
            for (int i = 0; i < 3; ++i) {
                float a1 = 0.f, a2 = 0.f;
#pragma unroll
                for (int d = 0; d < 4; ++d) { a1 = dot2(xr[i][d], 0x3F803F80u, a1); a2 = dot2(xr[i][d], xr[i][d], a2); }
                if (i < 2 || l < 32) { s1 += a1; s2 += a2; }
            }
            asm volatile("s_waitcnt lgkmcnt(0)" ::: "memory");
            if (l == 0) lds_add(L0 + L_CNT + 20, 1u);
            s1 += dppf<0x128>(s1); s2 += dppf<0x128>(s2);
            s1 = xsum4(s1); s2 = xsum4(s2);
            if (l < 8) stsf2(L0 + L_STP + (w * 8 + l) * 8, (f32x2){s1, s2});
        }
        gather_strip(ws, 40 * w, 160 + 40 * w, L0 + L_GS + w * 640, l, tag, ws, 1);
        STAMP(2);
        worker_sync(L0 + L_CNT + 4, l);
        {
            f32x2 st = {0.f, 0.f};
#pragma unroll
            for (int ww = 0; ww < 4; ++ww) st += *(LAS f32x2*)(size_t)(L0 + L_STP + (ww * 8 + tok) * 8);
            const float mu = st[0] * (1.f / 640.f), var = fmaxf(st[1] * (1.f / 640.f) - mu * mu, 0.f);
            const float rs = __builtin_amdgcn_rsqf(var + 1e-5f), nmr = -mu * rs;
#pragma unroll
            for (int i = 0; i < 3; ++i) {
                const int cl = i < 2 ? (l >> 3) + 8 * i : 16 + ((l >> 3) & 3);
                const u32x4 sa = lds128(L0 + L_GS + w * 640 + cl * 16), gg = lds128(L0 + L_GS + w * 640 + 320 + cl * 16);
                u32x4 h;
#pragma unroll
                for (int d = 0; d < 4; ++d)
                    h[d] = pack2((bflo(xr[i][d]) * rs + nmr) * bflo(gg[d]) + bflo(sa[d]), (bfhi(xr[i][d]) * rs + nmr) * bfhi(gg[d]) + bfhi(sa[d]));
                if (i < 2 || l < 32) ST16C(h, WS_H + (((rb * 8 + (j >> 1)) * 80 + 20 * w + cl) * 16 + 8 * (j & 1) + tok) * 16);
            }
        }
        VMCNT(0);
        worker_sync(L0 + L_CNT + 8, l);
        if (tid == 0) st_agent(ws + WS_FH + (rb * 16 + j) * FS, tag);
        if (w == 0) poll_flags(ws + WS_FH + rb * 16 * FS, 16, l, tag, ws, 11);
        __builtin_amdgcn_s_setprio(0);
    }
    BAR();
    asm volatile("s_waitcnt vmcnt(0)" : "+v"(wu0) :: "memory");
    STAMP(3);

    u32x4 xa[20];
    const u32 lo16 = l * 16;
    const char* hsrc = ws + WS_H + (rb * 8 + w) * 80 * 256;
#define XL(s) xa[s] = gld16c(((s) & 3) * 1024, hsrc + ((s) >> 2) * 4096, lo16)
    XL(0); XL(1); XL(2); XL(3); XL(4); XL(5);
    const char* kvx = ws + WS_KV + (rb * 16 + j) * 22528;
    const char* wtq = ws + WS_WT + j * 153600 + (100 + w) * 1024;
    {
        f32x4 acc[5];
#pragma unroll
        for (int n = 0; n < 5; ++n) acc[n] = Z4;
        const u32 b0 = L0 + L_WKV + lo16;
        u32x4 bf[3][5];
#pragma unroll
        for (int n = 0; n < 5; ++n) { bf[0][n] = lds128(b0 + n * 1024); bf[1][n] = lds128(b0 + 5120 + n * 1024); }
        static_for<0, 20>([&](auto S) {
            constexpr int s = S;
            vmw1<(s < 14 ? 5 : 6)>(xa[s]);
            if constexpr (s + 6 < 20) XL(s + 6);
            if constexpr (s >= 13) { if (s < 19 || w < 2) glds16c(wtq + (s - 13) * 8192, lo16, L0 + L_WKV + (100 + w) * 1024 + (s - 13) * 8192); }
            SB0();
#pragma unroll
            for (int n = 0; n < 5; ++n) {
                if (s < 18) bf[(s + 2) % 3][n] = lds128(b0 + (s + 2) * 5120 + n * 1024);
                acc[n] = mfma(bf[s % 3][n], xa[s], acc[n]);
            }
#pragma unroll
            for (int n = 0; n < 5; ++n) { SGB(M_DSR, 1); SGB(M_MFMA, 1); }
            SB0();
        });
        STAMP(18);
        BAR();
        const int row = 256 + 16 * w + i16;
#pragma unroll
        for (int n = 0; n < 5; ++n) {
            const int Q = 4 * n + g;
            const u32x2 v = {pack2(acc[n][0], acc[n][1]), pack2(acc[n][2], acc[n][3])};
            const int wi = Q - 10, hd = wi >= 5 ? 1 : 0;
            const u32 a = Q < 10 ? L_K + row * 80 + 8 * Q : L_V + row * 96 + 48 * hd + 8 * (wi - 5 * hd);
            sts64(L0 + a, v);
        }
        if (g < 2) sts64(L0 + L_V + row * 96 + 48 * g + 40, (u32x2){0x3F80u, 0u});
        if (rb < 2) {
            const int nrow = rb == 0 ? 256 : 128;
            for (int i = tid; i < nrow * 5; i += 512) sts128(L0 + L_K + i * 16, (u32x4){0, 0, 0, 0});
            for (int i = tid; i < nrow * 6; i += 512) sts128(L0 + L_V + i * 16, (u32x4){0, 0, 0, 0});
        }
        vmw<0>();
        BAR();
    }
#undef XL
    STAMP(4);
    {
        u32x4 kvd[3];
#pragma unroll
        for (int e = 0; e < 3; ++e) {
            const int n = min(w + 8 * e, 21);
            kvd[e] = lds128(L0 + (n < 10 ? L_K + 20480 + n * 1024 : L_V + 24576 + (n - 10) * 1024) + lo16);
        }
        const u32 kvo = WS_KV + (rb * 16 + j) * 22528 + w * 1024 + lo16;
        const u16* wroot = wpm ? p.wu : p.wg;
        const char* wsrc = (const char*)wroot + (160 * wpq + 8 * w) * 3456 + wpb * 128;
        const u32 wvo = (l >> 3) * 3456 + (l & 7) * 16;
        f32x4 acc[3];
#pragma unroll
        for (int n = 0; n < 3; ++n) acc[n] = Z4;
        const u32 bq = L0 + L_WQ + lo16, bq2 = L0 + L_WQ + 2048 + g * 128 + (i16 & 7) * 16;
#define QRD(s, n) lds128(((n) < 2 ? bq + (n) * 1024 : bq2) + (s) * 2560)
        u32x4 bf[3][3];
#pragma unroll
        for (int n = 0; n < 3; ++n) { bf[0][n] = QRD(0, n); bf[1][n] = QRD(1, n); }
        Gather G;
        const u32 gg0 = l < 40 ? 480 + 40 * wpq + l : 640 + 40 * wpq + (l - 40), gg1 = 640 + 40 * wpq + 24 + (l & 15);
        static_for<0, 20>([&](auto S) {
            constexpr int s = S;
            if constexpr (s == 6) { if (w == 7 && wpwg) gather_ask(ws, gg0, gg1, G); }
            if constexpr (s == 0) { if (rb < 15) ST16C(kvd[0], kvo); }
            if constexpr (s == 1) { if (rb < 15) ST16C(kvd[1], kvo + 8192); }
            if constexpr (s == 2) { if (rb < 15 && w < 6) ST16C(kvd[2], kvo + 16384); }
            if constexpr (s == 14) {
                vmw<0>();
                u32 old = 0;
                if (l == 0) old = lds_add(L0 + L_CNT + 28, 1u);
                if (__builtin_amdgcn_readfirstlane(old) == 7 && l == 0) st_agent(ws + WS_FKV + (j * 16 + rb) * FS, tag);
                if (w == 7 && wpwg) { f32x4 ra, rc; gather_finish(ws, gg0, gg1, G, tag, ws, 2, ra, rc); put_strip_spread(ra, rc, L0 + L_SB, l); }
            }
            if constexpr (s == 15) { if (wpwg) glds16(wroot, wsrc, wvo, L0 + L_WPRAW + w * 1024); }
            if constexpr (s == 16) { if (wpwg) glds16(wroot, wsrc + 64 * 3456, wvo, L0 + L_WPRAW + (w + 8) * 1024); }
            if constexpr (s == 17) { if (wpwg && w < 4) glds16(wroot, wsrc + 128 * 3456, wvo, L0 + L_WPRAW + (w + 16) * 1024); }
            SB0();
#pragma unroll
            for (int n = 0; n < 3; ++n) {
                if (s < 18) bf[(s + 2) % 3][n] = QRD(s + 2, n);
                acc[n] = mfma(bf[s % 3][n], xa[s], acc[n]);
            }
#pragma unroll
            for (int n = 0; n < 3; ++n) { SGB(M_DSR, 1); SGB(M_MFMA, 1); }
            SB0();
        });
#undef QRD
        STAMP(21);
#pragma unroll
        for (int n = 0; n < 3; ++n) {
            const int Q = 4 * n + g;
            const u32x2 v = {pack2(acc[n][0] * QSCALE, acc[n][1] * QSCALE), pack2(acc[n][2] * QSCALE, acc[n][3] * QSCALE)};
            if (Q < 10) sts64(L0 + L_Q + (16 * w + i16) * 80 + 8 * Q, v);
        }
    }
    if (w == 0 && rb >= 1) poll_flags(ws + WS_FKV + (j * 16 + (rb >= 2 ? rb - 2 : rb - 1)) * FS, rb >= 2 ? 2 : 1, l, tag, ws, 3);
    VMCNT(0);
    BAR();
    STAMP(5);
    if (rb >= 1) {
#pragma unroll
        for (int e = 0; e < 3; ++e) {
            const int n = w + 8 * e;
            if (n < 22) {
                const u32 d = n < 10 ? L_K + n * 1024 : L_V + (n - 10) * 1024, st = n < 10 ? 10240 : 12288;
                glds16c(kvx - 16 * 22528 + n * 1024, lo16, L0 + d + st);
                if (rb >= 2) glds16c(kvx - 32 * 22528 + n * 1024, lo16, L0 + d);
            }
        }
    }
    auto wp_slot = [&](int gam, int& jj, int& slot) {
        jj = gam < 84 ? gam / 7 : 12 + (gam - 84) / 6;
        const int ar = gam - (jj < 12 ? 7 * jj : 84 + 6 * (jj - 12)), a = (jj < 12 || ar < 3) ? ar : ar + 1;
        slot = a < 3 ? 2 * a + wpm : a == 3 ? (wpm ? 13 : 6) : 7 + 2 * (a - 4) + wpm;
    };
    if (wpwg) {
        const u32 one = i16 == 0 ? 0x3F803F80u : 0u;
        const u32x4 ones = {one, one, one, one};
#pragma unroll
        for (int i = 0; i < 3; ++i) {
            if (i < 2 || w < 4) {
                const int f = w + 8 * i, ks = f >> 2, t16 = f & 3;
                int jj, slot; wp_slot(4 * wpb + t16, jj, slot);
                const u32 a = L0 + L_WPRAW + (32 * ks + 8 * g + q4) * 128 + 32 * t16 + 8 * p4;
                u32x4 wr = ldtr2(a, a + 512);
                const u32x4 ga = lds128(L0 + L_G2 + (32 * ks + 8 * g) * 4), gc = lds128(L0 + L_G2 + (32 * ks + 8 * g) * 4 + 16);
                const u32x4 sbz = lds128(L0 + (i16 == 0 ? L_SB + (32 * ks + 8 * g) * 2 : L_Z16));
                const f32x4 vs = mfma(wr, sbz, Z4);
                u32x4 o; float t0, t1, t2, t3, t4, t5, t6, t7;
                asm volatile("v_dot2_f32_bf16 %4, %12, %16, 0\n\tv_dot2_f32_bf16 %5, %12, %17, 0\n\tv_dot2_f32_bf16 %6, %13, %18, 0\n\tv_dot2_f32_bf16 %7, %13, %19, 0\n\t"
                             "v_dot2_f32_bf16 %8, %14, %20, 0\n\tv_dot2_f32_bf16 %9, %14, %21, 0\n\tv_dot2_f32_bf16 %10, %15, %22, 0\n\tv_dot2_f32_bf16 %11, %15, %23, 0\n\t"
                             "v_cvt_pk_bf16_f32 %0, %4, %5\n\tv_cvt_pk_bf16_f32 %1, %6, %7\n\tv_cvt_pk_bf16_f32 %2, %8, %9\n\ts_nop 0\n\tv_cvt_pk_bf16_f32 %3, %10, %11\n\ts_nop 1"
                             : "=&v"(o[0]), "=&v"(o[1]), "=&v"(o[2]), "=&v"(o[3]),
                               "=&v"(t0), "=&v"(t1), "=&v"(t2), "=&v"(t3), "=&v"(t4), "=&v"(t5), "=&v"(t6), "=&v"(t7)
                             : "v"(wr[0]), "v"(wr[1]), "v"(wr[2]), "v"(wr[3]), "v"(ga[0]), "v"(ga[1]), "v"(ga[2]), "v"(ga[3]), "v"(gc[0]), "v"(gc[1]), "v"(gc[2]), "v"(gc[3]));
                const f32x4 us = mfma(o, ones, Z4);
                ST16C(o, WS_WP + ((jj * 20 + 5 * wpq + ks) * 14 + slot) * 1024 + lo16);
                if (i16 == 0) { stsf4(L0 + L_UVL + f * 128 + g * 16, us); stsf4(L0 + L_UVL + f * 128 + 64 + g * 16, vs); }
            }
        }
    }
    if (wpwg) { if (w < 4) vmw<3>(); else vmw<2>(); } else vmw<0>();
    BAR();
    STAMP(6);
    if (wpwg && w == 3) {
#pragma unroll
        for (int e = 0; e < 2; ++e) {
            const int t16 = 2 * e + (l >> 5);
            int jj, slot; wp_slot(4 * wpb + t16, jj, slot);
            float v = 0.f;
#pragma unroll
            for (int ks = 0; ks < 5; ++ks) v += ldsf(L0 + L_UVL + (4 * ks + t16) * 128 + (l & 31) * 4);
            st_agent32(ws + WS_UV + ((jj * 14 + slot) * 4 + wpq) * 128 + (l & 31) * 4, __float_as_uint(v));
        }
    }
    const int hf = j & 1, cb = j >> 1;
    u32 wvoff[5];
#pragma unroll
    for (int i = 0; i < 5; ++i) {
        const u32 sg = 64 * i + l, pos = (sg * 6554) >> 16, c = sg - pos * 10, k = pos ^ (((pos >> 3) & 1) << 2);
        wvoff[i] = k * 1280 + c * 16;
    }
    const char* wosrc = (const char*)p.wo + cb * 160;
#define WO_STEP(sx) do { _Pragma("unroll") for (int i = 0; i < 5; ++i) glds16(p.wo, wosrc + (sx) * 32 * 1280, wvoff[i], L0 + L_WO + (sx) * 5120 + i * 1024); } while (0)
    STAMP(7);

    {
        const int hd = w & 1, qi = w >> 1;
        const int kr0 = 32 * qi;
        u32x4 qf[2];
#pragma unroll
        for (int a = 0; a < 2; ++a) {
            const u32 qa = L0 + L_Q + (kr0 + 16 * a + i16) * 80 + hd * 40;
            const u32x2 q0 = lds64(qa + 8 * g);
            u32x2 q1 = lds64(qa + 32);
            if (g != 0) q1 = (u32x2){0, 0};
            qf[a] = (u32x4){q0[0], q0[1], q1[0], q1[1]};
        }
        const u32 kb0 = L0 + L_K + (kr0 + i16) * 80 + hd * 40 + 8 * g, kb1 = L0 + L_K + (kr0 + i16) * 80 + hd * 40 + 32;
        f32x4 mfirst, mlast;
#pragma unroll
        for (int r = 0; r < 4; ++r) { const bool lo = 4 * g + r <= i16; mfirst[r] = lo ? -INFINITY : 0.f; mlast[r] = lo ? 0.f : -INFINITY; }
        f32x4 s0[17], s1[17];
        {
            u32x4 kf[6];
#define KRD(t) do { const u32x2 k0 = lds64(kb0 + (t) * 1280), k1 = lds64(kb1 + (t) * 1280); kf[(t) % 6] = (u32x4){k0[0], k0[1], k1[0], k1[1]}; } while (0)
            KRD(0); KRD(1); KRD(2); KRD(3); KRD(4);
            static_for<0, 18>([&](auto T) {
                constexpr int t = T;
                SB0();
                if constexpr (t < 13) KRD(t + 5);
                if constexpr (t < 17) s0[t] = mfma(kf[t % 6], qf[0], t == 0 ? mfirst : t == 16 ? mlast : Z4);
                if constexpr (t > 0) s1[t - 1] = mfma(kf[t % 6], qf[1], t == 1 ? mfirst : t == 17 ? mlast : Z4);
                SGB(M_DSR, 2); SGB(M_MFMA, 2);
                SB0();
            });
#undef KRD
        }
        WO_STEP(4 + w);
        if (rb < 2) {
            const int fv = rb == 1 ? 128 : 256;
#pragma unroll
            for (int t = 0; t < 16; ++t) {
                const bool d0 = kr0 + 16 * t < fv, d1 = kr0 + 16 * t + 16 < fv;
#pragma unroll
                for (int r = 0; r < 4; ++r) { s0[t][r] = d0 ? -INFINITY : s0[t][r]; s1[t][r] = d1 ? -INFINITY : s1[t][r]; }
            }
        }
        STAMP(22);
        float m0 = fmaxf(s0[0][0], s0[0][1]), m1 = fmaxf(s1[0][0], s1[0][1]), n0 = fmaxf(s0[0][2], s0[0][3]), n1 = fmaxf(s1[0][2], s1[0][3]);
#pragma unroll
        for (int t = 1; t < 17; ++t) {
            m0 = fmaxf(fmaxf(m0, s0[t][0]), s0[t][1]); m1 = fmaxf(fmaxf(m1, s1[t][0]), s1[t][1]);
            n0 = fmaxf(fmaxf(n0, s0[t][2]), s0[t][3]); n1 = fmaxf(fmaxf(n1, s1[t][2]), s1[t][3]);
        }
        m0 = fmaxf(m0, n0); m1 = fmaxf(m1, n1);
        m0 = xmax4(m0); m1 = xmax4(m1);
        WO_STEP(12 + w);
        STAMP(23);
        f32x4 o00 = Z4, o01 = Z4, o10 = Z4, o11 = Z4;
        const u32 vb = L0 + L_V + (kr0 + 4 * g + q4) * 96 + hd * 48 + 8 * p4;
        auto pv = [&](auto SUB) {
            constexpr bool sub = SUB;
            u32x4 vf[2][2];
            vf[0][0] = ldtr2(vb, vb + 16 * 96); vf[0][1] = ldtr2(vb + 32, vb + 16 * 96 + 32);
#define EX(v, m) __builtin_amdgcn_exp2f(sub ? (v) - (m) : (v))
            static_for<0, 9>([&](auto U) {
                constexpr int u = U;
                if constexpr (u < 8) { const u32 a = vb + (u + 1) * 32 * 96; vf[(u + 1) & 1][0] = ldtr2(a, a + 16 * 96); vf[(u + 1) & 1][1] = ldtr2(a + 32, a + 16 * 96 + 32); }
                u32x4 pa, pb;
                pa[0] = pack2(EX(s0[2 * u][0], m0), EX(s0[2 * u][1], m0)); pa[1] = pack2(EX(s0[2 * u][2], m0), EX(s0[2 * u][3], m0));
                if constexpr (u < 8) { pa[2] = pack2(EX(s0[2 * u + 1][0], m0), EX(s0[2 * u + 1][1], m0)); pa[3] = pack2(EX(s0[2 * u + 1][2], m0), EX(s0[2 * u + 1][3], m0)); }
                else { pa[2] = 0; pa[3] = 0; }
                if constexpr (u > 0) { pb[0] = pack2(EX(s1[2 * u - 1][0], m1), EX(s1[2 * u - 1][1], m1)); pb[1] = pack2(EX(s1[2 * u - 1][2], m1), EX(s1[2 * u - 1][3], m1)); }
                else { pb[0] = 0; pb[1] = 0; }
                pb[2] = pack2(EX(s1[2 * u][0], m1), EX(s1[2 * u][1], m1)); pb[3] = pack2(EX(s1[2 * u][2], m1), EX(s1[2 * u][3], m1));
                o00 = mfma(vf[u & 1][0], pa, o00); o01 = mfma(vf[u & 1][1], pa, o01);
                o10 = mfma(vf[u & 1][0], pb, o10); o11 = mfma(vf[u & 1][1], pb, o11);
                SB0();
            });
#undef EX
        };
        if (__all(fabsf(m0) <= 64.f && fabsf(m1) <= 64.f)) pv(std::false_type{}); else pv(std::true_type{});
        STAMP(24);
        const float i0 = __builtin_amdgcn_rcpf(__shfl(o01[0], i16 + 16)), i1 = __builtin_amdgcn_rcpf(__shfl(o11[0], i16 + 16));
        const u32 oa = L0 + L_Q + (kr0 + i16) * 80 + hd * 40;
        sts64(oa + 8 * g, (u32x2){pack2(o00[0] * i0, o00[1] * i0), pack2(o00[2] * i0, o00[3] * i0)});
        sts64(oa + 1280 + 8 * g, (u32x2){pack2(o10[0] * i1, o10[1] * i1), pack2(o10[2] * i1, o10[3] * i1)});
        if (g == 0) {
            sts64(oa + 32, (u32x2){pack2(o01[0] * i0, o01[1] * i0), pack2(o01[2] * i0, o01[3] * i0)});
            sts64(oa + 1280 + 32, (u32x2){pack2(o11[0] * i1, o11[1] * i1), pack2(o11[2] * i1, o11[3] * i1)});
        }
    }
    BAR();
    {
        const int tok = 64 * (w & 1) + l;
        const u32 oo = WS_O + (((rb * 8 + (tok >> 4)) * 80 + 5 * j) * 16 + (tok & 15)) * 16;
        ST16C(lds128(L0 + L_Q + tok * 80 + (w >> 1) * 16), oo + (w >> 1) * 256);
        if (w < 2) ST16C(lds128(L0 + L_Q + tok * 80 + 64), oo + 4 * 256);
    }
    VMCNT(0);
    BAR();
    if (tid == 0) {
        st_agent(ws + WS_FO + (rb * 16 + j) * FS, tag);
        if (wpwg) st_agent(ws + WS_FWP + b * FS, tag);
    }
    STAMP(8);

    const int wm = w & 3, wk = w >> 2;
    const int mt3 = rb * 8 + 4 * hf + wm;
    u32x2 x1k[5];
    {
        const int wdu = b + 256 * (w - 6);
        const bool wdw = w >= 6 && wdu < 432;
        if (w >= 1 && w < 5) {
#pragma unroll
            for (int i = 0; i < 5; ++i) glds16(p.wo, wosrc + (w - 1) * 32 * 1280, wvoff[i], L0 + L_WO + (w - 1) * 5120 + i * 1024);
        } else if (w >= 5) {
#pragma unroll
            for (int e = 0; e < 4; ++e) {
                const int pc = w - 5 + 3 * e;
                if (pc < 10) {
                    const u32 sg = 64 * pc + l, r = (sg * 6554) >> 16, c = sg - r * 10;
                    glds16(p.x, (const char*)p.x + (rb * 128 + 64 * hf) * 1280 + cb * 160, r * 1280 + c * 16, L0 + L_XR + pc * 1024);
                }
            }
            if (wdw) {
                const int ucb = wdu / 54, us = wdu - 54 * ucb;
#pragma unroll
                for (int i = 0; i < 5; ++i) glds16(p.wd, (const char*)p.wd + ucb * 160 + us * 32 * 1280, wvoff[i], L0 + L_WDRAW + (w - 6) * 5120 + i * 1024);
            }
        }
        if (w == 1 && l < 20) stsf4(L0 + L_GA + l * 16, gather1(ws, 320 + 20 * cb + l, tag, ws, 4));
        if (w == 0) {
            const int bl = min((Gj >> 2) + ((l >> 2) % 3), (Gj + nj - 1) >> 2);
            poll_at(ws, WS_FWP + (108 * (l >= 12 && l < 24 ? 1 : 0) + 4 * bl + (l & 3)) * FS, l, tag, ws, 6);
            poll_flags(ws + WS_FO + rb * 16 * FS, 16, l, tag, ws, 5);
        }
        if (wdw) vmw<5>(); else vmw<0>();
        BAR();
        STAMP(9);
        u32x4 a[10];
        const char* asrc = ws + WS_O + (mt3 * 80 + 40 * wk) * 256;
#define AL(s) a[s] = gld16c(((s) & 3) * 1024, asrc + ((s) >> 2) * 4096, lo16)
        AL(0); AL(1); AL(2); AL(3);
        f32x4 acc[5];
#pragma unroll
        for (int n = 0; n < 5; ++n) acc[n] = Z4;
        const u32 wb = L0 + L_WO + wk * 51200;
        const u32 b0 = wb + 160 * (8 * g + 4 * (g & 1) + q4) + 8 * p4, b1 = wb + 160 * (8 * g + 4 * ((g & 1) ^ 1) + q4) + 8 * p4;
        u32x4 bf[3][5];
#pragma unroll
        for (int n = 0; n < 5; ++n) { bf[0][n] = ldtr2(b0 + n * 32, b1 + n * 32); bf[1][n] = ldtr2(b0 + 5120 + n * 32, b1 + 5120 + n * 32); }
        static_for<0, 10>([&](auto S) {
            constexpr int s = S;
            vmw1<(s < 7 ? 3 : 9 - s)>(a[s]);
            if constexpr (s + 4 < 10) AL(s + 4);
            SB0();
#pragma unroll
            for (int n = 0; n < 5; ++n) {
                if (s < 8) bf[(s + 2) % 3][n] = ldtr2(b0 + (s + 2) * 5120 + n * 32, b1 + (s + 2) * 5120 + n * 32);
                acc[n] = mfma(bf[s % 3][n], a[s], acc[n]);
            }
#pragma unroll
            for (int n = 0; n < 5; ++n) { SGB(M_DSR, 2); SGB(M_MFMA, 1); }
            SB0();
        });
#undef AL
        STAMP(25);
        if (wk == 1) {
#pragma unroll
            for (int n = 0; n < 5; ++n) stsf4(L0 + L_RED3 + (wm * 5 + n) * 1024 + lo16, acc[n]);
        }
        BAR();
        if (wk == 0) {
            float s1 = 0.f, s2 = 0.f;
#pragma unroll
            for (int n = 0; n < 5; ++n) {
                const f32x4 v = acc[n] + ldsf4(L0 + L_RED3 + (wm * 5 + n) * 1024 + lo16);
                const f32x4 ga = ldsf4(L0 + L_GA + (16 * n + 4 * g) * 4);
                const u32x2 xr = lds64(L0 + L_XR + (16 * wm + i16) * 160 + 32 * n + 8 * g);
                const u32x2 o = {pack2(bflo(xr[0]) + ga[0] * v[0], bfhi(xr[0]) + ga[1] * v[1]), pack2(bflo(xr[1]) + ga[2] * v[2], bfhi(xr[1]) + ga[3] * v[3])};
                x1k[n] = o;
                s1 = dot2(o[0], 0x3F803F80u, s1); s1 = dot2(o[1], 0x3F803F80u, s1);
                s2 = dot2(o[0], o[0], s2); s2 = dot2(o[1], o[1], s2);
            }
            const u32 xo = WS_X1 + ((mt3 * 80 + 10 * cb) * 16 + i16) * 16;
#pragma unroll
            for (int n = 0; n < 4; n += 2) {
                const auto a0 = __builtin_amdgcn_permlane16_swap(x1k[n][0], x1k[n + 1][0], false, false);
                const auto a1 = __builtin_amdgcn_permlane16_swap(x1k[n][1], x1k[n + 1][1], false, false);
                ST16C(((u32x4){a0[0], a1[0], a0[1], a1[1]}), xo + (2 * (n + (g & 1)) + (g >> 1)) * 256);
            }
            ST8C(x1k[4], xo + (8 + (g >> 1)) * 256 + 8 * (g & 1));
            s1 = xsum4(s1); s2 = xsum4(s2);
            if (l < 16) ST8C(((u32x2){__float_as_uint(s1), __float_as_uint(s2)}), WS_STAT + ((rb * 8 + cb) * 128 + 64 * hf + 16 * wm + l) * 8);
            STAMP(26);
            vmw<0>();
        } else {
#pragma unroll
            for (int n = 0; n < 5; ++n) x1k[n] = (u32x2){0, 0};
            if (wdw) {
                const u32 wr = L0 + L_WDRAW + (w - 6) * 5120;
                const u32 r0 = wr + 160 * (8 * g + 4 * (g & 1) + q4) + 8 * p4, r1 = wr + 160 * (8 * g + 4 * ((g & 1) ^ 1) + q4) + 8 * p4;
#pragma unroll
                for (int n = 0; n < 5; ++n) ST16C(ldtr2(r0 + n * 32, r1 + n * 32), WS_WD + (wdu * 5 + n) * 1024 + lo16);
            }
        }
        BAR();
        if (tid == 0) st_agent(ws + WS_FX1 + (rb * 16 + j) * FS, tag);
    }
    STAMP(10);

    const char* wdsrc = ws + WS_WD + (cb * 54 + wk * 27) * 5120;
    const u32 r5w = L0 + wk * 20480 + wm * 5120;
#define P5_B1(t, i) do { if ((i) == 0 || (i) == 4) glds16c(wdsrc + min(4 * (t) + wm, 26) * 5120 + (i) * 1024, lo16, r5w + r5_slot(t) + (i) * 1024); \
        else glds16c_next(((i) & 3) * 1024, wdsrc + min(4 * (t) + wm, 26) * 5120, lo16); } while (0)
    {
        const int wn = w >> 2;
        const int bst = w < 4 ? 4 * w : 16 + 3 * (w - 4);
        const char* wpj = ws + WS_WP + (j * 20 * 14 + bst) * 1024;
        const char* x1b = ws + WS_X1 + (rb * 8 + w) * 80 * 256;
        const u32 r4a = L0 + L_R4 + w * 2048, r4b = L0 + L_R4 + 16384 + bst * 1024;
#define P4_B1(t, i) do { if ((i) == 0) glds16c(wpj + (t) * 28 * 1024, lo16, r4b + ((t) % 3) * R4_STAGE); else glds16c_next((i) * 1024, wpj + (t) * 28 * 1024, lo16); } while (0)
#define P4_A1(t, i) do { if ((i) == 0) glds16c(x1b + 8 * (t) * 256, lo16, r4a + ((t) % 3) * R4_STAGE); else glds16c_next(1024, x1b + 8 * (t) * 256, lo16); } while (0)
#define P4_B(t) do { P4_B1(t, 0); P4_B1(t, 1); P4_B1(t, 2); if (w < 4) P4_B1(t, 3); } while (0)
        P4_B(0); P4_B(1); P4_B(2);
        if (w == 0) poll_flags(ws + WS_FX1 + rb * 16 * FS, 16, l, tag, ws, 7);
        BAR();
        STAMP(11);
        glds16c(ws + WS_STAT + rb * 8192 + w * 1024, lo16, L0 + L_ST4 + w * 1024);
        if (w < 7) glds16c(ws + WS_UV + j * 7168 + w * 1024, lo16, L0 + L_UV + w * 1024);
        P4_A1(0, 0); P4_A1(0, 1); P4_A1(1, 0); P4_A1(1, 1); P4_A1(2, 0); P4_A1(2, 1);
        f32x4 acc[7][2];
#pragma unroll
        for (int n = 0; n < 7; ++n) { acc[n][0] = Z4; acc[n][1] = Z4; }
        const u32 ra = opaque(L0 + L_R4 + 2 * wm * 2048 + lo16), rbb = opaque(L0 + L_R4 + 16384 + 7 * wn * 1024 + lo16);
        u32x4 fa[2][2], fb[2][7];
#define P4_RA(buf, t, ks) do { const u32 so = ((t) % 3) * R4_STAGE + (ks) * 1024; fa[buf][0] = lds128(ra + so); fa[buf][1] = lds128(ra + so + 2048); } while (0)
#define P4_RB(buf, t, ks, n) fb[buf][n] = lds128(rbb + ((t) % 3) * R4_STAGE + (ks) * 14336 + (n) * 1024)
#define P4_MM(buf, n) do { acc[n][0] = mfma(fb[buf][n], fa[buf][0], acc[n][0]); acc[n][1] = mfma(fb[buf][n], fa[buf][1], acc[n][1]); } while (0)
#define P4_HALF(buf, rd, t, ks, V0, V1, V2) do { \
            SB0(); if (rd) { P4_RA((buf) ^ 1, t, ks); P4_RB((buf) ^ 1, t, ks, 0); } P4_MM(buf, 0); SB0(); V0; \
            SB0(); if (rd) P4_RB((buf) ^ 1, t, ks, 1); P4_MM(buf, 1); SB0(); \
            SB0(); if (rd) P4_RB((buf) ^ 1, t, ks, 2); P4_MM(buf, 2); SB0(); V1; \
            SB0(); if (rd) P4_RB((buf) ^ 1, t, ks, 3); P4_MM(buf, 3); SB0(); \
            SB0(); if (rd) P4_RB((buf) ^ 1, t, ks, 4); P4_MM(buf, 4); SB0(); V2; \
            SB0(); if (rd) P4_RB((buf) ^ 1, t, ks, 5); P4_MM(buf, 5); SB0(); \
            SB0(); if (rd) P4_RB((buf) ^ 1, t, ks, 6); P4_MM(buf, 6); SB0(); } while (0)
        VMCNT(4);
        BAR();
        if (tid == 0) st_agent(ws + WS_FW2 + b * FS, tag);
        STAMP(27);
        u32x2 wdf = {0u, 0u};
        const u32 wdo = WS_FW2 + ((54 * cb + (l < 54 ? l : 0)) & 255) * FS;
        P4_RA(0, 0, 0);
#pragma unroll
        for (int n = 0; n < 7; ++n) P4_RB(0, 0, 0, n);
        static_for<0, 10>([&](auto T) {
            constexpr int t = T;
            constexpr bool f = t >= 1 && t + 2 <= 9, sN = t + 3 <= 9;
            if constexpr (t < 9) P4_HALF(0, true, t, 1, if (f && w < 4) P4_B1(t + 2, 3), if (f) P4_A1(t + 2, 0), if (f) P4_A1(t + 2, 1));
            else P4_HALF(0, true, t, 1, P5_B1(0, 3), P5_B1(0, 4), P5_B1(1, 0));
            if constexpr (t == 5) {
                if (tid < 128) {
                    f32x2 st = {0.f, 0.f};
#pragma unroll
                    for (int c = 0; c < 8; ++c) st += *(LAS f32x2*)(size_t)(L0 + L_ST4 + (c * 128 + tid) * 8);
                    const float mu = st[0] * (1.f / 640.f), var = fmaxf(st[1] * (1.f / 640.f) - mu * mu, 0.f), r = __builtin_amdgcn_rsqf(var + 1e-5f);
                    stsf2(L0 + L_MR + tid * 8, (f32x2){r, -mu * r});
                } else {
                    const int e = tid - 128;
                    stsf(L0 + L_UVS + e * 4, (ldsf(L0 + L_UV + (e >> 5) * 512 + (e & 31) * 4) + ldsf(L0 + L_UV + (e >> 5) * 512 + 128 + (e & 31) * 4))
                                           + (ldsf(L0 + L_UV + (e >> 5) * 512 + 256 + (e & 31) * 4) + ldsf(L0 + L_UV + (e >> 5) * 512 + 384 + (e & 31) * 4)));
                    if (e < 64) { const int e2 = e + 384;
                        stsf(L0 + L_UVS + e2 * 4, (ldsf(L0 + L_UV + (e2 >> 5) * 512 + (e2 & 31) * 4) + ldsf(L0 + L_UV + (e2 >> 5) * 512 + 128 + (e2 & 31) * 4))
                                                + (ldsf(L0 + L_UV + (e2 >> 5) * 512 + 256 + (e2 & 31) * 4) + ldsf(L0 + L_UV + (e2 >> 5) * 512 + 384 + (e2 & 31) * 4))); }
                }
            }
            if constexpr (t < 9) {
                if constexpr (t == 0) vmw<2>();
                else if constexpr (t == 8) {
                    asm volatile("s_waitcnt vmcnt(0)" : "+v"(wdf) :: "memory");
                    if (w == 7) { if (!__all(wdf[0] == (u32)tag && wdf[1] == (u32)(tag >> 32))) poll_at(ws, wdo, l, tag, ws, 12); }
                }
                else { if (w < 4) vmw<6>(); else vmw<5>(); }
                BAR();
                if constexpr (t == 7) { if (w == 7) asm volatile("s_nop 4\n\tglobal_load_dwordx2 %0, %1, %2 sc1" : "=v"(wdf) : "v"(wdo), "s"(ws) : "memory"); }
                if constexpr (t < 8) P4_HALF(1, true, t + 1, 0, if (sN) P4_B1(t + 3, 0), if (sN) P4_B1(t + 3, 1), if (sN) P4_B1(t + 3, 2));
                else P4_HALF(1, true, t + 1, 0, P5_B1(0, 0), P5_B1(0, 1), P5_B1(0, 2));
            } else {
                P4_HALF(1, false, 0, 0, P5_B1(1, 1), P5_B1(1, 2), P5_B1(1, 3));
                P5_B1(1, 4);
            }
        });
#undef P4_B
#undef P4_B1
#undef P4_A1
#undef P4_RA
#undef P4_RB
#undef P4_MM
#undef P4_HALF
        STAMP(12);
        f32x2 mr[2];
#pragma unroll
        for (int m = 0; m < 2; ++m) mr[m] = *(LAS f32x2*)(size_t)(L0 + L_MR + (32 * wm + 16 * m + i16) * 8);
#pragma unroll
        for (int n = 0; n < 7; ++n) {
            const f32x4 u = ldsf4(L0 + L_UVS + (7 * wn + n) * 128 + 16 * g), v = ldsf4(L0 + L_UVS + (7 * wn + n) * 128 + 64 + 16 * g);
#pragma unroll
            for (int m = 0; m < 2; ++m) acc[n][m] = acc[n][m] * mr[m][0] + (u * mr[m][1] + v);
        }
        if (nj == 7) stsf4(L0 + L_XCH + w * 1024 + lo16, wn == 0 ? acc[6][1] : acc[6][0]);
        auto silu_mul = [&](const f32x4 gt, const f32x4 up) -> u32x2 {
            const f32x4 e = gt * (-LOG2E), gu = gt * up;
            f32x4 d;
#pragma unroll
            for (int r = 0; r < 4; ++r) d[r] = __builtin_amdgcn_exp2f(e[r]);
            d = d + 1.f;
#pragma unroll
            for (int r = 0; r < 4; ++r) d[r] = __builtin_amdgcn_rcpf(d[r]);
            const f32x4 o = gu * d;
            return (u32x2){pack2(o[0], o[1]), pack2(o[2], o[3])};
        };
#pragma unroll
        for (int a = 0; a < 3; ++a) {
            const int la = a + 4 * wn, gam = Gj + (la <= 3 || nj == 7 ? la : la - 1);
            const u32x2 o0 = silu_mul(acc[2 * a][0], acc[2 * a + 1][0]), o1 = silu_mul(acc[2 * a][1], acc[2 * a + 1][1]);
            const auto s0 = __builtin_amdgcn_permlane16_swap(o0[0], o1[0], false, false);
            const auto s1 = __builtin_amdgcn_permlane16_swap(o0[1], o1[1], false, false);
            const u32x4 v = {s0[0], s1[0], s0[1], s1[1]};
            ST16C(v, WS_ACT + (((rb * 8 + 2 * wm + (g & 1)) * 216 + 2 * gam + (g >> 1)) * 16 + i16) * 16);
        }
        STAMP(28);
        BAR();
        if (nj == 7) {
            const f32x4 other = ldsf4(L0 + L_XCH + (w ^ 4) * 1024 + lo16);
            const u32x2 o = wn == 0 ? silu_mul(acc[6][0], other) : silu_mul(other, acc[6][1]);
            ST8C(o, WS_ACT + (((rb * 8 + 2 * wm + wn) * 216 + 2 * (Gj + 3) + (g >> 1)) * 16 + i16) * 16 + 8 * (g & 1));
        }
        P5_B1(2, 0); P5_B1(2, 1); P5_B1(2, 2); P5_B1(2, 3); P5_B1(2, 4);
        vmw<5>();
        BAR();
        if (tid == 0) st_agent(ws + WS_FACT + (rb * 16 + j) * FS, tag);
    }
    STAMP(13);

    {
        if (w == 1 && l < 20) stsf4(L0 + L_GB + l * 16, gather1(ws, 800 + 20 * cb + l, tag, ws, 8));
        if (w == 0) poll_flags(ws + WS_FACT + rb * 16 * FS, 16, l, tag, ws, 9);
        const bool bump = rb == 15 && j == 0;
        if (bump && w >= 4) poll_at(ws, WS_FO + (tid - 256) * FS, l, tag, ws, 10);
        VMCNT(0);
        BAR();
        if (bump && tid == 0) st_agent(ws + WS_EPOCH, tag);
        STAMP(14);
        const char* asrc = ws + WS_ACT + (mt3 * 216 + wk * 108) * 256;
        u32x4 a[3][4];
#define P5_A1(t, i) a[(t) % 3][i] = gld16c((i) * 1024, asrc + (t) * 4096, lo16)
        P5_A1(0, 0); P5_A1(0, 1); P5_A1(0, 2); P5_A1(0, 3); P5_A1(1, 0); P5_A1(1, 1); P5_A1(1, 2); P5_A1(1, 3); P5_A1(2, 0); P5_A1(2, 1); P5_A1(2, 2); P5_A1(2, 3);
        f32x4 acc[5];
#pragma unroll
        for (int n = 0; n < 5; ++n) acc[n] = Z4;
        const u32 rb5 = opaque(L0 + wk * 20480 + lo16);
        u32x4 fb[3][5];
#define P5_RD(buf, t, kk) do { _Pragma("unroll") for (int n = 0; n < 5; ++n) fb[buf][n] = lds128(rb5 + r5_slot(t) + (kk) * 5120 + n * 1024); } while (0)
#define P5_MM(buf, av) do { _Pragma("unroll") for (int n = 0; n < 5; ++n) acc[n] = mfma(fb[buf][n], (av), acc[n]); } while (0)
#define P5_MIX() do { _Pragma("unroll") for (int n = 0; n < 5; ++n) { SGB(M_DSR, 1); SGB(M_MFMA, 1); } } while (0)
        P5_RD(0, 0, 0); P5_RD(1, 0, 1);
        static_for<0, 7>([&](auto T) {
            constexpr int t = T, q = t % 3, k0 = 4 * t;
            constexpr bool nx = t + 3 < 7;
            constexpr int NA = t == 0 ? 8 : t == 1 ? 13 : t == 2 ? 18 : t == 3 ? 19 : t == 4 ? 18 : t == 5 ? 9 : 3;
            constexpr int NB = t == 2 ? 11 : t == 3 ? 11 : t == 4 ? 8 : t == 5 ? 0 : 63;
            vmw4<NA>(a[q][0], a[q][1], a[q][2], a[q][3]);
            SB0(); P5_RD((k0 + 2) % 3, t, 2); P5_MM(k0 % 3, a[q][0]); P5_MIX(); SB0();
            if constexpr (nx) P5_A1(t + 3, 0);
            if constexpr (t < 6) {
                SB0(); P5_RD((k0 + 3) % 3, t, 3); P5_MM((k0 + 1) % 3, a[q][1]); P5_MIX(); SB0();
                if constexpr (nx) P5_A1(t + 3, 1);
                vmw<NB>(); BAR();
                if constexpr (nx) { P5_B1(t + 3, 0); P5_B1(t + 3, 1); }
                SB0(); P5_RD((k0 + 4) % 3, t + 1, 0); P5_MM((k0 + 2) % 3, a[q][2]); P5_MIX(); SB0();
                if constexpr (nx) { P5_A1(t + 3, 2); P5_B1(t + 3, 2); P5_B1(t + 3, 3); }
                SB0(); P5_RD((k0 + 5) % 3, t + 1, 1); P5_MM((k0 + 3) % 3, a[q][3]); P5_MIX(); SB0();
                if constexpr (nx) { if constexpr (t + 3 < 6) P5_A1(t + 3, 3); P5_B1(t + 3, 4); }
            } else {
                SB0(); P5_MM((k0 + 1) % 3, a[q][1]); SB0(); P5_MM((k0 + 2) % 3, a[q][2]); SB0();
            }
        });
#undef P5_B1
#undef P5_A1
#undef P5_RD
#undef P5_MM
#undef P5_MIX
        STAMP(15);
        if (wk == 1) {
#pragma unroll
            for (int n = 0; n < 5; ++n) stsf4(L0 + L_RED5 + (wm * 5 + n) * 1024 + lo16, acc[n]);
        }
        BAR();
        if (wk == 0) {
#pragma unroll
            for (int n = 0; n < 5; ++n) {
                const f32x4 v = acc[n] + ldsf4(L0 + L_RED5 + (wm * 5 + n) * 1024 + lo16);
                const f32x4 gb = ldsf4(L0 + L_GB + (16 * n + 4 * g) * 4);
                const u32x2 o = {pack2(bflo(x1k[n][0]) + gb[0] * v[0], bfhi(x1k[n][0]) + gb[1] * v[1]), pack2(bflo(x1k[n][1]) + gb[2] * v[2], bfhi(x1k[n][1]) + gb[3] * v[3])};
                sts64(L0 + L_OS + (16 * wm + i16) * 160 + 32 * n + 8 * g, o);
            }
        }
        BAR();
#pragma unroll
        for (int i = 0; i < 2; ++i) {
            const int id = tid + 512 * i;
            if (id < 640) {
                const int r = (id * 6554) >> 16, c = id - 10 * r;
                *(GAS u32x4*)((char*)p.out + (rb * 128 + 64 * hf + r) * 1280 + cb * 160 + c * 16) = lds128(L0 + L_OS + id * 16);
            }
        }
    }
    STAMP(16); STAMP_RT(63); STAMP_OUT();
    asm volatile(".Lmk_code_to:");
}

extern "C" void kernel_launch(void* const* d_in, const int* in_sizes, int n_in, void* d_out, int out_size, void* d_ws, size_t ws_size, hipStream_t stream) {
    Params p;
    p.x = (const u16*)d_in[0]; p.wqkv = (const u16*)d_in[1]; p.wo = (const u16*)d_in[2];
    p.wg = (const u16*)d_in[4]; p.wu = (const u16*)d_in[5]; p.wd = (const u16*)d_in[6];
    p.c = (const u16*)d_in[7]; p.wmod = (const u16*)d_in[8];
    p.out = (u16*)d_out; p.ws = (char*)d_ws;
    mega_kernel<<<256, 512, 0, stream>>>(p);
}
```

```cpp
#include <hip/hip_runtime.h>
#include <hip/hip_bf16.h>
#include <stdint.h>
#include <type_traits>

typedef unsigned short u16;
typedef unsigned u32;
typedef unsigned long long u64;
typedef __attribute__((ext_vector_type(2))) unsigned u32x2;
typedef __attribute__((ext_vector_type(4))) unsigned u32x4;
typedef __attribute__((ext_vector_type(2))) float f32x2;
typedef __attribute__((ext_vector_type(4))) float f32x4;
typedef __attribute__((ext_vector_type(2))) __bf16 bf16x2;
typedef __attribute__((ext_vector_type(8))) __bf16 bf16x8;
typedef __attribute__((ext_vector_type(4))) short s16x4;
#define LAS __attribute__((address_space(3)))
#define GAS __attribute__((address_space(1)))
#define DI __device__ __forceinline__

constexpr int SEQ = 2048, DM = 640, FF = 1728;
constexpr float QSCALE = 0.22360679774997896f * 1.4426950408889634f;
constexpr float LOG2E = 1.4426950408889634f;

constexpr u32 WS_EPOCH = 0;
constexpr u32 WS_ERR   = 256;
constexpr u32 FS = 128;
constexpr u32 WS_FKV   = 4096;
constexpr u32 WS_FO    = WS_FKV + 256 * FS;
constexpr u32 WS_FX1   = WS_FO + 256 * FS;
constexpr u32 WS_FACT  = WS_FX1 + 256 * FS;
constexpr u32 WS_FH    = WS_FACT + 256 * FS;
constexpr u32 WS_FWT   = WS_FH + 256 * FS;
constexpr u32 WS_FWP   = WS_FWT + 256 * FS;
constexpr u32 WS_FW2   = WS_FWP + 256 * FS;
constexpr u32 WS_MODG  = WS_FW2 + 256 * FS;
constexpr u32 WS_UV    = WS_MODG + 61440;
constexpr u32 WS_STAT  = WS_UV + 114688;
constexpr u32 WS_KV    = 589824;
constexpr u32 WS_O     = WS_KV + 256 * 22528;
constexpr u32 WS_X1    = WS_O + SEQ * DM * 2;
constexpr u32 WS_ACT   = WS_X1 + SEQ * DM * 2;
constexpr u32 WS_WP    = WS_ACT + SEQ * FF * 2;
constexpr u32 WS_H     = WS_WP + 16 * 20 * 14 * 1024;
constexpr u32 WS_WD    = WS_H + SEQ * DM * 2;
constexpr u32 WS_WT    = WS_WD + 8 * 54 * 5 * 1024;
constexpr u32 WS_END   = WS_WT + 16 * 153600;

constexpr u32 L_WKV = 0, L_WQ = 102400, L_GS = 153600, L_STP = 156160, L_MODP = 156416, L_CNT = 157440, L_SB = 157696, L_G2 = 158016, L_Z16 = 158656;
constexpr u32 L_WTRAW = 116736, L_XS = 133120;
constexpr u32 L_K = 0, L_V = 30720, L_Q = 67584, L_WPRAW = 77824, L_UVL = 98304;
constexpr u32 L_WO = 61440, L_RED3 = 0, L_XR = 30720, L_GA = 40960, L_WDRAW = 43008;
constexpr u32 L_R4 = 0, R4_STAGE = 45056, L_ST4 = 135168, L_UV = 143360, L_MR = 150528, L_XCH = 151552, L_UVS = 159744;
constexpr u32 L_RED5 = 126976, L_GB = 147456, L_OS = 148480;
constexpr u32 r5_slot(int t) { return t % 3 == 0 ? 45056 : t % 3 == 1 ? 86016 : 0; }

struct Params {
    const u16* x; const u16* wqkv; const u16* wo; const u16* wg; const u16* wu; const u16* wd;
    const u16* c; const u16* wmod; u16* out; char* ws;
};

DI float bflo(u32 d) { return __uint_as_float(d << 16); }
DI float bfhi(u32 d) { return __uint_as_float(d & 0xffff0000u); }
DI u32 pack2(float a, float b) { f32x2 v = {a, b}; return __builtin_bit_cast(u32, __builtin_convertvector(v, bf16x2)); }
DI float rbf(float a) { return bflo(pack2(a, a)); }
DI f32x4 mfma(u32x4 a, u32x4 b, f32x4 c) {
    return __builtin_amdgcn_mfma_f32_16x16x32_bf16(__builtin_bit_cast(bf16x8, a), __builtin_bit_cast(bf16x8, b), c, 0, 0, 0);
}
DI u32x2 ldtr(u32 a) { return __builtin_bit_cast(u32x2, __builtin_amdgcn_ds_read_tr16_b64_v4i16((LAS s16x4*)(size_t)a)); }
DI u32x4 ldtr2(u32 a0, u32 a1) { u32x2 x = ldtr(a0), y = ldtr(a1); return (u32x4){x[0], x[1], y[0], y[1]}; }
DI u32x4 lds128(u32 a) { return *(LAS u32x4*)(size_t)a; }
DI u32x2 lds64(u32 a) { return *(LAS u32x2*)(size_t)a; }
DI f32x4 ldsf4(u32 a) { return *(LAS f32x4*)(size_t)a; }
DI float ldsf(u32 a) { return *(LAS float*)(size_t)a; }
DI void sts128(u32 a, u32x4 v) { *(LAS u32x4*)(size_t)a = v; }
DI void sts64(u32 a, u32x2 v) { *(LAS u32x2*)(size_t)a = v; }
DI void stsf(u32 a, float v) { *(LAS float*)(size_t)a = v; }
DI void stsf4(u32 a, f32x4 v) { *(LAS f32x4*)(size_t)a = v; }
DI void stsf2(u32 a, f32x2 v) { *(LAS f32x2*)(size_t)a = v; }

DI void glds16_(const void* root, u32 voff, u32 ldsdst) {
    asm volatile("s_mov_b32 m0, %2\n\ts_nop 0\n\tglobal_load_lds_dwordx4 %0, %1" :: "v"(voff), "s"(root), "s"(__builtin_amdgcn_readfirstlane(ldsdst)) : "memory");
}
DI void glds16c_(const void* root, u32 voff, u32 ldsdst) {
    asm volatile("s_mov_b32 m0, %2\n\ts_nop 0\n\tglobal_load_lds_dwordx4 %0, %1 sc1" :: "v"(voff), "s"(root), "s"(__builtin_amdgcn_readfirstlane(ldsdst)) : "memory");
}
template <int OFF> DI void glds16c_next_(const void* root, u32 voff) {
    asm volatile("global_load_lds_dwordx4 %0, %1 offset:%2 sc1" :: "v"(voff), "s"(root), "i"(OFF) : "memory");
}
template <int OFF> DI u32x4 gld16(const void* root, u32 voff) {
    u32x4 r; asm volatile("global_load_dwordx4 %0, %1, %2 offset:%3" : "=v"(r) : "v"(voff), "s"(root), "i"(OFF) : "memory"); return r;
}
template <int OFF> DI u32x4 gld16c_(const void* root, u32 voff) {
    u32x4 r; asm volatile("global_load_dwordx4 %0, %1, %2 offset:%3 sc1" : "=v"(r) : "v"(voff), "s"(root), "i"(OFF) : "memory"); return r;
}
#define ROFF(root, ptr) ((u32)((const char*)(ptr) - (const char*)(root)))
#define glds16(root, ptr, voff, dst) glds16_((root), ROFF(root, ptr) + (voff), (dst))
#define glds16c(ptr, voff, dst) glds16c_(ws, ROFF(ws, ptr) + (voff), (dst))
#define glds16c_next(OFF, ptr, voff) glds16c_next_<(OFF)>(ws, ROFF(ws, ptr) + (voff))
#define gld16c(OFF, ptr, voff) gld16c_<(OFF)>(ws, ROFF(ws, ptr) + (voff))
DI u32 gldu16(const void* sbase, u32 voff) {
    u32 r; asm volatile("global_load_ushort %0, %1, %2" : "=v"(r) : "v"(voff), "s"(sbase) : "memory"); return r;
}
#define VMCNT(n) asm volatile("s_waitcnt vmcnt(" #n ")" ::: "memory")
#define BAR() asm volatile("s_waitcnt lgkmcnt(0)\n\ts_barrier" ::: "memory")
#define RLX_AGENT __ATOMIC_RELAXED, __HIP_MEMORY_SCOPE_AGENT
DI u64 ld_agent(const char* p) { return __hip_atomic_load((GAS u64*)p, RLX_AGENT); }
DI void st_agent(char* p, u64 v) { __hip_atomic_store((GAS u64*)p, v, RLX_AGENT); }
DI void st_agent32(char* p, u32 v) { __hip_atomic_store((GAS u32*)p, v, RLX_AGENT); }
DI float xsum4(float x) {
    const auto s = __builtin_amdgcn_permlane16_swap(__float_as_uint(x), __float_as_uint(x), false, false);
    x = __uint_as_float(s[0]) + __uint_as_float(s[1]);
    const auto t = __builtin_amdgcn_permlane32_swap(__float_as_uint(x), __float_as_uint(x), false, false);
    return __uint_as_float(t[0]) + __uint_as_float(t[1]);
}
DI float xmax4(float x) {
    const auto s = __builtin_amdgcn_permlane16_swap(__float_as_uint(x), __float_as_uint(x), false, false);
    x = fmaxf(__uint_as_float(s[0]), __uint_as_float(s[1]));
    const auto t = __builtin_amdgcn_permlane32_swap(__float_as_uint(x), __float_as_uint(x), false, false);
    return fmaxf(__uint_as_float(t[0]), __uint_as_float(t[1]));
}
template <int CTRL> DI float dppf(float v) { return __builtin_bit_cast(float, __builtin_amdgcn_update_dpp(0, __builtin_bit_cast(int, v), CTRL, 0xf, 0xf, true)); }

constexpr u32 SPIN_CAP = 1u << 17;
DI void poll_flags(const char* base, int n, int lane, u64 tag, char* ws, u32 code) {
    const char* a = base + (lane < n ? lane : 0) * FS;
    u32 spins = 0;
    for (;;) {
        const u64 v = ld_agent(a);
        if (__all(v == tag)) break;
        if (++spins > SPIN_CAP) { if (lane == 0) st_agent32(ws + WS_ERR, code); break; }
        __builtin_amdgcn_s_sleep(1);
    }
}
typedef __attribute__((ext_vector_type(2))) _Float16 f16x2;
DI float hlo(u32 d) { return (float)__builtin_bit_cast(f16x2, d)[0]; }
DI float hhi(u32 d) { return (float)__builtin_bit_cast(f16x2, d)[1]; }
DI u32 packh(float a, float b) { const f16x2 v = {(_Float16)a, (_Float16)b}; return __builtin_bit_cast(u32, v); }
DI bool gran_ok(const u32x4& v, u64 tag) { return v[2] == (u32)tag && v[3] == (u32)(tag >> 32); }
DI f32x4 gran_sum(const u32x4& a, const u32x4& b, const u32x4& c, const u32x4& d) {
    const f32x4 s = {(hlo(a[0]) + hlo(b[0])) + (hlo(c[0]) + hlo(d[0])), (hhi(a[0]) + hhi(b[0])) + (hhi(c[0]) + hhi(d[0])),
                     (hlo(a[1]) + hlo(b[1])) + (hlo(c[1]) + hlo(d[1])), (hhi(a[1]) + hhi(b[1])) + (hhi(c[1]) + hhi(d[1]))};
    return (f32x4){rbf(s[0]), rbf(s[1]), rbf(s[2]), rbf(s[3])};
}
DI f32x4 gather1(const char* ws, u32 g0, u64 tag, char* wsw, u32 code) {
    u32 spins = 0; u32x4 v0, v1, v2, v3;
    const u32 o = WS_MODG + g0 * 16;
    for (;;) {
        asm volatile("s_nop 4\n\tglobal_load_dwordx4 %0, %4, %5 sc1\n\tglobal_load_dwordx4 %1, %6, %5 sc1\n\tglobal_load_dwordx4 %2, %7, %5 sc1\n\tglobal_load_dwordx4 %3, %8, %5 sc1\n\ts_waitcnt vmcnt(0)"
                     : "=&v"(v0), "=&v"(v1), "=&v"(v2), "=&v"(v3) : "v"(o), "s"(ws), "v"(o + 15360), "v"(o + 30720), "v"(o + 46080) : "memory");
        if (__all(gran_ok(v0, tag) && gran_ok(v1, tag) && gran_ok(v2, tag) && gran_ok(v3, tag))) break;
        if (++spins > SPIN_CAP) { st_agent32(wsw + WS_ERR, code); break; }
        __builtin_amdgcn_s_sleep(1);
    }
    return gran_sum(v0, v1, v2, v3);
}
DI void reask(u32x4& v, const char* ws, u32 off, u64 tag) {
    if (!gran_ok(v, tag)) asm volatile("global_load_dwordx4 %0, %1, %2 sc1" : "+v"(v) : "v"(off), "s"(ws) : "memory");
}
DI void gather2(const char* ws, u32 g0, u32 g1, u64 tag, char* wsw, u32 code, f32x4& ra, f32x4& rb) {
    u32 spins = 0; u32x4 v0, v1, v2, v3, u0, u1, u2, u3;
    const u32 o = WS_MODG + g0 * 16, q = WS_MODG + g1 * 16;
    asm volatile("global_load_dwordx4 %0, %8, %9 sc1\n\tglobal_load_dwordx4 %1, %10, %9 sc1\n\tglobal_load_dwordx4 %2, %11, %9 sc1\n\tglobal_load_dwordx4 %3, %12, %9 sc1\n\t"
                 "global_load_dwordx4 %4, %13, %9 sc1\n\tglobal_load_dwordx4 %5, %14, %9 sc1\n\tglobal_load_dwordx4 %6, %15, %9 sc1\n\tglobal_load_dwordx4 %7, %16, %9 sc1\n\ts_waitcnt vmcnt(0)"
                 : "=&v"(v0), "=&v"(v1), "=&v"(v2), "=&v"(v3), "=&v"(u0), "=&v"(u1), "=&v"(u2), "=&v"(u3)
                 : "v"(o), "s"(ws), "v"(o + 15360), "v"(o + 30720), "v"(o + 46080), "v"(q), "v"(q + 15360), "v"(q + 30720), "v"(q + 46080) : "memory");
    for (;;) {
        if (__all(gran_ok(v0, tag) && gran_ok(v1, tag) && gran_ok(v2, tag) && gran_ok(v3, tag) && gran_ok(u0, tag) && gran_ok(u1, tag) && gran_ok(u2, tag) && gran_ok(u3, tag))) break;
        if (++spins > SPIN_CAP) { st_agent32(wsw + WS_ERR, code); break; }
        reask(v0, ws, o, tag); reask(v1, ws, o + 15360, tag); reask(v2, ws, o + 30720, tag); reask(v3, ws, o + 46080, tag);
        reask(u0, ws, q, tag); reask(u1, ws, q + 15360, tag); reask(u2, ws, q + 30720, tag); reask(u3, ws, q + 46080, tag);
        asm volatile("s_waitcnt vmcnt(0)" : "+v"(v0), "+v"(v1), "+v"(v2), "+v"(v3), "+v"(u0), "+v"(u1), "+v"(u2), "+v"(u3) :: "memory");
    }
    ra = gran_sum(v0, v1, v2, v3); rb = gran_sum(u0, u1, u2, u3);
}
struct Gather { u32x4 v0, v1, v2, v3, u0, u1, u2, u3; };
DI void gather_ask(const char* ws, u32 g0, u32 g1, Gather& G) {
    const u32 o = WS_MODG + g0 * 16, q = WS_MODG + g1 * 16;
    asm volatile("global_load_dwordx4 %0, %8, %9 sc1\n\tglobal_load_dwordx4 %1, %10, %9 sc1\n\tglobal_load_dwordx4 %2, %11, %9 sc1\n\tglobal_load_dwordx4 %3, %12, %9 sc1\n\t"
                 "global_load_dwordx4 %4, %13, %9 sc1\n\tglobal_load_dwordx4 %5, %14, %9 sc1\n\tglobal_load_dwordx4 %6, %15, %9 sc1\n\tglobal_load_dwordx4 %7, %16, %9 sc1"
                 : "=&v"(G.v0), "=&v"(G.v1), "=&v"(G.v2), "=&v"(G.v3), "=&v"(G.u0), "=&v"(G.u1), "=&v"(G.u2), "=&v"(G.u3)
                 : "v"(o), "s"(ws), "v"(o + 15360), "v"(o + 30720), "v"(o + 46080), "v"(q), "v"(q + 15360), "v"(q + 30720), "v"(q + 46080) : "memory");
}
DI void gather_finish(const char* ws, u32 g0, u32 g1, Gather& G, u64 tag, char* wsw, u32 code, f32x4& ra, f32x4& rb) {
    asm volatile("" : "+v"(G.v0), "+v"(G.v1), "+v"(G.v2), "+v"(G.v3), "+v"(G.u0), "+v"(G.u1), "+v"(G.u2), "+v"(G.u3) :: "memory");
    if (__all(gran_ok(G.v0, tag) && gran_ok(G.v1, tag) && gran_ok(G.v2, tag) && gran_ok(G.v3, tag) && gran_ok(G.u0, tag) && gran_ok(G.u1, tag) && gran_ok(G.u2, tag) && gran_ok(G.u3, tag))) {
        ra = gran_sum(G.v0, G.v1, G.v2, G.v3); rb = gran_sum(G.u0, G.u1, G.u2, G.u3);
    } else gather2(ws, g0, g1, tag, wsw, code, ra, rb);
}
DI void gather_strip(const char* ws, u32 gs, u32 gc, u32 strip, int l, u64 tag, char* wsw, u32 code) {
    f32x4 a, c;
    gather2(ws, l < 40 ? gs + l : gc + (l - 40), gc + 24 + (l & 15), tag, wsw, code, a, c);
    if (l >= 40) a = a + 1.f;
    c = c + 1.f;
    *(LAS u32x2*)(size_t)(strip + (l < 40 ? l * 8 : 320 + (l - 40) * 8)) = (u32x2){pack2(a[0], a[1]), pack2(a[2], a[3])};
    if (l < 16) *(LAS u32x2*)(size_t)(strip + 320 + (24 + l) * 8) = (u32x2){pack2(c[0], c[1]), pack2(c[2], c[3])};
}
DI void put_strip_spread(const f32x4& a, const f32x4& c, u32 strip, int l) {
    if (l < 40) *(LAS u32x2*)(size_t)(strip + l * 8) = (u32x2){pack2(a[0], a[1]), pack2(a[2], a[3])};
    else {
        const u32 p0 = pack2(a[0] + 1.f, a[1] + 1.f), p1 = pack2(a[2] + 1.f, a[3] + 1.f);
        *(LAS u32x4*)(size_t)(strip + 320 + (l - 40) * 16) = (u32x4){p0 & 0xffffu, p0 & 0xffff0000u, p1 & 0xffffu, p1 & 0xffff0000u};
    }
    if (l < 16) {
        const u32 p0 = pack2(c[0] + 1.f, c[1] + 1.f), p1 = pack2(c[2] + 1.f, c[3] + 1.f);
        *(LAS u32x4*)(size_t)(strip + 320 + (24 + l) * 16) = (u32x4){p0 & 0xffffu, p0 & 0xffff0000u, p1 & 0xffffu, p1 & 0xffff0000u};
    }
    if (l == 63) *(LAS u32x4*)(size_t)(strip + 960) = (u32x4){0, 0, 0, 0};
}
DI void poll_at(const char* ws, u32 off, int lane, u64 tag, char* wsw, u32 code) {
    u32 spins = 0;
    for (;;) {
        const u64 v = ld_agent(ws + off);
        if (__all(v == tag)) break;
        if (++spins > SPIN_CAP) { if (lane == 0) st_agent32(wsw + WS_ERR, code); break; }
        __builtin_amdgcn_s_sleep(1);
    }
}

template <int N> using IC = std::integral_constant<int, N>;
template <int I, int E, class F> DI void static_for(F&& f) { if constexpr (I < E) { f(IC<I>{}); static_for<I + 1, E>(f); } }
template <int N> DI void vmw() { asm volatile("s_waitcnt vmcnt(%0)" :: "i"(N) : "memory"); }
template <int N> DI void vmw1(u32x4& a) { asm volatile("s_waitcnt vmcnt(%1)" : "+v"(a) : "i"(N) : "memory"); }
template <int N> DI void vmw4(u32x4& a, u32x4& b, u32x4& c, u32x4& d) { asm volatile("s_waitcnt vmcnt(%4)" : "+v"(a), "+v"(b), "+v"(c), "+v"(d) : "i"(N) : "memory"); }
DI u32 opaque(u32 a) { asm("" : "+v"(a)); return a; }
DI float dot2(u32 a, u32 b, float c) { return __builtin_amdgcn_fdot2_f32_bf16(__builtin_bit_cast(bf16x2, a), __builtin_bit_cast(bf16x2, b), c, false); }
#define SB0() __builtin_amdgcn_sched_barrier(0)
#define SGB(m, n) __builtin_amdgcn_sched_group_barrier((m), (n), 0)
constexpr int M_MFMA = 0x8, M_DSR = 0x100;
#define Z4 ((f32x4){0.f, 0.f, 0.f, 0.f})
DI u32 lds_add(u32 a, u32 v) { return __hip_atomic_fetch_add((LAS u32*)(size_t)a, v, __ATOMIC_RELAXED, __HIP_MEMORY_SCOPE_WORKGROUP); }
DI u32 lds_ld(u32 a) { return __hip_atomic_load((LAS u32*)(size_t)a, __ATOMIC_RELAXED, __HIP_MEMORY_SCOPE_WORKGROUP); }
DI void worker_sync(u32 cnt, int lane) {
    asm volatile("s_waitcnt lgkmcnt(0)" ::: "memory");
    if (lane == 0) lds_add(cnt, 1u);
    for (u32 spins = 0; lds_ld(cnt) < 4u && spins < (1u << 22); ++spins) {}
    asm volatile("" ::: "memory");
}

__global__ __launch_bounds__(512, 2) void mega_kernel(Params p) {
    __shared__ __attribute__((aligned(16))) char ldsmem[163840];
    const u32 L0 = (u32)(uintptr_t)(LAS char*)ldsmem;
    const int tid = threadIdx.x, l = tid & 63;
    const int w = __builtin_amdgcn_readfirstlane(tid >> 6);
    const int b = blockIdx.x, xg = b & 7, sidx = b >> 3;
    const int rb = 4 * (xg >> 1) + (sidx >> 3), j = 8 * (xg & 1) + (sidx & 7);
    const int g = l >> 4, i16 = l & 15, q4 = i16 >> 2, p4 = l & 3;
    u32 wu0 = 0;
    if (w == 7) {
        const char* pc; u32 span;
        asm volatile("s_getpc_b64 %0\n.Lmk_code_from:\n\ts_mov_b32 %1, .Lmk_code_to-.Lmk_code_from" : "=s"(pc), "=s"(span));
        const u32 per = (span + 4095u) >> 12;
        const u32 off = min((sidx * per + min((u32)l, per - 1u)) * 128u, span - 4u);
        asm volatile("s_nop 0\n\tglobal_load_dword %0, %1, %2" : "=v"(wu0) : "v"(off), "s"(pc) : "memory");
    }
    char* const ws = p.ws;
    asm volatile("" :: "s"(p.x), "s"(p.wqkv), "s"(p.wo), "s"(p.wg), "s"(p.wu), "s"(p.wd), "s"(p.c), "s"(p.wmod), "s"(p.out), "s"(p.ws));
    u64 tag;
#define ASK_TAG(ep) u32x2 ep; asm volatile("global_load_dwordx2 %0, %1, %2" : "=v"(ep) : "v"(WS_EPOCH), "s"(ws) : "memory")
#define TAKE_TAG(ep) do { asm volatile("s_waitcnt vmcnt(0)" : "+v"(ep) :: "memory"); \
        tag = (((u64)(u32)__builtin_amdgcn_readfirstlane(ep[1]) << 32) | (u32)__builtin_amdgcn_readfirstlane(ep[0])) + 1; } while (0)
    const __amdgpu_buffer_rsrc_t wsr = __builtin_amdgcn_make_buffer_rsrc(ws, 0, WS_END, 0x00020000);
#define ST16C(v, off) __builtin_amdgcn_raw_buffer_store_b128((v), wsr, (off), 0, 16)
#define ST8C(v, off)  __builtin_amdgcn_raw_buffer_store_b64((v), wsr, (off), 0, 16)

    const int nj = j < 12 ? 7 : 6, Gj = j < 12 ? 7 * j : 84 + 6 * (j - 12);
    const bool wpwg = b < 216;
    const int wpm = b >= 108 ? 1 : 0, wpb = (b - 108 * wpm) >> 2, wpq = b & 3;
    u32x4 mw[5]; u32 mc[5];
    auto mod_ask = [&]() {
#pragma unroll
        for (int i = 0; i < 5; ++i) {
            const int r = 160 * (b & 3) + ((tid & 255) >> 3) + 32 * i;
            mw[i] = gld16<0>(p.wmod, r * 7680 + (b >> 2) * 128 + (tid & 7) * 16);
            mc[i] = gldu16(p.c, r * 2);
        }
    };
    auto mod_finish = [&]() {
        float acc[8];
#pragma unroll
        for (int e = 0; e < 8; ++e) acc[e] = 0.f;
#pragma unroll
        for (int i = 0; i < 5; ++i) {
            const float cv = bflo(mc[i]);
            const float sv = cv * __builtin_amdgcn_rcpf(1.f + __builtin_amdgcn_exp2f(-cv * LOG2E));
            const u32 slo = pack2(sv, 0.f), shi = slo << 16;
#pragma unroll
            for (int d = 0; d < 4; ++d) { acc[2 * d] = dot2(mw[i][d], slo, acc[2 * d]); acc[2 * d + 1] = dot2(mw[i][d], shi, acc[2 * d + 1]); }
        }
#pragma unroll
        for (int e = 0; e < 8; ++e) { const float v = acc[e]; acc[e] = xsum4(v + dppf<0x128>(v)); }
        if (l < 8) {
            stsf4(L0 + L_MODP + (w & 3) * 256 + l * 32, (f32x4){acc[0], acc[1], acc[2], acc[3]});
            stsf4(L0 + L_MODP + (w & 3) * 256 + l * 32 + 16, (f32x4){acc[4], acc[5], acc[6], acc[7]});
        }
        asm volatile("s_waitcnt lgkmcnt(0)" ::: "memory");
        u32 old = 0;
        if (l == 0) old = lds_add(L0 + L_CNT, 1u);
        if (__builtin_amdgcn_readfirstlane(old) == 3) {
            float v = 0.f;
#pragma unroll
            for (int ww = 0; ww < 4; ++ww) v += ldsf(L0 + L_MODP + ww * 256 + l * 4);
            const float v1 = dppf<0x55>(v), v2 = dppf<0xAA>(v), v3 = dppf<0xFF>(v), v0 = dppf<0x00>(v);
            const u32x4 gr = {packh(v0, v1), packh(v2, v3), (u32)tag, (u32)(tag >> 32)};
            if (p4 == 0) ST16C(gr, WS_MODG + ((b & 3) * 960 + (b >> 2) * 16 + (l >> 2)) * 16);
        }
    };
#define MOD_LANDED() asm volatile("s_waitcnt vmcnt(0)" : "+v"(mw[0]), "+v"(mw[1]), "+v"(mw[2]), "+v"(mw[3]), "+v"(mw[4]), "+v"(mc[0]), "+v"(mc[1]), "+v"(mc[2]), "+v"(mc[3]), "+v"(mc[4]) :: "memory")
    if (w >= 4) {
        __builtin_amdgcn_s_setprio(3);
        const int u = w - 4;
        const bool tw = b < 150;
        const int tb = b / 5, tk = b - 5 * tb, ts = 4 * tk + u;
        if (tw) {
            const char* src = (const char*)p.wqkv + ts * 122880 + tb * 128;
            const u32 voff = (l >> 3) * 3840 + (l & 7) * 16;
#pragma unroll
            for (int i = 0; i < 4; ++i) glds16(p.wqkv, src + i * 8 * 3840, voff, L0 + L_WTRAW + u * 4096 + i * 1024);
        }
        ASK_TAG(epl);
        BAR();
        __builtin_amdgcn_s_setprio(0);
        TAKE_TAG(epl);
        if (tw) {
            const u32 a = L0 + L_WTRAW + u * 4096 + (8 * g + q4) * 128 + 8 * p4;
#pragma unroll
            for (int t = 0; t < 4; ++t) {
                const u32 cc = 8 * tb + 2 * t + (i16 >> 3);
                const u32 seg = cc >= 160 ? 2 : cc >= 80 ? 1 : 0, wi = cc - 80 * seg, jj = (wi * 13108) >> 16, c5 = wi - 5 * jj;
                const u32 c = seg == 0 ? 10 + c5 : seg == 1 ? c5 : 5 + c5, nt = c >> 1;
                const u32 dst = WS_WT + jj * 153600 + (nt < 5 ? ts * 5120 + nt * 1024 : 102400 + ts * 2560 + (nt - 5) * 1024)
                              + (nt < 7 ? (g * 16 + 8 * (c & 1) + (i16 & 7)) * 16 : g * 128 + (i16 & 7) * 16);
                ST16C(ldtr2(a + 32 * t, a + 32 * t + 512), dst);
            }
        }
        vmw<0>();
        {
            asm volatile("s_waitcnt lgkmcnt(0)" ::: "memory");
            u32 old = 0;
            if (l == 0) old = lds_add(L0 + L_CNT + 12, 1u);
            if (tw && __builtin_amdgcn_readfirstlane(old) == 3 && l == 0) st_agent(ws + WS_FWT + b * FS, tag);
        }
        if (b >= 80 && b < 240) {
            if (!tw) __builtin_amdgcn_s_sleep(32);
            mod_ask(); MOD_LANDED(); mod_finish();
        }
        if (u == 0) {
            const int wh = l >= 30 ? 0 : l / 5, kr = l >= 30 ? 0 : l - 5 * wh;
            const int bl = (1280 * (wh >> 1) + 80 * j + 79 * (wh & 1)) >> 7;
            poll_at(ws, WS_FWT + (bl * 5 + kr) * FS, l, tag, ws, 13);
            for (u32 spins = 0; lds_ld(L0 + L_CNT + 12) < 4u && spins < (1u << 22); ++spins) {}
            if (l == 0) lds_add(L0 + L_CNT + 24, 1u);
        } else {
            for (u32 spins = 0; lds_ld(L0 + L_CNT + 24) < 1u && spins < (1u << 22); ++spins) {}
        }
        asm volatile("" ::: "memory");
        {
            const char* src = ws + WS_WT + j * 153600 + u * 1024;
            const u32 lo = l * 16;
#pragma unroll
            for (int i = 0; i < 25; ++i) glds16c(src + i * 4096, lo, L0 + L_WKV + u * 1024 + i * 4096);
        }
        VMCNT(0);
    } else {
        __builtin_amdgcn_s_setprio(2);
        if (b < 80) mod_ask();
        ASK_TAG(epw);
        if (tid < 16) *(LAS u32*)(size_t)(L0 + L_CNT + tid * 4) = 0u;
        BAR();
        TAKE_TAG(epw);
        if (b < 80) { MOD_LANDED(); mod_finish(); }
        {
            const char* src = (const char*)p.x + (rb * 128 + 8 * j) * 1280 + w * 1024;
            glds16(p.x, src, l * 16, L0 + L_XS + w * 1024);
            glds16(p.x, src + 4096, l * 16, L0 + L_XS + (w + 4) * 1024);
            if (w < 2) glds16(p.x, src + 8192, l * 16, L0 + L_XS + (w + 8) * 1024);
        }
        VMCNT(0);
        worker_sync(L0 + L_CNT + 16, l);
        const int tok = l & 7;
        u32x4 xr[3];
#pragma unroll
        for (int i = 0; i < 3; ++i) xr[i] = lds128(L0 + L_XS + tok * 1280 + (20 * w + (i < 2 ? (l >> 3) + 8 * i : 16 + ((l >> 3) & 3))) * 16);
        {
            float s1 = 0.f, s2 = 0.f;
#pragma unroll
            for (int i = 0; i < 3; ++i) {
                float a1 = 0.f, a2 = 0.f;
#pragma unroll
                for (int d = 0; d < 4; ++d) { a1 = dot2(xr[i][d], 0x3F803F80u, a1); a2 = dot2(xr[i][d], xr[i][d], a2); }
                if (i < 2 || l < 32) { s1 += a1; s2 += a2; }
            }
            s1 += dppf<0x128>(s1); s2 += dppf<0x128>(s2);
            s1 = xsum4(s1); s2 = xsum4(s2);
            if (l < 8) stsf2(L0 + L_STP + (w * 8 + l) * 8, (f32x2){s1, s2});
        }
        gather_strip(ws, 40 * w, 160 + 40 * w, L0 + L_GS + w * 640, l, tag, ws, 1);
        worker_sync(L0 + L_CNT + 4, l);
        {
            f32x2 st = {0.f, 0.f};
#pragma unroll
            for (int ww = 0; ww < 4; ++ww) st += *(LAS f32x2*)(size_t)(L0 + L_STP + (ww * 8 + tok) * 8);
            const float mu = st[0] * (1.f / 640.f), var = fmaxf(st[1] * (1.f / 640.f) - mu * mu, 0.f);
            const float rs = __builtin_amdgcn_rsqf(var + 1e-5f), nmr = -mu * rs;
#pragma unroll
            for (int i = 0; i < 3; ++i) {
                const int cl = i < 2 ? (l >> 3) + 8 * i : 16 + ((l >> 3) & 3);
                const u32x4 sa = lds128(L0 + L_GS + w * 640 + cl * 16), gg = lds128(L0 + L_GS + w * 640 + 320 + cl * 16);
                u32x4 h;
#pragma unroll
                for (int d = 0; d < 4; ++d)
                    h[d] = pack2((bflo(xr[i][d]) * rs + nmr) * bflo(gg[d]) + bflo(sa[d]), (bfhi(xr[i][d]) * rs + nmr) * bfhi(gg[d]) + bfhi(sa[d]));
                if (i < 2 || l < 32) ST16C(h, WS_H + (((rb * 8 + (j >> 1)) * 80 + 20 * w + cl) * 16 + 8 * (j & 1) + tok) * 16);
            }
        }
        VMCNT(0);
        worker_sync(L0 + L_CNT + 8, l);
        if (tid == 0) st_agent(ws + WS_FH + (rb * 16 + j) * FS, tag);
        if (w == 0) poll_flags(ws + WS_FH + rb * 16 * FS, 16, l, tag, ws, 11);
        __builtin_amdgcn_s_setprio(0);
    }
    BAR();
    asm volatile("s_waitcnt vmcnt(0)" : "+v"(wu0) :: "memory");

    u32x4 xa[20];
    const u32 lo16 = l * 16;
    const char* hsrc = ws + WS_H + (rb * 8 + w) * 80 * 256;
#define XL(s) xa[s] = gld16c(((s) & 3) * 1024, hsrc + ((s) >> 2) * 4096, lo16)
    XL(0); XL(1); XL(2); XL(3); XL(4); XL(5);
    const char* kvx = ws + WS_KV + (rb * 16 + j) * 22528;
    const char* wtq = ws + WS_WT + j * 153600 + (100 + w) * 1024;
    {
        f32x4 acc[5];
#pragma unroll
        for (int n = 0; n < 5; ++n) acc[n] = Z4;
        const u32 b0 = L0 + L_WKV + lo16;
        u32x4 bf[3][5];
#pragma unroll
        for (int n = 0; n < 5; ++n) { bf[0][n] = lds128(b0 + n * 1024); bf[1][n] = lds128(b0 + 5120 + n * 1024); }
        static_for<0, 20>([&](auto S) {
            constexpr int s = S;
            vmw1<(s < 14 ? 5 : 6)>(xa[s]);
            if constexpr (s + 6 < 20) XL(s + 6);
            if constexpr (s >= 13) { if (s < 19 || w < 2) glds16c(wtq + (s - 13) * 8192, lo16, L0 + L_WKV + (100 + w) * 1024 + (s - 13) * 8192); }
            SB0();
#pragma unroll
            for (int n = 0; n < 5; ++n) {
                if (s < 18) bf[(s + 2) % 3][n] = lds128(b0 + (s + 2) * 5120 + n * 1024);
                acc[n] = mfma(bf[s % 3][n], xa[s], acc[n]);
            }
#pragma unroll
            for (int n = 0; n < 5; ++n) { SGB(M_DSR, 1); SGB(M_MFMA, 1); }
            SB0();
        });
        BAR();
        const int row = 256 + 16 * w + i16;
#pragma unroll
        for (int n = 0; n < 5; ++n) {
            const int Q = 4 * n + g;
            const u32x2 v = {pack2(acc[n][0], acc[n][1]), pack2(acc[n][2], acc[n][3])};
            const int wi = Q - 10, hd = wi >= 5 ? 1 : 0;
            const u32 a = Q < 10 ? L_K + row * 80 + 8 * Q : L_V + row * 96 + 48 * hd + 8 * (wi - 5 * hd);
            sts64(L0 + a, v);
        }
        if (g < 2) sts64(L0 + L_V + row * 96 + 48 * g + 40, (u32x2){0x3F80u, 0u});
        if (rb < 2) {
            const int nrow = rb == 0 ? 256 : 128;
            for (int i = tid; i < nrow * 5; i += 512) sts128(L0 + L_K + i * 16, (u32x4){0, 0, 0, 0});
            for (int i = tid; i < nrow * 6; i += 512) sts128(L0 + L_V + i * 16, (u32x4){0, 0, 0, 0});
        }
        vmw<0>();
        BAR();
    }
#undef XL
    {
        u32x4 kvd[3];
#pragma unroll
        for (int e = 0; e < 3; ++e) {
            const int n = min(w + 8 * e, 21);
            kvd[e] = lds128(L0 + (n < 10 ? L_K + 20480 + n * 1024 : L_V + 24576 + (n - 10) * 1024) + lo16);
        }
        const u32 kvo = WS_KV + (rb * 16 + j) * 22528 + w * 1024 + lo16;
        const u16* wroot = wpm ? p.wu : p.wg;
        const char* wsrc = (const char*)wroot + (160 * wpq + 8 * w) * 3456 + wpb * 128;
        const u32 wvo = (l >> 3) * 3456 + (l & 7) * 16;
        f32x4 acc[3];
#pragma unroll
        for (int n = 0; n < 3; ++n) acc[n] = Z4;
        const u32 bq = L0 + L_WQ + lo16, bq2 = L0 + L_WQ + 2048 + g * 128 + (i16 & 7) * 16;
#define QRD(s, n) lds128(((n) < 2 ? bq + (n) * 1024 : bq2) + (s) * 2560)
        u32x4 bf[3][3];
#pragma unroll
        for (int n = 0; n < 3; ++n) { bf[0][n] = QRD(0, n); bf[1][n] = QRD(1, n); }
        Gather G;
        const u32 gg0 = l < 40 ? 480 + 40 * wpq + l : 640 + 40 * wpq + (l - 40), gg1 = 640 + 40 * wpq + 24 + (l & 15);
        static_for<0, 20>([&](auto S) {
            constexpr int s = S;
            if constexpr (s == 6) { if (w == 7 && wpwg) gather_ask(ws, gg0, gg1, G); }
            if constexpr (s == 0) { if (rb < 15) ST16C(kvd[0], kvo); }
            if constexpr (s == 1) { if (rb < 15) ST16C(kvd[1], kvo + 8192); }
            if constexpr (s == 2) { if (rb < 15 && w < 6) ST16C(kvd[2], kvo + 16384); }
            if constexpr (s == 14) {
                vmw<0>();
                u32 old = 0;
                if (l == 0) old = lds_add(L0 + L_CNT + 28, 1u);
                if (__builtin_amdgcn_readfirstlane(old) == 7 && l == 0) st_agent(ws + WS_FKV + (j * 16 + rb) * FS, tag);
                if (w == 7 && wpwg) { f32x4 ra, rc; gather_finish(ws, gg0, gg1, G, tag, ws, 2, ra, rc); put_strip_spread(ra, rc, L0 + L_SB, l); }
            }
            if constexpr (s == 15) { if (wpwg) glds16(wroot, wsrc, wvo, L0 + L_WPRAW + w * 1024); }
            if constexpr (s == 16) { if (wpwg) glds16(wroot, wsrc + 64 * 3456, wvo, L0 + L_WPRAW + (w + 8) * 1024); }
            if constexpr (s == 17) { if (wpwg && w < 4) glds16(wroot, wsrc + 128 * 3456, wvo, L0 + L_WPRAW + (w + 16) * 1024); }
            SB0();
#pragma unroll
            for (int n = 0; n < 3; ++n) {
                if (s < 18) bf[(s + 2) % 3][n] = QRD(s + 2, n);
                acc[n] = mfma(bf[s % 3][n], xa[s], acc[n]);
            }
#pragma unroll
            for (int n = 0; n < 3; ++n) { SGB(M_DSR, 1); SGB(M_MFMA, 1); }
            SB0();
        });
#undef QRD
#pragma unroll
        for (int n = 0; n < 3; ++n) {
            const int Q = 4 * n + g;
            const u32x2 v = {pack2(acc[n][0] * QSCALE, acc[n][1] * QSCALE), pack2(acc[n][2] * QSCALE, acc[n][3] * QSCALE)};
            if (Q < 10) sts64(L0 + L_Q + (16 * w + i16) * 80 + 8 * Q, v);
        }
    }
    if (w == 0 && rb >= 1) poll_flags(ws + WS_FKV + (j * 16 + (rb >= 2 ? rb - 2 : rb - 1)) * FS, rb >= 2 ? 2 : 1, l, tag, ws, 3);
    VMCNT(0);
    BAR();
    if (rb >= 1) {
#pragma unroll
        for (int e = 0; e < 3; ++e) {
            const int n = w + 8 * e;
            if (n < 22) {
                const u32 d = n < 10 ? L_K + n * 1024 : L_V + (n - 10) * 1024, st = n < 10 ? 10240 : 12288;
                glds16c(kvx - 16 * 22528 + n * 1024, lo16, L0 + d + st);
                if (rb >= 2) glds16c(kvx - 32 * 22528 + n * 1024, lo16, L0 + d);
            }
        }
    }
    auto wp_slot = [&](int gam, int& jj, int& slot) {
        jj = gam < 84 ? gam / 7 : 12 + (gam - 84) / 6;
        const int ar = gam - (jj < 12 ? 7 * jj : 84 + 6 * (jj - 12)), a = (jj < 12 || ar < 3) ? ar : ar + 1;
        slot = a < 3 ? 2 * a + wpm : a == 3 ? (wpm ? 13 : 6) : 7 + 2 * (a - 4) + wpm;
    };
    if (wpwg) {
        const u32 one = i16 == 0 ? 0x3F803F80u : 0u;
        const u32x4 ones = {one, one, one, one};
#pragma unroll
        for (int i = 0; i < 3; ++i) {
            if (i < 2 || w < 4) {
                const int f = w + 8 * i, ks = f >> 2, t16 = f & 3;
                int jj, slot; wp_slot(4 * wpb + t16, jj, slot);
                const u32 a = L0 + L_WPRAW + (32 * ks + 8 * g + q4) * 128 + 32 * t16 + 8 * p4;
                u32x4 wr = ldtr2(a, a + 512);
                const u32x4 ga = lds128(L0 + L_G2 + (32 * ks + 8 * g) * 4), gc = lds128(L0 + L_G2 + (32 * ks + 8 * g) * 4 + 16);
                const u32x4 sbz = lds128(L0 + (i16 == 0 ? L_SB + (32 * ks + 8 * g) * 2 : L_Z16));
                const f32x4 vs = mfma(wr, sbz, Z4);
                u32x4 o; float t0, t1, t2, t3, t4, t5, t6, t7;
                asm volatile("v_dot2_f32_bf16 %4, %12, %16, 0\n\tv_dot2_f32_bf16 %5, %12, %17, 0\n\tv_dot2_f32_bf16 %6, %13, %18, 0\n\tv_dot2_f32_bf16 %7, %13, %19, 0\n\t"
                             "v_dot2_f32_bf16 %8, %14, %20, 0\n\tv_dot2_f32_bf16 %9, %14, %21, 0\n\tv_dot2_f32_bf16 %10, %15, %22, 0\n\tv_dot2_f32_bf16 %11, %15, %23, 0\n\t"
                             "v_cvt_pk_bf16_f32 %0, %4, %5\n\tv_cvt_pk_bf16_f32 %1, %6, %7\n\tv_cvt_pk_bf16_f32 %2, %8, %9\n\ts_nop 0\n\tv_cvt_pk_bf16_f32 %3, %10, %11\n\ts_nop 1"
                             : "=&v"(o[0]), "=&v"(o[1]), "=&v"(o[2]), "=&v"(o[3]),
                               "=&v"(t0), "=&v"(t1), "=&v"(t2), "=&v"(t3), "=&v"(t4), "=&v"(t5), "=&v"(t6), "=&v"(t7)
                             : "v"(wr[0]), "v"(wr[1]), "v"(wr[2]), "v"(wr[3]), "v"(ga[0]), "v"(ga[1]), "v"(ga[2]), "v"(ga[3]), "v"(gc[0]), "v"(gc[1]), "v"(gc[2]), "v"(gc[3]));
                const f32x4 us = mfma(o, ones, Z4);
                ST16C(o, WS_WP + ((jj * 20 + 5 * wpq + ks) * 14 + slot) * 1024 + lo16);
                if (i16 == 0) { stsf4(L0 + L_UVL + f * 128 + g * 16, us); stsf4(L0 + L_UVL + f * 128 + 64 + g * 16, vs); }
            }
        }
    }
    if (wpwg) { if (w < 4) vmw<3>(); else vmw<2>(); } else vmw<0>();
    BAR();
    if (wpwg && w == 3) {
#pragma unroll
        for (int e = 0; e < 2; ++e) {
            const int t16 = 2 * e + (l >> 5);
            int jj, slot; wp_slot(4 * wpb + t16, jj, slot);
            float v = 0.f;
#pragma unroll
            for (int ks = 0; ks < 5; ++ks) v += ldsf(L0 + L_UVL + (4 * ks + t16) * 128 + (l & 31) * 4);
            st_agent32(ws + WS_UV + ((jj * 14 + slot) * 4 + wpq) * 128 + (l & 31) * 4, __float_as_uint(v));
        }
    }
    const int hf = j & 1, cb = j >> 1;
    u32 wvoff[5];
#pragma unroll
    for (int i = 0; i < 5; ++i) {
        const u32 sg = 64 * i + l, pos = (sg * 6554) >> 16, c = sg - pos * 10, k = pos ^ (((pos >> 3) & 1) << 2);
        wvoff[i] = k * 1280 + c * 16;
    }
    const char* wosrc = (const char*)p.wo + cb * 160;
#define WO_STEP(sx) do { _Pragma("unroll") for (int i = 0; i < 5; ++i) glds16(p.wo, wosrc + (sx) * 32 * 1280, wvoff[i], L0 + L_WO + (sx) * 5120 + i * 1024); } while (0)

    {
        const int hd = w & 1, qi = w >> 1;
        const int kr0 = 32 * qi;
        u32x4 qf[2];
#pragma unroll
        for (int a = 0; a < 2; ++a) {
            const u32 qa = L0 + L_Q + (kr0 + 16 * a + i16) * 80 + hd * 40;
            const u32x2 q0 = lds64(qa + 8 * g);
            u32x2 q1 = lds64(qa + 32);
            if (g != 0) q1 = (u32x2){0, 0};
            qf[a] = (u32x4){q0[0], q0[1], q1[0], q1[1]};
        }
        const u32 kb0 = L0 + L_K + (kr0 + i16) * 80 + hd * 40 + 8 * g, kb1 = L0 + L_K + (kr0 + i16) * 80 + hd * 40 + 32;
        f32x4 mfirst, mlast;
#pragma unroll
        for (int r = 0; r < 4; ++r) { const bool lo = 4 * g + r <= i16; mfirst[r] = lo ? -INFINITY : 0.f; mlast[r] = lo ? 0.f : -INFINITY; }
        f32x4 s0[17], s1[17];
        {
            u32x4 kf[6];
#define KRD(t) do { const u32x2 k0 = lds64(kb0 + (t) * 1280), k1 = lds64(kb1 + (t) * 1280); kf[(t) % 6] = (u32x4){k0[0], k0[1], k1[0], k1[1]}; } while (0)
            KRD(0); KRD(1); KRD(2); KRD(3); KRD(4);
            static_for<0, 18>([&](auto T) {
                constexpr int t = T;
                SB0();
                if constexpr (t < 13) KRD(t + 5);
                if constexpr (t < 17) s0[t] = mfma(kf[t % 6], qf[0], t == 0 ? mfirst : t == 16 ? mlast : Z4);
                if constexpr (t > 0) s1[t - 1] = mfma(kf[t % 6], qf[1], t == 1 ? mfirst : t == 17 ? mlast : Z4);
                SGB(M_DSR, 2); SGB(M_MFMA, 2);
                SB0();
            });
#undef KRD
        }
        WO_STEP(4 + w);
        if (rb < 2) {
            const int fv = rb == 1 ? 128 : 256;
#pragma unroll
            for (int t = 0; t < 16; ++t) {
                const bool d0 = kr0 + 16 * t < fv, d1 = kr0 + 16 * t + 16 < fv;
#pragma unroll
                for (int r = 0; r < 4; ++r) { s0[t][r] = d0 ? -INFINITY : s0[t][r]; s1[t][r] = d1 ? -INFINITY : s1[t][r]; }
            }
        }
        float m0 = fmaxf(s0[0][0], s0[0][1]), m1 = fmaxf(s1[0][0], s1[0][1]), n0 = fmaxf(s0[0][2], s0[0][3]), n1 = fmaxf(s1[0][2], s1[0][3]);
#pragma unroll
        for (int t = 1; t < 17; ++t) {
            m0 = fmaxf(fmaxf(m0, s0[t][0]), s0[t][1]); m1 = fmaxf(fmaxf(m1, s1[t][0]), s1[t][1]);
            n0 = fmaxf(fmaxf(n0, s0[t][2]), s0[t][3]); n1 = fmaxf(fmaxf(n1, s1[t][2]), s1[t][3]);
        }
        m0 = fmaxf(m0, n0); m1 = fmaxf(m1, n1);
        m0 = xmax4(m0); m1 = xmax4(m1);
        WO_STEP(12 + w);
        f32x4 o00 = Z4, o01 = Z4, o10 = Z4, o11 = Z4;
        const u32 vb = L0 + L_V + (kr0 + 4 * g + q4) * 96 + hd * 48 + 8 * p4;
        auto pv = [&](auto SUB) {
            constexpr bool sub = SUB;
            u32x4 vf[2][2];
            vf[0][0] = ldtr2(vb, vb + 16 * 96); vf[0][1] = ldtr2(vb + 32, vb + 16 * 96 + 32);
#define EX(v, m) __builtin_amdgcn_exp2f(sub ? (v) - (m) : (v))
            static_for<0, 9>([&](auto U) {
                constexpr int u = U;
                if constexpr (u < 8) { const u32 a = vb + (u + 1) * 32 * 96; vf[(u + 1) & 1][0] = ldtr2(a, a + 16 * 96); vf[(u + 1) & 1][1] = ldtr2(a + 32, a + 16 * 96 + 32); }
                u32x4 pa, pb;
                pa[0] = pack2(EX(s0[2 * u][0], m0), EX(s0[2 * u][1], m0)); pa[1] = pack2(EX(s0[2 * u][2], m0), EX(s0[2 * u][3], m0));
                if constexpr (u < 8) { pa[2] = pack2(EX(s0[2 * u + 1][0], m0), EX(s0[2 * u + 1][1], m0)); pa[3] = pack2(EX(s0[2 * u + 1][2], m0), EX(s0[2 * u + 1][3], m0)); }
                else { pa[2] = 0; pa[3] = 0; }
                if constexpr (u > 0) { pb[0] = pack2(EX(s1[2 * u - 1][0], m1), EX(s1[2 * u - 1][1], m1)); pb[1] = pack2(EX(s1[2 * u - 1][2], m1), EX(s1[2 * u - 1][3], m1)); }
                else { pb[0] = 0; pb[1] = 0; }
                pb[2] = pack2(EX(s1[2 * u][0], m1), EX(s1[2 * u][1], m1)); pb[3] = pack2(EX(s1[2 * u][2], m1), EX(s1[2 * u][3], m1));
                o00 = mfma(vf[u & 1][0], pa, o00); o01 = mfma(vf[u & 1][1], pa, o01);
                o10 = mfma(vf[u & 1][0], pb, o10); o11 = mfma(vf[u & 1][1], pb, o11);
                SB0();
            });
#undef EX
        };
        if (__all(fabsf(m0) <= 64.f && fabsf(m1) <= 64.f)) pv(std::false_type{}); else pv(std::true_type{});
        const float i0 = __builtin_amdgcn_rcpf(__shfl(o01[0], i16 + 16)), i1 = __builtin_amdgcn_rcpf(__shfl(o11[0], i16 + 16));
        const u32 oa = L0 + L_Q + (kr0 + i16) * 80 + hd * 40;
        sts64(oa + 8 * g, (u32x2){pack2(o00[0] * i0, o00[1] * i0), pack2(o00[2] * i0, o00[3] * i0)});
        sts64(oa + 1280 + 8 * g, (u32x2){pack2(o10[0] * i1, o10[1] * i1), pack2(o10[2] * i1, o10[3] * i1)});
        if (g == 0) {
            sts64(oa + 32, (u32x2){pack2(o01[0] * i0, o01[1] * i0), pack2(o01[2] * i0, o01[3] * i0)});
            sts64(oa + 1280 + 32, (u32x2){pack2(o11[0] * i1, o11[1] * i1), pack2(o11[2] * i1, o11[3] * i1)});
        }
    }
    BAR();
    {
        const int tok = 64 * (w & 1) + l;
        const u32 oo = WS_O + (((rb * 8 + (tok >> 4)) * 80 + 5 * j) * 16 + (tok & 15)) * 16;
        ST16C(lds128(L0 + L_Q + tok * 80 + (w >> 1) * 16), oo + (w >> 1) * 256);
        if (w < 2) ST16C(lds128(L0 + L_Q + tok * 80 + 64), oo + 4 * 256);
    }
    VMCNT(0);
    BAR();
    if (tid == 0) {
        st_agent(ws + WS_FO + (rb * 16 + j) * FS, tag);
        if (wpwg) st_agent(ws + WS_FWP + b * FS, tag);
    }

    const int wm = w & 3, wk = w >> 2;
    const int mt3 = rb * 8 + 4 * hf + wm;
    u32x2 x1k[5];
    {
        const int wdu = b + 256 * (w - 6);
        const bool wdw = w >= 6 && wdu < 432;
        if (w >= 1 && w < 5) {
#pragma unroll
            for (int i = 0; i < 5; ++i) glds16(p.wo, wosrc + (w - 1) * 32 * 1280, wvoff[i], L0 + L_WO + (w - 1) * 5120 + i * 1024);
        } else if (w >= 5) {
#pragma unroll
            for (int e = 0; e < 4; ++e) {
                const int pc = w - 5 + 3 * e;
                if (pc < 10) {
                    const u32 sg = 64 * pc + l, r = (sg * 6554) >> 16, c = sg - r * 10;
                    glds16(p.x, (const char*)p.x + (rb * 128 + 64 * hf) * 1280 + cb * 160, r * 1280 + c * 16, L0 + L_XR + pc * 1024);
                }
            }
            if (wdw) {
                const int ucb = wdu / 54, us = wdu - 54 * ucb;
#pragma unroll
                for (int i = 0; i < 5; ++i) glds16(p.wd, (const char*)p.wd + ucb * 160 + us * 32 * 1280, wvoff[i], L0 + L_WDRAW + (w - 6) * 5120 + i * 1024);
            }
        }
        if (w == 1 && l < 20) stsf4(L0 + L_GA + l * 16, gather1(ws, 320 + 20 * cb + l, tag, ws, 4));
        if (w == 0) {
            const int bl = min((Gj >> 2) + ((l >> 2) % 3), (Gj + nj - 1) >> 2);
            poll_at(ws, WS_FWP + (108 * (l >= 12 && l < 24 ? 1 : 0) + 4 * bl + (l & 3)) * FS, l, tag, ws, 6);
            poll_flags(ws + WS_FO + rb * 16 * FS, 16, l, tag, ws, 5);
        }
        if (wdw) vmw<5>(); else vmw<0>();
        BAR();
        u32x4 a[10];
        const char* asrc = ws + WS_O + (mt3 * 80 + 40 * wk) * 256;
#define AL(s) a[s] = gld16c(((s) & 3) * 1024, asrc + ((s) >> 2) * 4096, lo16)
        AL(0); AL(1); AL(2); AL(3);
        f32x4 acc[5];
#pragma unroll
        for (int n = 0; n < 5; ++n) acc[n] = Z4;
        const u32 wb = L0 + L_WO + wk * 51200;
        const u32 b0 = wb + 160 * (8 * g + 4 * (g & 1) + q4) + 8 * p4, b1 = wb + 160 * (8 * g + 4 * ((g & 1) ^ 1) + q4) + 8 * p4;
        u32x4 bf[3][5];
#pragma unroll
        for (int n = 0; n < 5; ++n) { bf[0][n] = ldtr2(b0 + n * 32, b1 + n * 32); bf[1][n] = ldtr2(b0 + 5120 + n * 32, b1 + 5120 + n * 32); }
        static_for<0, 10>([&](auto S) {
            constexpr int s = S;
            vmw1<(s < 7 ? 3 : 9 - s)>(a[s]);
            if constexpr (s + 4 < 10) AL(s + 4);
            SB0();
#pragma unroll
            for (int n = 0; n < 5; ++n) {
                if (s < 8) bf[(s + 2) % 3][n] = ldtr2(b0 + (s + 2) * 5120 + n * 32, b1 + (s + 2) * 5120 + n * 32);
                acc[n] = mfma(bf[s % 3][n], a[s], acc[n]);
            }
#pragma unroll
            for (int n = 0; n < 5; ++n) { SGB(M_DSR, 2); SGB(M_MFMA, 1); }
            SB0();
        });
#undef AL
        if (wk == 1) {
#pragma unroll
            for (int n = 0; n < 5; ++n) stsf4(L0 + L_RED3 + (wm * 5 + n) * 1024 + lo16, acc[n]);
        }
        BAR();
        if (wk == 0) {
            float s1 = 0.f, s2 = 0.f;
#pragma unroll
            for (int n = 0; n < 5; ++n) {
                const f32x4 v = acc[n] + ldsf4(L0 + L_RED3 + (wm * 5 + n) * 1024 + lo16);
                const f32x4 ga = ldsf4(L0 + L_GA + (16 * n + 4 * g) * 4);
                const u32x2 xr = lds64(L0 + L_XR + (16 * wm + i16) * 160 + 32 * n + 8 * g);
                const u32x2 o = {pack2(bflo(xr[0]) + ga[0] * v[0], bfhi(xr[0]) + ga[1] * v[1]), pack2(bflo(xr[1]) + ga[2] * v[2], bfhi(xr[1]) + ga[3] * v[3])};
                x1k[n] = o;
                s1 = dot2(o[0], 0x3F803F80u, s1); s1 = dot2(o[1], 0x3F803F80u, s1);
                s2 = dot2(o[0], o[0], s2); s2 = dot2(o[1], o[1], s2);
            }
            const u32 xo = WS_X1 + ((mt3 * 80 + 10 * cb) * 16 + i16) * 16;
#pragma unroll
            for (int n = 0; n < 4; n += 2) {
                const auto a0 = __builtin_amdgcn_permlane16_swap(x1k[n][0], x1k[n + 1][0], false, false);
                const auto a1 = __builtin_amdgcn_permlane16_swap(x1k[n][1], x1k[n + 1][1], false, false);
                ST16C(((u32x4){a0[0], a1[0], a0[1], a1[1]}), xo + (2 * (n + (g & 1)) + (g >> 1)) * 256);
            }
            ST8C(x1k[4], xo + (8 + (g >> 1)) * 256 + 8 * (g & 1));
            s1 = xsum4(s1); s2 = xsum4(s2);
            if (l < 16) ST8C(((u32x2){__float_as_uint(s1), __float_as_uint(s2)}), WS_STAT + ((rb * 8 + cb) * 128 + 64 * hf + 16 * wm + l) * 8);
            vmw<0>();
        } else {
#pragma unroll
            for (int n = 0; n < 5; ++n) x1k[n] = (u32x2){0, 0};
            if (wdw) {
                const u32 wr = L0 + L_WDRAW + (w - 6) * 5120;
                const u32 r0 = wr + 160 * (8 * g + 4 * (g & 1) + q4) + 8 * p4, r1 = wr + 160 * (8 * g + 4 * ((g & 1) ^ 1) + q4) + 8 * p4;
#pragma unroll
                for (int n = 0; n < 5; ++n) ST16C(ldtr2(r0 + n * 32, r1 + n * 32), WS_WD + (wdu * 5 + n) * 1024 + lo16);
            }
        }
        BAR();
        if (tid == 0) st_agent(ws + WS_FX1 + (rb * 16 + j) * FS, tag);
    }

    const char* wdsrc = ws + WS_WD + (cb * 54 + wk * 27) * 5120;
    const u32 r5w = L0 + wk * 20480 + wm * 5120;
#define P5_B1(t, i) do { if ((i) == 0 || (i) == 4) glds16c(wdsrc + min(4 * (t) + wm, 26) * 5120 + (i) * 1024, lo16, r5w + r5_slot(t) + (i) * 1024); \
        else glds16c_next(((i) & 3) * 1024, wdsrc + min(4 * (t) + wm, 26) * 5120, lo16); } while (0)
    {
        const int wn = w >> 2;
        const int bst = w < 4 ? 4 * w : 16 + 3 * (w - 4);
        const char* wpj = ws + WS_WP + (j * 20 * 14 + bst) * 1024;
        const char* x1b = ws + WS_X1 + (rb * 8 + w) * 80 * 256;
        const u32 r4a = L0 + L_R4 + w * 2048, r4b = L0 + L_R4 + 16384 + bst * 1024;
#define P4_B1(t, i) do { if ((i) == 0) glds16c(wpj + (t) * 28 * 1024, lo16, r4b + ((t) % 3) * R4_STAGE); else glds16c_next((i) * 1024, wpj + (t) * 28 * 1024, lo16); } while (0)
#define P4_A1(t, i) do { if ((i) == 0) glds16c(x1b + 8 * (t) * 256, lo16, r4a + ((t) % 3) * R4_STAGE); else glds16c_next(1024, x1b + 8 * (t) * 256, lo16); } while (0)
#define P4_B(t) do { P4_B1(t, 0); P4_B1(t, 1); P4_B1(t, 2); if (w < 4) P4_B1(t, 3); } while (0)
        P4_B(0); P4_B(1); P4_B(2);
        if (w == 0) poll_flags(ws + WS_FX1 + rb * 16 * FS, 16, l, tag, ws, 7);
        BAR();
        glds16c(ws + WS_STAT + rb * 8192 + w * 1024, lo16, L0 + L_ST4 + w * 1024);
        if (w < 7) glds16c(ws + WS_UV + j * 7168 + w * 1024, lo16, L0 + L_UV + w * 1024);
        P4_A1(0, 0); P4_A1(0, 1); P4_A1(1, 0); P4_A1(1, 1); P4_A1(2, 0); P4_A1(2, 1);
        f32x4 acc[7][2];
#pragma unroll
        for (int n = 0; n < 7; ++n) { acc[n][0] = Z4; acc[n][1] = Z4; }
        const u32 ra = opaque(L0 + L_R4 + 2 * wm * 2048 + lo16), rbb = opaque(L0 + L_R4 + 16384 + 7 * wn * 1024 + lo16);
        u32x4 fa[2][2], fb[2][7];
#define P4_RA(buf, t, ks) do { const u32 so = ((t) % 3) * R4_STAGE + (ks) * 1024; fa[buf][0] = lds128(ra + so); fa[buf][1] = lds128(ra + so + 2048); } while (0)
#define P4_RB(buf, t, ks, n) fb[buf][n] = lds128(rbb + ((t) % 3) * R4_STAGE + (ks) * 14336 + (n) * 1024)
#define P4_MM(buf, n) do { acc[n][0] = mfma(fb[buf][n], fa[buf][0], acc[n][0]); acc[n][1] = mfma(fb[buf][n], fa[buf][1], acc[n][1]); } while (0)
#define P4_HALF(buf, rd, t, ks, V0, V1, V2) do { \
            SB0(); if (rd) { P4_RA((buf) ^ 1, t, ks); P4_RB((buf) ^ 1, t, ks, 0); } P4_MM(buf, 0); SB0(); V0; \
            SB0(); if (rd) P4_RB((buf) ^ 1, t, ks, 1); P4_MM(buf, 1); SB0(); \
            SB0(); if (rd) P4_RB((buf) ^ 1, t, ks, 2); P4_MM(buf, 2); SB0(); V1; \
            SB0(); if (rd) P4_RB((buf) ^ 1, t, ks, 3); P4_MM(buf, 3); SB0(); \
            SB0(); if (rd) P4_RB((buf) ^ 1, t, ks, 4); P4_MM(buf, 4); SB0(); V2; \
            SB0(); if (rd) P4_RB((buf) ^ 1, t, ks, 5); P4_MM(buf, 5); SB0(); \
            SB0(); if (rd) P4_RB((buf) ^ 1, t, ks, 6); P4_MM(buf, 6); SB0(); } while (0)
        VMCNT(4);
        BAR();
        if (tid == 0) st_agent(ws + WS_FW2 + b * FS, tag);
        u32x2 wdf = {0u, 0u};
        const u32 wdo = WS_FW2 + ((54 * cb + (l < 54 ? l : 0)) & 255) * FS;
        P4_RA(0, 0, 0);
#pragma unroll
        for (int n = 0; n < 7; ++n) P4_RB(0, 0, 0, n);
        static_for<0, 10>([&](auto T) {
            constexpr int t = T;
            constexpr bool f = t >= 1 && t + 2 <= 9, sN = t + 3 <= 9;
            if constexpr (t < 9) P4_HALF(0, true, t, 1, if (f && w < 4) P4_B1(t + 2, 3), if (f) P4_A1(t + 2, 0), if (f) P4_A1(t + 2, 1));
            else P4_HALF(0, true, t, 1, P5_B1(0, 3), P5_B1(0, 4), P5_B1(1, 0));
            if constexpr (t == 5) {
                if (tid < 128) {
                    f32x2 st = {0.f, 0.f};
#pragma unroll
                    for (int c = 0; c < 8; ++c) st += *(LAS f32x2*)(size_t)(L0 + L_ST4 + (c * 128 + tid) * 8);
                    const float mu = st[0] * (1.f / 640.f), var = fmaxf(st[1] * (1.f / 640.f) - mu * mu, 0.f), r = __builtin_amdgcn_rsqf(var + 1e-5f);
                    stsf2(L0 + L_MR + tid * 8, (f32x2){r, -mu * r});
                } else {
                    const int e = tid - 128;
                    stsf(L0 + L_UVS + e * 4, (ldsf(L0 + L_UV + (e >> 5) * 512 + (e & 31) * 4) + ldsf(L0 + L_UV + (e >> 5) * 512 + 128 + (e & 31) * 4))
                                           + (ldsf(L0 + L_UV + (e >> 5) * 512 + 256 + (e & 31) * 4) + ldsf(L0 + L_UV + (e >> 5) * 512 + 384 + (e & 31) * 4)));
                    if (e < 64) { const int e2 = e + 384;
                        stsf(L0 + L_UVS + e2 * 4, (ldsf(L0 + L_UV + (e2 >> 5) * 512 + (e2 & 31) * 4) + ldsf(L0 + L_UV + (e2 >> 5) * 512 + 128 + (e2 & 31) * 4))
                                                + (ldsf(L0 + L_UV + (e2 >> 5) * 512 + 256 + (e2 & 31) * 4) + ldsf(L0 + L_UV + (e2 >> 5) * 512 + 384 + (e2 & 31) * 4))); }
                }
            }
            if constexpr (t < 9) {
                if constexpr (t == 0) vmw<2>();
                else if constexpr (t == 8) {
                    asm volatile("s_waitcnt vmcnt(0)" : "+v"(wdf) :: "memory");
                    if (w == 7) { if (!__all(wdf[0] == (u32)tag && wdf[1] == (u32)(tag >> 32))) poll_at(ws, wdo, l, tag, ws, 12); }
                }
                else { if (w < 4) vmw<6>(); else vmw<5>(); }
                BAR();
                if constexpr (t == 7) { if (w == 7) asm volatile("s_nop 4\n\tglobal_load_dwordx2 %0, %1, %2 sc1" : "=v"(wdf) : "v"(wdo), "s"(ws) : "memory"); }
                if constexpr (t < 8) P4_HALF(1, true, t + 1, 0, if (sN) P4_B1(t + 3, 0), if (sN) P4_B1(t + 3, 1), if (sN) P4_B1(t + 3, 2));
                else P4_HALF(1, true, t + 1, 0, P5_B1(0, 0), P5_B1(0, 1), P5_B1(0, 2));
            } else {
                P4_HALF(1, false, 0, 0, P5_B1(1, 1), P5_B1(1, 2), P5_B1(1, 3));
                P5_B1(1, 4);
            }
        });
#undef P4_B
#undef P4_B1
#undef P4_A1
#undef P4_RA
#undef P4_RB
#undef P4_MM
#undef P4_HALF
        f32x2 mr[2];
#pragma unroll
        for (int m = 0; m < 2; ++m) mr[m] = *(LAS f32x2*)(size_t)(L0 + L_MR + (32 * wm + 16 * m + i16) * 8);
#pragma unroll
        for (int n = 0; n < 7; ++n) {
            const f32x4 u = ldsf4(L0 + L_UVS + (7 * wn + n) * 128 + 16 * g), v = ldsf4(L0 + L_UVS + (7 * wn + n) * 128 + 64 + 16 * g);
#pragma unroll
            for (int m = 0; m < 2; ++m) acc[n][m] = acc[n][m] * mr[m][0] + (u * mr[m][1] + v);
        }
        if (nj == 7) stsf4(L0 + L_XCH + w * 1024 + lo16, wn == 0 ? acc[6][1] : acc[6][0]);
        auto silu_mul = [&](const f32x4 gt, const f32x4 up) -> u32x2 {
            const f32x4 e = gt * (-LOG2E), gu = gt * up;
            f32x4 d;
#pragma unroll
            for (int r = 0; r < 4; ++r) d[r] = __builtin_amdgcn_exp2f(e[r]);
            d = d + 1.f;
#pragma unroll
            for (int r = 0; r < 4; ++r) d[r] = __builtin_amdgcn_rcpf(d[r]);
            const f32x4 o = gu * d;
            return (u32x2){pack2(o[0], o[1]), pack2(o[2], o[3])};
        };
#pragma unroll
        for (int a = 0; a < 3; ++a) {
            const int la = a + 4 * wn, gam = Gj + (la <= 3 || nj == 7 ? la : la - 1);
            const u32x2 o0 = silu_mul(acc[2 * a][0], acc[2 * a + 1][0]), o1 = silu_mul(acc[2 * a][1], acc[2 * a + 1][1]);
            const auto s0 = __builtin_amdgcn_permlane16_swap(o0[0], o1[0], false, false);
            const auto s1 = __builtin_amdgcn_permlane16_swap(o0[1], o1[1], false, false);
            const u32x4 v = {s0[0], s1[0], s0[1], s1[1]};
            ST16C(v, WS_ACT + (((rb * 8 + 2 * wm + (g & 1)) * 216 + 2 * gam + (g >> 1)) * 16 + i16) * 16);
        }
        BAR();
        if (nj == 7) {
            const f32x4 other = ldsf4(L0 + L_XCH + (w ^ 4) * 1024 + lo16);
            const u32x2 o = wn == 0 ? silu_mul(acc[6][0], other) : silu_mul(other, acc[6][1]);
            ST8C(o, WS_ACT + (((rb * 8 + 2 * wm + wn) * 216 + 2 * (Gj + 3) + (g >> 1)) * 16 + i16) * 16 + 8 * (g & 1));
        }
        P5_B1(2, 0); P5_B1(2, 1); P5_B1(2, 2); P5_B1(2, 3); P5_B1(2, 4);
        vmw<5>();
        BAR();
        if (tid == 0) st_agent(ws + WS_FACT + (rb * 16 + j) * FS, tag);
    }

    {
        if (w == 1 && l < 20) stsf4(L0 + L_GB + l * 16, gather1(ws, 800 + 20 * cb + l, tag, ws, 8));
        if (w == 0) poll_flags(ws + WS_FACT + rb * 16 * FS, 16, l, tag, ws, 9);
        const bool bump = rb == 15 && j == 0;
        if (bump && w >= 4) poll_at(ws, WS_FO + (tid - 256) * FS, l, tag, ws, 10);
        VMCNT(0);
        BAR();
        if (bump && tid == 0) st_agent(ws + WS_EPOCH, tag);
        const char* asrc = ws + WS_ACT + (mt3 * 216 + wk * 108) * 256;
        u32x4 a[3][4];
#define P5_A1(t, i) a[(t) % 3][i] = gld16c((i) * 1024, asrc + (t) * 4096, lo16)
        P5_A1(0, 0); P5_A1(0, 1); P5_A1(0, 2); P5_A1(0, 3); P5_A1(1, 0); P5_A1(1, 1); P5_A1(1, 2); P5_A1(1, 3); P5_A1(2, 0); P5_A1(2, 1); P5_A1(2, 2); P5_A1(2, 3);
        f32x4 acc[5];
#pragma unroll
        for (int n = 0; n < 5; ++n) acc[n] = Z4;
        const u32 rb5 = opaque(L0 + wk * 20480 + lo16);
        u32x4 fb[3][5];
#define P5_RD(buf, t, kk) do { _Pragma("unroll") for (int n = 0; n < 5; ++n) fb[buf][n] = lds128(rb5 + r5_slot(t) + (kk) * 5120 + n * 1024); } while (0)
#define P5_MM(buf, av) do { _Pragma("unroll") for (int n = 0; n < 5; ++n) acc[n] = mfma(fb[buf][n], (av), acc[n]); } while (0)
#define P5_MIX() do { _Pragma("unroll") for (int n = 0; n < 5; ++n) { SGB(M_DSR, 1); SGB(M_MFMA, 1); } } while (0)
        P5_RD(0, 0, 0); P5_RD(1, 0, 1);
        static_for<0, 7>([&](auto T) {
            constexpr int t = T, q = t % 3, k0 = 4 * t;
            constexpr bool nx = t + 3 < 7;
            constexpr int NA = t == 0 ? 8 : t == 1 ? 13 : t == 2 ? 18 : t == 3 ? 19 : t == 4 ? 18 : t == 5 ? 9 : 3;
            constexpr int NB = t == 2 ? 11 : t == 3 ? 11 : t == 4 ? 8 : t == 5 ? 0 : 63;
            vmw4<NA>(a[q][0], a[q][1], a[q][2], a[q][3]);
            SB0(); P5_RD((k0 + 2) % 3, t, 2); P5_MM(k0 % 3, a[q][0]); P5_MIX(); SB0();
            if constexpr (nx) P5_A1(t + 3, 0);
            if constexpr (t < 6) {
                SB0(); P5_RD((k0 + 3) % 3, t, 3); P5_MM((k0 + 1) % 3, a[q][1]); P5_MIX(); SB0();
                if constexpr (nx) P5_A1(t + 3, 1);
                vmw<NB>(); BAR();
                if constexpr (nx) { P5_B1(t + 3, 0); P5_B1(t + 3, 1); }
                SB0(); P5_RD((k0 + 4) % 3, t + 1, 0); P5_MM((k0 + 2) % 3, a[q][2]); P5_MIX(); SB0();
                if constexpr (nx) { P5_A1(t + 3, 2); P5_B1(t + 3, 2); P5_B1(t + 3, 3); }
                SB0(); P5_RD((k0 + 5) % 3, t + 1, 1); P5_MM((k0 + 3) % 3, a[q][3]); P5_MIX(); SB0();
                if constexpr (nx) { if constexpr (t + 3 < 6) P5_A1(t + 3, 3); P5_B1(t + 3, 4); }
            } else {
                SB0(); P5_MM((k0 + 1) % 3, a[q][1]); SB0(); P5_MM((k0 + 2) % 3, a[q][2]); SB0();
            }
        });
#undef P5_B1
#undef P5_A1
#undef P5_RD
#undef P5_MM
#undef P5_MIX
        if (wk == 1) {
#pragma unroll
            for (int n = 0; n < 5; ++n) stsf4(L0 + L_RED5 + (wm * 5 + n) * 1024 + lo16, acc[n]);
        }
        BAR();
        if (wk == 0) {
#pragma unroll
            for (int n = 0; n < 5; ++n) {
                const f32x4 v = acc[n] + ldsf4(L0 + L_RED5 + (wm * 5 + n) * 1024 + lo16);
                const f32x4 gb = ldsf4(L0 + L_GB + (16 * n + 4 * g) * 4);
                const u32x2 o = {pack2(bflo(x1k[n][0]) + gb[0] * v[0], bfhi(x1k[n][0]) + gb[1] * v[1]), pack2(bflo(x1k[n][1]) + gb[2] * v[2], bfhi(x1k[n][1]) + gb[3] * v[3])};
                sts64(L0 + L_OS + (16 * wm + i16) * 160 + 32 * n + 8 * g, o);
            }
        }
        BAR();
#pragma unroll
        for (int i = 0; i < 2; ++i) {
            const int id = tid + 512 * i;
            if (id < 640) {
                const int r = (id * 6554) >> 16, c = id - 10 * r;
                *(GAS u32x4*)((char*)p.out + (rb * 128 + 64 * hf + r) * 1280 + cb * 160 + c * 16) = lds128(L0 + L_OS + id * 16);
            }
        }
    }
    asm volatile(".Lmk_code_to:");
}

extern "C" void kernel_launch(void* const* d_in, const int* in_sizes, int n_in, void* d_out, int out_size, void* d_ws, size_t ws_size, hipStream_t stream) {
    Params p;
    p.x = (const u16*)d_in[0]; p.wqkv = (const u16*)d_in[1]; p.wo = (const u16*)d_in[2];
    p.wg = (const u16*)d_in[4]; p.wu = (const u16*)d_in[5]; p.wd = (const u16*)d_in[6];
    p.c = (const u16*)d_in[7]; p.wmod = (const u16*)d_in[8];
    p.out = (u16*)d_out; p.ws = (char*)d_ws;
    mega_kernel<<<256, 512, 0, stream>>>(p);
}
```

```cpp
#define PA 0
#define PB 0
#include <hip/hip_runtime.h>
#include <hip/hip_bf16.h>
#include <stdint.h>
#include <type_traits>

typedef unsigned short u16;
typedef unsigned u32;
typedef unsigned long long u64;
typedef __attribute__((ext_vector_type(2))) unsigned u32x2;
typedef __attribute__((ext_vector_type(4))) unsigned u32x4;
typedef __attribute__((ext_vector_type(2))) float f32x2;
typedef __attribute__((ext_vector_type(4))) float f32x4;
typedef __attribute__((ext_vector_type(2))) __bf16 bf16x2;
typedef __attribute__((ext_vector_type(8))) __bf16 bf16x8;
typedef __attribute__((ext_vector_type(4))) short s16x4;
#define LAS __attribute__((address_space(3)))
#define GAS __attribute__((address_space(1)))
#define DI __device__ __forceinline__

constexpr int SEQ = 2048, DM = 640, FF = 1728;
constexpr float QSCALE = 0.22360679774997896f * 1.4426950408889634f;
constexpr float LOG2E = 1.4426950408889634f;

constexpr u32 WS_EPOCH = 0;
constexpr u32 WS_ERR   = 256;
constexpr u32 FS = 128;
constexpr u32 WS_FKV   = 4096;
constexpr u32 WS_FO    = WS_FKV + 256 * FS;
constexpr u32 WS_FX1   = WS_FO + 256 * FS;
constexpr u32 WS_FACT  = WS_FX1 + 256 * FS;
constexpr u32 WS_FH    = WS_FACT + 256 * FS;
constexpr u32 WS_FWT   = WS_FH + 256 * FS;
constexpr u32 WS_FWP   = WS_FWT + 256 * FS;
constexpr u32 WS_FW2   = WS_FWP + 256 * FS;
constexpr u32 WS_MODG  = WS_FW2 + 256 * FS;
constexpr u32 WS_UV    = WS_MODG + 61440;
constexpr u32 WS_STAT  = WS_UV + 114688;
constexpr u32 WS_KV    = 589824;
constexpr u32 WS_O     = WS_KV + 256 * 22528;
constexpr u32 WS_X1    = WS_O + SEQ * DM * 2;
constexpr u32 WS_ACT   = WS_X1 + SEQ * DM * 2;
constexpr u32 WS_WP    = WS_ACT + SEQ * FF * 2;
constexpr u32 WS_H     = WS_WP + 16 * 20 * 14 * 1024;
constexpr u32 WS_WD    = WS_H + SEQ * DM * 2;
constexpr u32 WS_WO    = WS_WD + 8 * 54 * 5 * 1024;
constexpr u32 WS_WT    = WS_WO + 8 * 20 * 5 * 1024;
constexpr u32 WS_STAMP = WS_WT + 16 * 153600;
constexpr u32 WS_END   = WS_STAMP + 256 * 8 * 64 * 4;

constexpr u32 L_WKV = 0, L_WQ = 102400, L_GS = 153600, L_STP = 156160, L_MODP = 156416, L_CNT = 157440, L_SB = 157696, L_G2 = 158016, L_Z16 = 158656;
constexpr u32 L_ODRAW = 101376, L_WTRAW = 116736, L_XS = 133120;
constexpr u32 L_K = 0, L_V = 30720, L_Q = 67584, L_WPRAW = 77824, L_UVL = 98304;
constexpr u32 L_WO = 61440, L_RED3 = 0, L_X1S = 20480, L_XR = 30720, L_GA = 40960, L_ST3 = 41472, L_WDRAW = 43008;
constexpr u32 L_R4 = 0, R4_STAGE = 45056, L_ST4 = 135168, L_UV = 143360, L_MR = 150528, L_XCH = 151552, L_UVS = 159744;
constexpr u32 L_RED5 = 126976, L_GB = 147456, L_OS = 148480;
constexpr u32 r5_slot(int t) { return t % 3 == 0 ? 45056 : t % 3 == 1 ? 86016 : 0; }

struct Params {
    const u16* x; const u16* wqkv; const u16* wo; const u16* wg; const u16* wu; const u16* wd;
    const u16* c; const u16* wmod; u16* out; char* ws;
};

DI float bflo(u32 d) { return __uint_as_float(d << 16); }
DI float bfhi(u32 d) { return __uint_as_float(d & 0xffff0000u); }
DI u32 pack2(float a, float b) { f32x2 v = {a, b}; return __builtin_bit_cast(u32, __builtin_convertvector(v, bf16x2)); }
DI float rbf(float a) { return bflo(pack2(a, a)); }
DI f32x4 mfma(u32x4 a, u32x4 b, f32x4 c) {
    return __builtin_amdgcn_mfma_f32_16x16x32_bf16(__builtin_bit_cast(bf16x8, a), __builtin_bit_cast(bf16x8, b), c, 0, 0, 0);
}
DI u32x2 ldtr(u32 a) { return __builtin_bit_cast(u32x2, __builtin_amdgcn_ds_read_tr16_b64_v4i16((LAS s16x4*)(size_t)a)); }
DI u32x4 ldtr2(u32 a0, u32 a1) { u32x2 x = ldtr(a0), y = ldtr(a1); return (u32x4){x[0], x[1], y[0], y[1]}; }
DI u32x4 lds128(u32 a) { return *(LAS u32x4*)(size_t)a; }
DI u32x2 lds64(u32 a) { return *(LAS u32x2*)(size_t)a; }
DI f32x4 ldsf4(u32 a) { return *(LAS f32x4*)(size_t)a; }
DI float ldsf(u32 a) { return *(LAS float*)(size_t)a; }
DI void sts128(u32 a, u32x4 v) { *(LAS u32x4*)(size_t)a = v; }
DI void sts64(u32 a, u32x2 v) { *(LAS u32x2*)(size_t)a = v; }
DI void stsf(u32 a, float v) { *(LAS float*)(size_t)a = v; }
DI void stsf4(u32 a, f32x4 v) { *(LAS f32x4*)(size_t)a = v; }
DI void stsf2(u32 a, f32x2 v) { *(LAS f32x2*)(size_t)a = v; }
DI void sts16(u32 a, u16 v) { *(LAS u16*)(size_t)a = v; }

DI void glds16_(const void* root, u32 voff, u32 ldsdst) {
    asm volatile("s_mov_b32 m0, %2\n\ts_nop 0\n\tglobal_load_lds_dwordx4 %0, %1" :: "v"(voff), "s"(root), "s"(__builtin_amdgcn_readfirstlane(ldsdst)) : "memory");
}
DI void glds16c_(const void* root, u32 voff, u32 ldsdst) {
    asm volatile("s_mov_b32 m0, %2\n\ts_nop 0\n\tglobal_load_lds_dwordx4 %0, %1 sc1" :: "v"(voff), "s"(root), "s"(__builtin_amdgcn_readfirstlane(ldsdst)) : "memory");
}
template <int OFF> DI void glds16c_next_(const void* root, u32 voff) {
    asm volatile("global_load_lds_dwordx4 %0, %1 offset:%2 sc1" :: "v"(voff), "s"(root), "i"(OFF) : "memory");
}
template <int OFF> DI u32x4 gld16(const void* root, u32 voff) {
    u32x4 r; asm volatile("global_load_dwordx4 %0, %1, %2 offset:%3" : "=v"(r) : "v"(voff), "s"(root), "i"(OFF) : "memory"); return r;
}
template <int OFF> DI u32x4 gld16c_(const void* root, u32 voff) {
    u32x4 r; asm volatile("global_load_dwordx4 %0, %1, %2 offset:%3 sc1" : "=v"(r) : "v"(voff), "s"(root), "i"(OFF) : "memory"); return r;
}
#define ROFF(root, ptr) ((u32)((const char*)(ptr) - (const char*)(root)))
#define glds16(root, ptr, voff, dst) glds16_((root), ROFF(root, ptr) + (voff), (dst))
#define glds16c(ptr, voff, dst) glds16c_(ws, ROFF(ws, ptr) + (voff), (dst))
#define glds16c_next(OFF, ptr, voff) glds16c_next_<(OFF)>(ws, ROFF(ws, ptr) + (voff))
#define gld16c(OFF, ptr, voff) gld16c_<(OFF)>(ws, ROFF(ws, ptr) + (voff))
DI u32 gldu16(const void* sbase, u32 voff) {
    u32 r; asm volatile("global_load_ushort %0, %1, %2" : "=v"(r) : "v"(voff), "s"(sbase) : "memory"); return r;
}
#define VMCNT(n) asm volatile("s_waitcnt vmcnt(" #n ")" ::: "memory")
#define BAR() asm volatile("s_waitcnt lgkmcnt(0)\n\ts_barrier" ::: "memory")
#define RLX_AGENT __ATOMIC_RELAXED, __HIP_MEMORY_SCOPE_AGENT
DI u64 ld_agent(const char* p) { return __hip_atomic_load((GAS u64*)p, RLX_AGENT); }
DI void st_agent(char* p, u64 v) { __hip_atomic_store((GAS u64*)p, v, RLX_AGENT); }
DI void st_agent32(char* p, u32 v) { __hip_atomic_store((GAS u32*)p, v, RLX_AGENT); }
DI float ld_agentf(const char* p) { return __uint_as_float(__hip_atomic_load((GAS u32*)p, RLX_AGENT)); }
DI float xsum4(float x) {
    const auto s = __builtin_amdgcn_permlane16_swap(__float_as_uint(x), __float_as_uint(x), false, false);
    x = __uint_as_float(s[0]) + __uint_as_float(s[1]);
    const auto t = __builtin_amdgcn_permlane32_swap(__float_as_uint(x), __float_as_uint(x), false, false);
    return __uint_as_float(t[0]) + __uint_as_float(t[1]);
}
DI float xmax4(float x) {
    const auto s = __builtin_amdgcn_permlane16_swap(__float_as_uint(x), __float_as_uint(x), false, false);
    x = fmaxf(__uint_as_float(s[0]), __uint_as_float(s[1]));
    const auto t = __builtin_amdgcn_permlane32_swap(__float_as_uint(x), __float_as_uint(x), false, false);
    return fmaxf(__uint_as_float(t[0]), __uint_as_float(t[1]));
}
template <int CTRL> DI float dppf(float v) { return __builtin_bit_cast(float, __builtin_amdgcn_update_dpp(0, __builtin_bit_cast(int, v), CTRL, 0xf, 0xf, true)); }

constexpr u32 SPIN_CAP = 1u << 17;
DI void poll_flags(const char* base, int n, int lane, u64 tag, char* ws, u32 code) {
    const char* a = base + (lane < n ? lane : 0) * FS;
    u32 spins = 0;
    for (;;) {
        const u64 v = ld_agent(a);
        if (__all(v == tag)) break;
        if (++spins > SPIN_CAP) { if (lane == 0) st_agent32(ws + WS_ERR, code); break; }
        __builtin_amdgcn_s_sleep(1);
    }
}
typedef __attribute__((ext_vector_type(2))) _Float16 f16x2;
DI float hlo(u32 d) { return (float)__builtin_bit_cast(f16x2, d)[0]; }
DI float hhi(u32 d) { return (float)__builtin_bit_cast(f16x2, d)[1]; }
DI u32 packh(float a, float b) { const f16x2 v = {(_Float16)a, (_Float16)b}; return __builtin_bit_cast(u32, v); }
DI bool gran_ok(const u32x4& v, u64 tag) { return v[2] == (u32)tag && v[3] == (u32)(tag >> 32); }
DI f32x4 gran_sum(const u32x4& a, const u32x4& b, const u32x4& c, const u32x4& d) {
    const f32x4 s = {(hlo(a[0]) + hlo(b[0])) + (hlo(c[0]) + hlo(d[0])), (hhi(a[0]) + hhi(b[0])) + (hhi(c[0]) + hhi(d[0])),
                     (hlo(a[1]) + hlo(b[1])) + (hlo(c[1]) + hlo(d[1])), (hhi(a[1]) + hhi(b[1])) + (hhi(c[1]) + hhi(d[1]))};
    return (f32x4){rbf(s[0]), rbf(s[1]), rbf(s[2]), rbf(s[3])};
}
DI f32x4 gather1(const char* ws, u32 g0, u64 tag, char* wsw, u32 code) {
    u32 spins = 0; u32x4 v0, v1, v2, v3;
    const u32 o = WS_MODG + g0 * 16;
    for (;;) {
        asm volatile("s_nop 4\n\tglobal_load_dwordx4 %0, %4, %5 sc1\n\tglobal_load_dwordx4 %1, %6, %5 sc1\n\tglobal_load_dwordx4 %2, %7, %5 sc1\n\tglobal_load_dwordx4 %3, %8, %5 sc1\n\ts_waitcnt vmcnt(0)"
                     : "=&v"(v0), "=&v"(v1), "=&v"(v2), "=&v"(v3) : "v"(o), "s"(ws), "v"(o + 15360), "v"(o + 30720), "v"(o + 46080) : "memory");
        if (__all(gran_ok(v0, tag) && gran_ok(v1, tag) && gran_ok(v2, tag) && gran_ok(v3, tag))) break;
        if (++spins > SPIN_CAP) { st_agent32(wsw + WS_ERR, code); break; }
        __builtin_amdgcn_s_sleep(1);
    }
    return gran_sum(v0, v1, v2, v3);
}
DI void reask(u32x4& v, const char* ws, u32 off, u64 tag) {
    if (!gran_ok(v, tag)) asm volatile("global_load_dwordx4 %0, %1, %2 sc1" : "+v"(v) : "v"(off), "s"(ws) : "memory");
}
DI void gather2(const char* ws, u32 g0, u32 g1, u64 tag, char* wsw, u32 code, f32x4& ra, f32x4& rb) {
    u32 spins = 0; u32x4 v0, v1, v2, v3, u0, u1, u2, u3;
    const u32 o = WS_MODG + g0 * 16, q = WS_MODG + g1 * 16;
    asm volatile("global_load_dwordx4 %0, %8, %9 sc1\n\tglobal_load_dwordx4 %1, %10, %9 sc1\n\tglobal_load_dwordx4 %2, %11, %9 sc1\n\tglobal_load_dwordx4 %3, %12, %9 sc1\n\t"
                 "global_load_dwordx4 %4, %13, %9 sc1\n\tglobal_load_dwordx4 %5, %14, %9 sc1\n\tglobal_load_dwordx4 %6, %15, %9 sc1\n\tglobal_load_dwordx4 %7, %16, %9 sc1\n\ts_waitcnt vmcnt(0)"
                 : "=&v"(v0), "=&v"(v1), "=&v"(v2), "=&v"(v3), "=&v"(u0), "=&v"(u1), "=&v"(u2), "=&v"(u3)
                 : "v"(o), "s"(ws), "v"(o + 15360), "v"(o + 30720), "v"(o + 46080), "v"(q), "v"(q + 15360), "v"(q + 30720), "v"(q + 46080) : "memory");
    for (;;) {
        if (__all(gran_ok(v0, tag) && gran_ok(v1, tag) && gran_ok(v2, tag) && gran_ok(v3, tag) && gran_ok(u0, tag) && gran_ok(u1, tag) && gran_ok(u2, tag) && gran_ok(u3, tag))) break;
        if (++spins > SPIN_CAP) { st_agent32(wsw + WS_ERR, code); break; }
        reask(v0, ws, o, tag); reask(v1, ws, o + 15360, tag); reask(v2, ws, o + 30720, tag); reask(v3, ws, o + 46080, tag);
        reask(u0, ws, q, tag); reask(u1, ws, q + 15360, tag); reask(u2, ws, q + 30720, tag); reask(u3, ws, q + 46080, tag);
        asm volatile("s_waitcnt vmcnt(0)" : "+v"(v0), "+v"(v1), "+v"(v2), "+v"(v3), "+v"(u0), "+v"(u1), "+v"(u2), "+v"(u3) :: "memory");
    }
    ra = gran_sum(v0, v1, v2, v3); rb = gran_sum(u0, u1, u2, u3);
}
struct Gather { u32x4 v0, v1, v2, v3, u0, u1, u2, u3; };
DI void gather_ask(const char* ws, u32 g0, u32 g1, Gather& G) {
    const u32 o = WS_MODG + g0 * 16, q = WS_MODG + g1 * 16;
    asm volatile("global_load_dwordx4 %0, %8, %9 sc1\n\tglobal_load_dwordx4 %1, %10, %9 sc1\n\tglobal_load_dwordx4 %2, %11, %9 sc1\n\tglobal_load_dwordx4 %3, %12, %9 sc1\n\t"
                 "global_load_dwordx4 %4, %13, %9 sc1\n\tglobal_load_dwordx4 %5, %14, %9 sc1\n\tglobal_load_dwordx4 %6, %15, %9 sc1\n\tglobal_load_dwordx4 %7, %16, %9 sc1"
                 : "=&v"(G.v0), "=&v"(G.v1), "=&v"(G.v2), "=&v"(G.v3), "=&v"(G.u0), "=&v"(G.u1), "=&v"(G.u2), "=&v"(G.u3)
                 : "v"(o), "s"(ws), "v"(o + 15360), "v"(o + 30720), "v"(o + 46080), "v"(q), "v"(q + 15360), "v"(q + 30720), "v"(q + 46080) : "memory");
}
DI void gather_finish(const char* ws, u32 g0, u32 g1, Gather& G, u64 tag, char* wsw, u32 code, f32x4& ra, f32x4& rb) {
    asm volatile("" : "+v"(G.v0), "+v"(G.v1), "+v"(G.v2), "+v"(G.v3), "+v"(G.u0), "+v"(G.u1), "+v"(G.u2), "+v"(G.u3) :: "memory");
    if (__all(gran_ok(G.v0, tag) && gran_ok(G.v1, tag) && gran_ok(G.v2, tag) && gran_ok(G.v3, tag) && gran_ok(G.u0, tag) && gran_ok(G.u1, tag) && gran_ok(G.u2, tag) && gran_ok(G.u3, tag))) {
        ra = gran_sum(G.v0, G.v1, G.v2, G.v3); rb = gran_sum(G.u0, G.u1, G.u2, G.u3);
    } else gather2(ws, g0, g1, tag, wsw, code, ra, rb);
}
DI void gather_strip(const char* ws, u32 gs, u32 gc, u32 strip, int l, u64 tag, char* wsw, u32 code) {
    f32x4 a, c;
    gather2(ws, l < 40 ? gs + l : gc + (l - 40), gc + 24 + (l & 15), tag, wsw, code, a, c);
    if (l >= 40) a = a + 1.f;
    c = c + 1.f;
    *(LAS u32x2*)(size_t)(strip + (l < 40 ? l * 8 : 320 + (l - 40) * 8)) = (u32x2){pack2(a[0], a[1]), pack2(a[2], a[3])};
    if (l < 16) *(LAS u32x2*)(size_t)(strip + 320 + (24 + l) * 8) = (u32x2){pack2(c[0], c[1]), pack2(c[2], c[3])};
}
DI void put_strip_spread(const f32x4& a, const f32x4& c, u32 strip, int l) {
    if (l < 40) *(LAS u32x2*)(size_t)(strip + l * 8) = (u32x2){pack2(a[0], a[1]), pack2(a[2], a[3])};
    else {
        const u32 p0 = pack2(a[0] + 1.f, a[1] + 1.f), p1 = pack2(a[2] + 1.f, a[3] + 1.f);
        *(LAS u32x4*)(size_t)(strip + 320 + (l - 40) * 16) = (u32x4){p0 & 0xffffu, p0 & 0xffff0000u, p1 & 0xffffu, p1 & 0xffff0000u};
    }
    if (l < 16) {
        const u32 p0 = pack2(c[0] + 1.f, c[1] + 1.f), p1 = pack2(c[2] + 1.f, c[3] + 1.f);
        *(LAS u32x4*)(size_t)(strip + 320 + (24 + l) * 16) = (u32x4){p0 & 0xffffu, p0 & 0xffff0000u, p1 & 0xffffu, p1 & 0xffff0000u};
    }
    if (l == 63) *(LAS u32x4*)(size_t)(strip + 960) = (u32x4){0, 0, 0, 0};
}
DI void poll_at(const char* ws, u32 off, int lane, u64 tag, char* wsw, u32 code) {
    u32 spins = 0;
    for (;;) {
        const u64 v = ld_agent(ws + off);
        if (__all(v == tag)) break;
        if (++spins > SPIN_CAP) { if (lane == 0) st_agent32(wsw + WS_ERR, code); break; }
        __builtin_amdgcn_s_sleep(1);
    }
}

#define STAMP(i) do { if ((i) == PA) tA = __builtin_amdgcn_s_memrealtime(); if ((i) == PB) tB = __builtin_amdgcn_s_memrealtime(); } while (0)
#define STAMPL(i) do {} while (0)
#define STAMP_RT(i) do {} while (0)
#define STAMP_OUT() do { const u64 d_ = tB - tA, e_ = __builtin_amdgcn_s_memrealtime(); while (__builtin_amdgcn_s_memrealtime() - e_ < d_) __builtin_amdgcn_s_sleep(1); } while (0)

template <int N> using IC = std::integral_constant<int, N>;
template <int I, int E, class F> DI void static_for(F&& f) { if constexpr (I < E) { f(IC<I>{}); static_for<I + 1, E>(f); } }
template <int N> DI void vmw() { asm volatile("s_waitcnt vmcnt(%0)" :: "i"(N) : "memory"); }
template <int N> DI void vmw1(u32x4& a) { asm volatile("s_waitcnt vmcnt(%1)" : "+v"(a) : "i"(N) : "memory"); }
template <int N> DI void vmw4(u32x4& a, u32x4& b, u32x4& c, u32x4& d) { asm volatile("s_waitcnt vmcnt(%4)" : "+v"(a), "+v"(b), "+v"(c), "+v"(d) : "i"(N) : "memory"); }
DI u32 opaque(u32 a) { asm("" : "+v"(a)); return a; }
DI float dot2(u32 a, u32 b, float c) { return __builtin_amdgcn_fdot2_f32_bf16(__builtin_bit_cast(bf16x2, a), __builtin_bit_cast(bf16x2, b), c, false); }
#define SB0() __builtin_amdgcn_sched_barrier(0)
#define SGB(m, n) __builtin_amdgcn_sched_group_barrier((m), (n), 0)
constexpr int M_MFMA = 0x8, M_DSR = 0x100;
#define Z4 ((f32x4){0.f, 0.f, 0.f, 0.f})
DI u32 lds_add(u32 a, u32 v) { return __hip_atomic_fetch_add((LAS u32*)(size_t)a, v, __ATOMIC_RELAXED, __HIP_MEMORY_SCOPE_WORKGROUP); }
DI u32 lds_ld(u32 a) { return __hip_atomic_load((LAS u32*)(size_t)a, __ATOMIC_RELAXED, __HIP_MEMORY_SCOPE_WORKGROUP); }
DI void worker_sync(u32 cnt, int lane) {
    asm volatile("s_waitcnt lgkmcnt(0)" ::: "memory");
    if (lane == 0) lds_add(cnt, 1u);
    for (u32 spins = 0; lds_ld(cnt) < 4u && spins < (1u << 22); ++spins) {}
    asm volatile("" ::: "memory");
}

__global__ __launch_bounds__(512, 2) void mega_kernel(Params p) {
    __shared__ __attribute__((aligned(16))) char ldsmem[163840];
    const u32 L0 = (u32)(uintptr_t)(LAS char*)ldsmem;
    const int tid = threadIdx.x, l = tid & 63;
    const int w = __builtin_amdgcn_readfirstlane(tid >> 6);
    const int b = blockIdx.x, xg = b & 7, sidx = b >> 3;
    const int rb = 4 * (xg >> 1) + (sidx >> 3), j = 8 * (xg & 1) + (sidx & 7);
    const int g = l >> 4, i16 = l & 15, q4 = i16 >> 2, p4 = l & 3;
    u32 wu0 = 0;
    if (w == 7) {
        const char* pc; u32 span;
        asm volatile("s_getpc_b64 %0\n.Lmk_code_from:\n\ts_mov_b32 %1, .Lmk_code_to-.Lmk_code_from" : "=s"(pc), "=s"(span));
        const u32 per = (span + 4095u) >> 12;
        const u32 off = min((sidx * per + min((u32)l, per - 1u)) * 128u, span - 4u);
        asm volatile("s_nop 0\n\tglobal_load_dword %0, %1, %2" : "=v"(wu0) : "v"(off), "s"(pc) : "memory");
    }
    char* const ws = p.ws;
    asm volatile("" :: "s"(p.x), "s"(p.wqkv), "s"(p.wo), "s"(p.wg), "s"(p.wu), "s"(p.wd), "s"(p.c), "s"(p.wmod), "s"(p.out), "s"(p.ws));
    u64 tag;
#define ASK_TAG(ep) u32x2 ep; asm volatile("global_load_dwordx2 %0, %1, %2" : "=v"(ep) : "v"(WS_EPOCH), "s"(ws) : "memory")
#define TAKE_TAG(ep) do { asm volatile("s_waitcnt vmcnt(0)" : "+v"(ep) :: "memory"); \
        tag = (((u64)(u32)__builtin_amdgcn_readfirstlane(ep[1]) << 32) | (u32)__builtin_amdgcn_readfirstlane(ep[0])) + 1; } while (0)
    const __amdgpu_buffer_rsrc_t wsr = __builtin_amdgcn_make_buffer_rsrc(ws, 0, WS_END, 0x00020000);
#define ST16C(v, off) __builtin_amdgcn_raw_buffer_store_b128((v), wsr, (off), 0, 16)
#define ST8C(v, off)  __builtin_amdgcn_raw_buffer_store_b64((v), wsr, (off), 0, 16)
    u64 tA = 0, tB = 0;
    STAMP_RT(62); STAMP(0);

    const int nj = j < 12 ? 7 : 6, Gj = j < 12 ? 7 * j : 84 + 6 * (j - 12);
    const bool wpwg = b < 216;
    const int wpm = b >= 108 ? 1 : 0, wpb = (b - 108 * wpm) >> 2, wpq = b & 3;
    u32x4 mw[5]; u32 mc[5];
    auto mod_ask = [&]() {
#pragma unroll
        for (int i = 0; i < 5; ++i) {
            const int r = 160 * (b & 3) + ((tid & 255) >> 3) + 32 * i;
            mw[i] = gld16<0>(p.wmod, r * 7680 + (b >> 2) * 128 + (tid & 7) * 16);
            mc[i] = gldu16(p.c, r * 2);
        }
    };
    auto mod_finish = [&]() {
        float acc[8];
#pragma unroll
        for (int e = 0; e < 8; ++e) acc[e] = 0.f;
#pragma unroll
        for (int i = 0; i < 5; ++i) {
            const float cv = bflo(mc[i]);
            const float sv = cv * __builtin_amdgcn_rcpf(1.f + __builtin_amdgcn_exp2f(-cv * LOG2E));
            const u32 slo = pack2(sv, 0.f), shi = slo << 16;
#pragma unroll
            for (int d = 0; d < 4; ++d) { acc[2 * d] = dot2(mw[i][d], slo, acc[2 * d]); acc[2 * d + 1] = dot2(mw[i][d], shi, acc[2 * d + 1]); }
        }
#pragma unroll
        for (int e = 0; e < 8; ++e) { const float v = acc[e]; acc[e] = xsum4(v + dppf<0x128>(v)); }
        if (l < 8) {
            stsf4(L0 + L_MODP + (w & 3) * 256 + l * 32, (f32x4){acc[0], acc[1], acc[2], acc[3]});
            stsf4(L0 + L_MODP + (w & 3) * 256 + l * 32 + 16, (f32x4){acc[4], acc[5], acc[6], acc[7]});
        }
        asm volatile("s_waitcnt lgkmcnt(0)" ::: "memory");
        u32 old = 0;
        if (l == 0) old = lds_add(L0 + L_CNT, 1u);
        if (__builtin_amdgcn_readfirstlane(old) == 3) {
            float v = 0.f;
#pragma unroll
            for (int ww = 0; ww < 4; ++ww) v += ldsf(L0 + L_MODP + ww * 256 + l * 4);
            const float v1 = dppf<0x55>(v), v2 = dppf<0xAA>(v), v3 = dppf<0xFF>(v), v0 = dppf<0x00>(v);
            const u32x4 gr = {packh(v0, v1), packh(v2, v3), (u32)tag, (u32)(tag >> 32)};
            if (p4 == 0) ST16C(gr, WS_MODG + ((b & 3) * 960 + (b >> 2) * 16 + (l >> 2)) * 16);
        }
    };
#define MOD_LANDED() asm volatile("s_waitcnt vmcnt(0)" : "+v"(mw[0]), "+v"(mw[1]), "+v"(mw[2]), "+v"(mw[3]), "+v"(mw[4]), "+v"(mc[0]), "+v"(mc[1]), "+v"(mc[2]), "+v"(mc[3]), "+v"(mc[4]) :: "memory")
    if (w >= 4) {
        __builtin_amdgcn_s_setprio(3);
        const int u = w - 4;
        const bool tw = b < 150;
        const int tb = b / 5, tk = b - 5 * tb, ts = 4 * tk + u;
        if (tw) {
            const char* src = (const char*)p.wqkv + ts * 122880 + tb * 128;
            const u32 voff = (l >> 3) * 3840 + (l & 7) * 16;
#pragma unroll
            for (int i = 0; i < 4; ++i) glds16(p.wqkv, src + i * 8 * 3840, voff, L0 + L_WTRAW + u * 4096 + i * 1024);
        }
        ASK_TAG(epl);
        BAR();
        __builtin_amdgcn_s_setprio(0);
        TAKE_TAG(epl);
        if (tw) {
            const u32 a = L0 + L_WTRAW + u * 4096 + (8 * g + q4) * 128 + 8 * p4;
#pragma unroll
            for (int t = 0; t < 4; ++t) {
                const u32 cc = 8 * tb + 2 * t + (i16 >> 3);
                const u32 seg = cc >= 160 ? 2 : cc >= 80 ? 1 : 0, wi = cc - 80 * seg, jj = (wi * 13108) >> 16, c5 = wi - 5 * jj;
                const u32 c = seg == 0 ? 10 + c5 : seg == 1 ? c5 : 5 + c5, nt = c >> 1;
                const u32 dst = WS_WT + jj * 153600 + (nt < 5 ? ts * 5120 + nt * 1024 : 102400 + ts * 2560 + (nt - 5) * 1024)
                              + (nt < 7 ? (g * 16 + 8 * (c & 1) + (i16 & 7)) * 16 : g * 128 + (i16 & 7) * 16);
                ST16C(ldtr2(a + 32 * t, a + 32 * t + 512), dst);
            }
        }
        vmw<0>();
        {
            asm volatile("s_waitcnt lgkmcnt(0)" ::: "memory");
            u32 old = 0;
            if (l == 0) old = lds_add(L0 + L_CNT + 12, 1u);
            if (tw && __builtin_amdgcn_readfirstlane(old) == 3 && l == 0) st_agent(ws + WS_FWT + b * FS, tag);
        }
        STAMPL(19);
        if (b >= 80 && b < 240) {
            if (!tw) __builtin_amdgcn_s_sleep(32);
            mod_ask(); MOD_LANDED(); mod_finish();
        }
        if (u == 0) {
            const int wh = l >= 30 ? 0 : l / 5, kr = l >= 30 ? 0 : l - 5 * wh;
            const int bl = (1280 * (wh >> 1) + 80 * j + 79 * (wh & 1)) >> 7;
            poll_at(ws, WS_FWT + (bl * 5 + kr) * FS, l, tag, ws, 13);
            for (u32 spins = 0; lds_ld(L0 + L_CNT + 12) < 4u && spins < (1u << 22); ++spins) {}
            if (l == 0) lds_add(L0 + L_CNT + 24, 1u);
        } else {
            for (u32 spins = 0; lds_ld(L0 + L_CNT + 24) < 1u && spins < (1u << 22); ++spins) {}
        }
        asm volatile("" ::: "memory");
        STAMPL(20);
        {
            const char* src = ws + WS_WT + j * 153600 + u * 1024;
            const u32 lo = l * 16;
#pragma unroll
            for (int i = 0; i < 25; ++i) glds16c(src + i * 4096, lo, L0 + L_WKV + u * 1024 + i * 4096);
        }
        VMCNT(0);
        STAMPL(17);
    } else {
        __builtin_amdgcn_s_setprio(2);
        if (b < 80) mod_ask();
        ASK_TAG(epw);
        if (tid < 16) *(LAS u32*)(size_t)(L0 + L_CNT + tid * 4) = 0u;
        BAR();
        STAMP(29);
        TAKE_TAG(epw);
        STAMP(30);
        if (b < 80) { MOD_LANDED(); mod_finish(); }
        {
            const char* src = (const char*)p.x + (rb * 128 + 8 * j) * 1280 + w * 1024;
            glds16(p.x, src, l * 16, L0 + L_XS + w * 1024);
            glds16(p.x, src + 4096, l * 16, L0 + L_XS + (w + 4) * 1024);
            if (w < 2) glds16(p.x, src + 8192, l * 16, L0 + L_XS + (w + 8) * 1024);
        }
        VMCNT(0);
        STAMP(1);
        worker_sync(L0 + L_CNT + 16, l);
        const int tok = l & 7;
        u32x4 xr[3];
#pragma unroll
        for (int i = 0; i < 3; ++i) xr[i] = lds128(L0 + L_XS + tok * 1280 + (20 * w + (i < 2 ? (l >> 3) + 8 * i : 16 + ((l >> 3) & 3))) * 16);
        {
            float s1 = 0.f, s2 = 0.f;
#pragma unroll
            for (int i = 0; i < 3; ++i) {
                float a1 = 0.f, a2 = 0.f;
#pragma unroll
                for (int d = 0; d < 4; ++d) { a1 = dot2(xr[i][d], 0x3F803F80u, a1); a2 = dot2(xr[i][d], xr[i][d], a2); }
                if (i < 2 || l < 32) { s1 += a1; s2 += a2; }
            }
            asm volatile("s_waitcnt lgkmcnt(0)" ::: "memory");
            if (l == 0) lds_add(L0 + L_CNT + 20, 1u);
            s1 += dppf<0x128>(s1); s2 += dppf<0x128>(s2);
            s1 = xsum4(s1); s2 = xsum4(s2);
            if (l < 8) stsf2(L0 + L_STP + (w * 8 + l) * 8, (f32x2){s1, s2});
        }
        gather_strip(ws, 40 * w, 160 + 40 * w, L0 + L_GS + w * 640, l, tag, ws, 1);
        STAMP(2);
        worker_sync(L0 + L_CNT + 4, l);
        {
            f32x2 st = {0.f, 0.f};
#pragma unroll
            for (int ww = 0; ww < 4; ++ww) st += *(LAS f32x2*)(size_t)(L0 + L_STP + (ww * 8 + tok) * 8);
            const float mu = st[0] * (1.f / 640.f), var = fmaxf(st[1] * (1.f / 640.f) - mu * mu, 0.f);
            const float rs = __builtin_amdgcn_rsqf(var + 1e-5f), nmr = -mu * rs;
#pragma unroll
            for (int i = 0; i < 3; ++i) {
                const int cl = i < 2 ? (l >> 3) + 8 * i : 16 + ((l >> 3) & 3);
                const u32x4 sa = lds128(L0 + L_GS + w * 640 + cl * 16), gg = lds128(L0 + L_GS + w * 640 + 320 + cl * 16);
                u32x4 h;
#pragma unroll
                for (int d = 0; d < 4; ++d)
                    h[d] = pack2((bflo(xr[i][d]) * rs + nmr) * bflo(gg[d]) + bflo(sa[d]), (bfhi(xr[i][d]) * rs + nmr) * bfhi(gg[d]) + bfhi(sa[d]));
                if (i < 2 || l < 32) ST16C(h, WS_H + (((rb * 8 + (j >> 1)) * 80 + 20 * w + cl) * 16 + 8 * (j & 1) + tok) * 16);
            }
        }
        VMCNT(0);
        worker_sync(L0 + L_CNT + 8, l);
        if (tid == 0) st_agent(ws + WS_FH + (rb * 16 + j) * FS, tag);
        if (w == 0) poll_flags(ws + WS_FH + rb * 16 * FS, 16, l, tag, ws, 11);
        __builtin_amdgcn_s_setprio(0);
    }
    BAR();
    asm volatile("s_waitcnt vmcnt(0)" : "+v"(wu0) :: "memory");
    STAMP(3);

    u32x4 xa[20];
    const u32 lo16 = l * 16;
    const char* hsrc = ws + WS_H + (rb * 8 + w) * 80 * 256;
#define XL(s) xa[s] = gld16c(((s) & 3) * 1024, hsrc + ((s) >> 2) * 4096, lo16)
    XL(0); XL(1); XL(2); XL(3); XL(4); XL(5);
    const char* kvx = ws + WS_KV + (rb * 16 + j) * 22528;
    const char* wtq = ws + WS_WT + j * 153600 + (100 + w) * 1024;
    {
        f32x4 acc[5];
#pragma unroll
        for (int n = 0; n < 5; ++n) acc[n] = Z4;
        const u32 b0 = L0 + L_WKV + lo16;
        u32x4 bf[3][5];
#pragma unroll
        for (int n = 0; n < 5; ++n) { bf[0][n] = lds128(b0 + n * 1024); bf[1][n] = lds128(b0 + 5120 + n * 1024); }
        static_for<0, 20>([&](auto S) {
            constexpr int s = S;
            vmw1<(s < 14 ? 5 : 6)>(xa[s]);
            if constexpr (s + 6 < 20) XL(s + 6);
            if constexpr (s >= 13) { if (s < 19 || w < 2) glds16c(wtq + (s - 13) * 8192, lo16, L0 + L_WKV + (100 + w) * 1024 + (s - 13) * 8192); }
            SB0();
#pragma unroll
            for (int n = 0; n < 5; ++n) {
                if (s < 18) bf[(s + 2) % 3][n] = lds128(b0 + (s + 2) * 5120 + n * 1024);
                acc[n] = mfma(bf[s % 3][n], xa[s], acc[n]);
            }
#pragma unroll
            for (int n = 0; n < 5; ++n) { SGB(M_DSR, 1); SGB(M_MFMA, 1); }
            SB0();
        });
        STAMP(18);
        BAR();
        const int row = 256 + 16 * w + i16;
#pragma unroll
        for (int n = 0; n < 5; ++n) {
            const int Q = 4 * n + g;
            const u32x2 v = {pack2(acc[n][0], acc[n][1]), pack2(acc[n][2], acc[n][3])};
            const int wi = Q - 10, hd = wi >= 5 ? 1 : 0;
            const u32 a = Q < 10 ? L_K + row * 80 + 8 * Q : L_V + row * 96 + 48 * hd + 8 * (wi - 5 * hd);
            sts64(L0 + a, v);
        }
        if (g < 2) sts64(L0 + L_V + row * 96 + 48 * g + 40, (u32x2){0x3F80u, 0u});
        if (rb < 2) {
            const int nrow = rb == 0 ? 256 : 128;
            for (int i = tid; i < nrow * 5; i += 512) sts128(L0 + L_K + i * 16, (u32x4){0, 0, 0, 0});
            for (int i = tid; i < nrow * 6; i += 512) sts128(L0 + L_V + i * 16, (u32x4){0, 0, 0, 0});
        }
        vmw<0>();
        BAR();
    }
#undef XL
    STAMP(4);
    {
        u32x4 kvd[3];
#pragma unroll
        for (int e = 0; e < 3; ++e) {
            const int n = min(w + 8 * e, 21);
            kvd[e] = lds128(L0 + (n < 10 ? L_K + 20480 + n * 1024 : L_V + 24576 + (n - 10) * 1024) + lo16);
        }
        const u32 kvo = WS_KV + (rb * 16 + j) * 22528 + w * 1024 + lo16;
        const u16* wroot = wpm ? p.wu : p.wg;
        const char* wsrc = (const char*)wroot + (160 * wpq + 8 * w) * 3456 + wpb * 128;
        const u32 wvo = (l >> 3) * 3456 + (l & 7) * 16;
        f32x4 acc[3];
#pragma unroll
        for (int n = 0; n < 3; ++n) acc[n] = Z4;
        const u32 bq = L0 + L_WQ + lo16, bq2 = L0 + L_WQ + 2048 + g * 128 + (i16 & 7) * 16;
#define QRD(s, n) lds128(((n) < 2 ? bq + (n) * 1024 : bq2) + (s) * 2560)
        u32x4 bf[3][3];
#pragma unroll
        for (int n = 0; n < 3; ++n) { bf[0][n] = QRD(0, n); bf[1][n] = QRD(1, n); }
        Gather G;
        const u32 gg0 = l < 40 ? 480 + 40 * wpq + l : 640 + 40 * wpq + (l - 40), gg1 = 640 + 40 * wpq + 24 + (l & 15);
        static_for<0, 20>([&](auto S) {
            constexpr int s = S;
            if constexpr (s == 6) { if (w == 7 && wpwg) gather_ask(ws, gg0, gg1, G); }
            if constexpr (s == 0) { if (rb < 15) ST16C(kvd[0], kvo); }
            if constexpr (s == 1) { if (rb < 15) ST16C(kvd[1], kvo + 8192); }
            if constexpr (s == 2) { if (rb < 15 && w < 6) ST16C(kvd[2], kvo + 16384); }
            if constexpr (s == 14) {
                vmw<0>();
                u32 old = 0;
                if (l == 0) old = lds_add(L0 + L_CNT + 28, 1u);
                if (__builtin_amdgcn_readfirstlane(old) == 7 && l == 0) st_agent(ws + WS_FKV + (j * 16 + rb) * FS, tag);
                if (w == 7 && wpwg) { f32x4 ra, rc; gather_finish(ws, gg0, gg1, G, tag, ws, 2, ra, rc); put_strip_spread(ra, rc, L0 + L_SB, l); }
            }
            if constexpr (s == 15) { if (wpwg) glds16(wroot, wsrc, wvo, L0 + L_WPRAW + w * 1024); }
            if constexpr (s == 16) { if (wpwg) glds16(wroot, wsrc + 64 * 3456, wvo, L0 + L_WPRAW + (w + 8) * 1024); }
            if constexpr (s == 17) { if (wpwg && w < 4) glds16(wroot, wsrc + 128 * 3456, wvo, L0 + L_WPRAW + (w + 16) * 1024); }
            SB0();
#pragma unroll
            for (int n = 0; n < 3; ++n) {
                if (s < 18) bf[(s + 2) % 3][n] = QRD(s + 2, n);
                acc[n] = mfma(bf[s % 3][n], xa[s], acc[n]);
            }
#pragma unroll
            for (int n = 0; n < 3; ++n) { SGB(M_DSR, 1); SGB(M_MFMA, 1); }
            SB0();
        });
#undef QRD
        STAMP(21);
#pragma unroll
        for (int n = 0; n < 3; ++n) {
            const int Q = 4 * n + g;
            const u32x2 v = {pack2(acc[n][0] * QSCALE, acc[n][1] * QSCALE), pack2(acc[n][2] * QSCALE, acc[n][3] * QSCALE)};
            if (Q < 10) sts64(L0 + L_Q + (16 * w + i16) * 80 + 8 * Q, v);
        }
    }
    if (w == 0 && rb >= 1) poll_flags(ws + WS_FKV + (j * 16 + (rb >= 2 ? rb - 2 : rb - 1)) * FS, rb >= 2 ? 2 : 1, l, tag, ws, 3);
    VMCNT(0);
    BAR();
    STAMP(5);
    if (rb >= 1) {
#pragma unroll
        for (int e = 0; e < 3; ++e) {
            const int n = w + 8 * e;
            if (n < 22) {
                const u32 d = n < 10 ? L_K + n * 1024 : L_V + (n - 10) * 1024, st = n < 10 ? 10240 : 12288;
                glds16c(kvx - 16 * 22528 + n * 1024, lo16, L0 + d + st);
                if (rb >= 2) glds16c(kvx - 32 * 22528 + n * 1024, lo16, L0 + d);
            }
        }
    }
    auto wp_slot = [&](int gam, int& jj, int& slot) {
        jj = gam < 84 ? gam / 7 : 12 + (gam - 84) / 6;
        const int ar = gam - (jj < 12 ? 7 * jj : 84 + 6 * (jj - 12)), a = (jj < 12 || ar < 3) ? ar : ar + 1;
        slot = a < 3 ? 2 * a + wpm : a == 3 ? (wpm ? 13 : 6) : 7 + 2 * (a - 4) + wpm;
    };
    if (wpwg) {
        const u32 one = i16 == 0 ? 0x3F803F80u : 0u;
        const u32x4 ones = {one, one, one, one};
#pragma unroll
        for (int i = 0; i < 3; ++i) {
            if (i < 2 || w < 4) {
                const int f = w + 8 * i, ks = f >> 2, t16 = f & 3;
                int jj, slot; wp_slot(4 * wpb + t16, jj, slot);
                const u32 a = L0 + L_WPRAW + (32 * ks + 8 * g + q4) * 128 + 32 * t16 + 8 * p4;
                u32x4 wr = ldtr2(a, a + 512);
                const u32x4 ga = lds128(L0 + L_G2 + (32 * ks + 8 * g) * 4), gc = lds128(L0 + L_G2 + (32 * ks + 8 * g) * 4 + 16);
                const u32x4 sbz = lds128(L0 + (i16 == 0 ? L_SB + (32 * ks + 8 * g) * 2 : L_Z16));
                const f32x4 vs = mfma(wr, sbz, Z4);
                u32x4 o; float t0, t1, t2, t3, t4, t5, t6, t7;
                asm volatile("v_dot2_f32_bf16 %4, %12, %16, 0\n\tv_dot2_f32_bf16 %5, %12, %17, 0\n\tv_dot2_f32_bf16 %6, %13, %18, 0\n\tv_dot2_f32_bf16 %7, %13, %19, 0\n\t"
                             "v_dot2_f32_bf16 %8, %14, %20, 0\n\tv_dot2_f32_bf16 %9, %14, %21, 0\n\tv_dot2_f32_bf16 %10, %15, %22, 0\n\tv_dot2_f32_bf16 %11, %15, %23, 0\n\t"
                             "v_cvt_pk_bf16_f32 %0, %4, %5\n\tv_cvt_pk_bf16_f32 %1, %6, %7\n\tv_cvt_pk_bf16_f32 %2, %8, %9\n\ts_nop 0\n\tv_cvt_pk_bf16_f32 %3, %10, %11\n\ts_nop 1"
                             : "=&v"(o[0]), "=&v"(o[1]), "=&v"(o[2]), "=&v"(o[3]),
                               "=&v"(t0), "=&v"(t1), "=&v"(t2), "=&v"(t3), "=&v"(t4), "=&v"(t5), "=&v"(t6), "=&v"(t7)
                             : "v"(wr[0]), "v"(wr[1]), "v"(wr[2]), "v"(wr[3]), "v"(ga[0]), "v"(ga[1]), "v"(ga[2]), "v"(ga[3]), "v"(gc[0]), "v"(gc[1]), "v"(gc[2]), "v"(gc[3]));
                const f32x4 us = mfma(o, ones, Z4);
                ST16C(o, WS_WP + ((jj * 20 + 5 * wpq + ks) * 14 + slot) * 1024 + lo16);
                if (i16 == 0) { stsf4(L0 + L_UVL + f * 128 + g * 16, us); stsf4(L0 + L_UVL + f * 128 + 64 + g * 16, vs); }
            }
        }
    }
    if (wpwg) { if (w < 4) vmw<3>(); else vmw<2>(); } else vmw<0>();
    BAR();
    STAMP(6);
    if (wpwg && w == 3) {
#pragma unroll
        for (int e = 0; e < 2; ++e) {
            const int t16 = 2 * e + (l >> 5);
            int jj, slot; wp_slot(4 * wpb + t16, jj, slot);
            float v = 0.f;
#pragma unroll
            for (int ks = 0; ks < 5; ++ks) v += ldsf(L0 + L_UVL + (4 * ks + t16) * 128 + (l & 31) * 4);
            st_agent32(ws + WS_UV + ((jj * 14 + slot) * 4 + wpq) * 128 + (l & 31) * 4, __float_as_uint(v));
        }
    }
    const int hf = j & 1, cb = j >> 1;
    u32 wvoff[5];
#pragma unroll
    for (int i = 0; i < 5; ++i) {
        const u32 sg = 64 * i + l, pos = (sg * 6554) >> 16, c = sg - pos * 10, k = pos ^ (((pos >> 3) & 1) << 2);
        wvoff[i] = k * 1280 + c * 16;
    }
    const char* wosrc = (const char*)p.wo + cb * 160;
#define WO_STEP(sx) do { _Pragma("unroll") for (int i = 0; i < 5; ++i) glds16(p.wo, wosrc + (sx) * 32 * 1280, wvoff[i], L0 + L_WO + (sx) * 5120 + i * 1024); } while (0)
    STAMP(7);

    {
        const int hd = w & 1, qi = w >> 1;
        const int kr0 = 32 * qi;
        u32x4 qf[2];
#pragma unroll
        for (int a = 0; a < 2; ++a) {
            const u32 qa = L0 + L_Q + (kr0 + 16 * a + i16) * 80 + hd * 40;
            const u32x2 q0 = lds64(qa + 8 * g);
            u32x2 q1 = lds64(qa + 32);
            if (g != 0) q1 = (u32x2){0, 0};
            qf[a] = (u32x4){q0[0], q0[1], q1[0], q1[1]};
        }
        const u32 kb0 = L0 + L_K + (kr0 + i16) * 80 + hd * 40 + 8 * g, kb1 = L0 + L_K + (kr0 + i16) * 80 + hd * 40 + 32;
        f32x4 mfirst, mlast;
#pragma unroll
        for (int r = 0; r < 4; ++r) { const bool lo = 4 * g + r <= i16; mfirst[r] = lo ? -INFINITY : 0.f; mlast[r] = lo ? 0.f : -INFINITY; }
        f32x4 s0[17], s1[17];
        {
            u32x4 kf[6];
#define KRD(t) do { const u32x2 k0 = lds64(kb0 + (t) * 1280), k1 = lds64(kb1 + (t) * 1280); kf[(t) % 6] = (u32x4){k0[0], k0[1], k1[0], k1[1]}; } while (0)
            KRD(0); KRD(1); KRD(2); KRD(3); KRD(4);
            static_for<0, 18>([&](auto T) {
                constexpr int t = T;
                SB0();
                if constexpr (t < 13) KRD(t + 5);
                if constexpr (t < 17) s0[t] = mfma(kf[t % 6], qf[0], t == 0 ? mfirst : t == 16 ? mlast : Z4);
                if constexpr (t > 0) s1[t - 1] = mfma(kf[t % 6], qf[1], t == 1 ? mfirst : t == 17 ? mlast : Z4);
                SGB(M_DSR, 2); SGB(M_MFMA, 2);
                SB0();
            });
#undef KRD
        }
        WO_STEP(4 + w);
        if (rb < 2) {
            const int fv = rb == 1 ? 128 : 256;
#pragma unroll
            for (int t = 0; t < 16; ++t) {
                const bool d0 = kr0 + 16 * t < fv, d1 = kr0 + 16 * t + 16 < fv;
#pragma unroll
                for (int r = 0; r < 4; ++r) { s0[t][r] = d0 ? -INFINITY : s0[t][r]; s1[t][r] = d1 ? -INFINITY : s1[t][r]; }
            }
        }
        STAMP(22);
        float m0 = fmaxf(s0[0][0], s0[0][1]), m1 = fmaxf(s1[0][0], s1[0][1]), n0 = fmaxf(s0[0][2], s0[0][3]), n1 = fmaxf(s1[0][2], s1[0][3]);
#pragma unroll
        for (int t = 1; t < 17; ++t) {
            m0 = fmaxf(fmaxf(m0, s0[t][0]), s0[t][1]); m1 = fmaxf(fmaxf(m1, s1[t][0]), s1[t][1]);
            n0 = fmaxf(fmaxf(n0, s0[t][2]), s0[t][3]); n1 = fmaxf(fmaxf(n1, s1[t][2]), s1[t][3]);
        }
        m0 = fmaxf(m0, n0); m1 = fmaxf(m1, n1);
        m0 = xmax4(m0); m1 = xmax4(m1);
        WO_STEP(12 + w);
        STAMP(23);
        f32x4 o00 = Z4, o01 = Z4, o10 = Z4, o11 = Z4;
        const u32 vb = L0 + L_V + (kr0 + 4 * g + q4) * 96 + hd * 48 + 8 * p4;
        auto pv = [&](auto SUB) {
            constexpr bool sub = SUB;
            u32x4 vf[2][2];
            vf[0][0] = ldtr2(vb, vb + 16 * 96); vf[0][1] = ldtr2(vb + 32, vb + 16 * 96 + 32);
#define EX(v, m) __builtin_amdgcn_exp2f(sub ? (v) - (m) : (v))
            static_for<0, 9>([&](auto U) {
                constexpr int u = U;
                if constexpr (u < 8) { const u32 a = vb + (u + 1) * 32 * 96; vf[(u + 1) & 1][0] = ldtr2(a, a + 16 * 96); vf[(u + 1) & 1][1] = ldtr2(a + 32, a + 16 * 96 + 32); }
                u32x4 pa, pb;
                pa[0] = pack2(EX(s0[2 * u][0], m0), EX(s0[2 * u][1], m0)); pa[1] = pack2(EX(s0[2 * u][2], m0), EX(s0[2 * u][3], m0));
                if constexpr (u < 8) { pa[2] = pack2(EX(s0[2 * u + 1][0], m0), EX(s0[2 * u + 1][1], m0)); pa[3] = pack2(EX(s0[2 * u + 1][2], m0), EX(s0[2 * u + 1][3], m0)); }
                else { pa[2] = 0; pa[3] = 0; }
                if constexpr (u > 0) { pb[0] = pack2(EX(s1[2 * u - 1][0], m1), EX(s1[2 * u - 1][1], m1)); pb[1] = pack2(EX(s1[2 * u - 1][2], m1), EX(s1[2 * u - 1][3], m1)); }
                else { pb[0] = 0; pb[1] = 0; }
                pb[2] = pack2(EX(s1[2 * u][0], m1), EX(s1[2 * u][1], m1)); pb[3] = pack2(EX(s1[2 * u][2], m1), EX(s1[2 * u][3], m1));
                o00 = mfma(vf[u & 1][0], pa, o00); o01 = mfma(vf[u & 1][1], pa, o01);
                o10 = mfma(vf[u & 1][0], pb, o10); o11 = mfma(vf[u & 1][1], pb, o11);
                SB0();
            });
#undef EX
        };
        if (__all(fabsf(m0) <= 64.f && fabsf(m1) <= 64.f)) pv(std::false_type{}); else pv(std::true_type{});
        STAMP(24);
        const float i0 = __builtin_amdgcn_rcpf(__shfl(o01[0], i16 + 16)), i1 = __builtin_amdgcn_rcpf(__shfl(o11[0], i16 + 16));
        const u32 oa = L0 + L_Q + (kr0 + i16) * 80 + hd * 40;
        sts64(oa + 8 * g, (u32x2){pack2(o00[0] * i0, o00[1] * i0), pack2(o00[2] * i0, o00[3] * i0)});
        sts64(oa + 1280 + 8 * g, (u32x2){pack2(o10[0] * i1, o10[1] * i1), pack2(o10[2] * i1, o10[3] * i1)});
        if (g == 0) {
            sts64(oa + 32, (u32x2){pack2(o01[0] * i0, o01[1] * i0), pack2(o01[2] * i0, o01[3] * i0)});
            sts64(oa + 1280 + 32, (u32x2){pack2(o11[0] * i1, o11[1] * i1), pack2(o11[2] * i1, o11[3] * i1)});
        }
    }
    BAR();
    {
        const int tok = 64 * (w & 1) + l;
        const u32 oo = WS_O + (((rb * 8 + (tok >> 4)) * 80 + 5 * j) * 16 + (tok & 15)) * 16;
        ST16C(lds128(L0 + L_Q + tok * 80 + (w >> 1) * 16), oo + (w >> 1) * 256);
        if (w < 2) ST16C(lds128(L0 + L_Q + tok * 80 + 64), oo + 4 * 256);
    }
    VMCNT(0);
    BAR();
    if (tid == 0) {
        st_agent(ws + WS_FO + (rb * 16 + j) * FS, tag);
        if (wpwg) st_agent(ws + WS_FWP + b * FS, tag);
    }
    STAMP(8);

    const int wm = w & 3, wk = w >> 2;
    const int mt3 = rb * 8 + 4 * hf + wm;
    u32x2 x1k[5];
    {
        const int wdu = b + 256 * (w - 6);
        const bool wdw = w >= 6 && wdu < 432;
        if (w >= 1 && w < 5) {
#pragma unroll
            for (int i = 0; i < 5; ++i) glds16(p.wo, wosrc + (w - 1) * 32 * 1280, wvoff[i], L0 + L_WO + (w - 1) * 5120 + i * 1024);
        } else if (w >= 5) {
#pragma unroll
            for (int e = 0; e < 4; ++e) {
                const int pc = w - 5 + 3 * e;
                if (pc < 10) {
                    const u32 sg = 64 * pc + l, r = (sg * 6554) >> 16, c = sg - r * 10;
                    glds16(p.x, (const char*)p.x + (rb * 128 + 64 * hf) * 1280 + cb * 160, r * 1280 + c * 16, L0 + L_XR + pc * 1024);
                }
            }
            if (wdw) {
                const int ucb = wdu / 54, us = wdu - 54 * ucb;
#pragma unroll
                for (int i = 0; i < 5; ++i) glds16(p.wd, (const char*)p.wd + ucb * 160 + us * 32 * 1280, wvoff[i], L0 + L_WDRAW + (w - 6) * 5120 + i * 1024);
            }
        }
        if (w == 1 && l < 20) stsf4(L0 + L_GA + l * 16, gather1(ws, 320 + 20 * cb + l, tag, ws, 4));
        if (w == 0) {
            const int bl = min((Gj >> 2) + ((l >> 2) % 3), (Gj + nj - 1) >> 2);
            poll_at(ws, WS_FWP + (108 * (l >= 12 && l < 24 ? 1 : 0) + 4 * bl + (l & 3)) * FS, l, tag, ws, 6);
            poll_flags(ws + WS_FO + rb * 16 * FS, 16, l, tag, ws, 5);
        }
        if (wdw) vmw<5>(); else vmw<0>();
        BAR();
        STAMP(9);
        u32x4 a[10];
        const char* asrc = ws + WS_O + (mt3 * 80 + 40 * wk) * 256;
#define AL(s) a[s] = gld16c(((s) & 3) * 1024, asrc + ((s) >> 2) * 4096, lo16)
        AL(0); AL(1); AL(2); AL(3);
        f32x4 acc[5];
#pragma unroll
        for (int n = 0; n < 5; ++n) acc[n] = Z4;
        const u32 wb = L0 + L_WO + wk * 51200;
        const u32 b0 = wb + 160 * (8 * g + 4 * (g & 1) + q4) + 8 * p4, b1 = wb + 160 * (8 * g + 4 * ((g & 1) ^ 1) + q4) + 8 * p4;
        u32x4 bf[3][5];
#pragma unroll
        for (int n = 0; n < 5; ++n) { bf[0][n] = ldtr2(b0 + n * 32, b1 + n * 32); bf[1][n] = ldtr2(b0 + 5120 + n * 32, b1 + 5120 + n * 32); }
        static_for<0, 10>([&](auto S) {
            constexpr int s = S;
            vmw1<(s < 7 ? 3 : 9 - s)>(a[s]);
            if constexpr (s + 4 < 10) AL(s + 4);
            SB0();
#pragma unroll
            for (int n = 0; n < 5; ++n) {
                if (s < 8) bf[(s + 2) % 3][n] = ldtr2(b0 + (s + 2) * 5120 + n * 32, b1 + (s + 2) * 5120 + n * 32);
                acc[n] = mfma(bf[s % 3][n], a[s], acc[n]);
            }
#pragma unroll
            for (int n = 0; n < 5; ++n) { SGB(M_DSR, 2); SGB(M_MFMA, 1); }
            SB0();
        });
#undef AL
        STAMP(25);
        if (wk == 1) {
#pragma unroll
            for (int n = 0; n < 5; ++n) stsf4(L0 + L_RED3 + (wm * 5 + n) * 1024 + lo16, acc[n]);
        }
        BAR();
        if (wk == 0) {
            float s1 = 0.f, s2 = 0.f;
#pragma unroll
            for (int n = 0; n < 5; ++n) {
                const f32x4 v = acc[n] + ldsf4(L0 + L_RED3 + (wm * 5 + n) * 1024 + lo16);
                const f32x4 ga = ldsf4(L0 + L_GA + (16 * n + 4 * g) * 4);
                const u32x2 xr = lds64(L0 + L_XR + (16 * wm + i16) * 160 + 32 * n + 8 * g);
                const u32x2 o = {pack2(bflo(xr[0]) + ga[0] * v[0], bfhi(xr[0]) + ga[1] * v[1]), pack2(bflo(xr[1]) + ga[2] * v[2], bfhi(xr[1]) + ga[3] * v[3])};
                x1k[n] = o;
                s1 = dot2(o[0], 0x3F803F80u, s1); s1 = dot2(o[1], 0x3F803F80u, s1);
                s2 = dot2(o[0], o[0], s2); s2 = dot2(o[1], o[1], s2);
            }
            const u32 xo = WS_X1 + ((mt3 * 80 + 10 * cb) * 16 + i16) * 16;
#pragma unroll
            for (int n = 0; n < 4; n += 2) {
                const auto a0 = __builtin_amdgcn_permlane16_swap(x1k[n][0], x1k[n + 1][0], false, false);
                const auto a1 = __builtin_amdgcn_permlane16_swap(x1k[n][1], x1k[n + 1][1], false, false);
                ST16C(((u32x4){a0[0], a1[0], a0[1], a1[1]}), xo + (2 * (n + (g & 1)) + (g >> 1)) * 256);
            }
            ST8C(x1k[4], xo + (8 + (g >> 1)) * 256 + 8 * (g & 1));
            s1 = xsum4(s1); s2 = xsum4(s2);
            if (l < 16) ST8C(((u32x2){__float_as_uint(s1), __float_as_uint(s2)}), WS_STAT + ((rb * 8 + cb) * 128 + 64 * hf + 16 * wm + l) * 8);
            STAMP(26);
            vmw<0>();
        } else {
#pragma unroll
            for (int n = 0; n < 5; ++n) x1k[n] = (u32x2){0, 0};
            if (wdw) {
                const u32 wr = L0 + L_WDRAW + (w - 6) * 5120;
                const u32 r0 = wr + 160 * (8 * g + 4 * (g & 1) + q4) + 8 * p4, r1 = wr + 160 * (8 * g + 4 * ((g & 1) ^ 1) + q4) + 8 * p4;
#pragma unroll
                for (int n = 0; n < 5; ++n) ST16C(ldtr2(r0 + n * 32, r1 + n * 32), WS_WD + (wdu * 5 + n) * 1024 + lo16);
            }
        }
        BAR();
        if (tid == 0) st_agent(ws + WS_FX1 + (rb * 16 + j) * FS, tag);
    }
    STAMP(10);

    const char* wdsrc = ws + WS_WD + (cb * 54 + wk * 27) * 5120;
    const u32 r5w = L0 + wk * 20480 + wm * 5120;
#define P5_B1(t, i) do { if ((i) == 0 || (i) == 4) glds16c(wdsrc + min(4 * (t) + wm, 26) * 5120 + (i) * 1024, lo16, r5w + r5_slot(t) + (i) * 1024); \
        else glds16c_next(((i) & 3) * 1024, wdsrc + min(4 * (t) + wm, 26) * 5120, lo16); } while (0)
    {
        const int wn = w >> 2;
        const int bst = w < 4 ? 4 * w : 16 + 3 * (w - 4);
        const char* wpj = ws + WS_WP + (j * 20 * 14 + bst) * 1024;
        const char* x1b = ws + WS_X1 + (rb * 8 + w) * 80 * 256;
        const u32 r4a = L0 + L_R4 + w * 2048, r4b = L0 + L_R4 + 16384 + bst * 1024;
#define P4_B1(t, i) do { if ((i) == 0) glds16c(wpj + (t) * 28 * 1024, lo16, r4b + ((t) % 3) * R4_STAGE); else glds16c_next((i) * 1024, wpj + (t) * 28 * 1024, lo16); } while (0)
#define P4_A1(t, i) do { if ((i) == 0) glds16c(x1b + 8 * (t) * 256, lo16, r4a + ((t) % 3) * R4_STAGE); else glds16c_next(1024, x1b + 8 * (t) * 256, lo16); } while (0)
#define P4_B(t) do { P4_B1(t, 0); P4_B1(t, 1); P4_B1(t, 2); if (w < 4) P4_B1(t, 3); } while (0)
        P4_B(0); P4_B(1); P4_B(2);
        if (w == 0) poll_flags(ws + WS_FX1 + rb * 16 * FS, 16, l, tag, ws, 7);
        BAR();
        STAMP(11);
        glds16c(ws + WS_STAT + rb * 8192 + w * 1024, lo16, L0 + L_ST4 + w * 1024);
        if (w < 7) glds16c(ws + WS_UV + j * 7168 + w * 1024, lo16, L0 + L_UV + w * 1024);
        P4_A1(0, 0); P4_A1(0, 1); P4_A1(1, 0); P4_A1(1, 1); P4_A1(2, 0); P4_A1(2, 1);
        f32x4 acc[7][2];
#pragma unroll
        for (int n = 0; n < 7; ++n) { acc[n][0] = Z4; acc[n][1] = Z4; }
        const u32 ra = opaque(L0 + L_R4 + 2 * wm * 2048 + lo16), rbb = opaque(L0 + L_R4 + 16384 + 7 * wn * 1024 + lo16);
        u32x4 fa[2][2], fb[2][7];
#define P4_RA(buf, t, ks) do { const u32 so = ((t) % 3) * R4_STAGE + (ks) * 1024; fa[buf][0] = lds128(ra + so); fa[buf][1] = lds128(ra + so + 2048); } while (0)
#define P4_RB(buf, t, ks, n) fb[buf][n] = lds128(rbb + ((t) % 3) * R4_STAGE + (ks) * 14336 + (n) * 1024)
#define P4_MM(buf, n) do { acc[n][0] = mfma(fb[buf][n], fa[buf][0], acc[n][0]); acc[n][1] = mfma(fb[buf][n], fa[buf][1], acc[n][1]); } while (0)
#define P4_HALF(buf, rd, t, ks, V0, V1, V2) do { \
            SB0(); if (rd) { P4_RA((buf) ^ 1, t, ks); P4_RB((buf) ^ 1, t, ks, 0); } P4_MM(buf, 0); SB0(); V0; \
            SB0(); if (rd) P4_RB((buf) ^ 1, t, ks, 1); P4_MM(buf, 1); SB0(); \
            SB0(); if (rd) P4_RB((buf) ^ 1, t, ks, 2); P4_MM(buf, 2); SB0(); V1; \
            SB0(); if (rd) P4_RB((buf) ^ 1, t, ks, 3); P4_MM(buf, 3); SB0(); \
            SB0(); if (rd) P4_RB((buf) ^ 1, t, ks, 4); P4_MM(buf, 4); SB0(); V2; \
            SB0(); if (rd) P4_RB((buf) ^ 1, t, ks, 5); P4_MM(buf, 5); SB0(); \
            SB0(); if (rd) P4_RB((buf) ^ 1, t, ks, 6); P4_MM(buf, 6); SB0(); } while (0)
        VMCNT(4);
        BAR();
        if (tid == 0) st_agent(ws + WS_FW2 + b * FS, tag);
        STAMP(27);
        u32x2 wdf = {0u, 0u};
        const u32 wdo = WS_FW2 + ((54 * cb + (l < 54 ? l : 0)) & 255) * FS;
        P4_RA(0, 0, 0);
#pragma unroll
        for (int n = 0; n < 7; ++n) P4_RB(0, 0, 0, n);
        static_for<0, 10>([&](auto T) {
            constexpr int t = T;
            constexpr bool f = t >= 1 && t + 2 <= 9, sN = t + 3 <= 9;
            if constexpr (t < 9) P4_HALF(0, true, t, 1, if (f && w < 4) P4_B1(t + 2, 3), if (f) P4_A1(t + 2, 0), if (f) P4_A1(t + 2, 1));
            else P4_HALF(0, true, t, 1, P5_B1(0, 3), P5_B1(0, 4), P5_B1(1, 0));
            if constexpr (t == 5) {
                if (tid < 128) {
                    f32x2 st = {0.f, 0.f};
#pragma unroll
                    for (int c = 0; c < 8; ++c) st += *(LAS f32x2*)(size_t)(L0 + L_ST4 + (c * 128 + tid) * 8);
                    const float mu = st[0] * (1.f / 640.f), var = fmaxf(st[1] * (1.f / 640.f) - mu * mu, 0.f), r = __builtin_amdgcn_rsqf(var + 1e-5f);
                    stsf2(L0 + L_MR + tid * 8, (f32x2){r, -mu * r});
                } else {
                    const int e = tid - 128;
                    stsf(L0 + L_UVS + e * 4, (ldsf(L0 + L_UV + (e >> 5) * 512 + (e & 31) * 4) + ldsf(L0 + L_UV + (e >> 5) * 512 + 128 + (e & 31) * 4))
                                           + (ldsf(L0 + L_UV + (e >> 5) * 512 + 256 + (e & 31) * 4) + ldsf(L0 + L_UV + (e >> 5) * 512 + 384 + (e & 31) * 4)));
                    if (e < 64) { const int e2 = e + 384;
                        stsf(L0 + L_UVS + e2 * 4, (ldsf(L0 + L_UV + (e2 >> 5) * 512 + (e2 & 31) * 4) + ldsf(L0 + L_UV + (e2 >> 5) * 512 + 128 + (e2 & 31) * 4))
                                                + (ldsf(L0 + L_UV + (e2 >> 5) * 512 + 256 + (e2 & 31) * 4) + ldsf(L0 + L_UV + (e2 >> 5) * 512 + 384 + (e2 & 31) * 4))); }
                }
            }
            if constexpr (t < 9) {
                if constexpr (t == 0) vmw<2>();
                else if constexpr (t == 8) {
                    asm volatile("s_waitcnt vmcnt(0)" : "+v"(wdf) :: "memory");
                    if (w == 7) { if (!__all(wdf[0] == (u32)tag && wdf[1] == (u32)(tag >> 32))) poll_at(ws, wdo, l, tag, ws, 12); }
                }
                else { if (w < 4) vmw<6>(); else vmw<5>(); }
                BAR();
                if constexpr (t == 7) { if (w == 7) asm volatile("s_nop 4\n\tglobal_load_dwordx2 %0, %1, %2 sc1" : "=v"(wdf) : "v"(wdo), "s"(ws) : "memory"); }
                if constexpr (t < 8) P4_HALF(1, true, t + 1, 0, if (sN) P4_B1(t + 3, 0), if (sN) P4_B1(t + 3, 1), if (sN) P4_B1(t + 3, 2));
                else P4_HALF(1, true, t + 1, 0, P5_B1(0, 0), P5_B1(0, 1), P5_B1(0, 2));
            } else {
                P4_HALF(1, false, 0, 0, P5_B1(1, 1), P5_B1(1, 2), P5_B1(1, 3));
                P5_B1(1, 4);
            }
        });
#undef P4_B
#undef P4_B1
#undef P4_A1
#undef P4_RA
#undef P4_RB
#undef P4_MM
#undef P4_HALF
        STAMP(12);
        f32x2 mr[2];
#pragma unroll
        for (int m = 0; m < 2; ++m) mr[m] = *(LAS f32x2*)(size_t)(L0 + L_MR + (32 * wm + 16 * m + i16) * 8);
#pragma unroll
        for (int n = 0; n < 7; ++n) {
            const f32x4 u = ldsf4(L0 + L_UVS + (7 * wn + n) * 128 + 16 * g), v = ldsf4(L0 + L_UVS + (7 * wn + n) * 128 + 64 + 16 * g);
#pragma unroll
            for (int m = 0; m < 2; ++m) acc[n][m] = acc[n][m] * mr[m][0] + (u * mr[m][1] + v);
        }
        if (nj == 7) stsf4(L0 + L_XCH + w * 1024 + lo16, wn == 0 ? acc[6][1] : acc[6][0]);
        auto silu_mul = [&](const f32x4 gt, const f32x4 up) -> u32x2 {
            const f32x4 e = gt * (-LOG2E), gu = gt * up;
            f32x4 d;
#pragma unroll
            for (int r = 0; r < 4; ++r) d[r] = __builtin_amdgcn_exp2f(e[r]);
            d = d + 1.f;
#pragma unroll
            for (int r = 0; r < 4; ++r) d[r] = __builtin_amdgcn_rcpf(d[r]);
            const f32x4 o = gu * d;
            return (u32x2){pack2(o[0], o[1]), pack2(o[2], o[3])};
        };
#pragma unroll
        for (int a = 0; a < 3; ++a) {
            const int la = a + 4 * wn, gam = Gj + (la <= 3 || nj == 7 ? la : la - 1);
            const u32x2 o0 = silu_mul(acc[2 * a][0], acc[2 * a + 1][0]), o1 = silu_mul(acc[2 * a][1], acc[2 * a + 1][1]);
            const auto s0 = __builtin_amdgcn_permlane16_swap(o0[0], o1[0], false, false);
            const auto s1 = __builtin_amdgcn_permlane16_swap(o0[1], o1[1], false, false);
            const u32x4 v = {s0[0], s1[0], s0[1], s1[1]};
            ST16C(v, WS_ACT + (((rb * 8 + 2 * wm + (g & 1)) * 216 + 2 * gam + (g >> 1)) * 16 + i16) * 16);
        }
        STAMP(28);
        BAR();
        if (nj == 7) {
            const f32x4 other = ldsf4(L0 + L_XCH + (w ^ 4) * 1024 + lo16);
            const u32x2 o = wn == 0 ? silu_mul(acc[6][0], other) : silu_mul(other, acc[6][1]);
            ST8C(o, WS_ACT + (((rb * 8 + 2 * wm + wn) * 216 + 2 * (Gj + 3) + (g >> 1)) * 16 + i16) * 16 + 8 * (g & 1));
        }
        P5_B1(2, 0); P5_B1(2, 1); P5_B1(2, 2); P5_B1(2, 3); P5_B1(2, 4);
        vmw<5>();
        BAR();
        if (tid == 0) st_agent(ws + WS_FACT + (rb * 16 + j) * FS, tag);
    }
    STAMP(13);

    {
        if (w == 1 && l < 20) stsf4(L0 + L_GB + l * 16, gather1(ws, 800 + 20 * cb + l, tag, ws, 8));
        if (w == 0) poll_flags(ws + WS_FACT + rb * 16 * FS, 16, l, tag, ws, 9);
        const bool bump = rb == 15 && j == 0;
        if (bump && w >= 4) poll_at(ws, WS_FO + (tid - 256) * FS, l, tag, ws, 10);
        VMCNT(0);
        BAR();
        if (bump && tid == 0) st_agent(ws + WS_EPOCH, tag);
        STAMP(14);
        const char* asrc = ws + WS_ACT + (mt3 * 216 + wk * 108) * 256;
        u32x4 a[3][4];
#define P5_A1(t, i) a[(t) % 3][i] = gld16c((i) * 1024, asrc + (t) * 4096, lo16)
        P5_A1(0, 0); P5_A1(0, 1); P5_A1(0, 2); P5_A1(0, 3); P5_A1(1, 0); P5_A1(1, 1); P5_A1(1, 2); P5_A1(1, 3); P5_A1(2, 0); P5_A1(2, 1); P5_A1(2, 2); P5_A1(2, 3);
        f32x4 acc[5];
#pragma unroll
        for (int n = 0; n < 5; ++n) acc[n] = Z4;
        const u32 rb5 = opaque(L0 + wk * 20480 + lo16);
        u32x4 fb[3][5];
#define P5_RD(buf, t, kk) do { _Pragma("unroll") for (int n = 0; n < 5; ++n) fb[buf][n] = lds128(rb5 + r5_slot(t) + (kk) * 5120 + n * 1024); } while (0)
#define P5_MM(buf, av) do { _Pragma("unroll") for (int n = 0; n < 5; ++n) acc[n] = mfma(fb[buf][n], (av), acc[n]); } while (0)
#define P5_MIX() do { _Pragma("unroll") for (int n = 0; n < 5; ++n) { SGB(M_DSR, 1); SGB(M_MFMA, 1); } } while (0)
        P5_RD(0, 0, 0); P5_RD(1, 0, 1);
        static_for<0, 7>([&](auto T) {
            constexpr int t = T, q = t % 3, k0 = 4 * t;
            constexpr bool nx = t + 3 < 7;
            constexpr int NA = t == 0 ? 8 : t == 1 ? 13 : t == 2 ? 18 : t == 3 ? 19 : t == 4 ? 18 : t == 5 ? 9 : 3;
            constexpr int NB = t == 2 ? 11 : t == 3 ? 11 : t == 4 ? 8 : t == 5 ? 0 : 63;
            vmw4<NA>(a[q][0], a[q][1], a[q][2], a[q][3]);
            SB0(); P5_RD((k0 + 2) % 3, t, 2); P5_MM(k0 % 3, a[q][0]); P5_MIX(); SB0();
            if constexpr (nx) P5_A1(t + 3, 0);
            if constexpr (t < 6) {
                SB0(); P5_RD((k0 + 3) % 3, t, 3); P5_MM((k0 + 1) % 3, a[q][1]); P5_MIX(); SB0();
                if constexpr (nx) P5_A1(t + 3, 1);
                vmw<NB>(); BAR();
                if constexpr (nx) { P5_B1(t + 3, 0); P5_B1(t + 3, 1); }
                SB0(); P5_RD((k0 + 4) % 3, t + 1, 0); P5_MM((k0 + 2) % 3, a[q][2]); P5_MIX(); SB0();
                if constexpr (nx) { P5_A1(t + 3, 2); P5_B1(t + 3, 2); P5_B1(t + 3, 3); }
                SB0(); P5_RD((k0 + 5) % 3, t + 1, 1); P5_MM((k0 + 3) % 3, a[q][3]); P5_MIX(); SB0();
                if constexpr (nx) { if constexpr (t + 3 < 6) P5_A1(t + 3, 3); P5_B1(t + 3, 4); }
            } else {
                SB0(); P5_MM((k0 + 1) % 3, a[q][1]); SB0(); P5_MM((k0 + 2) % 3, a[q][2]); SB0();
            }
        });
#undef P5_B1
#undef P5_A1
#undef P5_RD
#undef P5_MM
#undef P5_MIX
        STAMP(15);
        if (wk == 1) {
#pragma unroll
            for (int n = 0; n < 5; ++n) stsf4(L0 + L_RED5 + (wm * 5 + n) * 1024 + lo16, acc[n]);
        }
        BAR();
        if (wk == 0) {
#pragma unroll
            for (int n = 0; n < 5; ++n) {
                const f32x4 v = acc[n] + ldsf4(L0 + L_RED5 + (wm * 5 + n) * 1024 + lo16);
                const f32x4 gb = ldsf4(L0 + L_GB + (16 * n + 4 * g) * 4);
                const u32x2 o = {pack2(bflo(x1k[n][0]) + gb[0] * v[0], bfhi(x1k[n][0]) + gb[1] * v[1]), pack2(bflo(x1k[n][1]) + gb[2] * v[2], bfhi(x1k[n][1]) + gb[3] * v[3])};
                sts64(L0 + L_OS + (16 * wm + i16) * 160 + 32 * n + 8 * g, o);
            }
        }
        BAR();
#pragma unroll
        for (int i = 0; i < 2; ++i) {
            const int id = tid + 512 * i;
            if (id < 640) {
                const int r = (id * 6554) >> 16, c = id - 10 * r;
                *(GAS u32x4*)((char*)p.out + (rb * 128 + 64 * hf + r) * 1280 + cb * 160 + c * 16) = lds128(L0 + L_OS + id * 16);
            }
        }
    }
    STAMP(16); STAMP_RT(63); STAMP_OUT();
    asm volatile(".Lmk_code_to:");
}

extern "C" void kernel_launch(void* const* d_in, const int* in_sizes, int n_in, void* d_out, int out_size, void* d_ws, size_t ws_size, hipStream_t stream) {
    Params p;
    p.x = (const u16*)d_in[0]; p.wqkv = (const u16*)d_in[1]; p.wo = (const u16*)d_in[2];
    p.wg = (const u16*)d_in[4]; p.wu = (const u16*)d_in[5]; p.wd = (const u16*)d_in[6];
    p.c = (const u16*)d_in[7]; p.wmod = (const u16*)d_in[8];
    p.out = (u16*)d_out; p.ws = (char*)d_ws;
    mega_kernel<<<256, 512, 0, stream>>>(p);
}
```

```cpp
#define PA 0
#define PB 0
#include <hip/hip_runtime.h>
#include <hip/hip_bf16.h>
#include <stdint.h>
#include <type_traits>

typedef unsigned short u16;
typedef unsigned u32;
typedef unsigned long long u64;
typedef __attribute__((ext_vector_type(2))) unsigned u32x2;
typedef __attribute__((ext_vector_type(4))) unsigned u32x4;
typedef __attribute__((ext_vector_type(2))) float f32x2;
typedef __attribute__((ext_vector_type(4))) float f32x4;
typedef __attribute__((ext_vector_type(2))) __bf16 bf16x2;
typedef __attribute__((ext_vector_type(8))) __bf16 bf16x8;
typedef __attribute__((ext_vector_type(4))) short s16x4;
#define LAS __attribute__((address_space(3)))
#define GAS __attribute__((address_space(1)))
#define DI __device__ __forceinline__

constexpr int SEQ = 2048, DM = 640, FF = 1728;
constexpr float QSCALE = 0.22360679774997896f * 1.4426950408889634f;
constexpr float LOG2E = 1.4426950408889634f;

constexpr u32 WS_EPOCH = 0;
constexpr u32 WS_ERR   = 256;
constexpr u32 FS = 128;
constexpr u32 WS_FKV   = 4096;
constexpr u32 WS_FO    = WS_FKV + 256 * FS;
constexpr u32 WS_FX1   = WS_FO + 256 * FS;
constexpr u32 WS_FACT  = WS_FX1 + 256 * FS;
constexpr u32 WS_FH    = WS_FACT + 256 * FS;
constexpr u32 WS_FWT   = WS_FH + 256 * FS;
constexpr u32 WS_FWP   = WS_FWT + 256 * FS;
constexpr u32 WS_FW2   = WS_FWP + 256 * FS;
constexpr u32 WS_MODG  = WS_FW2 + 256 * FS;
constexpr u32 WS_UV    = WS_MODG + 61440;
constexpr u32 WS_STAT  = WS_UV + 114688;
constexpr u32 WS_KV    = 589824;
constexpr u32 WS_O     = WS_KV + 256 * 22528;
constexpr u32 WS_X1    = WS_O + SEQ * DM * 2;
constexpr u32 WS_ACT   = WS_X1 + SEQ * DM * 2;
constexpr u32 WS_WP    = WS_ACT + SEQ * FF * 2;
constexpr u32 WS_H     = WS_WP + 16 * 20 * 14 * 1024;
constexpr u32 WS_WD    = WS_H + SEQ * DM * 2;
constexpr u32 WS_WO    = WS_WD + 8 * 54 * 5 * 1024;
constexpr u32 WS_WT    = WS_WO + 8 * 20 * 5 * 1024;
constexpr u32 WS_STAMP = WS_WT + 16 * 153600;
constexpr u32 WS_END   = WS_STAMP + 256 * 8 * 64 * 4;

constexpr u32 L_WKV = 0, L_WQ = 102400, L_GS = 153600, L_STP = 156160, L_MODP = 156416, L_CNT = 157440, L_SB = 157696, L_G2 = 158016, L_Z16 = 158656;
constexpr u32 L_ODRAW = 101376, L_WTRAW = 116736, L_XS = 133120;
constexpr u32 L_K = 0, L_V = 30720, L_Q = 67584, L_WPRAW = 77824, L_UVL = 98304;
constexpr u32 L_WO = 61440, L_RED3 = 0, L_X1S = 20480, L_XR = 30720, L_GA = 40960, L_ST3 = 41472, L_WDRAW = 43008;
constexpr u32 L_R4 = 0, R4_STAGE = 45056, L_ST4 = 135168, L_UV = 143360, L_MR = 150528, L_XCH = 151552, L_UVS = 159744;
constexpr u32 L_RED5 = 126976, L_GB = 147456, L_OS = 148480;
constexpr u32 r5_slot(int t) { return t % 3 == 0 ? 45056 : t % 3 == 1 ? 86016 : 0; }

struct Params {
    const u16* x; const u16* wqkv; const u16* wo; const u16* wg; const u16* wu; const u16* wd;
    const u16* c; const u16* wmod; u16* out; char* ws;
};

DI float bflo(u32 d) { return __uint_as_float(d << 16); }
DI float bfhi(u32 d) { return __uint_as_float(d & 0xffff0000u); }
DI u32 pack2(float a, float b) { f32x2 v = {a, b}; return __builtin_bit_cast(u32, __builtin_convertvector(v, bf16x2)); }
DI float rbf(float a) { return bflo(pack2(a, a)); }
DI f32x4 mfma(u32x4 a, u32x4 b, f32x4 c) {
    return __builtin_amdgcn_mfma_f32_16x16x32_bf16(__builtin_bit_cast(bf16x8, a), __builtin_bit_cast(bf16x8, b), c, 0, 0, 0);
}
DI u32x2 ldtr(u32 a) { return __builtin_bit_cast(u32x2, __builtin_amdgcn_ds_read_tr16_b64_v4i16((LAS s16x4*)(size_t)a)); }
DI u32x4 ldtr2(u32 a0, u32 a1) { u32x2 x = ldtr(a0), y = ldtr(a1); return (u32x4){x[0], x[1], y[0], y[1]}; }
DI u32x4 lds128(u32 a) { return *(LAS u32x4*)(size_t)a; }
DI u32x2 lds64(u32 a) { return *(LAS u32x2*)(size_t)a; }
DI f32x4 ldsf4(u32 a) { return *(LAS f32x4*)(size_t)a; }
DI float ldsf(u32 a) { return *(LAS float*)(size_t)a; }
DI void sts128(u32 a, u32x4 v) { *(LAS u32x4*)(size_t)a = v; }
DI void sts64(u32 a, u32x2 v) { *(LAS u32x2*)(size_t)a = v; }
DI void stsf(u32 a, float v) { *(LAS float*)(size_t)a = v; }
DI void stsf4(u32 a, f32x4 v) { *(LAS f32x4*)(size_t)a = v; }
DI void stsf2(u32 a, f32x2 v) { *(LAS f32x2*)(size_t)a = v; }
DI void sts16(u32 a, u16 v) { *(LAS u16*)(size_t)a = v; }

DI void glds16_(const void* root, u32 voff, u32 ldsdst) {
    asm volatile("s_mov_b32 m0, %2\n\ts_nop 0\n\tglobal_load_lds_dwordx4 %0, %1" :: "v"(voff), "s"(root), "s"(__builtin_amdgcn_readfirstlane(ldsdst)) : "memory");
}
DI void glds16c_(const void* root, u32 voff, u32 ldsdst) {
    asm volatile("s_mov_b32 m0, %2\n\ts_nop 0\n\tglobal_load_lds_dwordx4 %0, %1 sc1" :: "v"(voff), "s"(root), "s"(__builtin_amdgcn_readfirstlane(ldsdst)) : "memory");
}
template <int OFF> DI void glds16c_next_(const void* root, u32 voff) {
    asm volatile("global_load_lds_dwordx4 %0, %1 offset:%2 sc1" :: "v"(voff), "s"(root), "i"(OFF) : "memory");
}
template <int OFF> DI u32x4 gld16(const void* root, u32 voff) {
    u32x4 r; asm volatile("global_load_dwordx4 %0, %1, %2 offset:%3" : "=v"(r) : "v"(voff), "s"(root), "i"(OFF) : "memory"); return r;
}
template <int OFF> DI u32x4 gld16c_(const void* root, u32 voff) {
    u32x4 r; asm volatile("global_load_dwordx4 %0, %1, %2 offset:%3 sc1" : "=v"(r) : "v"(voff), "s"(root), "i"(OFF) : "memory"); return r;
}
#define ROFF(root, ptr) ((u32)((const char*)(ptr) - (const char*)(root)))
#define glds16(root, ptr, voff, dst) glds16_((root), ROFF(root, ptr) + (voff), (dst))
#define glds16c(ptr, voff, dst) glds16c_(ws, ROFF(ws, ptr) + (voff), (dst))
#define glds16c_next(OFF, ptr, voff) glds16c_next_<(OFF)>(ws, ROFF(ws, ptr) + (voff))
#define gld16c(OFF, ptr, voff) gld16c_<(OFF)>(ws, ROFF(ws, ptr) + (voff))
DI u32 gldu16(const void* sbase, u32 voff) {
    u32 r; asm volatile("global_load_ushort %0, %1, %2" : "=v"(r) : "v"(voff), "s"(sbase) : "memory"); return r;
}
#define VMCNT(n) asm volatile("s_waitcnt vmcnt(" #n ")" ::: "memory")
#define BAR() asm volatile("s_waitcnt lgkmcnt(0)\n\ts_barrier" ::: "memory")
#define RLX_AGENT __ATOMIC_RELAXED, __HIP_MEMORY_SCOPE_AGENT
DI u64 ld_agent(const char* p) { return __hip_atomic_load((GAS u64*)p, RLX_AGENT); }
DI void st_agent(char* p, u64 v) { __hip_atomic_store((GAS u64*)p, v, RLX_AGENT); }
DI void st_agent32(char* p, u32 v) { __hip_atomic_store((GAS u32*)p, v, RLX_AGENT); }
DI float ld_agentf(const char* p) { return __uint_as_float(__hip_atomic_load((GAS u32*)p, RLX_AGENT)); }
DI float xsum4(float x) {
    const auto s = __builtin_amdgcn_permlane16_swap(__float_as_uint(x), __float_as_uint(x), false, false);
    x = __uint_as_float(s[0]) + __uint_as_float(s[1]);
    const auto t = __builtin_amdgcn_permlane32_swap(__float_as_uint(x), __float_as_uint(x), false, false);
    return __uint_as_float(t[0]) + __uint_as_float(t[1]);
}
DI float xmax4(float x) {
    const auto s = __builtin_amdgcn_permlane16_swap(__float_as_uint(x), __float_as_uint(x), false, false);
    x = fmaxf(__uint_as_float(s[0]), __uint_as_float(s[1]));
    const auto t = __builtin_amdgcn_permlane32_swap(__float_as_uint(x), __float_as_uint(x), false, false);
    return fmaxf(__uint_as_float(t[0]), __uint_as_float(t[1]));
}
template <int CTRL> DI float dppf(float v) { return __builtin_bit_cast(float, __builtin_amdgcn_update_dpp(0, __builtin_bit_cast(int, v), CTRL, 0xf, 0xf, true)); }

constexpr u32 SPIN_CAP = 1u << 17;
DI void poll_flags(const char* base, int n, int lane, u64 tag, char* ws, u32 code) {
    const char* a = base + (lane < n ? lane : 0) * FS;
    u32 spins = 0;
    for (;;) {
        const u64 v = ld_agent(a);
        if (__all(v == tag)) break;
        if (++spins > SPIN_CAP) { if (lane == 0) st_agent32(ws + WS_ERR, code); break; }
        __builtin_amdgcn_s_sleep(1);
    }
}
typedef __attribute__((ext_vector_type(2))) _Float16 f16x2;
DI float hlo(u32 d) { return (float)__builtin_bit_cast(f16x2, d)[0]; }
DI float hhi(u32 d) { return (float)__builtin_bit_cast(f16x2, d)[1]; }
DI u32 packh(float a, float b) { const f16x2 v = {(_Float16)a, (_Float16)b}; return __builtin_bit_cast(u32, v); }
DI bool gran_ok(const u32x4& v, u64 tag) { return v[2] == (u32)tag && v[3] == (u32)(tag >> 32); }
DI f32x4 gran_sum(const u32x4& a, const u32x4& b, const u32x4& c, const u32x4& d) {
    const f32x4 s = {(hlo(a[0]) + hlo(b[0])) + (hlo(c[0]) + hlo(d[0])), (hhi(a[0]) + hhi(b[0])) + (hhi(c[0]) + hhi(d[0])),
                     (hlo(a[1]) + hlo(b[1])) + (hlo(c[1]) + hlo(d[1])), (hhi(a[1]) + hhi(b[1])) + (hhi(c[1]) + hhi(d[1]))};
    return (f32x4){rbf(s[0]), rbf(s[1]), rbf(s[2]), rbf(s[3])};
}
DI f32x4 gather1(const char* ws, u32 g0, u64 tag, char* wsw, u32 code) {
    u32 spins = 0; u32x4 v0, v1, v2, v3;
    const u32 o = WS_MODG + g0 * 16;
    for (;;) {
        asm volatile("s_nop 4\n\tglobal_load_dwordx4 %0, %4, %5 sc1\n\tglobal_load_dwordx4 %1, %6, %5 sc1\n\tglobal_load_dwordx4 %2, %7, %5 sc1\n\tglobal_load_dwordx4 %3, %8, %5 sc1\n\ts_waitcnt vmcnt(0)"
                     : "=&v"(v0), "=&v"(v1), "=&v"(v2), "=&v"(v3) : "v"(o), "s"(ws), "v"(o + 15360), "v"(o + 30720), "v"(o + 46080) : "memory");
        if (__all(gran_ok(v0, tag) && gran_ok(v1, tag) && gran_ok(v2, tag) && gran_ok(v3, tag))) break;
        if (++spins > SPIN_CAP) { st_agent32(wsw + WS_ERR, code); break; }
        __builtin_amdgcn_s_sleep(1);
    }
    return gran_sum(v0, v1, v2, v3);
}
DI void reask(u32x4& v, const char* ws, u32 off, u64 tag) {
    if (!gran_ok(v, tag)) asm volatile("global_load_dwordx4 %0, %1, %2 sc1" : "+v"(v) : "v"(off), "s"(ws) : "memory");
}
DI void gather2(const char* ws, u32 g0, u32 g1, u64 tag, char* wsw, u32 code, f32x4& ra, f32x4& rb) {
    u32 spins = 0; u32x4 v0, v1, v2, v3, u0, u1, u2, u3;
    const u32 o = WS_MODG + g0 * 16, q = WS_MODG + g1 * 16;
    asm volatile("global_load_dwordx4 %0, %8, %9 sc1\n\tglobal_load_dwordx4 %1, %10, %9 sc1\n\tglobal_load_dwordx4 %2, %11, %9 sc1\n\tglobal_load_dwordx4 %3, %12, %9 sc1\n\t"
                 "global_load_dwordx4 %4, %13, %9 sc1\n\tglobal_load_dwordx4 %5, %14, %9 sc1\n\tglobal_load_dwordx4 %6, %15, %9 sc1\n\tglobal_load_dwordx4 %7, %16, %9 sc1\n\ts_waitcnt vmcnt(0)"
                 : "=&v"(v0), "=&v"(v1), "=&v"(v2), "=&v"(v3), "=&v"(u0), "=&v"(u1), "=&v"(u2), "=&v"(u3)
                 : "v"(o), "s"(ws), "v"(o + 15360), "v"(o + 30720), "v"(o + 46080), "v"(q), "v"(q + 15360), "v"(q + 30720), "v"(q + 46080) : "memory");
    for (;;) {
        if (__all(gran_ok(v0, tag) && gran_ok(v1, tag) && gran_ok(v2, tag) && gran_ok(v3, tag) && gran_ok(u0, tag) && gran_ok(u1, tag) && gran_ok(u2, tag) && gran_ok(u3, tag))) break;
        if (++spins > SPIN_CAP) { st_agent32(wsw + WS_ERR, code); break; }
        reask(v0, ws, o, tag); reask(v1, ws, o + 15360, tag); reask(v2, ws, o + 30720, tag); reask(v3, ws, o + 46080, tag);
        reask(u0, ws, q, tag); reask(u1, ws, q + 15360, tag); reask(u2, ws, q + 30720, tag); reask(u3, ws, q + 46080, tag);
        asm volatile("s_waitcnt vmcnt(0)" : "+v"(v0), "+v"(v1), "+v"(v2), "+v"(v3), "+v"(u0), "+v"(u1), "+v"(u2), "+v"(u3) :: "memory");
    }
    ra = gran_sum(v0, v1, v2, v3); rb = gran_sum(u0, u1, u2, u3);
}
struct Gather { u32x4 v0, v1, v2, v3, u0, u1, u2, u3; };
DI void gather_ask(const char* ws, u32 g0, u32 g1, Gather& G) {
    const u32 o = WS_MODG + g0 * 16, q = WS_MODG + g1 * 16;
    asm volatile("global_load_dwordx4 %0, %8, %9 sc1\n\tglobal_load_dwordx4 %1, %10, %9 sc1\n\tglobal_load_dwordx4 %2, %11, %9 sc1\n\tglobal_load_dwordx4 %3, %12, %9 sc1\n\t"
                 "global_load_dwordx4 %4, %13, %9 sc1\n\tglobal_load_dwordx4 %5, %14, %9 sc1\n\tglobal_load_dwordx4 %6, %15, %9 sc1\n\tglobal_load_dwordx4 %7, %16, %9 sc1"
                 : "=&v"(G.v0), "=&v"(G.v1), "=&v"(G.v2), "=&v"(G.v3), "=&v"(G.u0), "=&v"(G.u1), "=&v"(G.u2), "=&v"(G.u3)
                 : "v"(o), "s"(ws), "v"(o + 15360), "v"(o + 30720), "v"(o + 46080), "v"(q), "v"(q + 15360), "v"(q + 30720), "v"(q + 46080) : "memory");
}
DI void gather_finish(const char* ws, u32 g0, u32 g1, Gather& G, u64 tag, char* wsw, u32 code, f32x4& ra, f32x4& rb) {
    asm volatile("" : "+v"(G.v0), "+v"(G.v1), "+v"(G.v2), "+v"(G.v3), "+v"(G.u0), "+v"(G.u1), "+v"(G.u2), "+v"(G.u3) :: "memory");
    if (__all(gran_ok(G.v0, tag) && gran_ok(G.v1, tag) && gran_ok(G.v2, tag) && gran_ok(G.v3, tag) && gran_ok(G.u0, tag) && gran_ok(G.u1, tag) && gran_ok(G.u2, tag) && gran_ok(G.u3, tag))) {
        ra = gran_sum(G.v0, G.v1, G.v2, G.v3); rb = gran_sum(G.u0, G.u1, G.u2, G.u3);
    } else gather2(ws, g0, g1, tag, wsw, code, ra, rb);
}
DI void gather_strip(const char* ws, u32 gs, u32 gc, u32 strip, int l, u64 tag, char* wsw, u32 code) {
    f32x4 a, c;
    gather2(ws, l < 40 ? gs + l : gc + (l - 40), gc + 24 + (l & 15), tag, wsw, code, a, c);
    if (l >= 40) a = a + 1.f;
    c = c + 1.f;
    *(LAS u32x2*)(size_t)(strip + (l < 40 ? l * 8 : 320 + (l - 40) * 8)) = (u32x2){pack2(a[0], a[1]), pack2(a[2], a[3])};
    if (l < 16) *(LAS u32x2*)(size_t)(strip + 320 + (24 + l) * 8) = (u32x2){pack2(c[0], c[1]), pack2(c[2], c[3])};
}
DI void put_strip_spread(const f32x4& a, const f32x4& c, u32 strip, int l) {
    if (l < 40) *(LAS u32x2*)(size_t)(strip + l * 8) = (u32x2){pack2(a[0], a[1]), pack2(a[2], a[3])};
    else {
        const u32 p0 = pack2(a[0] + 1.f, a[1] + 1.f), p1 = pack2(a[2] + 1.f, a[3] + 1.f);
        *(LAS u32x4*)(size_t)(strip + 320 + (l - 40) * 16) = (u32x4){p0 & 0xffffu, p0 & 0xffff0000u, p1 & 0xffffu, p1 & 0xffff0000u};
    }
    if (l < 16) {
        const u32 p0 = pack2(c[0] + 1.f, c[1] + 1.f), p1 = pack2(c[2] + 1.f, c[3] + 1.f);
        *(LAS u32x4*)(size_t)(strip + 320 + (24 + l) * 16) = (u32x4){p0 & 0xffffu, p0 & 0xffff0000u, p1 & 0xffffu, p1 & 0xffff0000u};
    }
    if (l == 63) *(LAS u32x4*)(size_t)(strip + 960) = (u32x4){0, 0, 0, 0};
}
DI void poll_at(const char* ws, u32 off, int lane, u64 tag, char* wsw, u32 code) {
    u32 spins = 0;
    for (;;) {
        const u64 v = ld_agent(ws + off);
        if (__all(v == tag)) break;
        if (++spins > SPIN_CAP) { if (lane == 0) st_agent32(wsw + WS_ERR, code); break; }
        __builtin_amdgcn_s_sleep(1);
    }
}

#define STAMP(i) do { if ((i) == PA) tA = __builtin_amdgcn_s_memrealtime(); if ((i) == PB) tB = __builtin_amdgcn_s_memrealtime(); } while (0)
#define STAMPL(i) do {} while (0)
#define STAMP_RT(i) do {} while (0)
#define STAMP_OUT() do { const u64 t0_ = ld_agent(ws + WS_ERR + 64); const u64 d_ = tE > t0_ ? tE - t0_ : 0, e_ = __builtin_amdgcn_s_memrealtime(); while (__builtin_amdgcn_s_memrealtime() - e_ < d_) __builtin_amdgcn_s_sleep(1); } while (0)

template <int N> using IC = std::integral_constant<int, N>;
template <int I, int E, class F> DI void static_for(F&& f) { if constexpr (I < E) { f(IC<I>{}); static_for<I + 1, E>(f); } }
template <int N> DI void vmw() { asm volatile("s_waitcnt vmcnt(%0)" :: "i"(N) : "memory"); }
template <int N> DI void vmw1(u32x4& a) { asm volatile("s_waitcnt vmcnt(%1)" : "+v"(a) : "i"(N) : "memory"); }
template <int N> DI void vmw4(u32x4& a, u32x4& b, u32x4& c, u32x4& d) { asm volatile("s_waitcnt vmcnt(%4)" : "+v"(a), "+v"(b), "+v"(c), "+v"(d) : "i"(N) : "memory"); }
DI u32 opaque(u32 a) { asm("" : "+v"(a)); return a; }
DI float dot2(u32 a, u32 b, float c) { return __builtin_amdgcn_fdot2_f32_bf16(__builtin_bit_cast(bf16x2, a), __builtin_bit_cast(bf16x2, b), c, false); }
#define SB0() __builtin_amdgcn_sched_barrier(0)
#define SGB(m, n) __builtin_amdgcn_sched_group_barrier((m), (n), 0)
constexpr int M_MFMA = 0x8, M_DSR = 0x100;
#define Z4 ((f32x4){0.f, 0.f, 0.f, 0.f})
DI u32 lds_add(u32 a, u32 v) { return __hip_atomic_fetch_add((LAS u32*)(size_t)a, v, __ATOMIC_RELAXED, __HIP_MEMORY_SCOPE_WORKGROUP); }
DI u32 lds_ld(u32 a) { return __hip_atomic_load((LAS u32*)(size_t)a, __ATOMIC_RELAXED, __HIP_MEMORY_SCOPE_WORKGROUP); }
DI void worker_sync(u32 cnt, int lane) {
    asm volatile("s_waitcnt lgkmcnt(0)" ::: "memory");
    if (lane == 0) lds_add(cnt, 1u);
    for (u32 spins = 0; lds_ld(cnt) < 4u && spins < (1u << 22); ++spins) {}
    asm volatile("" ::: "memory");
}

__global__ __launch_bounds__(512, 2) void mega_kernel(Params p) {
    __shared__ __attribute__((aligned(16))) char ldsmem[163840];
    const u32 L0 = (u32)(uintptr_t)(LAS char*)ldsmem;
    u64 tE = __builtin_amdgcn_s_memrealtime();
    asm volatile("" : "+s"(tE));
    const int tid = threadIdx.x, l = tid & 63;
    const int w = __builtin_amdgcn_readfirstlane(tid >> 6);
    const int b = blockIdx.x, xg = b & 7, sidx = b >> 3;
    const int rb = 4 * (xg >> 1) + (sidx >> 3), j = 8 * (xg & 1) + (sidx & 7);
    const int g = l >> 4, i16 = l & 15, q4 = i16 >> 2, p4 = l & 3;
    u32 wu0 = 0;
    if (w == 7) {
        const char* pc; u32 span;
        asm volatile("s_getpc_b64 %0\n.Lmk_code_from:\n\ts_mov_b32 %1, .Lmk_code_to-.Lmk_code_from" : "=s"(pc), "=s"(span));
        const u32 per = (span + 4095u) >> 12;
        const u32 off = min((sidx * per + min((u32)l, per - 1u)) * 128u, span - 4u);
        asm volatile("s_nop 0\n\tglobal_load_dword %0, %1, %2" : "=v"(wu0) : "v"(off), "s"(pc) : "memory");
    }
    char* const ws = p.ws;
    asm volatile("" :: "s"(p.x), "s"(p.wqkv), "s"(p.wo), "s"(p.wg), "s"(p.wu), "s"(p.wd), "s"(p.c), "s"(p.wmod), "s"(p.out), "s"(p.ws));
    u64 tag;
#define ASK_TAG(ep) u32x2 ep; asm volatile("global_load_dwordx2 %0, %1, %2" : "=v"(ep) : "v"(WS_EPOCH), "s"(ws) : "memory")
#define TAKE_TAG(ep) do { asm volatile("s_waitcnt vmcnt(0)" : "+v"(ep) :: "memory"); \
        tag = (((u64)(u32)__builtin_amdgcn_readfirstlane(ep[1]) << 32) | (u32)__builtin_amdgcn_readfirstlane(ep[0])) + 1; } while (0)
    const __amdgpu_buffer_rsrc_t wsr = __builtin_amdgcn_make_buffer_rsrc(ws, 0, WS_END, 0x00020000);
#define ST16C(v, off) __builtin_amdgcn_raw_buffer_store_b128((v), wsr, (off), 0, 16)
#define ST8C(v, off)  __builtin_amdgcn_raw_buffer_store_b64((v), wsr, (off), 0, 16)
    u64 tA = 0, tB = 0;
    if (b == 0 && tid == 0) st_agent(ws + WS_ERR + 64, tE);
    STAMP_RT(62); STAMP(0);

    const int nj = j < 12 ? 7 : 6, Gj = j < 12 ? 7 * j : 84 + 6 * (j - 12);
    const bool wpwg = b < 216;
    const int wpm = b >= 108 ? 1 : 0, wpb = (b - 108 * wpm) >> 2, wpq = b & 3;
    u32x4 mw[5]; u32 mc[5];
    auto mod_ask = [&]() {
#pragma unroll
        for (int i = 0; i < 5; ++i) {
            const int r = 160 * (b & 3) + ((tid & 255) >> 3) + 32 * i;
            mw[i] = gld16<0>(p.wmod, r * 7680 + (b >> 2) * 128 + (tid & 7) * 16);
            mc[i] = gldu16(p.c, r * 2);
        }
    };
    auto mod_finish = [&]() {
        float acc[8];
#pragma unroll
        for (int e = 0; e < 8; ++e) acc[e] = 0.f;
#pragma unroll
        for (int i = 0; i < 5; ++i) {
            const float cv = bflo(mc[i]);
            const float sv = cv * __builtin_amdgcn_rcpf(1.f + __builtin_amdgcn_exp2f(-cv * LOG2E));
            const u32 slo = pack2(sv, 0.f), shi = slo << 16;
#pragma unroll
            for (int d = 0; d < 4; ++d) { acc[2 * d] = dot2(mw[i][d], slo, acc[2 * d]); acc[2 * d + 1] = dot2(mw[i][d], shi, acc[2 * d + 1]); }
        }
#pragma unroll
        for (int e = 0; e < 8; ++e) { const float v = acc[e]; acc[e] = xsum4(v + dppf<0x128>(v)); }
        if (l < 8) {
            stsf4(L0 + L_MODP + (w & 3) * 256 + l * 32, (f32x4){acc[0], acc[1], acc[2], acc[3]});
            stsf4(L0 + L_MODP + (w & 3) * 256 + l * 32 + 16, (f32x4){acc[4], acc[5], acc[6], acc[7]});
        }
        asm volatile("s_waitcnt lgkmcnt(0)" ::: "memory");
        u32 old = 0;
        if (l == 0) old = lds_add(L0 + L_CNT, 1u);
        if (__builtin_amdgcn_readfirstlane(old) == 3) {
            float v = 0.f;
#pragma unroll
            for (int ww = 0; ww < 4; ++ww) v += ldsf(L0 + L_MODP + ww * 256 + l * 4);
            const float v1 = dppf<0x55>(v), v2 = dppf<0xAA>(v), v3 = dppf<0xFF>(v), v0 = dppf<0x00>(v);
            const u32x4 gr = {packh(v0, v1), packh(v2, v3), (u32)tag, (u32)(tag >> 32)};
            if (p4 == 0) ST16C(gr, WS_MODG + ((b & 3) * 960 + (b >> 2) * 16 + (l >> 2)) * 16);
        }
    };
#define MOD_LANDED() asm volatile("s_waitcnt vmcnt(0)" : "+v"(mw[0]), "+v"(mw[1]), "+v"(mw[2]), "+v"(mw[3]), "+v"(mw[4]), "+v"(mc[0]), "+v"(mc[1]), "+v"(mc[2]), "+v"(mc[3]), "+v"(mc[4]) :: "memory")
    if (w >= 4) {
        __builtin_amdgcn_s_setprio(3);
        const int u = w - 4;
        const bool tw = b < 150;
        const int tb = b / 5, tk = b - 5 * tb, ts = 4 * tk + u;
        if (tw) {
            const char* src = (const char*)p.wqkv + ts * 122880 + tb * 128;
            const u32 voff = (l >> 3) * 3840 + (l & 7) * 16;
#pragma unroll
            for (int i = 0; i < 4; ++i) glds16(p.wqkv, src + i * 8 * 3840, voff, L0 + L_WTRAW + u * 4096 + i * 1024);
        }
        ASK_TAG(epl);
        BAR();
        __builtin_amdgcn_s_setprio(0);
        TAKE_TAG(epl);
        if (tw) {
            const u32 a = L0 + L_WTRAW + u * 4096 + (8 * g + q4) * 128 + 8 * p4;
#pragma unroll
            for (int t = 0; t < 4; ++t) {
                const u32 cc = 8 * tb + 2 * t + (i16 >> 3);
                const u32 seg = cc >= 160 ? 2 : cc >= 80 ? 1 : 0, wi = cc - 80 * seg, jj = (wi * 13108) >> 16, c5 = wi - 5 * jj;
                const u32 c = seg == 0 ? 10 + c5 : seg == 1 ? c5 : 5 + c5, nt = c >> 1;
                const u32 dst = WS_WT + jj * 153600 + (nt < 5 ? ts * 5120 + nt * 1024 : 102400 + ts * 2560 + (nt - 5) * 1024)
                              + (nt < 7 ? (g * 16 + 8 * (c & 1) + (i16 & 7)) * 16 : g * 128 + (i16 & 7) * 16);
                ST16C(ldtr2(a + 32 * t, a + 32 * t + 512), dst);
            }
        }
        vmw<0>();
        {
            asm volatile("s_waitcnt lgkmcnt(0)" ::: "memory");
            u32 old = 0;
            if (l == 0) old = lds_add(L0 + L_CNT + 12, 1u);
            if (tw && __builtin_amdgcn_readfirstlane(old) == 3 && l == 0) st_agent(ws + WS_FWT + b * FS, tag);
        }
        STAMPL(19);
        if (b >= 80 && b < 240) {
            if (!tw) __builtin_amdgcn_s_sleep(32);
            mod_ask(); MOD_LANDED(); mod_finish();
        }
        if (u == 0) {
            const int wh = l >= 30 ? 0 : l / 5, kr = l >= 30 ? 0 : l - 5 * wh;
            const int bl = (1280 * (wh >> 1) + 80 * j + 79 * (wh & 1)) >> 7;
            poll_at(ws, WS_FWT + (bl * 5 + kr) * FS, l, tag, ws, 13);
            for (u32 spins = 0; lds_ld(L0 + L_CNT + 12) < 4u && spins < (1u << 22); ++spins) {}
            if (l == 0) lds_add(L0 + L_CNT + 24, 1u);
        } else {
            for (u32 spins = 0; lds_ld(L0 + L_CNT + 24) < 1u && spins < (1u << 22); ++spins) {}
        }
        asm volatile("" ::: "memory");
        STAMPL(20);
        {
            const char* src = ws + WS_WT + j * 153600 + u * 1024;
            const u32 lo = l * 16;
#pragma unroll
            for (int i = 0; i < 25; ++i) glds16c(src + i * 4096, lo, L0 + L_WKV + u * 1024 + i * 4096);
        }
        VMCNT(0);
        STAMPL(17);
    } else {
        __builtin_amdgcn_s_setprio(2);
        if (b < 80) mod_ask();
        ASK_TAG(epw);
        if (tid < 16) *(LAS u32*)(size_t)(L0 + L_CNT + tid * 4) = 0u;
        BAR();
        STAMP(29);
        TAKE_TAG(epw);
        STAMP(30);
        if (b < 80) { MOD_LANDED(); mod_finish(); }
        {
            const char* src = (const char*)p.x + (rb * 128 + 8 * j) * 1280 + w * 1024;
            glds16(p.x, src, l * 16, L0 + L_XS + w * 1024);
            glds16(p.x, src + 4096, l * 16, L0 + L_XS + (w + 4) * 1024);
            if (w < 2) glds16(p.x, src + 8192, l * 16, L0 + L_XS + (w + 8) * 1024);
        }
        VMCNT(0);
        STAMP(1);
        worker_sync(L0 + L_CNT + 16, l);
        const int tok = l & 7;
        u32x4 xr[3];
#pragma unroll
        for (int i = 0; i < 3; ++i) xr[i] = lds128(L0 + L_XS + tok * 1280 + (20 * w + (i < 2 ? (l >> 3) + 8 * i : 16 + ((l >> 3) & 3))) * 16);
        {
            float s1 = 0.f, s2 = 0.f;
#pragma unroll
            for (int i = 0; i < 3; ++i) {
                float a1 = 0.f, a2 = 0.f;
#pragma unroll
                for (int d = 0; d < 4; ++d) { a1 = dot2(xr[i][d], 0x3F803F80u, a1); a2 = dot2(xr[i][d], xr[i][d], a2); }
                if (i < 2 || l < 32) { s1 += a1; s2 += a2; }
            }
            asm volatile("s_waitcnt lgkmcnt(0)" ::: "memory");
            if (l == 0) lds_add(L0 + L_CNT + 20, 1u);
            s1 += dppf<0x128>(s1); s2 += dppf<0x128>(s2);
            s1 = xsum4(s1); s2 = xsum4(s2);
            if (l < 8) stsf2(L0 + L_STP + (w * 8 + l) * 8, (f32x2){s1, s2});
        }
        gather_strip(ws, 40 * w, 160 + 40 * w, L0 + L_GS + w * 640, l, tag, ws, 1);
        STAMP(2);
        worker_sync(L0 + L_CNT + 4, l);
        {
            f32x2 st = {0.f, 0.f};
#pragma unroll
            for (int ww = 0; ww < 4; ++ww) st += *(LAS f32x2*)(size_t)(L0 + L_STP + (ww * 8 + tok) * 8);
            const float mu = st[0] * (1.f / 640.f), var = fmaxf(st[1] * (1.f / 640.f) - mu * mu, 0.f);
            const float rs = __builtin_amdgcn_rsqf(var + 1e-5f), nmr = -mu * rs;
#pragma unroll
            for (int i = 0; i < 3; ++i) {
                const int cl = i < 2 ? (l >> 3) + 8 * i : 16 + ((l >> 3) & 3);
                const u32x4 sa = lds128(L0 + L_GS + w * 640 + cl * 16), gg = lds128(L0 + L_GS + w * 640 + 320 + cl * 16);
                u32x4 h;
#pragma unroll
                for (int d = 0; d < 4; ++d)
                    h[d] = pack2((bflo(xr[i][d]) * rs + nmr) * bflo(gg[d]) + bflo(sa[d]), (bfhi(xr[i][d]) * rs + nmr) * bfhi(gg[d]) + bfhi(sa[d]));
                if (i < 2 || l < 32) ST16C(h, WS_H + (((rb * 8 + (j >> 1)) * 80 + 20 * w + cl) * 16 + 8 * (j & 1) + tok) * 16);
            }
        }
        VMCNT(0);
        worker_sync(L0 + L_CNT + 8, l);
        if (tid == 0) st_agent(ws + WS_FH + (rb * 16 + j) * FS, tag);
        if (w == 0) poll_flags(ws + WS_FH + rb * 16 * FS, 16, l, tag, ws, 11);
        __builtin_amdgcn_s_setprio(0);
    }
    BAR();
    asm volatile("s_waitcnt vmcnt(0)" : "+v"(wu0) :: "memory");
    STAMP(3);

    u32x4 xa[20];
    const u32 lo16 = l * 16;
    const char* hsrc = ws + WS_H + (rb * 8 + w) * 80 * 256;
#define XL(s) xa[s] = gld16c(((s) & 3) * 1024, hsrc + ((s) >> 2) * 4096, lo16)
    XL(0); XL(1); XL(2); XL(3); XL(4); XL(5);
    const char* kvx = ws + WS_KV + (rb * 16 + j) * 22528;
    const char* wtq = ws + WS_WT + j * 153600 + (100 + w) * 1024;
    {
        f32x4 acc[5];
#pragma unroll
        for (int n = 0; n < 5; ++n) acc[n] = Z4;
        const u32 b0 = L0 + L_WKV + lo16;
        u32x4 bf[3][5];
#pragma unroll
        for (int n = 0; n < 5; ++n) { bf[0][n] = lds128(b0 + n * 1024); bf[1][n] = lds128(b0 + 5120 + n * 1024); }
        static_for<0, 20>([&](auto S) {
            constexpr int s = S;
            vmw1<(s < 14 ? 5 : 6)>(xa[s]);
            if constexpr (s + 6 < 20) XL(s + 6);
            if constexpr (s >= 13) { if (s < 19 || w < 2) glds16c(wtq + (s - 13) * 8192, lo16, L0 + L_WKV + (100 + w) * 1024 + (s - 13) * 8192); }
            SB0();
#pragma unroll
            for (int n = 0; n < 5; ++n) {
                if (s < 18) bf[(s + 2) % 3][n] = lds128(b0 + (s + 2) * 5120 + n * 1024);
                acc[n] = mfma(bf[s % 3][n], xa[s], acc[n]);
            }
#pragma unroll
            for (int n = 0; n < 5; ++n) { SGB(M_DSR, 1); SGB(M_MFMA, 1); }
            SB0();
        });
        STAMP(18);
        BAR();
        const int row = 256 + 16 * w + i16;
#pragma unroll
        for (int n = 0; n < 5; ++n) {
            const int Q = 4 * n + g;
            const u32x2 v = {pack2(acc[n][0], acc[n][1]), pack2(acc[n][2], acc[n][3])};
            const int wi = Q - 10, hd = wi >= 5 ? 1 : 0;
            const u32 a = Q < 10 ? L_K + row * 80 + 8 * Q : L_V + row * 96 + 48 * hd + 8 * (wi - 5 * hd);
            sts64(L0 + a, v);
        }
        if (g < 2) sts64(L0 + L_V + row * 96 + 48 * g + 40, (u32x2){0x3F80u, 0u});
        if (rb < 2) {
            const int nrow = rb == 0 ? 256 : 128;
            for (int i = tid; i < nrow * 5; i += 512) sts128(L0 + L_K + i * 16, (u32x4){0, 0, 0, 0});
            for (int i = tid; i < nrow * 6; i += 512) sts128(L0 + L_V + i * 16, (u32x4){0, 0, 0, 0});
        }
        vmw<0>();
        BAR();
    }
#undef XL
    STAMP(4);
    {
        u32x4 kvd[3];
#pragma unroll
        for (int e = 0; e < 3; ++e) {
            const int n = min(w + 8 * e, 21);
            kvd[e] = lds128(L0 + (n < 10 ? L_K + 20480 + n * 1024 : L_V + 24576 + (n - 10) * 1024) + lo16);
        }
        const u32 kvo = WS_KV + (rb * 16 + j) * 22528 + w * 1024 + lo16;
        const u16* wroot = wpm ? p.wu : p.wg;
        const char* wsrc = (const char*)wroot + (160 * wpq + 8 * w) * 3456 + wpb * 128;
        const u32 wvo = (l >> 3) * 3456 + (l & 7) * 16;
        f32x4 acc[3];
#pragma unroll
        for (int n = 0; n < 3; ++n) acc[n] = Z4;
        const u32 bq = L0 + L_WQ + lo16, bq2 = L0 + L_WQ + 2048 + g * 128 + (i16 & 7) * 16;
#define QRD(s, n) lds128(((n) < 2 ? bq + (n) * 1024 : bq2) + (s) * 2560)
        u32x4 bf[3][3];
#pragma unroll
        for (int n = 0; n < 3; ++n) { bf[0][n] = QRD(0, n); bf[1][n] = QRD(1, n); }
        Gather G;
        const u32 gg0 = l < 40 ? 480 + 40 * wpq + l : 640 + 40 * wpq + (l - 40), gg1 = 640 + 40 * wpq + 24 + (l & 15);
        static_for<0, 20>([&](auto S) {
            constexpr int s = S;
            if constexpr (s == 6) { if (w == 7 && wpwg) gather_ask(ws, gg0, gg1, G); }
            if constexpr (s == 0) { if (rb < 15) ST16C(kvd[0], kvo); }
            if constexpr (s == 1) { if (rb < 15) ST16C(kvd[1], kvo + 8192); }
            if constexpr (s == 2) { if (rb < 15 && w < 6) ST16C(kvd[2], kvo + 16384); }
            if constexpr (s == 14) {
                vmw<0>();
                u32 old = 0;
                if (l == 0) old = lds_add(L0 + L_CNT + 28, 1u);
                if (__builtin_amdgcn_readfirstlane(old) == 7 && l == 0) st_agent(ws + WS_FKV + (j * 16 + rb) * FS, tag);
                if (w == 7 && wpwg) { f32x4 ra, rc; gather_finish(ws, gg0, gg1, G, tag, ws, 2, ra, rc); put_strip_spread(ra, rc, L0 + L_SB, l); }
            }
            if constexpr (s == 15) { if (wpwg) glds16(wroot, wsrc, wvo, L0 + L_WPRAW + w * 1024); }
            if constexpr (s == 16) { if (wpwg) glds16(wroot, wsrc + 64 * 3456, wvo, L0 + L_WPRAW + (w + 8) * 1024); }
            if constexpr (s == 17) { if (wpwg && w < 4) glds16(wroot, wsrc + 128 * 3456, wvo, L0 + L_WPRAW + (w + 16) * 1024); }
            SB0();
#pragma unroll
            for (int n = 0; n < 3; ++n) {
                if (s < 18) bf[(s + 2) % 3][n] = QRD(s + 2, n);
                acc[n] = mfma(bf[s % 3][n], xa[s], acc[n]);
            }
#pragma unroll
            for (int n = 0; n < 3; ++n) { SGB(M_DSR, 1); SGB(M_MFMA, 1); }
            SB0();
        });
#undef QRD
        STAMP(21);
#pragma unroll
        for (int n = 0; n < 3; ++n) {
            const int Q = 4 * n + g;
            const u32x2 v = {pack2(acc[n][0] * QSCALE, acc[n][1] * QSCALE), pack2(acc[n][2] * QSCALE, acc[n][3] * QSCALE)};
            if (Q < 10) sts64(L0 + L_Q + (16 * w + i16) * 80 + 8 * Q, v);
        }
    }
    if (w == 0 && rb >= 1) poll_flags(ws + WS_FKV + (j * 16 + (rb >= 2 ? rb - 2 : rb - 1)) * FS, rb >= 2 ? 2 : 1, l, tag, ws, 3);
    VMCNT(0);
    BAR();
    STAMP(5);
    if (rb >= 1) {
#pragma unroll
        for (int e = 0; e < 3; ++e) {
            const int n = w + 8 * e;
            if (n < 22) {
                const u32 d = n < 10 ? L_K + n * 1024 : L_V + (n - 10) * 1024, st = n < 10 ? 10240 : 12288;
                glds16c(kvx - 16 * 22528 + n * 1024, lo16, L0 + d + st);
                if (rb >= 2) glds16c(kvx - 32 * 22528 + n * 1024, lo16, L0 + d);
            }
        }
    }
    auto wp_slot = [&](int gam, int& jj, int& slot) {
        jj = gam < 84 ? gam / 7 : 12 + (gam - 84) / 6;
        const int ar = gam - (jj < 12 ? 7 * jj : 84 + 6 * (jj - 12)), a = (jj < 12 || ar < 3) ? ar : ar + 1;
        slot = a < 3 ? 2 * a + wpm : a == 3 ? (wpm ? 13 : 6) : 7 + 2 * (a - 4) + wpm;
    };
    if (wpwg) {
        const u32 one = i16 == 0 ? 0x3F803F80u : 0u;
        const u32x4 ones = {one, one, one, one};
#pragma unroll
        for (int i = 0; i < 3; ++i) {
            if (i < 2 || w < 4) {
                const int f = w + 8 * i, ks = f >> 2, t16 = f & 3;
                int jj, slot; wp_slot(4 * wpb + t16, jj, slot);
                const u32 a = L0 + L_WPRAW + (32 * ks + 8 * g + q4) * 128 + 32 * t16 + 8 * p4;
                u32x4 wr = ldtr2(a, a + 512);
                const u32x4 ga = lds128(L0 + L_G2 + (32 * ks + 8 * g) * 4), gc = lds128(L0 + L_G2 + (32 * ks + 8 * g) * 4 + 16);
                const u32x4 sbz = lds128(L0 + (i16 == 0 ? L_SB + (32 * ks + 8 * g) * 2 : L_Z16));
                const f32x4 vs = mfma(wr, sbz, Z4);
                u32x4 o; float t0, t1, t2, t3, t4, t5, t6, t7;
                asm volatile("v_dot2_f32_bf16 %4, %12, %16, 0\n\tv_dot2_f32_bf16 %5, %12, %17, 0\n\tv_dot2_f32_bf16 %6, %13, %18, 0\n\tv_dot2_f32_bf16 %7, %13, %19, 0\n\t"
                             "v_dot2_f32_bf16 %8, %14, %20, 0\n\tv_dot2_f32_bf16 %9, %14, %21, 0\n\tv_dot2_f32_bf16 %10, %15, %22, 0\n\tv_dot2_f32_bf16 %11, %15, %23, 0\n\t"
                             "v_cvt_pk_bf16_f32 %0, %4, %5\n\tv_cvt_pk_bf16_f32 %1, %6, %7\n\tv_cvt_pk_bf16_f32 %2, %8, %9\n\ts_nop 0\n\tv_cvt_pk_bf16_f32 %3, %10, %11\n\ts_nop 1"
                             : "=&v"(o[0]), "=&v"(o[1]), "=&v"(o[2]), "=&v"(o[3]),
                               "=&v"(t0), "=&v"(t1), "=&v"(t2), "=&v"(t3), "=&v"(t4), "=&v"(t5), "=&v"(t6), "=&v"(t7)
                             : "v"(wr[0]), "v"(wr[1]), "v"(wr[2]), "v"(wr[3]), "v"(ga[0]), "v"(ga[1]), "v"(ga[2]), "v"(ga[3]), "v"(gc[0]), "v"(gc[1]), "v"(gc[2]), "v"(gc[3]));
                const f32x4 us = mfma(o, ones, Z4);
                ST16C(o, WS_WP + ((jj * 20 + 5 * wpq + ks) * 14 + slot) * 1024 + lo16);
                if (i16 == 0) { stsf4(L0 + L_UVL + f * 128 + g * 16, us); stsf4(L0 + L_UVL + f * 128 + 64 + g * 16, vs); }
            }
        }
    }
    if (wpwg) { if (w < 4) vmw<3>(); else vmw<2>(); } else vmw<0>();
    BAR();
    STAMP(6);
    if (wpwg && w == 3) {
#pragma unroll
        for (int e = 0; e < 2; ++e) {
            const int t16 = 2 * e + (l >> 5);
            int jj, slot; wp_slot(4 * wpb + t16, jj, slot);
            float v = 0.f;
#pragma unroll
            for (int ks = 0; ks < 5; ++ks) v += ldsf(L0 + L_UVL + (4 * ks + t16) * 128 + (l & 31) * 4);
            st_agent32(ws + WS_UV + ((jj * 14 + slot) * 4 + wpq) * 128 + (l & 31) * 4, __float_as_uint(v));
        }
    }
    const int hf = j & 1, cb = j >> 1;
    u32 wvoff[5];
#pragma unroll
    for (int i = 0; i < 5; ++i) {
        const u32 sg = 64 * i + l, pos = (sg * 6554) >> 16, c = sg - pos * 10, k = pos ^ (((pos >> 3) & 1) << 2);
        wvoff[i] = k * 1280 + c * 16;
    }
    const char* wosrc = (const char*)p.wo + cb * 160;
#define WO_STEP(sx) do { _Pragma("unroll") for (int i = 0; i < 5; ++i) glds16(p.wo, wosrc + (sx) * 32 * 1280, wvoff[i], L0 + L_WO + (sx) * 5120 + i * 1024); } while (0)
    STAMP(7);

    {
        const int hd = w & 1, qi = w >> 1;
        const int kr0 = 32 * qi;
        u32x4 qf[2];
#pragma unroll
        for (int a = 0; a < 2; ++a) {
            const u32 qa = L0 + L_Q + (kr0 + 16 * a + i16) * 80 + hd * 40;
            const u32x2 q0 = lds64(qa + 8 * g);
            u32x2 q1 = lds64(qa + 32);
            if (g != 0) q1 = (u32x2){0, 0};
            qf[a] = (u32x4){q0[0], q0[1], q1[0], q1[1]};
        }
        const u32 kb0 = L0 + L_K + (kr0 + i16) * 80 + hd * 40 + 8 * g, kb1 = L0 + L_K + (kr0 + i16) * 80 + hd * 40 + 32;
        f32x4 mfirst, mlast;
#pragma unroll
        for (int r = 0; r < 4; ++r) { const bool lo = 4 * g + r <= i16; mfirst[r] = lo ? -INFINITY : 0.f; mlast[r] = lo ? 0.f : -INFINITY; }
        f32x4 s0[17], s1[17];
        {
            u32x4 kf[6];
#define KRD(t) do { const u32x2 k0 = lds64(kb0 + (t) * 1280), k1 = lds64(kb1 + (t) * 1280); kf[(t) % 6] = (u32x4){k0[0], k0[1], k1[0], k1[1]}; } while (0)
            KRD(0); KRD(1); KRD(2); KRD(3); KRD(4);
            static_for<0, 18>([&](auto T) {
                constexpr int t = T;
                SB0();
                if constexpr (t < 13) KRD(t + 5);
                if constexpr (t < 17) s0[t] = mfma(kf[t % 6], qf[0], t == 0 ? mfirst : t == 16 ? mlast : Z4);
                if constexpr (t > 0) s1[t - 1] = mfma(kf[t % 6], qf[1], t == 1 ? mfirst : t == 17 ? mlast : Z4);
                SGB(M_DSR, 2); SGB(M_MFMA, 2);
                SB0();
            });
#undef KRD
        }
        WO_STEP(4 + w);
        if (rb < 2) {
            const int fv = rb == 1 ? 128 : 256;
#pragma unroll
            for (int t = 0; t < 16; ++t) {
                const bool d0 = kr0 + 16 * t < fv, d1 = kr0 + 16 * t + 16 < fv;
#pragma unroll
                for (int r = 0; r < 4; ++r) { s0[t][r] = d0 ? -INFINITY : s0[t][r]; s1[t][r] = d1 ? -INFINITY : s1[t][r]; }
            }
        }
        STAMP(22);
        float m0 = fmaxf(s0[0][0], s0[0][1]), m1 = fmaxf(s1[0][0], s1[0][1]), n0 = fmaxf(s0[0][2], s0[0][3]), n1 = fmaxf(s1[0][2], s1[0][3]);
#pragma unroll
        for (int t = 1; t < 17; ++t) {
            m0 = fmaxf(fmaxf(m0, s0[t][0]), s0[t][1]); m1 = fmaxf(fmaxf(m1, s1[t][0]), s1[t][1]);
            n0 = fmaxf(fmaxf(n0, s0[t][2]), s0[t][3]); n1 = fmaxf(fmaxf(n1, s1[t][2]), s1[t][3]);
        }
        m0 = fmaxf(m0, n0); m1 = fmaxf(m1, n1);
        m0 = xmax4(m0); m1 = xmax4(m1);
        WO_STEP(12 + w);
        STAMP(23);
        f32x4 o00 = Z4, o01 = Z4, o10 = Z4, o11 = Z4;
        const u32 vb = L0 + L_V + (kr0 + 4 * g + q4) * 96 + hd * 48 + 8 * p4;
        auto pv = [&](auto SUB) {
            constexpr bool sub = SUB;
            u32x4 vf[2][2];
            vf[0][0] = ldtr2(vb, vb + 16 * 96); vf[0][1] = ldtr2(vb + 32, vb + 16 * 96 + 32);
#define EX(v, m) __builtin_amdgcn_exp2f(sub ? (v) - (m) : (v))
            static_for<0, 9>([&](auto U) {
                constexpr int u = U;
                if constexpr (u < 8) { const u32 a = vb + (u + 1) * 32 * 96; vf[(u + 1) & 1][0] = ldtr2(a, a + 16 * 96); vf[(u + 1) & 1][1] = ldtr2(a + 32, a + 16 * 96 + 32); }
                u32x4 pa, pb;
                pa[0] = pack2(EX(s0[2 * u][0], m0), EX(s0[2 * u][1], m0)); pa[1] = pack2(EX(s0[2 * u][2], m0), EX(s0[2 * u][3], m0));
                if constexpr (u < 8) { pa[2] = pack2(EX(s0[2 * u + 1][0], m0), EX(s0[2 * u + 1][1], m0)); pa[3] = pack2(EX(s0[2 * u + 1][2], m0), EX(s0[2 * u + 1][3], m0)); }
                else { pa[2] = 0; pa[3] = 0; }
                if constexpr (u > 0) { pb[0] = pack2(EX(s1[2 * u - 1][0], m1), EX(s1[2 * u - 1][1], m1)); pb[1] = pack2(EX(s1[2 * u - 1][2], m1), EX(s1[2 * u - 1][3], m1)); }
                else { pb[0] = 0; pb[1] = 0; }
                pb[2] = pack2(EX(s1[2 * u][0], m1), EX(s1[2 * u][1], m1)); pb[3] = pack2(EX(s1[2 * u][2], m1), EX(s1[2 * u][3], m1));
                o00 = mfma(vf[u & 1][0], pa, o00); o01 = mfma(vf[u & 1][1], pa, o01);
                o10 = mfma(vf[u & 1][0], pb, o10); o11 = mfma(vf[u & 1][1], pb, o11);
                SB0();
            });
#undef EX
        };
        if (__all(fabsf(m0) <= 64.f && fabsf(m1) <= 64.f)) pv(std::false_type{}); else pv(std::true_type{});
        STAMP(24);
        const float i0 = __builtin_amdgcn_rcpf(__shfl(o01[0], i16 + 16)), i1 = __builtin_amdgcn_rcpf(__shfl(o11[0], i16 + 16));
        const u32 oa = L0 + L_Q + (kr0 + i16) * 80 + hd * 40;
        sts64(oa + 8 * g, (u32x2){pack2(o00[0] * i0, o00[1] * i0), pack2(o00[2] * i0, o00[3] * i0)});
        sts64(oa + 1280 + 8 * g, (u32x2){pack2(o10[0] * i1, o10[1] * i1), pack2(o10[2] * i1, o10[3] * i1)});
        if (g == 0) {
            sts64(oa + 32, (u32x2){pack2(o01[0] * i0, o01[1] * i0), pack2(o01[2] * i0, o01[3] * i0)});
            sts64(oa + 1280 + 32, (u32x2){pack2(o11[0] * i1, o11[1] * i1), pack2(o11[2] * i1, o11[3] * i1)});
        }
    }
    BAR();
    {
        const int tok = 64 * (w & 1) + l;
        const u32 oo = WS_O + (((rb * 8 + (tok >> 4)) * 80 + 5 * j) * 16 + (tok & 15)) * 16;
        ST16C(lds128(L0 + L_Q + tok * 80 + (w >> 1) * 16), oo + (w >> 1) * 256);
        if (w < 2) ST16C(lds128(L0 + L_Q + tok * 80 + 64), oo + 4 * 256);
    }
    VMCNT(0);
    BAR();
    if (tid == 0) {
        st_agent(ws + WS_FO + (rb * 16 + j) * FS, tag);
        if (wpwg) st_agent(ws + WS_FWP + b * FS, tag);
    }
    STAMP(8);

    const int wm = w & 3, wk = w >> 2;
    const int mt3 = rb * 8 + 4 * hf + wm;
    u32x2 x1k[5];
    {
        const int wdu = b + 256 * (w - 6);
        const bool wdw = w >= 6 && wdu < 432;
        if (w >= 1 && w < 5) {
#pragma unroll
            for (int i = 0; i < 5; ++i) glds16(p.wo, wosrc + (w - 1) * 32 * 1280, wvoff[i], L0 + L_WO + (w - 1) * 5120 + i * 1024);
        } else if (w >= 5) {
#pragma unroll
            for (int e = 0; e < 4; ++e) {
                const int pc = w - 5 + 3 * e;
                if (pc < 10) {
                    const u32 sg = 64 * pc + l, r = (sg * 6554) >> 16, c = sg - r * 10;
                    glds16(p.x, (const char*)p.x + (rb * 128 + 64 * hf) * 1280 + cb * 160, r * 1280 + c * 16, L0 + L_XR + pc * 1024);
                }
            }
            if (wdw) {
                const int ucb = wdu / 54, us = wdu - 54 * ucb;
#pragma unroll
                for (int i = 0; i < 5; ++i) glds16(p.wd, (const char*)p.wd + ucb * 160 + us * 32 * 1280, wvoff[i], L0 + L_WDRAW + (w - 6) * 5120 + i * 1024);
            }
        }
        if (w == 1 && l < 20) stsf4(L0 + L_GA + l * 16, gather1(ws, 320 + 20 * cb + l, tag, ws, 4));
        if (w == 0) {
            const int bl = min((Gj >> 2) + ((l >> 2) % 3), (Gj + nj - 1) >> 2);
            poll_at(ws, WS_FWP + (108 * (l >= 12 && l < 24 ? 1 : 0) + 4 * bl + (l & 3)) * FS, l, tag, ws, 6);
            poll_flags(ws + WS_FO + rb * 16 * FS, 16, l, tag, ws, 5);
        }
        if (wdw) vmw<5>(); else vmw<0>();
        BAR();
        STAMP(9);
        u32x4 a[10];
        const char* asrc = ws + WS_O + (mt3 * 80 + 40 * wk) * 256;
#define AL(s) a[s] = gld16c(((s) & 3) * 1024, asrc + ((s) >> 2) * 4096, lo16)
        AL(0); AL(1); AL(2); AL(3);
        f32x4 acc[5];
#pragma unroll
        for (int n = 0; n < 5; ++n) acc[n] = Z4;
        const u32 wb = L0 + L_WO + wk * 51200;
        const u32 b0 = wb + 160 * (8 * g + 4 * (g & 1) + q4) + 8 * p4, b1 = wb + 160 * (8 * g + 4 * ((g & 1) ^ 1) + q4) + 8 * p4;
        u32x4 bf[3][5];
#pragma unroll
        for (int n = 0; n < 5; ++n) { bf[0][n] = ldtr2(b0 + n * 32, b1 + n * 32); bf[1][n] = ldtr2(b0 + 5120 + n * 32, b1 + 5120 + n * 32); }
        static_for<0, 10>([&](auto S) {
            constexpr int s = S;
            vmw1<(s < 7 ? 3 : 9 - s)>(a[s]);
            if constexpr (s + 4 < 10) AL(s + 4);
            SB0();
#pragma unroll
            for (int n = 0; n < 5; ++n) {
                if (s < 8) bf[(s + 2) % 3][n] = ldtr2(b0 + (s + 2) * 5120 + n * 32, b1 + (s + 2) * 5120 + n * 32);
                acc[n] = mfma(bf[s % 3][n], a[s], acc[n]);
            }
#pragma unroll
            for (int n = 0; n < 5; ++n) { SGB(M_DSR, 2); SGB(M_MFMA, 1); }
            SB0();
        });
#undef AL
        STAMP(25);
        if (wk == 1) {
#pragma unroll
            for (int n = 0; n < 5; ++n) stsf4(L0 + L_RED3 + (wm * 5 + n) * 1024 + lo16, acc[n]);
        }
        BAR();
        if (wk == 0) {
            float s1 = 0.f, s2 = 0.f;
#pragma unroll
            for (int n = 0; n < 5; ++n) {
                const f32x4 v = acc[n] + ldsf4(L0 + L_RED3 + (wm * 5 + n) * 1024 + lo16);
                const f32x4 ga = ldsf4(L0 + L_GA + (16 * n + 4 * g) * 4);
                const u32x2 xr = lds64(L0 + L_XR + (16 * wm + i16) * 160 + 32 * n + 8 * g);
                const u32x2 o = {pack2(bflo(xr[0]) + ga[0] * v[0], bfhi(xr[0]) + ga[1] * v[1]), pack2(bflo(xr[1]) + ga[2] * v[2], bfhi(xr[1]) + ga[3] * v[3])};
                x1k[n] = o;
                s1 = dot2(o[0], 0x3F803F80u, s1); s1 = dot2(o[1], 0x3F803F80u, s1);
                s2 = dot2(o[0], o[0], s2); s2 = dot2(o[1], o[1], s2);
            }
            const u32 xo = WS_X1 + ((mt3 * 80 + 10 * cb) * 16 + i16) * 16;
#pragma unroll
            for (int n = 0; n < 4; n += 2) {
                const auto a0 = __builtin_amdgcn_permlane16_swap(x1k[n][0], x1k[n + 1][0], false, false);
                const auto a1 = __builtin_amdgcn_permlane16_swap(x1k[n][1], x1k[n + 1][1], false, false);
                ST16C(((u32x4){a0[0], a1[0], a0[1], a1[1]}), xo + (2 * (n + (g & 1)) + (g >> 1)) * 256);
            }
            ST8C(x1k[4], xo + (8 + (g >> 1)) * 256 + 8 * (g & 1));
            s1 = xsum4(s1); s2 = xsum4(s2);
            if (l < 16) ST8C(((u32x2){__float_as_uint(s1), __float_as_uint(s2)}), WS_STAT + ((rb * 8 + cb) * 128 + 64 * hf + 16 * wm + l) * 8);
            STAMP(26);
            vmw<0>();
        } else {
#pragma unroll
            for (int n = 0; n < 5; ++n) x1k[n] = (u32x2){0, 0};
            if (wdw) {
                const u32 wr = L0 + L_WDRAW + (w - 6) * 5120;
                const u32 r0 = wr + 160 * (8 * g + 4 * (g & 1) + q4) + 8 * p4, r1 = wr + 160 * (8 * g + 4 * ((g & 1) ^ 1) + q4) + 8 * p4;
#pragma unroll
                for (int n = 0; n < 5; ++n) ST16C(ldtr2(r0 + n * 32, r1 + n * 32), WS_WD + (wdu * 5 + n) * 1024 + lo16);
            }
        }
        BAR();
        if (tid == 0) st_agent(ws + WS_FX1 + (rb * 16 + j) * FS, tag);
    }
    STAMP(10);

    const char* wdsrc = ws + WS_WD + (cb * 54 + wk * 27) * 5120;
    const u32 r5w = L0 + wk * 20480 + wm * 5120;
#define P5_B1(t, i) do { if ((i) == 0 || (i) == 4) glds16c(wdsrc + min(4 * (t) + wm, 26) * 5120 + (i) * 1024, lo16, r5w + r5_slot(t) + (i) * 1024); \
        else glds16c_next(((i) & 3) * 1024, wdsrc + min(4 * (t) + wm, 26) * 5120, lo16); } while (0)
    {
        const int wn = w >> 2;
        const int bst = w < 4 ? 4 * w : 16 + 3 * (w - 4);
        const char* wpj = ws + WS_WP + (j * 20 * 14 + bst) * 1024;
        const char* x1b = ws + WS_X1 + (rb * 8 + w) * 80 * 256;
        const u32 r4a = L0 + L_R4 + w * 2048, r4b = L0 + L_R4 + 16384 + bst * 1024;
#define P4_B1(t, i) do { if ((i) == 0) glds16c(wpj + (t) * 28 * 1024, lo16, r4b + ((t) % 3) * R4_STAGE); else glds16c_next((i) * 1024, wpj + (t) * 28 * 1024, lo16); } while (0)
#define P4_A1(t, i) do { if ((i) == 0) glds16c(x1b + 8 * (t) * 256, lo16, r4a + ((t) % 3) * R4_STAGE); else glds16c_next(1024, x1b + 8 * (t) * 256, lo16); } while (0)
#define P4_B(t) do { P4_B1(t, 0); P4_B1(t, 1); P4_B1(t, 2); if (w < 4) P4_B1(t, 3); } while (0)
        P4_B(0); P4_B(1); P4_B(2);
        if (w == 0) poll_flags(ws + WS_FX1 + rb * 16 * FS, 16, l, tag, ws, 7);
        BAR();
        STAMP(11);
        glds16c(ws + WS_STAT + rb * 8192 + w * 1024, lo16, L0 + L_ST4 + w * 1024);
        if (w < 7) glds16c(ws + WS_UV + j * 7168 + w * 1024, lo16, L0 + L_UV + w * 1024);
        P4_A1(0, 0); P4_A1(0, 1); P4_A1(1, 0); P4_A1(1, 1); P4_A1(2, 0); P4_A1(2, 1);
        f32x4 acc[7][2];
#pragma unroll
        for (int n = 0; n < 7; ++n) { acc[n][0] = Z4; acc[n][1] = Z4; }
        const u32 ra = opaque(L0 + L_R4 + 2 * wm * 2048 + lo16), rbb = opaque(L0 + L_R4 + 16384 + 7 * wn * 1024 + lo16);
        u32x4 fa[2][2], fb[2][7];
#define P4_RA(buf, t, ks) do { const u32 so = ((t) % 3) * R4_STAGE + (ks) * 1024; fa[buf][0] = lds128(ra + so); fa[buf][1] = lds128(ra + so + 2048); } while (0)
#define P4_RB(buf, t, ks, n) fb[buf][n] = lds128(rbb + ((t) % 3) * R4_STAGE + (ks) * 14336 + (n) * 1024)
#define P4_MM(buf, n) do { acc[n][0] = mfma(fb[buf][n], fa[buf][0], acc[n][0]); acc[n][1] = mfma(fb[buf][n], fa[buf][1], acc[n][1]); } while (0)
#define P4_HALF(buf, rd, t, ks, V0, V1, V2) do { \
            SB0(); if (rd) { P4_RA((buf) ^ 1, t, ks); P4_RB((buf) ^ 1, t, ks, 0); } P4_MM(buf, 0); SB0(); V0; \
            SB0(); if (rd) P4_RB((buf) ^ 1, t, ks, 1); P4_MM(buf, 1); SB0(); \
            SB0(); if (rd) P4_RB((buf) ^ 1, t, ks, 2); P4_MM(buf, 2); SB0(); V1; \
            SB0(); if (rd) P4_RB((buf) ^ 1, t, ks, 3); P4_MM(buf, 3); SB0(); \
            SB0(); if (rd) P4_RB((buf) ^ 1, t, ks, 4); P4_MM(buf, 4); SB0(); V2; \
            SB0(); if (rd) P4_RB((buf) ^ 1, t, ks, 5); P4_MM(buf, 5); SB0(); \
            SB0(); if (rd) P4_RB((buf) ^ 1, t, ks, 6); P4_MM(buf, 6); SB0(); } while (0)
        VMCNT(4);
        BAR();
        if (tid == 0) st_agent(ws + WS_FW2 + b * FS, tag);
        STAMP(27);
        u32x2 wdf = {0u, 0u};
        const u32 wdo = WS_FW2 + ((54 * cb + (l < 54 ? l : 0)) & 255) * FS;
        P4_RA(0, 0, 0);
#pragma unroll
        for (int n = 0; n < 7; ++n) P4_RB(0, 0, 0, n);
        static_for<0, 10>([&](auto T) {
            constexpr int t = T;
            constexpr bool f = t >= 1 && t + 2 <= 9, sN = t + 3 <= 9;
            if constexpr (t < 9) P4_HALF(0, true, t, 1, if (f && w < 4) P4_B1(t + 2, 3), if (f) P4_A1(t + 2, 0), if (f) P4_A1(t + 2, 1));
            else P4_HALF(0, true, t, 1, P5_B1(0, 3), P5_B1(0, 4), P5_B1(1, 0));
            if constexpr (t == 5) {
                if (tid < 128) {
                    f32x2 st = {0.f, 0.f};
#pragma unroll
                    for (int c = 0; c < 8; ++c) st += *(LAS f32x2*)(size_t)(L0 + L_ST4 + (c * 128 + tid) * 8);
                    const float mu = st[0] * (1.f / 640.f), var = fmaxf(st[1] * (1.f / 640.f) - mu * mu, 0.f), r = __builtin_amdgcn_rsqf(var + 1e-5f);
                    stsf2(L0 + L_MR + tid * 8, (f32x2){r, -mu * r});
                } else {
                    const int e = tid - 128;
                    stsf(L0 + L_UVS + e * 4, (ldsf(L0 + L_UV + (e >> 5) * 512 + (e & 31) * 4) + ldsf(L0 + L_UV + (e >> 5) * 512 + 128 + (e & 31) * 4))
                                           + (ldsf(L0 + L_UV + (e >> 5) * 512 + 256 + (e & 31) * 4) + ldsf(L0 + L_UV + (e >> 5) * 512 + 384 + (e & 31) * 4)));
                    if (e < 64) { const int e2 = e + 384;
                        stsf(L0 + L_UVS + e2 * 4, (ldsf(L0 + L_UV + (e2 >> 5) * 512 + (e2 & 31) * 4) + ldsf(L0 + L_UV + (e2 >> 5) * 512 + 128 + (e2 & 31) * 4))
                                                + (ldsf(L0 + L_UV + (e2 >> 5) * 512 + 256 + (e2 & 31) * 4) + ldsf(L0 + L_UV + (e2 >> 5) * 512 + 384 + (e2 & 31) * 4))); }
                }
            }
            if constexpr (t < 9) {
                if constexpr (t == 0) vmw<2>();
                else if constexpr (t == 8) {
                    asm volatile("s_waitcnt vmcnt(0)" : "+v"(wdf) :: "memory");
                    if (w == 7) { if (!__all(wdf[0] == (u32)tag && wdf[1] == (u32)(tag >> 32))) poll_at(ws, wdo, l, tag, ws, 12); }
                }
                else { if (w < 4) vmw<6>(); else vmw<5>(); }
                BAR();
                if constexpr (t == 7) { if (w == 7) asm volatile("s_nop 4\n\tglobal_load_dwordx2 %0, %1, %2 sc1" : "=v"(wdf) : "v"(wdo), "s"(ws) : "memory"); }
                if constexpr (t < 8) P4_HALF(1, true, t + 1, 0, if (sN) P4_B1(t + 3, 0), if (sN) P4_B1(t + 3, 1), if (sN) P4_B1(t + 3, 2));
                else P4_HALF(1, true, t + 1, 0, P5_B1(0, 0), P5_B1(0, 1), P5_B1(0, 2));
            } else {
                P4_HALF(1, false, 0, 0, P5_B1(1, 1), P5_B1(1, 2), P5_B1(1, 3));
                P5_B1(1, 4);
            }
        });
#undef P4_B
#undef P4_B1
#undef P4_A1
#undef P4_RA
#undef P4_RB
#undef P4_MM
#undef P4_HALF
        STAMP(12);
        f32x2 mr[2];
#pragma unroll
        for (int m = 0; m < 2; ++m) mr[m] = *(LAS f32x2*)(size_t)(L0 + L_MR + (32 * wm + 16 * m + i16) * 8);
#pragma unroll
        for (int n = 0; n < 7; ++n) {
            const f32x4 u = ldsf4(L0 + L_UVS + (7 * wn + n) * 128 + 16 * g), v = ldsf4(L0 + L_UVS + (7 * wn + n) * 128 + 64 + 16 * g);
#pragma unroll
            for (int m = 0; m < 2; ++m) acc[n][m] = acc[n][m] * mr[m][0] + (u * mr[m][1] + v);
        }
        if (nj == 7) stsf4(L0 + L_XCH + w * 1024 + lo16, wn == 0 ? acc[6][1] : acc[6][0]);
        auto silu_mul = [&](const f32x4 gt, const f32x4 up) -> u32x2 {
            const f32x4 e = gt * (-LOG2E), gu = gt * up;
            f32x4 d;
#pragma unroll
            for (int r = 0; r < 4; ++r) d[r] = __builtin_amdgcn_exp2f(e[r]);
            d = d + 1.f;
#pragma unroll
            for (int r = 0; r < 4; ++r) d[r] = __builtin_amdgcn_rcpf(d[r]);
            const f32x4 o = gu * d;
            return (u32x2){pack2(o[0], o[1]), pack2(o[2], o[3])};
        };
#pragma unroll
        for (int a = 0; a < 3; ++a) {
            const int la = a + 4 * wn, gam = Gj + (la <= 3 || nj == 7 ? la : la - 1);
            const u32x2 o0 = silu_mul(acc[2 * a][0], acc[2 * a + 1][0]), o1 = silu_mul(acc[2 * a][1], acc[2 * a + 1][1]);
            const auto s0 = __builtin_amdgcn_permlane16_swap(o0[0], o1[0], false, false);
            const auto s1 = __builtin_amdgcn_permlane16_swap(o0[1], o1[1], false, false);
            const u32x4 v = {s0[0], s1[0], s0[1], s1[1]};
            ST16C(v, WS_ACT + (((rb * 8 + 2 * wm + (g & 1)) * 216 + 2 * gam + (g >> 1)) * 16 + i16) * 16);
        }
        STAMP(28);
        BAR();
        if (nj == 7) {
            const f32x4 other = ldsf4(L0 + L_XCH + (w ^ 4) * 1024 + lo16);
            const u32x2 o = wn == 0 ? silu_mul(acc[6][0], other) : silu_mul(other, acc[6][1]);
            ST8C(o, WS_ACT + (((rb * 8 + 2 * wm + wn) * 216 + 2 * (Gj + 3) + (g >> 1)) * 16 + i16) * 16 + 8 * (g & 1));
        }
        P5_B1(2, 0); P5_B1(2, 1); P5_B1(2, 2); P5_B1(2, 3); P5_B1(2, 4);
        vmw<5>();
        BAR();
        if (tid == 0) st_agent(ws + WS_FACT + (rb * 16 + j) * FS, tag);
    }
    STAMP(13);

    {
        if (w == 1 && l < 20) stsf4(L0 + L_GB + l * 16, gather1(ws, 800 + 20 * cb + l, tag, ws, 8));
        if (w == 0) poll_flags(ws + WS_FACT + rb * 16 * FS, 16, l, tag, ws, 9);
        const bool bump = rb == 15 && j == 0;
        if (bump && w >= 4) poll_at(ws, WS_FO + (tid - 256) * FS, l, tag, ws, 10);
        VMCNT(0);
        BAR();
        if (bump && tid == 0) st_agent(ws + WS_EPOCH, tag);
        STAMP(14);
        const char* asrc = ws + WS_ACT + (mt3 * 216 + wk * 108) * 256;
        u32x4 a[3][4];
#define P5_A1(t, i) a[(t) % 3][i] = gld16c((i) * 1024, asrc + (t) * 4096, lo16)
        P5_A1(0, 0); P5_A1(0, 1); P5_A1(0, 2); P5_A1(0, 3); P5_A1(1, 0); P5_A1(1, 1); P5_A1(1, 2); P5_A1(1, 3); P5_A1(2, 0); P5_A1(2, 1); P5_A1(2, 2); P5_A1(2, 3);
        f32x4 acc[5];
#pragma unroll
        for (int n = 0; n < 5; ++n) acc[n] = Z4;
        const u32 rb5 = opaque(L0 + wk * 20480 + lo16);
        u32x4 fb[3][5];
#define P5_RD(buf, t, kk) do { _Pragma("unroll") for (int n = 0; n < 5; ++n) fb[buf][n] = lds128(rb5 + r5_slot(t) + (kk) * 5120 + n * 1024); } while (0)
#define P5_MM(buf, av) do { _Pragma("unroll") for (int n = 0; n < 5; ++n) acc[n] = mfma(fb[buf][n], (av), acc[n]); } while (0)
#define P5_MIX() do { _Pragma("unroll") for (int n = 0; n < 5; ++n) { SGB(M_DSR, 1); SGB(M_MFMA, 1); } } while (0)
        P5_RD(0, 0, 0); P5_RD(1, 0, 1);
        static_for<0, 7>([&](auto T) {
            constexpr int t = T, q = t % 3, k0 = 4 * t;
            constexpr bool nx = t + 3 < 7;
            constexpr int NA = t == 0 ? 8 : t == 1 ? 13 : t == 2 ? 18 : t == 3 ? 19 : t == 4 ? 18 : t == 5 ? 9 : 3;
            constexpr int NB = t == 2 ? 11 : t == 3 ? 11 : t == 4 ? 8 : t == 5 ? 0 : 63;
            vmw4<NA>(a[q][0], a[q][1], a[q][2], a[q][3]);
            SB0(); P5_RD((k0 + 2) % 3, t, 2); P5_MM(k0 % 3, a[q][0]); P5_MIX(); SB0();
            if constexpr (nx) P5_A1(t + 3, 0);
            if constexpr (t < 6) {
                SB0(); P5_RD((k0 + 3) % 3, t, 3); P5_MM((k0 + 1) % 3, a[q][1]); P5_MIX(); SB0();
                if constexpr (nx) P5_A1(t + 3, 1);
                vmw<NB>(); BAR();
                if constexpr (nx) { P5_B1(t + 3, 0); P5_B1(t + 3, 1); }
                SB0(); P5_RD((k0 + 4) % 3, t + 1, 0); P5_MM((k0 + 2) % 3, a[q][2]); P5_MIX(); SB0();
                if constexpr (nx) { P5_A1(t + 3, 2); P5_B1(t + 3, 2); P5_B1(t + 3, 3); }
                SB0(); P5_RD((k0 + 5) % 3, t + 1, 1); P5_MM((k0 + 3) % 3, a[q][3]); P5_MIX(); SB0();
                if constexpr (nx) { if constexpr (t + 3 < 6) P5_A1(t + 3, 3); P5_B1(t + 3, 4); }
            } else {
                SB0(); P5_MM((k0 + 1) % 3, a[q][1]); SB0(); P5_MM((k0 + 2) % 3, a[q][2]); SB0();
            }
        });
#undef P5_B1
#undef P5_A1
#undef P5_RD
#undef P5_MM
#undef P5_MIX
        STAMP(15);
        if (wk == 1) {
#pragma unroll
            for (int n = 0; n < 5; ++n) stsf4(L0 + L_RED5 + (wm * 5 + n) * 1024 + lo16, acc[n]);
        }
        BAR();
        if (wk == 0) {
#pragma unroll
            for (int n = 0; n < 5; ++n) {
                const f32x4 v = acc[n] + ldsf4(L0 + L_RED5 + (wm * 5 + n) * 1024 + lo16);
                const f32x4 gb = ldsf4(L0 + L_GB + (16 * n + 4 * g) * 4);
                const u32x2 o = {pack2(bflo(x1k[n][0]) + gb[0] * v[0], bfhi(x1k[n][0]) + gb[1] * v[1]), pack2(bflo(x1k[n][1]) + gb[2] * v[2], bfhi(x1k[n][1]) + gb[3] * v[3])};
                sts64(L0 + L_OS + (16 * wm + i16) * 160 + 32 * n + 8 * g, o);
            }
        }
        BAR();
#pragma unroll
        for (int i = 0; i < 2; ++i) {
            const int id = tid + 512 * i;
            if (id < 640) {
                const int r = (id * 6554) >> 16, c = id - 10 * r;
                *(GAS u32x4*)((char*)p.out + (rb * 128 + 64 * hf + r) * 1280 + cb * 160 + c * 16) = lds128(L0 + L_OS + id * 16);
            }
        }
    }
    STAMP(16); STAMP_RT(63); STAMP_OUT();
    asm volatile(".Lmk_code_to:");
}

extern "C" void kernel_launch(void* const* d_in, const int* in_sizes, int n_in, void* d_out, int out_size, void* d_ws, size_t ws_size, hipStream_t stream) {
    Params p;
    p.x = (const u16*)d_in[0]; p.wqkv = (const u16*)d_in[1]; p.wo = (const u16*)d_in[2];
    p.wg = (const u16*)d_in[4]; p.wu = (const u16*)d_in[5]; p.wd = (const u16*)d_in[6];
    p.c = (const u16*)d_in[7]; p.wmod = (const u16*)d_in[8];
    p.out = (u16*)d_out; p.ws = (char*)d_ws;
    mega_kernel<<<256, 512, 0, stream>>>(p);
}
```

```cpp
#include <hip/hip_runtime.h>
#include <hip/hip_bf16.h>
#include <stdint.h>
#include <type_traits>

typedef unsigned short u16;
typedef unsigned u32;
typedef unsigned long long u64;
typedef __attribute__((ext_vector_type(2))) unsigned u32x2;
typedef __attribute__((ext_vector_type(4))) unsigned u32x4;
typedef __attribute__((ext_vector_type(2))) float f32x2;
typedef __attribute__((ext_vector_type(4))) float f32x4;
typedef __attribute__((ext_vector_type(2))) __bf16 bf16x2;
typedef __attribute__((ext_vector_type(8))) __bf16 bf16x8;
typedef __attribute__((ext_vector_type(4))) short s16x4;
#define LAS __attribute__((address_space(3)))
#define GAS __attribute__((address_space(1)))
#define DI __device__ __forceinline__

constexpr int SEQ = 2048, DM = 640, FF = 1728;
constexpr float QSCALE = 0.22360679774997896f * 1.4426950408889634f;
constexpr float LOG2E = 1.4426950408889634f;

constexpr u32 WS_EPOCH = 0;
constexpr u32 WS_ERR   = 256;
constexpr u32 FS = 128;
constexpr u32 WS_FKV   = 4096;
constexpr u32 WS_FO    = WS_FKV + 256 * FS;
constexpr u32 WS_FX1   = WS_FO + 256 * FS;
constexpr u32 WS_FACT  = WS_FX1 + 256 * FS;
constexpr u32 WS_FH    = WS_FACT + 256 * FS;
constexpr u32 WS_FWT   = WS_FH + 256 * FS;
constexpr u32 WS_FWP   = WS_FWT + 256 * FS;
constexpr u32 WS_FW2   = WS_FWP + 256 * FS;
constexpr u32 WS_MODG  = WS_FW2 + 256 * FS;
constexpr u32 WS_UV    = WS_MODG + 61440;
constexpr u32 WS_STAT  = WS_UV + 114688;
constexpr u32 WS_KV    = 589824;
constexpr u32 WS_O     = WS_KV + 256 * 22528;
constexpr u32 WS_X1    = WS_O + SEQ * DM * 2;
constexpr u32 WS_ACT   = WS_X1 + SEQ * DM * 2;
constexpr u32 WS_WP    = WS_ACT + SEQ * FF * 2;
constexpr u32 WS_H     = WS_WP + 16 * 20 * 14 * 1024;
constexpr u32 WS_WD    = WS_H + SEQ * DM * 2;
constexpr u32 WS_WT    = WS_WD + 8 * 54 * 5 * 1024;
constexpr u32 WS_END   = WS_WT + 16 * 153600;

constexpr u32 L_WKV = 0, L_WQ = 102400, L_GS = 153600, L_STP = 156160, L_MODP = 156416, L_CNT = 157440, L_SB = 157696, L_G2 = 158016, L_Z16 = 158656;
constexpr u32 L_WTRAW = 116736, L_XS = 133120;
constexpr u32 L_K = 0, L_V = 30720, L_Q = 67584, L_WPRAW = 77824, L_UVL = 98304;
constexpr u32 L_WO = 61440, L_RED3 = 0, L_XR = 30720, L_GA = 40960, L_WDRAW = 43008;
constexpr u32 L_R4 = 0, R4_STAGE = 45056, L_ST4 = 135168, L_UV = 143360, L_MR = 150528, L_XCH = 151552, L_UVS = 159744;
constexpr u32 L_RED5 = 126976, L_GB = 147456, L_OS = 148480;
constexpr u32 r5_slot(int t) { return t % 3 == 0 ? 45056 : t % 3 == 1 ? 86016 : 0; }

struct Params {
    const u16* x; const u16* wqkv; const u16* wo; const u16* wg; const u16* wu; const u16* wd;
    const u16* c; const u16* wmod; u16* out; char* ws;
};

DI float bflo(u32 d) { return __uint_as_float(d << 16); }
DI float bfhi(u32 d) { return __uint_as_float(d & 0xffff0000u); }
DI u32 pack2(float a, float b) { f32x2 v = {a, b}; return __builtin_bit_cast(u32, __builtin_convertvector(v, bf16x2)); }
DI float rbf(float a) { return bflo(pack2(a, a)); }
DI f32x4 mfma(u32x4 a, u32x4 b, f32x4 c) {
    return __builtin_amdgcn_mfma_f32_16x16x32_bf16(__builtin_bit_cast(bf16x8, a), __builtin_bit_cast(bf16x8, b), c, 0, 0, 0);
}
DI u32x2 ldtr(u32 a) { return __builtin_bit_cast(u32x2, __builtin_amdgcn_ds_read_tr16_b64_v4i16((LAS s16x4*)(size_t)a)); }
DI u32x4 ldtr2(u32 a0, u32 a1) { u32x2 x = ldtr(a0), y = ldtr(a1); return (u32x4){x[0], x[1], y[0], y[1]}; }
DI u32x4 lds128(u32 a) { return *(LAS u32x4*)(size_t)a; }
DI u32x2 lds64(u32 a) { return *(LAS u32x2*)(size_t)a; }
DI f32x4 ldsf4(u32 a) { return *(LAS f32x4*)(size_t)a; }
DI float ldsf(u32 a) { return *(LAS float*)(size_t)a; }
DI void sts128(u32 a, u32x4 v) { *(LAS u32x4*)(size_t)a = v; }
DI void sts64(u32 a, u32x2 v) { *(LAS u32x2*)(size_t)a = v; }
DI void stsf(u32 a, float v) { *(LAS float*)(size_t)a = v; }
DI void stsf4(u32 a, f32x4 v) { *(LAS f32x4*)(size_t)a = v; }
DI void stsf2(u32 a, f32x2 v) { *(LAS f32x2*)(size_t)a = v; }

DI void glds16_(const void* root, u32 voff, u32 ldsdst) {
    asm volatile("s_mov_b32 m0, %2\n\ts_nop 0\n\tglobal_load_lds_dwordx4 %0, %1" :: "v"(voff), "s"(root), "s"(__builtin_amdgcn_readfirstlane(ldsdst)) : "memory");
}
DI void glds16c_(const void* root, u32 voff, u32 ldsdst) {
    asm volatile("s_mov_b32 m0, %2\n\ts_nop 0\n\tglobal_load_lds_dwordx4 %0, %1 sc1" :: "v"(voff), "s"(root), "s"(__builtin_amdgcn_readfirstlane(ldsdst)) : "memory");
}
template <int OFF> DI void glds16c_next_(const void* root, u32 voff) {
    asm volatile("global_load_lds_dwordx4 %0, %1 offset:%2 sc1" :: "v"(voff), "s"(root), "i"(OFF) : "memory");
}
template <int OFF> DI u32x4 gld16(const void* root, u32 voff) {
    u32x4 r; asm volatile("global_load_dwordx4 %0, %1, %2 offset:%3" : "=v"(r) : "v"(voff), "s"(root), "i"(OFF) : "memory"); return r;
}
template <int OFF> DI u32x4 gld16c_(const void* root, u32 voff) {
    u32x4 r; asm volatile("global_load_dwordx4 %0, %1, %2 offset:%3 sc1" : "=v"(r) : "v"(voff), "s"(root), "i"(OFF) : "memory"); return r;
}
#define ROFF(root, ptr) ((u32)((const char*)(ptr) - (const char*)(root)))
#define glds16(root, ptr, voff, dst) glds16_((root), ROFF(root, ptr) + (voff), (dst))
#define glds16c(ptr, voff, dst) glds16c_(ws, ROFF(ws, ptr) + (voff), (dst))
#define glds16c_next(OFF, ptr, voff) glds16c_next_<(OFF)>(ws, ROFF(ws, ptr) + (voff))
#define gld16c(OFF, ptr, voff) gld16c_<(OFF)>(ws, ROFF(ws, ptr) + (voff))
DI u32 gldu16(const void* sbase, u32 voff) {
    u32 r; asm volatile("global_load_ushort %0, %1, %2" : "=v"(r) : "v"(voff), "s"(sbase) : "memory"); return r;
}
#define VMCNT(n) asm volatile("s_waitcnt vmcnt(" #n ")" ::: "memory")
#define BAR() asm volatile("s_waitcnt lgkmcnt(0)\n\ts_barrier" ::: "memory")
#define RLX_AGENT __ATOMIC_RELAXED, __HIP_MEMORY_SCOPE_AGENT
DI u64 ld_agent(const char* p) { return __hip_atomic_load((GAS u64*)p, RLX_AGENT); }
DI void st_agent(char* p, u64 v) { __hip_atomic_store((GAS u64*)p, v, RLX_AGENT); }
DI void st_agent32(char* p, u32 v) { __hip_atomic_store((GAS u32*)p, v, RLX_AGENT); }
DI float xsum4(float x) {
    const auto s = __builtin_amdgcn_permlane16_swap(__float_as_uint(x), __float_as_uint(x), false, false);
    x = __uint_as_float(s[0]) + __uint_as_float(s[1]);
    const auto t = __builtin_amdgcn_permlane32_swap(__float_as_uint(x), __float_as_uint(x), false, false);
    return __uint_as_float(t[0]) + __uint_as_float(t[1]);
}
DI float xmax4(float x) {
    const auto s = __builtin_amdgcn_permlane16_swap(__float_as_uint(x), __float_as_uint(x), false, false);
    x = fmaxf(__uint_as_float(s[0]), __uint_as_float(s[1]));
    const auto t = __builtin_amdgcn_permlane32_swap(__float_as_uint(x), __float_as_uint(x), false, false);
    return fmaxf(__uint_as_float(t[0]), __uint_as_float(t[1]));
}
template <int CTRL> DI float dppf(float v) { return __builtin_bit_cast(float, __builtin_amdgcn_update_dpp(0, __builtin_bit_cast(int, v), CTRL, 0xf, 0xf, true)); }

constexpr u32 SPIN_CAP = 1u << 17;
DI void poll_flags(const char* base, int n, int lane, u64 tag, char* ws, u32 code) {
    const char* a = base + (lane < n ? lane : 0) * FS;
    u32 spins = 0;
    for (;;) {
        const u64 v = ld_agent(a);
        if (__all(v == tag)) break;
        if (++spins > SPIN_CAP) { if (lane == 0) st_agent32(ws + WS_ERR, code); break; }
        __builtin_amdgcn_s_sleep(1);
    }
}
typedef __attribute__((ext_vector_type(2))) _Float16 f16x2;
DI float hlo(u32 d) { return (float)__builtin_bit_cast(f16x2, d)[0]; }
DI float hhi(u32 d) { return (float)__builtin_bit_cast(f16x2, d)[1]; }
DI u32 packh(float a, float b) { const f16x2 v = {(_Float16)a, (_Float16)b}; return __builtin_bit_cast(u32, v); }
DI bool gran_ok(const u32x4& v, u64 tag) { return v[2] == (u32)tag && v[3] == (u32)(tag >> 32); }
DI f32x4 gran_sum(const u32x4& a, const u32x4& b, const u32x4& c, const u32x4& d) {
    const f32x4 s = {(hlo(a[0]) + hlo(b[0])) + (hlo(c[0]) + hlo(d[0])), (hhi(a[0]) + hhi(b[0])) + (hhi(c[0]) + hhi(d[0])),
                     (hlo(a[1]) + hlo(b[1])) + (hlo(c[1]) + hlo(d[1])), (hhi(a[1]) + hhi(b[1])) + (hhi(c[1]) + hhi(d[1]))};
    return (f32x4){rbf(s[0]), rbf(s[1]), rbf(s[2]), rbf(s[3])};
}
DI f32x4 gather1(const char* ws, u32 g0, u64 tag, char* wsw, u32 code) {
    u32 spins = 0; u32x4 v0, v1, v2, v3;
    const u32 o = WS_MODG + g0 * 16;
    for (;;) {
        asm volatile("s_nop 4\n\tglobal_load_dwordx4 %0, %4, %5 sc1\n\tglobal_load_dwordx4 %1, %6, %5 sc1\n\tglobal_load_dwordx4 %2, %7, %5 sc1\n\tglobal_load_dwordx4 %3, %8, %5 sc1\n\ts_waitcnt vmcnt(0)"
                     : "=&v"(v0), "=&v"(v1), "=&v"(v2), "=&v"(v3) : "v"(o), "s"(ws), "v"(o + 15360), "v"(o + 30720), "v"(o + 46080) : "memory");
        if (__all(gran_ok(v0, tag) && gran_ok(v1, tag) && gran_ok(v2, tag) && gran_ok(v3, tag))) break;
        if (++spins > SPIN_CAP) { st_agent32(wsw + WS_ERR, code); break; }
        __builtin_amdgcn_s_sleep(1);
    }
    return gran_sum(v0, v1, v2, v3);
}
DI void reask(u32x4& v, const char* ws, u32 off, u64 tag) {
    if (!gran_ok(v, tag)) asm volatile("global_load_dwordx4 %0, %1, %2 sc1" : "+v"(v) : "v"(off), "s"(ws) : "memory");
}
DI void gather2(const char* ws, u32 g0, u32 g1, u64 tag, char* wsw, u32 code, f32x4& ra, f32x4& rb) {
    u32 spins = 0; u32x4 v0, v1, v2, v3, u0, u1, u2, u3;
    const u32 o = WS_MODG + g0 * 16, q = WS_MODG + g1 * 16;
    asm volatile("global_load_dwordx4 %0, %8, %9 sc1\n\tglobal_load_dwordx4 %1, %10, %9 sc1\n\tglobal_load_dwordx4 %2, %11, %9 sc1\n\tglobal_load_dwordx4 %3, %12, %9 sc1\n\t"
                 "global_load_dwordx4 %4, %13, %9 sc1\n\tglobal_load_dwordx4 %5, %14, %9 sc1\n\tglobal_load_dwordx4 %6, %15, %9 sc1\n\tglobal_load_dwordx4 %7, %16, %9 sc1\n\ts_waitcnt vmcnt(0)"
                 : "=&v"(v0), "=&v"(v1), "=&v"(v2), "=&v"(v3), "=&v"(u0), "=&v"(u1), "=&v"(u2), "=&v"(u3)
                 : "v"(o), "s"(ws), "v"(o + 15360), "v"(o + 30720), "v"(o + 46080), "v"(q), "v"(q + 15360), "v"(q + 30720), "v"(q + 46080) : "memory");
    for (;;) {
        if (__all(gran_ok(v0, tag) && gran_ok(v1, tag) && gran_ok(v2, tag) && gran_ok(v3, tag) && gran_ok(u0, tag) && gran_ok(u1, tag) && gran_ok(u2, tag) && gran_ok(u3, tag))) break;
        if (++spins > SPIN_CAP) { st_agent32(wsw + WS_ERR, code); break; }
        reask(v0, ws, o, tag); reask(v1, ws, o + 15360, tag); reask(v2, ws, o + 30720, tag); reask(v3, ws, o + 46080, tag);
        reask(u0, ws, q, tag); reask(u1, ws, q + 15360, tag); reask(u2, ws, q + 30720, tag); reask(u3, ws, q + 46080, tag);
        asm volatile("s_waitcnt vmcnt(0)" : "+v"(v0), "+v"(v1), "+v"(v2), "+v"(v3), "+v"(u0), "+v"(u1), "+v"(u2), "+v"(u3) :: "memory");
    }
    ra = gran_sum(v0, v1, v2, v3); rb = gran_sum(u0, u1, u2, u3);
}
struct Gather { u32x4 v0, v1, v2, v3, u0, u1, u2, u3; };
DI void gather_ask(const char* ws, u32 g0, u32 g1, Gather& G) {
    const u32 o = WS_MODG + g0 * 16, q = WS_MODG + g1 * 16;
    asm volatile("global_load_dwordx4 %0, %8, %9 sc1\n\tglobal_load_dwordx4 %1, %10, %9 sc1\n\tglobal_load_dwordx4 %2, %11, %9 sc1\n\tglobal_load_dwordx4 %3, %12, %9 sc1\n\t"
                 "global_load_dwordx4 %4, %13, %9 sc1\n\tglobal_load_dwordx4 %5, %14, %9 sc1\n\tglobal_load_dwordx4 %6, %15, %9 sc1\n\tglobal_load_dwordx4 %7, %16, %9 sc1"
                 : "=&v"(G.v0), "=&v"(G.v1), "=&v"(G.v2), "=&v"(G.v3), "=&v"(G.u0), "=&v"(G.u1), "=&v"(G.u2), "=&v"(G.u3)
                 : "v"(o), "s"(ws), "v"(o + 15360), "v"(o + 30720), "v"(o + 46080), "v"(q), "v"(q + 15360), "v"(q + 30720), "v"(q + 46080) : "memory");
}
DI void gather_finish(const char* ws, u32 g0, u32 g1, Gather& G, u64 tag, char* wsw, u32 code, f32x4& ra, f32x4& rb) {
    asm volatile("" : "+v"(G.v0), "+v"(G.v1), "+v"(G.v2), "+v"(G.v3), "+v"(G.u0), "+v"(G.u1), "+v"(G.u2), "+v"(G.u3) :: "memory");
    if (__all(gran_ok(G.v0, tag) && gran_ok(G.v1, tag) && gran_ok(G.v2, tag) && gran_ok(G.v3, tag) && gran_ok(G.u0, tag) && gran_ok(G.u1, tag) && gran_ok(G.u2, tag) && gran_ok(G.u3, tag))) {
        ra = gran_sum(G.v0, G.v1, G.v2, G.v3); rb = gran_sum(G.u0, G.u1, G.u2, G.u3);
    } else gather2(ws, g0, g1, tag, wsw, code, ra, rb);
}
DI void gather_strip(const char* ws, u32 gs, u32 gc, u32 strip, int l, u64 tag, char* wsw, u32 code) {
    f32x4 a, c;
    gather2(ws, l < 40 ? gs + l : gc + (l - 40), gc + 24 + (l & 15), tag, wsw, code, a, c);
    if (l >= 40) a = a + 1.f;
    c = c + 1.f;
    *(LAS u32x2*)(size_t)(strip + (l < 40 ? l * 8 : 320 + (l - 40) * 8)) = (u32x2){pack2(a[0], a[1]), pack2(a[2], a[3])};
    if (l < 16) *(LAS u32x2*)(size_t)(strip + 320 + (24 + l) * 8) = (u32x2){pack2(c[0], c[1]), pack2(c[2], c[3])};
}
DI void put_strip_spread(const f32x4& a, const f32x4& c, u32 strip, int l) {
    if (l < 40) *(LAS u32x2*)(size_t)(strip + l * 8) = (u32x2){pack2(a[0], a[1]), pack2(a[2], a[3])};
    else {
        const u32 p0 = pack2(a[0] + 1.f, a[1] + 1.f), p1 = pack2(a[2] + 1.f, a[3] + 1.f);
        *(LAS u32x4*)(size_t)(strip + 320 + (l - 40) * 16) = (u32x4){p0 & 0xffffu, p0 & 0xffff0000u, p1 & 0xffffu, p1 & 0xffff0000u};
    }
    if (l < 16) {
        const u32 p0 = pack2(c[0] + 1.f, c[1] + 1.f), p1 = pack2(c[2] + 1.f, c[3] + 1.f);
        *(LAS u32x4*)(size_t)(strip + 320 + (24 + l) * 16) = (u32x4){p0 & 0xffffu, p0 & 0xffff0000u, p1 & 0xffffu, p1 & 0xffff0000u};
    }
    if (l == 63) *(LAS u32x4*)(size_t)(strip + 960) = (u32x4){0, 0, 0, 0};
}
DI void poll_at(const char* ws, u32 off, int lane, u64 tag, char* wsw, u32 code) {
    u32 spins = 0;
    for (;;) {
        const u64 v = ld_agent(ws + off);
        if (__all(v == tag)) break;
        if (++spins > SPIN_CAP) { if (lane == 0) st_agent32(wsw + WS_ERR, code); break; }
        __builtin_amdgcn_s_sleep(1);
    }
}

template <int N> using IC = std::integral_constant<int, N>;
template <int I, int E, class F> DI void static_for(F&& f) { if constexpr (I < E) { f(IC<I>{}); static_for<I + 1, E>(f); } }
template <int N> DI void vmw() { asm volatile("s_waitcnt vmcnt(%0)" :: "i"(N) : "memory"); }
template <int N> DI void vmw1(u32x4& a) { asm volatile("s_waitcnt vmcnt(%1)" : "+v"(a) : "i"(N) : "memory"); }
template <int N> DI void vmw4(u32x4& a, u32x4& b, u32x4& c, u32x4& d) { asm volatile("s_waitcnt vmcnt(%4)" : "+v"(a), "+v"(b), "+v"(c), "+v"(d) : "i"(N) : "memory"); }
DI u32 opaque(u32 a) { asm("" : "+v"(a)); return a; }
DI float dot2(u32 a, u32 b, float c) { return __builtin_amdgcn_fdot2_f32_bf16(__builtin_bit_cast(bf16x2, a), __builtin_bit_cast(bf16x2, b), c, false); }
#define SB0() __builtin_amdgcn_sched_barrier(0)
#define SGB(m, n) __builtin_amdgcn_sched_group_barrier((m), (n), 0)
constexpr int M_MFMA = 0x8, M_DSR = 0x100;
#define Z4 ((f32x4){0.f, 0.f, 0.f, 0.f})
DI u32 lds_add(u32 a, u32 v) { return __hip_atomic_fetch_add((LAS u32*)(size_t)a, v, __ATOMIC_RELAXED, __HIP_MEMORY_SCOPE_WORKGROUP); }
DI u32 lds_ld(u32 a) { return __hip_atomic_load((LAS u32*)(size_t)a, __ATOMIC_RELAXED, __HIP_MEMORY_SCOPE_WORKGROUP); }
DI void worker_sync(u32 cnt, int lane) {
    asm volatile("s_waitcnt lgkmcnt(0)" ::: "memory");
    if (lane == 0) lds_add(cnt, 1u);
    for (u32 spins = 0; lds_ld(cnt) < 4u && spins < (1u << 22); ++spins) {}
    asm volatile("" ::: "memory");
}

__global__ __launch_bounds__(512, 2) void mega_kernel(Params p) {
    __shared__ __attribute__((aligned(16))) char ldsmem[163840];
    const u32 L0 = (u32)(uintptr_t)(LAS char*)ldsmem;
    const int tid = threadIdx.x, l = tid & 63;
    const int w = __builtin_amdgcn_readfirstlane(tid >> 6);
    const int b = blockIdx.x, xg = b & 7, sidx = b >> 3;
    const int rb = 4 * (xg >> 1) + (sidx >> 3), j = 8 * (xg & 1) + (sidx & 7);
    const int g = l >> 4, i16 = l & 15, q4 = i16 >> 2, p4 = l & 3;
    u32 wu0 = 0;
    if (w == 7) {
        const char* pc; u32 span;
        asm volatile("s_getpc_b64 %0\n.Lmk_code_from:\n\ts_mov_b32 %1, .Lmk_code_to-.Lmk_code_from" : "=s"(pc), "=s"(span));
        const u32 per = (span + 4095u) >> 12;
        const u32 off = min((sidx * per + min((u32)l, per - 1u)) * 128u, span - 4u);
        asm volatile("s_nop 0\n\tglobal_load_dword %0, %1, %2" : "=v"(wu0) : "v"(off), "s"(pc) : "memory");
    }
    char* const ws = p.ws;
    asm volatile("" :: "s"(p.x), "s"(p.wqkv), "s"(p.wo), "s"(p.wg), "s"(p.wu), "s"(p.wd), "s"(p.c), "s"(p.wmod), "s"(p.out), "s"(p.ws));
    u64 tag;
#define ASK_TAG(ep) u32x2 ep; asm volatile("global_load_dwordx2 %0, %1, %2" : "=v"(ep) : "v"(WS_EPOCH), "s"(ws) : "memory")
#define TAKE_TAG(ep) do { asm volatile("s_waitcnt vmcnt(0)" : "+v"(ep) :: "memory"); \
        tag = (((u64)(u32)__builtin_amdgcn_readfirstlane(ep[1]) << 32) | (u32)__builtin_amdgcn_readfirstlane(ep[0])) + 1; } while (0)
    const __amdgpu_buffer_rsrc_t wsr = __builtin_amdgcn_make_buffer_rsrc(ws, 0, WS_END, 0x00020000);
#define ST16C(v, off) __builtin_amdgcn_raw_buffer_store_b128((v), wsr, (off), 0, 16)
#define ST8C(v, off)  __builtin_amdgcn_raw_buffer_store_b64((v), wsr, (off), 0, 16)

    const int nj = j < 12 ? 7 : 6, Gj = j < 12 ? 7 * j : 84 + 6 * (j - 12);
    const bool wpwg = b < 216;
    const int wpm = b >= 108 ? 1 : 0, wpb = (b - 108 * wpm) >> 2, wpq = b & 3;
    u32x4 mw[5]; u32 mc[5];
    auto mod_ask = [&]() {
#pragma unroll
        for (int i = 0; i < 5; ++i) {
            const int r = 160 * (b & 3) + ((tid & 255) >> 3) + 32 * i;
            mw[i] = gld16<0>(p.wmod, r * 7680 + (b >> 2) * 128 + (tid & 7) * 16);
            mc[i] = gldu16(p.c, r * 2);
        }
    };
    auto mod_finish = [&]() {
        float acc[8];
#pragma unroll
        for (int e = 0; e < 8; ++e) acc[e] = 0.f;
#pragma unroll
        for (int i = 0; i < 5; ++i) {
            const float cv = bflo(mc[i]);
            const float sv = cv * __builtin_amdgcn_rcpf(1.f + __builtin_amdgcn_exp2f(-cv * LOG2E));
            const u32 slo = pack2(sv, 0.f), shi = slo << 16;
#pragma unroll
            for (int d = 0; d < 4; ++d) { acc[2 * d] = dot2(mw[i][d], slo, acc[2 * d]); acc[2 * d + 1] = dot2(mw[i][d], shi, acc[2 * d + 1]); }
        }
#pragma unroll
        for (int e = 0; e < 8; ++e) { const float v = acc[e]; acc[e] = xsum4(v + dppf<0x128>(v)); }
        if (l < 8) {
            stsf4(L0 + L_MODP + (w & 3) * 256 + l * 32, (f32x4){acc[0], acc[1], acc[2], acc[3]});
            stsf4(L0 + L_MODP + (w & 3) * 256 + l * 32 + 16, (f32x4){acc[4], acc[5], acc[6], acc[7]});
        }
        asm volatile("s_waitcnt lgkmcnt(0)" ::: "memory");
        u32 old = 0;
        if (l == 0) old = lds_add(L0 + L_CNT, 1u);
        if (__builtin_amdgcn_readfirstlane(old) == 3) {
            float v = 0.f;
#pragma unroll
            for (int ww = 0; ww < 4; ++ww) v += ldsf(L0 + L_MODP + ww * 256 + l * 4);
            const float v1 = dppf<0x55>(v), v2 = dppf<0xAA>(v), v3 = dppf<0xFF>(v), v0 = dppf<0x00>(v);
            const u32x4 gr = {packh(v0, v1), packh(v2, v3), (u32)tag, (u32)(tag >> 32)};
            if (p4 == 0) ST16C(gr, WS_MODG + ((b & 3) * 960 + (b >> 2) * 16 + (l >> 2)) * 16);
        }
    };
#define MOD_LANDED() asm volatile("s_waitcnt vmcnt(0)" : "+v"(mw[0]), "+v"(mw[1]), "+v"(mw[2]), "+v"(mw[3]), "+v"(mw[4]), "+v"(mc[0]), "+v"(mc[1]), "+v"(mc[2]), "+v"(mc[3]), "+v"(mc[4]) :: "memory")
    if (w >= 4) {
        __builtin_amdgcn_s_setprio(3);
        const int u = w - 4;
        const bool tw = b < 150;
        const int tb = b / 5, tk = b - 5 * tb, ts = 4 * tk + u;
        if (tw) {
            const char* src = (const char*)p.wqkv + ts * 122880 + tb * 128;
            const u32 voff = (l >> 3) * 3840 + (l & 7) * 16;
#pragma unroll
            for (int i = 0; i < 4; ++i) glds16(p.wqkv, src + i * 8 * 3840, voff, L0 + L_WTRAW + u * 4096 + i * 1024);
        }
        ASK_TAG(epl);
        BAR();
        __builtin_amdgcn_s_setprio(0);
        TAKE_TAG(epl);
        if (tw) {
            const u32 a = L0 + L_WTRAW + u * 4096 + (8 * g + q4) * 128 + 8 * p4;
#pragma unroll
            for (int t = 0; t < 4; ++t) {
                const u32 cc = 8 * tb + 2 * t + (i16 >> 3);
                const u32 seg = cc >= 160 ? 2 : cc >= 80 ? 1 : 0, wi = cc - 80 * seg, jj = (wi * 13108) >> 16, c5 = wi - 5 * jj;
                const u32 c = seg == 0 ? 10 + c5 : seg == 1 ? c5 : 5 + c5, nt = c >> 1;
                const u32 dst = WS_WT + jj * 153600 + (nt < 5 ? ts * 5120 + nt * 1024 : 102400 + ts * 2560 + (nt - 5) * 1024)
                              + (nt < 7 ? (g * 16 + 8 * (c & 1) + (i16 & 7)) * 16 : g * 128 + (i16 & 7) * 16);
                ST16C(ldtr2(a + 32 * t, a + 32 * t + 512), dst);
            }
        }
        vmw<0>();
        {
            asm volatile("s_waitcnt lgkmcnt(0)" ::: "memory");
            u32 old = 0;
            if (l == 0) old = lds_add(L0 + L_CNT + 12, 1u);
            if (tw && __builtin_amdgcn_readfirstlane(old) == 3 && l == 0) st_agent(ws + WS_FWT + b * FS, tag);
        }
        if (b >= 80 && b < 240) {
            if (!tw) __builtin_amdgcn_s_sleep(32);
            mod_ask(); MOD_LANDED(); mod_finish();
        }
        if (u == 0) {
            const int wh = l >= 30 ? 0 : l / 5, kr = l >= 30 ? 0 : l - 5 * wh;
            const int bl = (1280 * (wh >> 1) + 80 * j + 79 * (wh & 1)) >> 7;
            poll_at(ws, WS_FWT + (bl * 5 + kr) * FS, l, tag, ws, 13);
            for (u32 spins = 0; lds_ld(L0 + L_CNT + 12) < 4u && spins < (1u << 22); ++spins) {}
            if (l == 0) lds_add(L0 + L_CNT + 24, 1u);
        } else {
            for (u32 spins = 0; lds_ld(L0 + L_CNT + 24) < 1u && spins < (1u << 22); ++spins) {}
        }
        asm volatile("" ::: "memory");
        {
            const char* src = ws + WS_WT + j * 153600 + u * 1024;
            const u32 lo = l * 16;
#pragma unroll
            for (int i = 0; i < 25; ++i) glds16c(src + i * 4096, lo, L0 + L_WKV + u * 1024 + i * 4096);
        }
        VMCNT(0);
    } else {
        __builtin_amdgcn_s_setprio(2);
        if (b < 80) mod_ask();
        ASK_TAG(epw);
        if (tid < 16) *(LAS u32*)(size_t)(L0 + L_CNT + tid * 4) = 0u;
        BAR();
        TAKE_TAG(epw);
        if (b < 80) { MOD_LANDED(); mod_finish(); }
        {
            const char* src = (const char*)p.x + (rb * 128 + 8 * j) * 1280 + w * 1024;
            glds16(p.x, src, l * 16, L0 + L_XS + w * 1024);
            glds16(p.x, src + 4096, l * 16, L0 + L_XS + (w + 4) * 1024);
            if (w < 2) glds16(p.x, src + 8192, l * 16, L0 + L_XS + (w + 8) * 1024);
        }
        VMCNT(0);
        worker_sync(L0 + L_CNT + 16, l);
        const int tok = l & 7;
        u32x4 xr[3];
#pragma unroll
        for (int i = 0; i < 3; ++i) xr[i] = lds128(L0 + L_XS + tok * 1280 + (20 * w + (i < 2 ? (l >> 3) + 8 * i : 16 + ((l >> 3) & 3))) * 16);
        {
            float s1 = 0.f, s2 = 0.f;
#pragma unroll
            for (int i = 0; i < 3; ++i) {
                float a1 = 0.f, a2 = 0.f;
#pragma unroll
                for (int d = 0; d < 4; ++d) { a1 = dot2(xr[i][d], 0x3F803F80u, a1); a2 = dot2(xr[i][d], xr[i][d], a2); }
                if (i < 2 || l < 32) { s1 += a1; s2 += a2; }
            }
            s1 += dppf<0x128>(s1); s2 += dppf<0x128>(s2);
            s1 = xsum4(s1); s2 = xsum4(s2);
            if (l < 8) stsf2(L0 + L_STP + (w * 8 + l) * 8, (f32x2){s1, s2});
        }
        gather_strip(ws, 40 * w, 160 + 40 * w, L0 + L_GS + w * 640, l, tag, ws, 1);
        worker_sync(L0 + L_CNT + 4, l);
        {
            f32x2 st = {0.f, 0.f};
#pragma unroll
            for (int ww = 0; ww < 4; ++ww) st += *(LAS f32x2*)(size_t)(L0 + L_STP + (ww * 8 + tok) * 8);
            const float mu = st[0] * (1.f / 640.f), var = fmaxf(st[1] * (1.f / 640.f) - mu * mu, 0.f);
            const float rs = __builtin_amdgcn_rsqf(var + 1e-5f), nmr = -mu * rs;
#pragma unroll
            for (int i = 0; i < 3; ++i) {
                const int cl = i < 2 ? (l >> 3) + 8 * i : 16 + ((l >> 3) & 3);
                const u32x4 sa = lds128(L0 + L_GS + w * 640 + cl * 16), gg = lds128(L0 + L_GS + w * 640 + 320 + cl * 16);
                u32x4 h;
#pragma unroll
                for (int d = 0; d < 4; ++d)
                    h[d] = pack2((bflo(xr[i][d]) * rs + nmr) * bflo(gg[d]) + bflo(sa[d]), (bfhi(xr[i][d]) * rs + nmr) * bfhi(gg[d]) + bfhi(sa[d]));
                if (i < 2 || l < 32) ST16C(h, WS_H + (((rb * 8 + (j >> 1)) * 80 + 20 * w + cl) * 16 + 8 * (j & 1) + tok) * 16);
            }
        }
        VMCNT(0);
        worker_sync(L0 + L_CNT + 8, l);
        if (tid == 0) st_agent(ws + WS_FH + (rb * 16 + j) * FS, tag);
        if (w == 0) poll_flags(ws + WS_FH + rb * 16 * FS, 16, l, tag, ws, 11);
        __builtin_amdgcn_s_setprio(0);
    }
    BAR();
    asm volatile("s_waitcnt vmcnt(0)" : "+v"(wu0) :: "memory");

    u32x4 xa[20];
    const u32 lo16 = l * 16;
    const char* hsrc = ws + WS_H + (rb * 8 + w) * 80 * 256;
#define XL(s) xa[s] = gld16c(((s) & 3) * 1024, hsrc + ((s) >> 2) * 4096, lo16)
    XL(0); XL(1); XL(2); XL(3); XL(4); XL(5);
    const char* kvx = ws + WS_KV + (rb * 16 + j) * 22528;
    const char* wtq = ws + WS_WT + j * 153600 + (100 + w) * 1024;
    {
        f32x4 acc[5];
#pragma unroll
        for (int n = 0; n < 5; ++n) acc[n] = Z4;
        const u32 b0 = L0 + L_WKV + lo16;
        u32x4 bf[3][5];
#pragma unroll
        for (int n = 0; n < 5; ++n) { bf[0][n] = lds128(b0 + n * 1024); bf[1][n] = lds128(b0 + 5120 + n * 1024); }
        static_for<0, 20>([&](auto S) {
            constexpr int s = S;
            vmw1<(s < 14 ? 5 : 6)>(xa[s]);
            if constexpr (s + 6 < 20) XL(s + 6);
            if constexpr (s >= 13) { if (s < 19 || w < 2) glds16c(wtq + (s - 13) * 8192, lo16, L0 + L_WKV + (100 + w) * 1024 + (s - 13) * 8192); }
            SB0();
#pragma unroll
            for (int n = 0; n < 5; ++n) {
                if (s < 18) bf[(s + 2) % 3][n] = lds128(b0 + (s + 2) * 5120 + n * 1024);
                acc[n] = mfma(bf[s % 3][n], xa[s], acc[n]);
            }
#pragma unroll
            for (int n = 0; n < 5; ++n) { SGB(M_DSR, 1); SGB(M_MFMA, 1); }
            SB0();
        });
        BAR();
        const int row = 256 + 16 * w + i16;
#pragma unroll
        for (int n = 0; n < 5; ++n) {
            const int Q = 4 * n + g;
            const u32x2 v = {pack2(acc[n][0], acc[n][1]), pack2(acc[n][2], acc[n][3])};
            const int wi = Q - 10, hd = wi >= 5 ? 1 : 0;
            const u32 a = Q < 10 ? L_K + row * 80 + 8 * Q : L_V + row * 96 + 48 * hd + 8 * (wi - 5 * hd);
            sts64(L0 + a, v);
        }
        if (g < 2) sts64(L0 + L_V + row * 96 + 48 * g + 40, (u32x2){0x3F80u, 0u});
        if (rb < 2) {
            const int nrow = rb == 0 ? 256 : 128;
            for (int i = tid; i < nrow * 5; i += 512) sts128(L0 + L_K + i * 16, (u32x4){0, 0, 0, 0});
            for (int i = tid; i < nrow * 6; i += 512) sts128(L0 + L_V + i * 16, (u32x4){0, 0, 0, 0});
        }
        vmw<0>();
        BAR();
    }
#undef XL
    {
        u32x4 kvd[3];
#pragma unroll
        for (int e = 0; e < 3; ++e) {
            const int n = min(w + 8 * e, 21);
            kvd[e] = lds128(L0 + (n < 10 ? L_K + 20480 + n * 1024 : L_V + 24576 + (n - 10) * 1024) + lo16);
        }
        const u32 kvo = WS_KV + (rb * 16 + j) * 22528 + w * 1024 + lo16;
        const u16* wroot = wpm ? p.wu : p.wg;
        const char* wsrc = (const char*)wroot + (160 * wpq + 8 * w) * 3456 + wpb * 128;
        const u32 wvo = (l >> 3) * 3456 + (l & 7) * 16;
        f32x4 acc[3];
#pragma unroll
        for (int n = 0; n < 3; ++n) acc[n] = Z4;
        const u32 bq = L0 + L_WQ + lo16, bq2 = L0 + L_WQ + 2048 + g * 128 + (i16 & 7) * 16;
#define QRD(s, n) lds128(((n) < 2 ? bq + (n) * 1024 : bq2) + (s) * 2560)
        u32x4 bf[3][3];
#pragma unroll
        for (int n = 0; n < 3; ++n) { bf[0][n] = QRD(0, n); bf[1][n] = QRD(1, n); }
        Gather G;
        const u32 gg0 = l < 40 ? 480 + 40 * wpq + l : 640 + 40 * wpq + (l - 40), gg1 = 640 + 40 * wpq + 24 + (l & 15);
        static_for<0, 20>([&](auto S) {
            constexpr int s = S;
            if constexpr (s == 6) { if (w == 7 && wpwg) gather_ask(ws, gg0, gg1, G); }
            if constexpr (s == 0) { if (rb < 15) ST16C(kvd[0], kvo); }
            if constexpr (s == 1) { if (rb < 15) ST16C(kvd[1], kvo + 8192); }
            if constexpr (s == 2) { if (rb < 15 && w < 6) ST16C(kvd[2], kvo + 16384); }
            if constexpr (s == 14) {
                vmw<0>();
                u32 old = 0;
                if (l == 0) old = lds_add(L0 + L_CNT + 28, 1u);
                if (__builtin_amdgcn_readfirstlane(old) == 7 && l == 0) st_agent(ws + WS_FKV + (j * 16 + rb) * FS, tag);
                if (w == 7 && wpwg) { f32x4 ra, rc; gather_finish(ws, gg0, gg1, G, tag, ws, 2, ra, rc); put_strip_spread(ra, rc, L0 + L_SB, l); }
            }
            if constexpr (s == 15) { if (wpwg) glds16(wroot, wsrc, wvo, L0 + L_WPRAW + w * 1024); }
            if constexpr (s == 16) { if (wpwg) glds16(wroot, wsrc + 64 * 3456, wvo, L0 + L_WPRAW + (w + 8) * 1024); }
            if constexpr (s == 17) { if (wpwg && w < 4) glds16(wroot, wsrc + 128 * 3456, wvo, L0 + L_WPRAW + (w + 16) * 1024); }
            SB0();
#pragma unroll
            for (int n = 0; n < 3; ++n) {
                if (s < 18) bf[(s + 2) % 3][n] = QRD(s + 2, n);
                acc[n] = mfma(bf[s % 3][n], xa[s], acc[n]);
            }
#pragma unroll
            for (int n = 0; n < 3; ++n) { SGB(M_DSR, 1); SGB(M_MFMA, 1); }
            SB0();
        });
#undef QRD
#pragma unroll
        for (int n = 0; n < 3; ++n) {
            const int Q = 4 * n + g;
            const u32x2 v = {pack2(acc[n][0] * QSCALE, acc[n][1] * QSCALE), pack2(acc[n][2] * QSCALE, acc[n][3] * QSCALE)};
            if (Q < 10) sts64(L0 + L_Q + (16 * w + i16) * 80 + 8 * Q, v);
        }
    }
    if (w == 0 && rb >= 1) poll_flags(ws + WS_FKV + (j * 16 + (rb >= 2 ? rb - 2 : rb - 1)) * FS, rb >= 2 ? 2 : 1, l, tag, ws, 3);
    VMCNT(0);
    BAR();
    if (rb >= 1) {
#pragma unroll
        for (int e = 0; e < 3; ++e) {
            const int n = w + 8 * e;
            if (n < 22) {
                const u32 d = n < 10 ? L_K + n * 1024 : L_V + (n - 10) * 1024, st = n < 10 ? 10240 : 12288;
                glds16c(kvx - 16 * 22528 + n * 1024, lo16, L0 + d + st);
                if (rb >= 2) glds16c(kvx - 32 * 22528 + n * 1024, lo16, L0 + d);
            }
        }
    }
    auto wp_slot = [&](int gam, int& jj, int& slot) {
        jj = gam < 84 ? gam / 7 : 12 + (gam - 84) / 6;
        const int ar = gam - (jj < 12 ? 7 * jj : 84 + 6 * (jj - 12)), a = (jj < 12 || ar < 3) ? ar : ar + 1;
        slot = a < 3 ? 2 * a + wpm : a == 3 ? (wpm ? 13 : 6) : 7 + 2 * (a - 4) + wpm;
    };
    if (wpwg) {
        const u32 one = i16 == 0 ? 0x3F803F80u : 0u;
        const u32x4 ones = {one, one, one, one};
#pragma unroll
        for (int i = 0; i < 3; ++i) {
            if (i < 2 || w < 4) {
                const int f = w + 8 * i, ks = f >> 2, t16 = f & 3;
                int jj, slot; wp_slot(4 * wpb + t16, jj, slot);
                const u32 a = L0 + L_WPRAW + (32 * ks + 8 * g + q4) * 128 + 32 * t16 + 8 * p4;
                u32x4 wr = ldtr2(a, a + 512);
                const u32x4 ga = lds128(L0 + L_G2 + (32 * ks + 8 * g) * 4), gc = lds128(L0 + L_G2 + (32 * ks + 8 * g) * 4 + 16);
                const u32x4 sbz = lds128(L0 + (i16 == 0 ? L_SB + (32 * ks + 8 * g) * 2 : L_Z16));
                const f32x4 vs = mfma(wr, sbz, Z4);
                u32x4 o; float t0, t1, t2, t3, t4, t5, t6, t7;
                asm volatile("v_dot2_f32_bf16 %4, %12, %16, 0\n\tv_dot2_f32_bf16 %5, %12, %17, 0\n\tv_dot2_f32_bf16 %6, %13, %18, 0\n\tv_dot2_f32_bf16 %7, %13, %19, 0\n\t"
                             "v_dot2_f32_bf16 %8, %14, %20, 0\n\tv_dot2_f32_bf16 %9, %14, %21, 0\n\tv_dot2_f32_bf16 %10, %15, %22, 0\n\tv_dot2_f32_bf16 %11, %15, %23, 0\n\t"
                             "v_cvt_pk_bf16_f32 %0, %4, %5\n\tv_cvt_pk_bf16_f32 %1, %6, %7\n\tv_cvt_pk_bf16_f32 %2, %8, %9\n\ts_nop 0\n\tv_cvt_pk_bf16_f32 %3, %10, %11\n\ts_nop 1"
                             : "=&v"(o[0]), "=&v"(o[1]), "=&v"(o[2]), "=&v"(o[3]),
                               "=&v"(t0), "=&v"(t1), "=&v"(t2), "=&v"(t3), "=&v"(t4), "=&v"(t5), "=&v"(t6), "=&v"(t7)
                             : "v"(wr[0]), "v"(wr[1]), "v"(wr[2]), "v"(wr[3]), "v"(ga[0]), "v"(ga[1]), "v"(ga[2]), "v"(ga[3]), "v"(gc[0]), "v"(gc[1]), "v"(gc[2]), "v"(gc[3]));
                const f32x4 us = mfma(o, ones, Z4);
                ST16C(o, WS_WP + ((jj * 20 + 5 * wpq + ks) * 14 + slot) * 1024 + lo16);
                if (i16 == 0) { stsf4(L0 + L_UVL + f * 128 + g * 16, us); stsf4(L0 + L_UVL + f * 128 + 64 + g * 16, vs); }
            }
        }
    }
    if (wpwg) { if (w < 4) vmw<3>(); else vmw<2>(); } else vmw<0>();
    BAR();
    if (wpwg && w == 3) {
#pragma unroll
        for (int e = 0; e < 2; ++e) {
            const int t16 = 2 * e + (l >> 5);
            int jj, slot; wp_slot(4 * wpb + t16, jj, slot);
            float v = 0.f;
#pragma unroll
            for (int ks = 0; ks < 5; ++ks) v += ldsf(L0 + L_UVL + (4 * ks + t16) * 128 + (l & 31) * 4);
            st_agent32(ws + WS_UV + ((jj * 14 + slot) * 4 + wpq) * 128 + (l & 31) * 4, __float_as_uint(v));
        }
    }
    const int hf = j & 1, cb = j >> 1;
    u32 wvoff[5];
#pragma unroll
    for (int i = 0; i < 5; ++i) {
        const u32 sg = 64 * i + l, pos = (sg * 6554) >> 16, c = sg - pos * 10, k = pos ^ (((pos >> 3) & 1) << 2);
        wvoff[i] = k * 1280 + c * 16;
    }
    const char* wosrc = (const char*)p.wo + cb * 160;
#define WO_STEP(sx) do { _Pragma("unroll") for (int i = 0; i < 5; ++i) glds16(p.wo, wosrc + (sx) * 32 * 1280, wvoff[i], L0 + L_WO + (sx) * 5120 + i * 1024); } while (0)

    {
        const int hd = w & 1, qi = w >> 1;
        const int kr0 = 32 * qi;
        u32x4 qf[2];
#pragma unroll
        for (int a = 0; a < 2; ++a) {
            const u32 qa = L0 + L_Q + (kr0 + 16 * a + i16) * 80 + hd * 40;
            const u32x2 q0 = lds64(qa + 8 * g);
            u32x2 q1 = lds64(qa + 32);
            if (g != 0) q1 = (u32x2){0, 0};
            qf[a] = (u32x4){q0[0], q0[1], q1[0], q1[1]};
        }
        const u32 kb0 = L0 + L_K + (kr0 + i16) * 80 + hd * 40 + 8 * g, kb1 = L0 + L_K + (kr0 + i16) * 80 + hd * 40 + 32;
        f32x4 mfirst, mlast;
#pragma unroll
        for (int r = 0; r < 4; ++r) { const bool lo = 4 * g + r <= i16; mfirst[r] = lo ? -INFINITY : 0.f; mlast[r] = lo ? 0.f : -INFINITY; }
        f32x4 s0[17], s1[17];
        {
            u32x4 kf[6];
#define KRD(t) do { const u32x2 k0 = lds64(kb0 + (t) * 1280), k1 = lds64(kb1 + (t) * 1280); kf[(t) % 6] = (u32x4){k0[0], k0[1], k1[0], k1[1]}; } while (0)
            KRD(0); KRD(1); KRD(2); KRD(3); KRD(4);
            static_for<0, 18>([&](auto T) {
                constexpr int t = T;
                SB0();
                if constexpr (t < 13) KRD(t + 5);
                if constexpr (t < 17) s0[t] = mfma(kf[t % 6], qf[0], t == 0 ? mfirst : t == 16 ? mlast : Z4);
                if constexpr (t > 0) s1[t - 1] = mfma(kf[t % 6], qf[1], t == 1 ? mfirst : t == 17 ? mlast : Z4);
                SGB(M_DSR, 2); SGB(M_MFMA, 2);
                SB0();
            });
#undef KRD
        }
        WO_STEP(4 + w);
        if (rb < 2) {
            const int fv = rb == 1 ? 128 : 256;
#pragma unroll
            for (int t = 0; t < 16; ++t) {
                const bool d0 = kr0 + 16 * t < fv, d1 = kr0 + 16 * t + 16 < fv;
#pragma unroll
                for (int r = 0; r < 4; ++r) { s0[t][r] = d0 ? -INFINITY : s0[t][r]; s1[t][r] = d1 ? -INFINITY : s1[t][r]; }
            }
        }
        float m0 = fmaxf(s0[0][0], s0[0][1]), m1 = fmaxf(s1[0][0], s1[0][1]), n0 = fmaxf(s0[0][2], s0[0][3]), n1 = fmaxf(s1[0][2], s1[0][3]);
#pragma unroll
        for (int t = 1; t < 17; ++t) {
            m0 = fmaxf(fmaxf(m0, s0[t][0]), s0[t][1]); m1 = fmaxf(fmaxf(m1, s1[t][0]), s1[t][1]);
            n0 = fmaxf(fmaxf(n0, s0[t][2]), s0[t][3]); n1 = fmaxf(fmaxf(n1, s1[t][2]), s1[t][3]);
        }
        m0 = fmaxf(m0, n0); m1 = fmaxf(m1, n1);
        m0 = xmax4(m0); m1 = xmax4(m1);
        WO_STEP(12 + w);
        f32x4 o00 = Z4, o01 = Z4, o10 = Z4, o11 = Z4;
        const u32 vb = L0 + L_V + (kr0 + 4 * g + q4) * 96 + hd * 48 + 8 * p4;
        auto pv = [&](auto SUB) {
            constexpr bool sub = SUB;
            u32x4 vf[2][2];
            vf[0][0] = ldtr2(vb, vb + 16 * 96); vf[0][1] = ldtr2(vb + 32, vb + 16 * 96 + 32);
#define EX(v, m) __builtin_amdgcn_exp2f(sub ? (v) - (m) : (v))
            static_for<0, 9>([&](auto U) {
                constexpr int u = U;
                if constexpr (u < 8) { const u32 a = vb + (u + 1) * 32 * 96; vf[(u + 1) & 1][0] = ldtr2(a, a + 16 * 96); vf[(u + 1) & 1][1] = ldtr2(a + 32, a + 16 * 96 + 32); }
                u32x4 pa, pb;
                pa[0] = pack2(EX(s0[2 * u][0], m0), EX(s0[2 * u][1], m0)); pa[1] = pack2(EX(s0[2 * u][2], m0), EX(s0[2 * u][3], m0));
                if constexpr (u < 8) { pa[2] = pack2(EX(s0[2 * u + 1][0], m0), EX(s0[2 * u + 1][1], m0)); pa[3] = pack2(EX(s0[2 * u + 1][2], m0), EX(s0[2 * u + 1][3], m0)); }
                else { pa[2] = 0; pa[3] = 0; }
                if constexpr (u > 0) { pb[0] = pack2(EX(s1[2 * u - 1][0], m1), EX(s1[2 * u - 1][1], m1)); pb[1] = pack2(EX(s1[2 * u - 1][2], m1), EX(s1[2 * u - 1][3], m1)); }
                else { pb[0] = 0; pb[1] = 0; }
                pb[2] = pack2(EX(s1[2 * u][0], m1), EX(s1[2 * u][1], m1)); pb[3] = pack2(EX(s1[2 * u][2], m1), EX(s1[2 * u][3], m1));
                o00 = mfma(vf[u & 1][0], pa, o00); o01 = mfma(vf[u & 1][1], pa, o01);
                o10 = mfma(vf[u & 1][0], pb, o10); o11 = mfma(vf[u & 1][1], pb, o11);
                SB0();
            });
#undef EX
        };
        if (__all(fabsf(m0) <= 64.f && fabsf(m1) <= 64.f)) pv(std::false_type{}); else pv(std::true_type{});
        const float i0 = __builtin_amdgcn_rcpf(__shfl(o01[0], i16 + 16)), i1 = __builtin_amdgcn_rcpf(__shfl(o11[0], i16 + 16));
        const u32 oa = L0 + L_Q + (kr0 + i16) * 80 + hd * 40;
        sts64(oa + 8 * g, (u32x2){pack2(o00[0] * i0, o00[1] * i0), pack2(o00[2] * i0, o00[3] * i0)});
        sts64(oa + 1280 + 8 * g, (u32x2){pack2(o10[0] * i1, o10[1] * i1), pack2(o10[2] * i1, o10[3] * i1)});
        if (g == 0) {
            sts64(oa + 32, (u32x2){pack2(o01[0] * i0, o01[1] * i0), pack2(o01[2] * i0, o01[3] * i0)});
            sts64(oa + 1280 + 32, (u32x2){pack2(o11[0] * i1, o11[1] * i1), pack2(o11[2] * i1, o11[3] * i1)});
        }
    }
    BAR();
    {
        const int tok = 64 * (w & 1) + l;
        const u32 oo = WS_O + (((rb * 8 + (tok >> 4)) * 80 + 5 * j) * 16 + (tok & 15)) * 16;
        ST16C(lds128(L0 + L_Q + tok * 80 + (w >> 1) * 16), oo + (w >> 1) * 256);
        if (w < 2) ST16C(lds128(L0 + L_Q + tok * 80 + 64), oo + 4 * 256);
    }
    VMCNT(0);
    BAR();
    if (tid == 0) {
        st_agent(ws + WS_FO + (rb * 16 + j) * FS, tag);
        if (wpwg) st_agent(ws + WS_FWP + b * FS, tag);
    }

    const int wm = w & 3, wk = w >> 2;
    const int mt3 = rb * 8 + 4 * hf + wm;
    u32x2 x1k[5];
    {
        const int wdu = b + 256 * (w - 6);
        const bool wdw = w >= 6 && wdu < 432;
        if (w >= 1 && w < 5) {
#pragma unroll
            for (int i = 0; i < 5; ++i) glds16(p.wo, wosrc + (w - 1) * 32 * 1280, wvoff[i], L0 + L_WO + (w - 1) * 5120 + i * 1024);
        } else if (w >= 5) {
#pragma unroll
            for (int e = 0; e < 4; ++e) {
                const int pc = w - 5 + 3 * e;
                if (pc < 10) {
                    const u32 sg = 64 * pc + l, r = (sg * 6554) >> 16, c = sg - r * 10;
                    glds16(p.x, (const char*)p.x + (rb * 128 + 64 * hf) * 1280 + cb * 160, r * 1280 + c * 16, L0 + L_XR + pc * 1024);
                }
            }
            if (wdw) {
                const int ucb = wdu / 54, us = wdu - 54 * ucb;
#pragma unroll
                for (int i = 0; i < 5; ++i) glds16(p.wd, (const char*)p.wd + ucb * 160 + us * 32 * 1280, wvoff[i], L0 + L_WDRAW + (w - 6) * 5120 + i * 1024);
            }
        }
        if (w == 1 && l < 20) stsf4(L0 + L_GA + l * 16, gather1(ws, 320 + 20 * cb + l, tag, ws, 4));
        if (w == 0) {
            const int bl = min((Gj >> 2) + ((l >> 2) % 3), (Gj + nj - 1) >> 2);
            poll_at(ws, WS_FWP + (108 * (l >= 12 && l < 24 ? 1 : 0) + 4 * bl + (l & 3)) * FS, l, tag, ws, 6);
            poll_flags(ws + WS_FO + rb * 16 * FS, 16, l, tag, ws, 5);
        }
        if (wdw) vmw<5>(); else vmw<0>();
        BAR();
        u32x4 a[10];
        const char* asrc = ws + WS_O + (mt3 * 80 + 40 * wk) * 256;
#define AL(s) a[s] = gld16c(((s) & 3) * 1024, asrc + ((s) >> 2) * 4096, lo16)
        AL(0); AL(1); AL(2); AL(3);
        f32x4 acc[5];
#pragma unroll
        for (int n = 0; n < 5; ++n) acc[n] = Z4;
        const u32 wb = L0 + L_WO + wk * 51200;
        const u32 b0 = wb + 160 * (8 * g + 4 * (g & 1) + q4) + 8 * p4, b1 = wb + 160 * (8 * g + 4 * ((g & 1) ^ 1) + q4) + 8 * p4;
        u32x4 bf[3][5];
#pragma unroll
        for (int n = 0; n < 5; ++n) { bf[0][n] = ldtr2(b0 + n * 32, b1 + n * 32); bf[1][n] = ldtr2(b0 + 5120 + n * 32, b1 + 5120 + n * 32); }
        static_for<0, 10>([&](auto S) {
            constexpr int s = S;
            vmw1<(s < 7 ? 3 : 9 - s)>(a[s]);
            if constexpr (s + 4 < 10) AL(s + 4);
            SB0();
#pragma unroll
            for (int n = 0; n < 5; ++n) {
                if (s < 8) bf[(s + 2) % 3][n] = ldtr2(b0 + (s + 2) * 5120 + n * 32, b1 + (s + 2) * 5120 + n * 32);
                acc[n] = mfma(bf[s % 3][n], a[s], acc[n]);
            }
#pragma unroll
            for (int n = 0; n < 5; ++n) { SGB(M_DSR, 2); SGB(M_MFMA, 1); }
            SB0();
        });
#undef AL
        if (wk == 1) {
#pragma unroll
            for (int n = 0; n < 5; ++n) stsf4(L0 + L_RED3 + (wm * 5 + n) * 1024 + lo16, acc[n]);
        }
        BAR();
        if (wk == 0) {
            float s1 = 0.f, s2 = 0.f;
#pragma unroll
            for (int n = 0; n < 5; ++n) {
                const f32x4 v = acc[n] + ldsf4(L0 + L_RED3 + (wm * 5 + n) * 1024 + lo16);
                const f32x4 ga = ldsf4(L0 + L_GA + (16 * n + 4 * g) * 4);
                const u32x2 xr = lds64(L0 + L_XR + (16 * wm + i16) * 160 + 32 * n + 8 * g);
                const u32x2 o = {pack2(bflo(xr[0]) + ga[0] * v[0], bfhi(xr[0]) + ga[1] * v[1]), pack2(bflo(xr[1]) + ga[2] * v[2], bfhi(xr[1]) + ga[3] * v[3])};
                x1k[n] = o;
                s1 = dot2(o[0], 0x3F803F80u, s1); s1 = dot2(o[1], 0x3F803F80u, s1);
                s2 = dot2(o[0], o[0], s2); s2 = dot2(o[1], o[1], s2);
            }
            const u32 xo = WS_X1 + ((mt3 * 80 + 10 * cb) * 16 + i16) * 16;
#pragma unroll
            for (int n = 0; n < 4; n += 2) {
                const auto a0 = __builtin_amdgcn_permlane16_swap(x1k[n][0], x1k[n + 1][0], false, false);
                const auto a1 = __builtin_amdgcn_permlane16_swap(x1k[n][1], x1k[n + 1][1], false, false);
                ST16C(((u32x4){a0[0], a1[0], a0[1], a1[1]}), xo + (2 * (n + (g & 1)) + (g >> 1)) * 256);
            }
            ST8C(x1k[4], xo + (8 + (g >> 1)) * 256 + 8 * (g & 1));
            s1 = xsum4(s1); s2 = xsum4(s2);
            if (l < 16) ST8C(((u32x2){__float_as_uint(s1), __float_as_uint(s2)}), WS_STAT + ((rb * 8 + cb) * 128 + 64 * hf + 16 * wm + l) * 8);
            vmw<0>();
        } else {
#pragma unroll
            for (int n = 0; n < 5; ++n) x1k[n] = (u32x2){0, 0};
            if (wdw) {
                const u32 wr = L0 + L_WDRAW + (w - 6) * 5120;
                const u32 r0 = wr + 160 * (8 * g + 4 * (g & 1) + q4) + 8 * p4, r1 = wr + 160 * (8 * g + 4 * ((g & 1) ^ 1) + q4) + 8 * p4;
#pragma unroll
                for (int n = 0; n < 5; ++n) ST16C(ldtr2(r0 + n * 32, r1 + n * 32), WS_WD + (wdu * 5 + n) * 1024 + lo16);
            }
        }
        BAR();
        if (tid == 0) st_agent(ws + WS_FX1 + (rb * 16 + j) * FS, tag);
    }

    const char* wdsrc = ws + WS_WD + (cb * 54 + wk * 27) * 5120;
    const u32 r5w = L0 + wk * 20480 + wm * 5120;
#define P5_B1(t, i) do { if ((i) == 0 || (i) == 4) glds16c(wdsrc + min(4 * (t) + wm, 26) * 5120 + (i) * 1024, lo16, r5w + r5_slot(t) + (i) * 1024); \
        else glds16c_next(((i) & 3) * 1024, wdsrc + min(4 * (t) + wm, 26) * 5120, lo16); } while (0)
    {
        const int wn = w >> 2;
        const int bst = w < 4 ? 4 * w : 16 + 3 * (w - 4);
        const char* wpj = ws + WS_WP + (j * 20 * 14 + bst) * 1024;
        const char* x1b = ws + WS_X1 + (rb * 8 + w) * 80 * 256;
        const u32 r4a = L0 + L_R4 + w * 2048, r4b = L0 + L_R4 + 16384 + bst * 1024;
#define P4_B1(t, i) do { if ((i) == 0) glds16c(wpj + (t) * 28 * 1024, lo16, r4b + ((t) % 3) * R4_STAGE); else glds16c_next((i) * 1024, wpj + (t) * 28 * 1024, lo16); } while (0)
#define P4_A1(t, i) do { if ((i) == 0) glds16c(x1b + 8 * (t) * 256, lo16, r4a + ((t) % 3) * R4_STAGE); else glds16c_next(1024, x1b + 8 * (t) * 256, lo16); } while (0)
#define P4_B(t) do { P4_B1(t, 0); P4_B1(t, 1); P4_B1(t, 2); if (w < 4) P4_B1(t, 3); } while (0)
        P4_B(0); P4_B(1); P4_B(2);
        if (w == 0) poll_flags(ws + WS_FX1 + rb * 16 * FS, 16, l, tag, ws, 7);
        BAR();
        glds16c(ws + WS_STAT + rb * 8192 + w * 1024, lo16, L0 + L_ST4 + w * 1024);
        if (w < 7) glds16c(ws + WS_UV + j * 7168 + w * 1024, lo16, L0 + L_UV + w * 1024);
        P4_A1(0, 0); P4_A1(0, 1); P4_A1(1, 0); P4_A1(1, 1); P4_A1(2, 0); P4_A1(2, 1);
        f32x4 acc[7][2];
#pragma unroll
        for (int n = 0; n < 7; ++n) { acc[n][0] = Z4; acc[n][1] = Z4; }
        const u32 ra = opaque(L0 + L_R4 + 2 * wm * 2048 + lo16), rbb = opaque(L0 + L_R4 + 16384 + 7 * wn * 1024 + lo16);
        u32x4 fa[2][2], fb[2][7];
#define P4_RA(buf, t, ks) do { const u32 so = ((t) % 3) * R4_STAGE + (ks) * 1024; fa[buf][0] = lds128(ra + so); fa[buf][1] = lds128(ra + so + 2048); } while (0)
#define P4_RB(buf, t, ks, n) fb[buf][n] = lds128(rbb + ((t) % 3) * R4_STAGE + (ks) * 14336 + (n) * 1024)
#define P4_MM(buf, n) do { acc[n][0] = mfma(fb[buf][n], fa[buf][0], acc[n][0]); acc[n][1] = mfma(fb[buf][n], fa[buf][1], acc[n][1]); } while (0)
#define P4_HALF(buf, rd, t, ks, V0, V1, V2) do { \
            SB0(); if (rd) { P4_RA((buf) ^ 1, t, ks); P4_RB((buf) ^ 1, t, ks, 0); } P4_MM(buf, 0); SB0(); V0; \
            SB0(); if (rd) P4_RB((buf) ^ 1, t, ks, 1); P4_MM(buf, 1); SB0(); \
            SB0(); if (rd) P4_RB((buf) ^ 1, t, ks, 2); P4_MM(buf, 2); SB0(); V1; \
            SB0(); if (rd) P4_RB((buf) ^ 1, t, ks, 3); P4_MM(buf, 3); SB0(); \
            SB0(); if (rd) P4_RB((buf) ^ 1, t, ks, 4); P4_MM(buf, 4); SB0(); V2; \
            SB0(); if (rd) P4_RB((buf) ^ 1, t, ks, 5); P4_MM(buf, 5); SB0(); \
            SB0(); if (rd) P4_RB((buf) ^ 1, t, ks, 6); P4_MM(buf, 6); SB0(); } while (0)
        VMCNT(4);
        BAR();
        if (tid == 0) st_agent(ws + WS_FW2 + b * FS, tag);
        u32x2 wdf = {0u, 0u};
        const u32 wdo = WS_FW2 + ((54 * cb + (l < 54 ? l : 0)) & 255) * FS;
        P4_RA(0, 0, 0);
#pragma unroll
        for (int n = 0; n < 7; ++n) P4_RB(0, 0, 0, n);
        static_for<0, 10>([&](auto T) {
            constexpr int t = T;
            constexpr bool f = t >= 1 && t + 2 <= 9, sN = t + 3 <= 9;
            if constexpr (t < 9) P4_HALF(0, true, t, 1, if (f && w < 4) P4_B1(t + 2, 3), if (f) P4_A1(t + 2, 0), if (f) P4_A1(t + 2, 1));
            else P4_HALF(0, true, t, 1, P5_B1(0, 3), P5_B1(0, 4), P5_B1(1, 0));
            if constexpr (t == 5) {
                if (tid < 128) {
                    f32x2 st = {0.f, 0.f};
#pragma unroll
                    for (int c = 0; c < 8; ++c) st += *(LAS f32x2*)(size_t)(L0 + L_ST4 + (c * 128 + tid) * 8);
                    const float mu = st[0] * (1.f / 640.f), var = fmaxf(st[1] * (1.f / 640.f) - mu * mu, 0.f), r = __builtin_amdgcn_rsqf(var + 1e-5f);
                    stsf2(L0 + L_MR + tid * 8, (f32x2){r, -mu * r});
                } else {
                    const int e = tid - 128;
                    stsf(L0 + L_UVS + e * 4, (ldsf(L0 + L_UV + (e >> 5) * 512 + (e & 31) * 4) + ldsf(L0 + L_UV + (e >> 5) * 512 + 128 + (e & 31) * 4))
                                           + (ldsf(L0 + L_UV + (e >> 5) * 512 + 256 + (e & 31) * 4) + ldsf(L0 + L_UV + (e >> 5) * 512 + 384 + (e & 31) * 4)));
                    if (e < 64) { const int e2 = e + 384;
                        stsf(L0 + L_UVS + e2 * 4, (ldsf(L0 + L_UV + (e2 >> 5) * 512 + (e2 & 31) * 4) + ldsf(L0 + L_UV + (e2 >> 5) * 512 + 128 + (e2 & 31) * 4))
                                                + (ldsf(L0 + L_UV + (e2 >> 5) * 512 + 256 + (e2 & 31) * 4) + ldsf(L0 + L_UV + (e2 >> 5) * 512 + 384 + (e2 & 31) * 4))); }
                }
            }
            if constexpr (t < 9) {
                if constexpr (t == 0) vmw<2>();
                else if constexpr (t == 8) {
                    asm volatile("s_waitcnt vmcnt(0)" : "+v"(wdf) :: "memory");
                    if (w == 7) { if (!__all(wdf[0] == (u32)tag && wdf[1] == (u32)(tag >> 32))) poll_at(ws, wdo, l, tag, ws, 12); }
                }
                else { if (w < 4) vmw<6>(); else vmw<5>(); }
                BAR();
                if constexpr (t == 7) { if (w == 7) asm volatile("s_nop 4\n\tglobal_load_dwordx2 %0, %1, %2 sc1" : "=v"(wdf) : "v"(wdo), "s"(ws) : "memory"); }
                if constexpr (t < 8) P4_HALF(1, true, t + 1, 0, if (sN) P4_B1(t + 3, 0), if (sN) P4_B1(t + 3, 1), if (sN) P4_B1(t + 3, 2));
                else P4_HALF(1, true, t + 1, 0, P5_B1(0, 0), P5_B1(0, 1), P5_B1(0, 2));
            } else {
                P4_HALF(1, false, 0, 0, P5_B1(1, 1), P5_B1(1, 2), P5_B1(1, 3));
                P5_B1(1, 4);
            }
        });
#undef P4_B
#undef P4_B1
#undef P4_A1
#undef P4_RA
#undef P4_RB
#undef P4_MM
#undef P4_HALF
        f32x2 mr[2];
#pragma unroll
        for (int m = 0; m < 2; ++m) mr[m] = *(LAS f32x2*)(size_t)(L0 + L_MR + (32 * wm + 16 * m + i16) * 8);
#pragma unroll
        for (int n = 0; n < 7; ++n) {
            const f32x4 u = ldsf4(L0 + L_UVS + (7 * wn + n) * 128 + 16 * g), v = ldsf4(L0 + L_UVS + (7 * wn + n) * 128 + 64 + 16 * g);
#pragma unroll
            for (int m = 0; m < 2; ++m) acc[n][m] = acc[n][m] * mr[m][0] + (u * mr[m][1] + v);
        }
        if (nj == 7) stsf4(L0 + L_XCH + w * 1024 + lo16, wn == 0 ? acc[6][1] : acc[6][0]);
        auto silu_mul = [&](const f32x4 gt, const f32x4 up) -> u32x2 {
            const f32x4 e = gt * (-LOG2E), gu = gt * up;
            f32x4 d;
#pragma unroll
            for (int r = 0; r < 4; ++r) d[r] = __builtin_amdgcn_exp2f(e[r]);
            d = d + 1.f;
#pragma unroll
            for (int r = 0; r < 4; ++r) d[r] = __builtin_amdgcn_rcpf(d[r]);
            const f32x4 o = gu * d;
            return (u32x2){pack2(o[0], o[1]), pack2(o[2], o[3])};
        };
#pragma unroll
        for (int a = 0; a < 3; ++a) {
            const int la = a + 4 * wn, gam = Gj + (la <= 3 || nj == 7 ? la : la - 1);
            const u32x2 o0 = silu_mul(acc[2 * a][0], acc[2 * a + 1][0]), o1 = silu_mul(acc[2 * a][1], acc[2 * a + 1][1]);
            const auto s0 = __builtin_amdgcn_permlane16_swap(o0[0], o1[0], false, false);
            const auto s1 = __builtin_amdgcn_permlane16_swap(o0[1], o1[1], false, false);
            const u32x4 v = {s0[0], s1[0], s0[1], s1[1]};
            ST16C(v, WS_ACT + (((rb * 8 + 2 * wm + (g & 1)) * 216 + 2 * gam + (g >> 1)) * 16 + i16) * 16);
        }
        BAR();
        if (nj == 7) {
            const f32x4 other = ldsf4(L0 + L_XCH + (w ^ 4) * 1024 + lo16);
            const u32x2 o = wn == 0 ? silu_mul(acc[6][0], other) : silu_mul(other, acc[6][1]);
            ST8C(o, WS_ACT + (((rb * 8 + 2 * wm + wn) * 216 + 2 * (Gj + 3) + (g >> 1)) * 16 + i16) * 16 + 8 * (g & 1));
        }
        P5_B1(2, 0); P5_B1(2, 1); P5_B1(2, 2); P5_B1(2, 3); P5_B1(2, 4);
        vmw<5>();
        BAR();
        if (tid == 0) st_agent(ws + WS_FACT + (rb * 16 + j) * FS, tag);
    }

    {
        if (w == 1 && l < 20) stsf4(L0 + L_GB + l * 16, gather1(ws, 800 + 20 * cb + l, tag, ws, 8));
        if (w == 0) poll_flags(ws + WS_FACT + rb * 16 * FS, 16, l, tag, ws, 9);
        const bool bump = rb == 15 && j == 0;
        if (bump && w >= 4) poll_at(ws, WS_FO + (tid - 256) * FS, l, tag, ws, 10);
        VMCNT(0);
        BAR();
        if (bump && tid == 0) st_agent(ws + WS_EPOCH, tag);
        const char* asrc = ws + WS_ACT + (mt3 * 216 + wk * 108) * 256;
        u32x4 a[3][4];
#define P5_A1(t, i) a[(t) % 3][i] = gld16c((i) * 1024, asrc + (t) * 4096, lo16)
        P5_A1(0, 0); P5_A1(0, 1); P5_A1(0, 2); P5_A1(0, 3); P5_A1(1, 0); P5_A1(1, 1); P5_A1(1, 2); P5_A1(1, 3); P5_A1(2, 0); P5_A1(2, 1); P5_A1(2, 2); P5_A1(2, 3);
        f32x4 acc[5];
#pragma unroll
        for (int n = 0; n < 5; ++n) acc[n] = Z4;
        const u32 rb5 = opaque(L0 + wk * 20480 + lo16);
        u32x4 fb[3][5];
#define P5_RD(buf, t, kk) do { _Pragma("unroll") for (int n = 0; n < 5; ++n) fb[buf][n] = lds128(rb5 + r5_slot(t) + (kk) * 5120 + n * 1024); } while (0)
#define P5_MM(buf, av) do { _Pragma("unroll") for (int n = 0; n < 5; ++n) acc[n] = mfma(fb[buf][n], (av), acc[n]); } while (0)
#define P5_MIX() do { _Pragma("unroll") for (int n = 0; n < 5; ++n) { SGB(M_DSR, 1); SGB(M_MFMA, 1); } } while (0)
        P5_RD(0, 0, 0); P5_RD(1, 0, 1);
        static_for<0, 7>([&](auto T) {
            constexpr int t = T, q = t % 3, k0 = 4 * t;
            constexpr bool nx = t + 3 < 7;
            constexpr int NA = t == 0 ? 8 : t == 1 ? 13 : t == 2 ? 18 : t == 3 ? 19 : t == 4 ? 18 : t == 5 ? 9 : 3;
            constexpr int NB = t == 2 ? 11 : t == 3 ? 11 : t == 4 ? 8 : t == 5 ? 0 : 63;
            vmw4<NA>(a[q][0], a[q][1], a[q][2], a[q][3]);
            SB0(); P5_RD((k0 + 2) % 3, t, 2); P5_MM(k0 % 3, a[q][0]); P5_MIX(); SB0();
            if constexpr (nx) P5_A1(t + 3, 0);
            if constexpr (t < 6) {
                SB0(); P5_RD((k0 + 3) % 3, t, 3); P5_MM((k0 + 1) % 3, a[q][1]); P5_MIX(); SB0();
                if constexpr (nx) P5_A1(t + 3, 1);
                vmw<NB>(); BAR();
                if constexpr (nx) { P5_B1(t + 3, 0); P5_B1(t + 3, 1); }
                SB0(); P5_RD((k0 + 4) % 3, t + 1, 0); P5_MM((k0 + 2) % 3, a[q][2]); P5_MIX(); SB0();
                if constexpr (nx) { P5_A1(t + 3, 2); P5_B1(t + 3, 2); P5_B1(t + 3, 3); }
                SB0(); P5_RD((k0 + 5) % 3, t + 1, 1); P5_MM((k0 + 3) % 3, a[q][3]); P5_MIX(); SB0();
                if constexpr (nx) { if constexpr (t + 3 < 6) P5_A1(t + 3, 3); P5_B1(t + 3, 4); }
            } else {
                SB0(); P5_MM((k0 + 1) % 3, a[q][1]); SB0(); P5_MM((k0 + 2) % 3, a[q][2]); SB0();
            }
        });
#undef P5_B1
#undef P5_A1
#undef P5_RD
#undef P5_MM
#undef P5_MIX
        if (wk == 1) {
#pragma unroll
            for (int n = 0; n < 5; ++n) stsf4(L0 + L_RED5 + (wm * 5 + n) * 1024 + lo16, acc[n]);
        }
        BAR();
        if (wk == 0) {
#pragma unroll
            for (int n = 0; n < 5; ++n) {
                const f32x4 v = acc[n] + ldsf4(L0 + L_RED5 + (wm * 5 + n) * 1024 + lo16);
                const f32x4 gb = ldsf4(L0 + L_GB + (16 * n + 4 * g) * 4);
                const u32x2 o = {pack2(bflo(x1k[n][0]) + gb[0] * v[0], bfhi(x1k[n][0]) + gb[1] * v[1]), pack2(bflo(x1k[n][1]) + gb[2] * v[2], bfhi(x1k[n][1]) + gb[3] * v[3])};
                sts64(L0 + L_OS + (16 * wm + i16) * 160 + 32 * n + 8 * g, o);
            }
        }
        BAR();
#pragma unroll
        for (int i = 0; i < 2; ++i) {
            const int id = tid + 512 * i;
            if (id < 640) {
                const int r = (id * 6554) >> 16, c = id - 10 * r;
                { const u32x4 ov = lds128(L0 + L_OS + id * 16); asm volatile("global_store_dwordx4 %0, %1, %2 sc1\n\ts_nop 1" :: "v"((u32)((rb * 128 + 64 * hf + r) * 1280 + cb * 160 + c * 16)), "v"(ov), "s"(p.out) : "memory"); }
            }
        }
    }
    asm volatile(".Lmk_code_to:");
}

extern "C" void kernel_launch(void* const* d_in, const int* in_sizes, int n_in, void* d_out, int out_size, void* d_ws, size_t ws_size, hipStream_t stream) {
    Params p;
    p.x = (const u16*)d_in[0]; p.wqkv = (const u16*)d_in[1]; p.wo = (const u16*)d_in[2];
    p.wg = (const u16*)d_in[4]; p.wu = (const u16*)d_in[5]; p.wd = (const u16*)d_in[6];
    p.c = (const u16*)d_in[7]; p.wmod = (const u16*)d_in[8];
    p.out = (u16*)d_out; p.ws = (char*)d_ws;
    mega_kernel<<<256, 512, 0, stream>>>(p);
}
```

```cpp
#include <hip/hip_runtime.h>
#include <hip/hip_bf16.h>
#include <stdint.h>
#include <type_traits>

typedef unsigned short u16;
typedef unsigned u32;
typedef unsigned long long u64;
typedef __attribute__((ext_vector_type(2))) unsigned u32x2;
typedef __attribute__((ext_vector_type(4))) unsigned u32x4;
typedef __attribute__((ext_vector_type(2))) float f32x2;
typedef __attribute__((ext_vector_type(4))) float f32x4;
typedef __attribute__((ext_vector_type(2))) __bf16 bf16x2;
typedef __attribute__((ext_vector_type(8))) __bf16 bf16x8;
typedef __attribute__((ext_vector_type(4))) short s16x4;
#define LAS __attribute__((address_space(3)))
#define GAS __attribute__((address_space(1)))
#define DI __device__ __forceinline__

constexpr int SEQ = 2048, DM = 640, FF = 1728;
constexpr float QSCALE = 0.22360679774997896f * 1.4426950408889634f;
constexpr float LOG2E = 1.4426950408889634f;

constexpr u32 WS_EPOCH = 0;
constexpr u32 WS_ERR   = 256;
constexpr u32 FS = 128;
constexpr u32 WS_FKV   = 4096;
constexpr u32 WS_FO    = WS_FKV + 256 * FS;
constexpr u32 WS_FX1   = WS_FO + 256 * FS;
constexpr u32 WS_FACT  = WS_FX1 + 256 * FS;
constexpr u32 WS_FH    = WS_FACT + 256 * FS;
constexpr u32 WS_FWT   = WS_FH + 256 * FS;
constexpr u32 WS_FWP   = WS_FWT + 256 * FS;
constexpr u32 WS_FW2   = WS_FWP + 256 * FS;
constexpr u32 WS_MODG  = WS_FW2 + 256 * FS;
constexpr u32 WS_UV    = WS_MODG + 61440;
constexpr u32 WS_STAT  = WS_UV + 114688;
constexpr u32 WS_KV    = 589824;
constexpr u32 WS_O     = WS_KV + 256 * 22528;
constexpr u32 WS_X1    = WS_O + SEQ * DM * 2;
constexpr u32 WS_ACT   = WS_X1 + SEQ * DM * 2;
constexpr u32 WS_WP    = WS_ACT + SEQ * FF * 2;
constexpr u32 WS_H     = WS_WP + 16 * 20 * 14 * 1024;
constexpr u32 WS_WD    = WS_H + SEQ * DM * 2;
constexpr u32 WS_WT    = WS_WD + 8 * 54 * 5 * 1024;
constexpr u32 WS_END   = WS_WT + 16 * 153600;

constexpr u32 L_WKV = 0, L_WQ = 102400, L_GS = 153600, L_STP = 156160, L_MODP = 156416, L_CNT = 157440, L_SB = 157696, L_G2 = 158016, L_Z16 = 158656;
constexpr u32 L_WTRAW = 116736, L_XS = 133120;
constexpr u32 L_K = 0, L_V = 30720, L_Q = 67584, L_WPRAW = 77824, L_UVL = 98304;
constexpr u32 L_WO = 61440, L_RED3 = 0, L_XR = 30720, L_GA = 40960, L_WDRAW = 43008;
constexpr u32 L_R4 = 0, R4_STAGE = 45056, L_ST4 = 135168, L_UV = 143360, L_MR = 150528, L_XCH = 151552, L_UVS = 159744;
constexpr u32 L_RED5 = 126976, L_GB = 147456, L_OS = 148480;
constexpr u32 r5_slot(int t) { return t % 3 == 0 ? 45056 : t % 3 == 1 ? 86016 : 0; }

struct Params {
    const u16* x; const u16* wqkv; const u16* wo; const u16* wg; const u16* wu; const u16* wd;
    const u16* c; const u16* wmod; u16* out; char* ws;
};

DI float bflo(u32 d) { return __uint_as_float(d << 16); }
DI float bfhi(u32 d) { return __uint_as_float(d & 0xffff0000u); }
DI u32 pack2(float a, float b) { f32x2 v = {a, b}; return __builtin_bit_cast(u32, __builtin_convertvector(v, bf16x2)); }
DI float rbf(float a) { return bflo(pack2(a, a)); }
DI f32x4 mfma(u32x4 a, u32x4 b, f32x4 c) {
    return __builtin_amdgcn_mfma_f32_16x16x32_bf16(__builtin_bit_cast(bf16x8, a), __builtin_bit_cast(bf16x8, b), c, 0, 0, 0);
}
DI u32x2 ldtr(u32 a) { return __builtin_bit_cast(u32x2, __builtin_amdgcn_ds_read_tr16_b64_v4i16((LAS s16x4*)(size_t)a)); }
DI u32x4 ldtr2(u32 a0, u32 a1) { u32x2 x = ldtr(a0), y = ldtr(a1); return (u32x4){x[0], x[1], y[0], y[1]}; }
DI u32x4 lds128(u32 a) { return *(LAS u32x4*)(size_t)a; }
DI u32x2 lds64(u32 a) { return *(LAS u32x2*)(size_t)a; }
DI f32x4 ldsf4(u32 a) { return *(LAS f32x4*)(size_t)a; }
DI float ldsf(u32 a) { return *(LAS float*)(size_t)a; }
DI void sts128(u32 a, u32x4 v) { *(LAS u32x4*)(size_t)a = v; }
DI void sts64(u32 a, u32x2 v) { *(LAS u32x2*)(size_t)a = v; }
DI void stsf(u32 a, float v) { *(LAS float*)(size_t)a = v; }
DI void stsf4(u32 a, f32x4 v) { *(LAS f32x4*)(size_t)a = v; }
DI void stsf2(u32 a, f32x2 v) { *(LAS f32x2*)(size_t)a = v; }

DI void glds16_(const void* root, u32 voff, u32 ldsdst) {
    asm volatile("s_mov_b32 m0, %2\n\ts_nop 0\n\tglobal_load_lds_dwordx4 %0, %1" :: "v"(voff), "s"(root), "s"(__builtin_amdgcn_readfirstlane(ldsdst)) : "memory");
}
DI void glds16c_(const void* root, u32 voff, u32 ldsdst) {
    asm volatile("s_mov_b32 m0, %2\n\ts_nop 0\n\tglobal_load_lds_dwordx4 %0, %1 sc1" :: "v"(voff), "s"(root), "s"(__builtin_amdgcn_readfirstlane(ldsdst)) : "memory");
}
template <int OFF> DI void glds16c_next_(const void* root, u32 voff) {
    asm volatile("global_load_lds_dwordx4 %0, %1 offset:%2 sc1" :: "v"(voff), "s"(root), "i"(OFF) : "memory");
}
template <int OFF> DI u32x4 gld16(const void* root, u32 voff) {
    u32x4 r; asm volatile("global_load_dwordx4 %0, %1, %2 offset:%3" : "=v"(r) : "v"(voff), "s"(root), "i"(OFF) : "memory"); return r;
}
template <int OFF> DI u32x4 gld16c_(const void* root, u32 voff) {
    u32x4 r; asm volatile("global_load_dwordx4 %0, %1, %2 offset:%3 sc1" : "=v"(r) : "v"(voff), "s"(root), "i"(OFF) : "memory"); return r;
}
#define ROFF(root, ptr) ((u32)((const char*)(ptr) - (const char*)(root)))
#define glds16(root, ptr, voff, dst) glds16_((root), ROFF(root, ptr) + (voff), (dst))
#define glds16c(ptr, voff, dst) glds16c_(ws, ROFF(ws, ptr) + (voff), (dst))
#define glds16c_next(OFF, ptr, voff) glds16c_next_<(OFF)>(ws, ROFF(ws, ptr) + (voff))
#define gld16c(OFF, ptr, voff) gld16c_<(OFF)>(ws, ROFF(ws, ptr) + (voff))
DI u32 gldu16(const void* sbase, u32 voff) {
    u32 r; asm volatile("global_load_ushort %0, %1, %2" : "=v"(r) : "v"(voff), "s"(sbase) : "memory"); return r;
}
#define VMCNT(n) asm volatile("s_waitcnt vmcnt(" #n ")" ::: "memory")
#define BAR() asm volatile("s_waitcnt lgkmcnt(0)\n\ts_barrier" ::: "memory")
#define RLX_AGENT __ATOMIC_RELAXED, __HIP_MEMORY_SCOPE_AGENT
DI u64 ld_agent(const char* p) { return __hip_atomic_load((GAS u64*)p, RLX_AGENT); }
DI void st_agent(char* p, u64 v) { __hip_atomic_store((GAS u64*)p, v, RLX_AGENT); }
DI void st_agent32(char* p, u32 v) { __hip_atomic_store((GAS u32*)p, v, RLX_AGENT); }
DI float xsum4(float x) {
    const auto s = __builtin_amdgcn_permlane16_swap(__float_as_uint(x), __float_as_uint(x), false, false);
    x = __uint_as_float(s[0]) + __uint_as_float(s[1]);
    const auto t = __builtin_amdgcn_permlane32_swap(__float_as_uint(x), __float_as_uint(x), false, false);
    return __uint_as_float(t[0]) + __uint_as_float(t[1]);
}
DI float xmax4(float x) {
    const auto s = __builtin_amdgcn_permlane16_swap(__float_as_uint(x), __float_as_uint(x), false, false);
    x = fmaxf(__uint_as_float(s[0]), __uint_as_float(s[1]));
    const auto t = __builtin_amdgcn_permlane32_swap(__float_as_uint(x), __float_as_uint(x), false, false);
    return fmaxf(__uint_as_float(t[0]), __uint_as_float(t[1]));
}
template <int CTRL> DI float dppf(float v) { return __builtin_bit_cast(float, __builtin_amdgcn_update_dpp(0, __builtin_bit_cast(int, v), CTRL, 0xf, 0xf, true)); }

constexpr u32 SPIN_CAP = 1u << 17;
DI void poll_flags(const char* base, int n, int lane, u64 tag, char* ws, u32 code) {
    const char* a = base + (lane < n ? lane : 0) * FS;
    u32 spins = 0;
    for (;;) {
        const u64 v = ld_agent(a);
        if (__all(v == tag)) break;
        if (++spins > SPIN_CAP) { if (lane == 0) st_agent32(ws + WS_ERR, code); break; }
        __builtin_amdgcn_s_sleep(1);
    }
}
typedef __attribute__((ext_vector_type(2))) _Float16 f16x2;
DI float hlo(u32 d) { return (float)__builtin_bit_cast(f16x2, d)[0]; }
DI float hhi(u32 d) { return (float)__builtin_bit_cast(f16x2, d)[1]; }
DI u32 packh(float a, float b) { const f16x2 v = {(_Float16)a, (_Float16)b}; return __builtin_bit_cast(u32, v); }
DI bool gran_ok(const u32x4& v, u64 tag) { return v[2] == (u32)tag && v[3] == (u32)(tag >> 32); }
DI f32x4 gran_sum(const u32x4& a, const u32x4& b, const u32x4& c, const u32x4& d) {
    const f32x4 s = {(hlo(a[0]) + hlo(b[0])) + (hlo(c[0]) + hlo(d[0])), (hhi(a[0]) + hhi(b[0])) + (hhi(c[0]) + hhi(d[0])),
                     (hlo(a[1]) + hlo(b[1])) + (hlo(c[1]) + hlo(d[1])), (hhi(a[1]) + hhi(b[1])) + (hhi(c[1]) + hhi(d[1]))};
    return (f32x4){rbf(s[0]), rbf(s[1]), rbf(s[2]), rbf(s[3])};
}
DI f32x4 gather1(const char* ws, u32 g0, u64 tag, char* wsw, u32 code) {
    u32 spins = 0; u32x4 v0, v1, v2, v3;
    const u32 o = WS_MODG + g0 * 16;
    for (;;) {
        asm volatile("s_nop 4\n\tglobal_load_dwordx4 %0, %4, %5 sc1\n\tglobal_load_dwordx4 %1, %6, %5 sc1\n\tglobal_load_dwordx4 %2, %7, %5 sc1\n\tglobal_load_dwordx4 %3, %8, %5 sc1\n\ts_waitcnt vmcnt(0)"
                     : "=&v"(v0), "=&v"(v1), "=&v"(v2), "=&v"(v3) : "v"(o), "s"(ws), "v"(o + 15360), "v"(o + 30720), "v"(o + 46080) : "memory");
        if (__all(gran_ok(v0, tag) && gran_ok(v1, tag) && gran_ok(v2, tag) && gran_ok(v3, tag))) break;
        if (++spins > SPIN_CAP) { st_agent32(wsw + WS_ERR, code); break; }
        __builtin_amdgcn_s_sleep(1);
    }
    return gran_sum(v0, v1, v2, v3);
}
DI void reask(u32x4& v, const char* ws, u32 off, u64 tag) {
    if (!gran_ok(v, tag)) asm volatile("global_load_dwordx4 %0, %1, %2 sc1" : "+v"(v) : "v"(off), "s"(ws) : "memory");
}
DI void gather2(const char* ws, u32 g0, u32 g1, u64 tag, char* wsw, u32 code, f32x4& ra, f32x4& rb) {
    u32 spins = 0; u32x4 v0, v1, v2, v3, u0, u1, u2, u3;
    const u32 o = WS_MODG + g0 * 16, q = WS_MODG + g1 * 16;
    asm volatile("global_load_dwordx4 %0, %8, %9 sc1\n\tglobal_load_dwordx4 %1, %10, %9 sc1\n\tglobal_load_dwordx4 %2, %11, %9 sc1\n\tglobal_load_dwordx4 %3, %12, %9 sc1\n\t"
                 "global_load_dwordx4 %4, %13, %9 sc1\n\tglobal_load_dwordx4 %5, %14, %9 sc1\n\tglobal_load_dwordx4 %6, %15, %9 sc1\n\tglobal_load_dwordx4 %7, %16, %9 sc1\n\ts_waitcnt vmcnt(0)"
                 : "=&v"(v0), "=&v"(v1), "=&v"(v2), "=&v"(v3), "=&v"(u0), "=&v"(u1), "=&v"(u2), "=&v"(u3)
                 : "v"(o), "s"(ws), "v"(o + 15360), "v"(o + 30720), "v"(o + 46080), "v"(q), "v"(q + 15360), "v"(q + 30720), "v"(q + 46080) : "memory");
    for (;;) {
        if (__all(gran_ok(v0, tag) && gran_ok(v1, tag) && gran_ok(v2, tag) && gran_ok(v3, tag) && gran_ok(u0, tag) && gran_ok(u1, tag) && gran_ok(u2, tag) && gran_ok(u3, tag))) break;
        if (++spins > SPIN_CAP) { st_agent32(wsw + WS_ERR, code); break; }
        reask(v0, ws, o, tag); reask(v1, ws, o + 15360, tag); reask(v2, ws, o + 30720, tag); reask(v3, ws, o + 46080, tag);
        reask(u0, ws, q, tag); reask(u1, ws, q + 15360, tag); reask(u2, ws, q + 30720, tag); reask(u3, ws, q + 46080, tag);
        asm volatile("s_waitcnt vmcnt(0)" : "+v"(v0), "+v"(v1), "+v"(v2), "+v"(v3), "+v"(u0), "+v"(u1), "+v"(u2), "+v"(u3) :: "memory");
    }
    ra = gran_sum(v0, v1, v2, v3); rb = gran_sum(u0, u1, u2, u3);
}
struct Gather { u32x4 v0, v1, v2, v3, u0, u1, u2, u3; };
DI void gather_ask(const char* ws, u32 g0, u32 g1, Gather& G) {
    const u32 o = WS_MODG + g0 * 16, q = WS_MODG + g1 * 16;
    asm volatile("global_load_dwordx4 %0, %8, %9 sc1\n\tglobal_load_dwordx4 %1, %10, %9 sc1\n\tglobal_load_dwordx4 %2, %11, %9 sc1\n\tglobal_load_dwordx4 %3, %12, %9 sc1\n\t"
                 "global_load_dwordx4 %4, %13, %9 sc1\n\tglobal_load_dwordx4 %5, %14, %9 sc1\n\tglobal_load_dwordx4 %6, %15, %9 sc1\n\tglobal_load_dwordx4 %7, %16, %9 sc1"
                 : "=&v"(G.v0), "=&v"(G.v1), "=&v"(G.v2), "=&v"(G.v3), "=&v"(G.u0), "=&v"(G.u1), "=&v"(G.u2), "=&v"(G.u3)
                 : "v"(o), "s"(ws), "v"(o + 15360), "v"(o + 30720), "v"(o + 46080), "v"(q), "v"(q + 15360), "v"(q + 30720), "v"(q + 46080) : "memory");
}
DI void gather_finish(const char* ws, u32 g0, u32 g1, Gather& G, u64 tag, char* wsw, u32 code, f32x4& ra, f32x4& rb) {
    asm volatile("" : "+v"(G.v0), "+v"(G.v1), "+v"(G.v2), "+v"(G.v3), "+v"(G.u0), "+v"(G.u1), "+v"(G.u2), "+v"(G.u3) :: "memory");
    if (__all(gran_ok(G.v0, tag) && gran_ok(G.v1, tag) && gran_ok(G.v2, tag) && gran_ok(G.v3, tag) && gran_ok(G.u0, tag) && gran_ok(G.u1, tag) && gran_ok(G.u2, tag) && gran_ok(G.u3, tag))) {
        ra = gran_sum(G.v0, G.v1, G.v2, G.v3); rb = gran_sum(G.u0, G.u1, G.u2, G.u3);
    } else gather2(ws, g0, g1, tag, wsw, code, ra, rb);
}
DI void gather_strip(const char* ws, u32 gs, u32 gc, u32 strip, int l, u64 tag, char* wsw, u32 code) {
    f32x4 a, c;
    gather2(ws, l < 40 ? gs + l : gc + (l - 40), gc + 24 + (l & 15), tag, wsw, code, a, c);
    if (l >= 40) a = a + 1.f;
    c = c + 1.f;
    *(LAS u32x2*)(size_t)(strip + (l < 40 ? l * 8 : 320 + (l - 40) * 8)) = (u32x2){pack2(a[0], a[1]), pack2(a[2], a[3])};
    if (l < 16) *(LAS u32x2*)(size_t)(strip + 320 + (24 + l) * 8) = (u32x2){pack2(c[0], c[1]), pack2(c[2], c[3])};
}
DI void put_strip_spread(const f32x4& a, const f32x4& c, u32 strip, int l) {
    if (l < 40) *(LAS u32x2*)(size_t)(strip + l * 8) = (u32x2){pack2(a[0], a[1]), pack2(a[2], a[3])};
    else {
        const u32 p0 = pack2(a[0] + 1.f, a[1] + 1.f), p1 = pack2(a[2] + 1.f, a[3] + 1.f);
        *(LAS u32x4*)(size_t)(strip + 320 + (l - 40) * 16) = (u32x4){p0 & 0xffffu, p0 & 0xffff0000u, p1 & 0xffffu, p1 & 0xffff0000u};
    }
    if (l < 16) {
        const u32 p0 = pack2(c[0] + 1.f, c[1] + 1.f), p1 = pack2(c[2] + 1.f, c[3] + 1.f);
        *(LAS u32x4*)(size_t)(strip + 320 + (24 + l) * 16) = (u32x4){p0 & 0xffffu, p0 & 0xffff0000u, p1 & 0xffffu, p1 & 0xffff0000u};
    }
    if (l == 63) *(LAS u32x4*)(size_t)(strip + 960) = (u32x4){0, 0, 0, 0};
}
DI void poll_at(const char* ws, u32 off, int lane, u64 tag, char* wsw, u32 code) {
    u32 spins = 0;
    for (;;) {
        const u64 v = ld_agent(ws + off);
        if (__all(v == tag)) break;
        if (++spins > SPIN_CAP) { if (lane == 0) st_agent32(wsw + WS_ERR, code); break; }
        __builtin_amdgcn_s_sleep(1);
    }
}

template <int N> using IC = std::integral_constant<int, N>;
template <int I, int E, class F> DI void static_for(F&& f) { if constexpr (I < E) { f(IC<I>{}); static_for<I + 1, E>(f); } }
template <int N> DI void vmw() { asm volatile("s_waitcnt vmcnt(%0)" :: "i"(N) : "memory"); }
template <int N> DI void vmw1(u32x4& a) { asm volatile("s_waitcnt vmcnt(%1)" : "+v"(a) : "i"(N) : "memory"); }
template <int N> DI void vmw4(u32x4& a, u32x4& b, u32x4& c, u32x4& d) { asm volatile("s_waitcnt vmcnt(%4)" : "+v"(a), "+v"(b), "+v"(c), "+v"(d) : "i"(N) : "memory"); }
DI u32 opaque(u32 a) { asm("" : "+v"(a)); return a; }
DI float dot2(u32 a, u32 b, float c) { return __builtin_amdgcn_fdot2_f32_bf16(__builtin_bit_cast(bf16x2, a), __builtin_bit_cast(bf16x2, b), c, false); }
#define SB0() __builtin_amdgcn_sched_barrier(0)
#define SGB(m, n) __builtin_amdgcn_sched_group_barrier((m), (n), 0)
constexpr int M_MFMA = 0x8, M_DSR = 0x100;
#define Z4 ((f32x4){0.f, 0.f, 0.f, 0.f})
DI u32 lds_add(u32 a, u32 v) { return __hip_atomic_fetch_add((LAS u32*)(size_t)a, v, __ATOMIC_RELAXED, __HIP_MEMORY_SCOPE_WORKGROUP); }
DI u32 lds_ld(u32 a) { return __hip_atomic_load((LAS u32*)(size_t)a, __ATOMIC_RELAXED, __HIP_MEMORY_SCOPE_WORKGROUP); }
DI void worker_sync(u32 cnt, int lane) {
    asm volatile("s_waitcnt lgkmcnt(0)" ::: "memory");
    if (lane == 0) lds_add(cnt, 1u);
    for (u32 spins = 0; lds_ld(cnt) < 4u && spins < (1u << 22); ++spins) {}
    asm volatile("" ::: "memory");
}

__global__ __launch_bounds__(512, 2) void mega_kernel(Params p) {
    __shared__ __attribute__((aligned(16))) char ldsmem[163840];
    const u32 L0 = (u32)(uintptr_t)(LAS char*)ldsmem;
    const int tid = threadIdx.x, l = tid & 63;
    const int w = __builtin_amdgcn_readfirstlane(tid >> 6);
    const int b = blockIdx.x, xg = b & 7, sidx = b >> 3;
    const int rb = 4 * (xg >> 1) + (sidx >> 3), j = 8 * (xg & 1) + (sidx & 7);
    const int g = l >> 4, i16 = l & 15, q4 = i16 >> 2, p4 = l & 3;
    u32 wu0 = 0;
    if (w == 7) {
        const char* pc; u32 span;
        asm volatile("s_getpc_b64 %0\n.Lmk_code_from:\n\ts_mov_b32 %1, .Lmk_code_to-.Lmk_code_from" : "=s"(pc), "=s"(span));
        const u32 per = (span + 4095u) >> 12;
        const u32 off = min((sidx * per + min((u32)l, per - 1u)) * 128u, span - 4u);
        asm volatile("s_nop 0\n\tglobal_load_dword %0, %1, %2" : "=v"(wu0) : "v"(off), "s"(pc) : "memory");
    }
    char* const ws = p.ws;
    asm volatile("" :: "s"(p.x), "s"(p.wqkv), "s"(p.wo), "s"(p.wg), "s"(p.wu), "s"(p.wd), "s"(p.c), "s"(p.wmod), "s"(p.out), "s"(p.ws));
    u64 tag;
#define ASK_TAG(ep) u32x2 ep; asm volatile("global_load_dwordx2 %0, %1, %2" : "=v"(ep) : "v"(WS_EPOCH), "s"(ws) : "memory")
#define TAKE_TAG(ep) do { asm volatile("s_waitcnt vmcnt(0)" : "+v"(ep) :: "memory"); \
        tag = (((u64)(u32)__builtin_amdgcn_readfirstlane(ep[1]) << 32) | (u32)__builtin_amdgcn_readfirstlane(ep[0])) + 1; } while (0)
    const __amdgpu_buffer_rsrc_t wsr = __builtin_amdgcn_make_buffer_rsrc(ws, 0, WS_END, 0x00020000);
#define ST16C(v, off) __builtin_amdgcn_raw_buffer_store_b128((v), wsr, (off), 0, 16)
#define ST8C(v, off)  __builtin_amdgcn_raw_buffer_store_b64((v), wsr, (off), 0, 16)

    const int nj = j < 12 ? 7 : 6, Gj = j < 12 ? 7 * j : 84 + 6 * (j - 12);
    const bool wpwg = b < 216;
    const int wpm = b >= 108 ? 1 : 0, wpb = (b - 108 * wpm) >> 2, wpq = b & 3;
    u32x4 mw[5]; u32 mc[5];
    auto mod_ask = [&]() {
#pragma unroll
        for (int i = 0; i < 5; ++i) {
            const int r = 160 * (b & 3) + ((tid & 255) >> 3) + 32 * i;
            mw[i] = gld16<0>(p.wmod, r * 7680 + (b >> 2) * 128 + (tid & 7) * 16);
            mc[i] = gldu16(p.c, r * 2);
        }
    };
    auto mod_finish = [&]() {
        float acc[8];
#pragma unroll
        for (int e = 0; e < 8; ++e) acc[e] = 0.f;
#pragma unroll
        for (int i = 0; i < 5; ++i) {
            const float cv = bflo(mc[i]);
            const float sv = cv * __builtin_amdgcn_rcpf(1.f + __builtin_amdgcn_exp2f(-cv * LOG2E));
            const u32 slo = pack2(sv, 0.f), shi = slo << 16;
#pragma unroll
            for (int d = 0; d < 4; ++d) { acc[2 * d] = dot2(mw[i][d], slo, acc[2 * d]); acc[2 * d + 1] = dot2(mw[i][d], shi, acc[2 * d + 1]); }
        }
#pragma unroll
        for (int e = 0; e < 8; ++e) { const float v = acc[e]; acc[e] = xsum4(v + dppf<0x128>(v)); }
        if (l < 8) {
            stsf4(L0 + L_MODP + (w & 3) * 256 + l * 32, (f32x4){acc[0], acc[1], acc[2], acc[3]});
            stsf4(L0 + L_MODP + (w & 3) * 256 + l * 32 + 16, (f32x4){acc[4], acc[5], acc[6], acc[7]});
        }
        asm volatile("s_waitcnt lgkmcnt(0)" ::: "memory");
        u32 old = 0;
        if (l == 0) old = lds_add(L0 + L_CNT, 1u);
        if (__builtin_amdgcn_readfirstlane(old) == 3) {
            float v = 0.f;
#pragma unroll
            for (int ww = 0; ww < 4; ++ww) v += ldsf(L0 + L_MODP + ww * 256 + l * 4);
            const float v1 = dppf<0x55>(v), v2 = dppf<0xAA>(v), v3 = dppf<0xFF>(v), v0 = dppf<0x00>(v);
            const u32x4 gr = {packh(v0, v1), packh(v2, v3), (u32)tag, (u32)(tag >> 32)};
            if (p4 == 0) ST16C(gr, WS_MODG + ((b & 3) * 960 + (b >> 2) * 16 + (l >> 2)) * 16);
        }
    };
#define MOD_LANDED() asm volatile("s_waitcnt vmcnt(0)" : "+v"(mw[0]), "+v"(mw[1]), "+v"(mw[2]), "+v"(mw[3]), "+v"(mw[4]), "+v"(mc[0]), "+v"(mc[1]), "+v"(mc[2]), "+v"(mc[3]), "+v"(mc[4]) :: "memory")
    if (w >= 4) {
        __builtin_amdgcn_s_setprio(3);
        const int u = w - 4;
        const bool tw = b < 150;
        const int tb = b / 5, tk = b - 5 * tb, ts = 4 * tk + u;
        if (tw) {
            const char* src = (const char*)p.wqkv + ts * 122880 + tb * 128;
            const u32 voff = (l >> 3) * 3840 + (l & 7) * 16;
#pragma unroll
            for (int i = 0; i < 4; ++i) glds16(p.wqkv, src + i * 8 * 3840, voff, L0 + L_WTRAW + u * 4096 + i * 1024);
        }
        ASK_TAG(epl);
        BAR();
        __builtin_amdgcn_s_setprio(0);
        TAKE_TAG(epl);
        if (tw) {
            const u32 a = L0 + L_WTRAW + u * 4096 + (8 * g + q4) * 128 + 8 * p4;
#pragma unroll
            for (int t = 0; t < 4; ++t) {
                const u32 cc = 8 * tb + 2 * t + (i16 >> 3);
                const u32 seg = cc >= 160 ? 2 : cc >= 80 ? 1 : 0, wi = cc - 80 * seg, jj = (wi * 13108) >> 16, c5 = wi - 5 * jj;
                const u32 c = seg == 0 ? 10 + c5 : seg == 1 ? c5 : 5 + c5, nt = c >> 1;
                const u32 dst = WS_WT + jj * 153600 + (nt < 5 ? ts * 5120 + nt * 1024 : 102400 + ts * 2560 + (nt - 5) * 1024)
                              + (nt < 7 ? (g * 16 + 8 * (c & 1) + (i16 & 7)) * 16 : g * 128 + (i16 & 7) * 16);
                ST16C(ldtr2(a + 32 * t, a + 32 * t + 512), dst);
            }
        }
        vmw<0>();
        {
            asm volatile("s_waitcnt lgkmcnt(0)" ::: "memory");
            u32 old = 0;
            if (l == 0) old = lds_add(L0 + L_CNT + 12, 1u);
            if (tw && __builtin_amdgcn_readfirstlane(old) == 3 && l == 0) st_agent(ws + WS_FWT + b * FS, tag);
        }
        if (b >= 80 && b < 240) {
            if (!tw) __builtin_amdgcn_s_sleep(32);
            mod_ask(); MOD_LANDED(); mod_finish();
        }
        if (u == 0) {
            const int wh = l >= 30 ? 0 : l / 5, kr = l >= 30 ? 0 : l - 5 * wh;
            const int bl = (1280 * (wh >> 1) + 80 * j + 79 * (wh & 1)) >> 7;
            poll_at(ws, WS_FWT + (bl * 5 + kr) * FS, l, tag, ws, 13);
            for (u32 spins = 0; lds_ld(L0 + L_CNT + 12) < 4u && spins < (1u << 22); ++spins) {}
            if (l == 0) lds_add(L0 + L_CNT + 24, 1u);
        } else {
            for (u32 spins = 0; lds_ld(L0 + L_CNT + 24) < 1u && spins < (1u << 22); ++spins) {}
        }
        asm volatile("" ::: "memory");
        {
            const char* src = ws + WS_WT + j * 153600 + u * 1024;
            const u32 lo = l * 16;
#pragma unroll
            for (int i = 0; i < 25; ++i) glds16c(src + i * 4096, lo, L0 + L_WKV + u * 1024 + i * 4096);
        }
        VMCNT(0);
    } else {
        __builtin_amdgcn_s_setprio(2);
        if (b < 80) mod_ask();
        ASK_TAG(epw);
        if (tid < 16) *(LAS u32*)(size_t)(L0 + L_CNT + tid * 4) = 0u;
        BAR();
        TAKE_TAG(epw);
        if (b < 80) { MOD_LANDED(); mod_finish(); }
        {
            const char* src = (const char*)p.x + (rb * 128 + 8 * j) * 1280 + w * 1024;
            glds16(p.x, src, l * 16, L0 + L_XS + w * 1024);
            glds16(p.x, src + 4096, l * 16, L0 + L_XS + (w + 4) * 1024);
            if (w < 2) glds16(p.x, src + 8192, l * 16, L0 + L_XS + (w + 8) * 1024);
        }
        VMCNT(0);
        worker_sync(L0 + L_CNT + 16, l);
        const int tok = l & 7;
        u32x4 xr[3];
#pragma unroll
        for (int i = 0; i < 3; ++i) xr[i] = lds128(L0 + L_XS + tok * 1280 + (20 * w + (i < 2 ? (l >> 3) + 8 * i : 16 + ((l >> 3) & 3))) * 16);
        {
            float s1 = 0.f, s2 = 0.f;
#pragma unroll
            for (int i = 0; i < 3; ++i) {
                float a1 = 0.f, a2 = 0.f;
#pragma unroll
                for (int d = 0; d < 4; ++d) { a1 = dot2(xr[i][d], 0x3F803F80u, a1); a2 = dot2(xr[i][d], xr[i][d], a2); }
                if (i < 2 || l < 32) { s1 += a1; s2 += a2; }
            }
            s1 += dppf<0x128>(s1); s2 += dppf<0x128>(s2);
            s1 = xsum4(s1); s2 = xsum4(s2);
            if (l < 8) stsf2(L0 + L_STP + (w * 8 + l) * 8, (f32x2){s1, s2});
        }
        gather_strip(ws, 40 * w, 160 + 40 * w, L0 + L_GS + w * 640, l, tag, ws, 1);
        worker_sync(L0 + L_CNT + 4, l);
        {
            f32x2 st = {0.f, 0.f};
#pragma unroll
            for (int ww = 0; ww < 4; ++ww) st += *(LAS f32x2*)(size_t)(L0 + L_STP + (ww * 8 + tok) * 8);
            const float mu = st[0] * (1.f / 640.f), var = fmaxf(st[1] * (1.f / 640.f) - mu * mu, 0.f);
            const float rs = __builtin_amdgcn_rsqf(var + 1e-5f), nmr = -mu * rs;
#pragma unroll
            for (int i = 0; i < 3; ++i) {
                const int cl = i < 2 ? (l >> 3) + 8 * i : 16 + ((l >> 3) & 3);
                const u32x4 sa = lds128(L0 + L_GS + w * 640 + cl * 16), gg = lds128(L0 + L_GS + w * 640 + 320 + cl * 16);
                u32x4 h;
#pragma unroll
                for (int d = 0; d < 4; ++d)
                    h[d] = pack2((bflo(xr[i][d]) * rs + nmr) * bflo(gg[d]) + bflo(sa[d]), (bfhi(xr[i][d]) * rs + nmr) * bfhi(gg[d]) + bfhi(sa[d]));
                if (i < 2 || l < 32) ST16C(h, WS_H + (((rb * 8 + (j >> 1)) * 80 + 20 * w + cl) * 16 + 8 * (j & 1) + tok) * 16);
            }
        }
        VMCNT(0);
        worker_sync(L0 + L_CNT + 8, l);
        if (tid == 0) st_agent(ws + WS_FH + (rb * 16 + j) * FS, tag);
        if (w == 0) poll_flags(ws + WS_FH + rb * 16 * FS, 16, l, tag, ws, 11);
        __builtin_amdgcn_s_setprio(0);
    }
    BAR();
    asm volatile("s_waitcnt vmcnt(0)" : "+v"(wu0) :: "memory");

    u32x4 xa[20];
    const u32 lo16 = l * 16;
    const char* hsrc = ws + WS_H + (rb * 8 + w) * 80 * 256;
#define XL(s) xa[s] = gld16c(((s) & 3) * 1024, hsrc + ((s) >> 2) * 4096, lo16)
    XL(0); XL(1); XL(2); XL(3); XL(4); XL(5);
    const char* kvx = ws + WS_KV + (rb * 16 + j) * 22528;
    const char* wtq = ws + WS_WT + j * 153600 + (100 + w) * 1024;
    {
        f32x4 acc[5];
#pragma unroll
        for (int n = 0; n < 5; ++n) acc[n] = Z4;
        const u32 b0 = L0 + L_WKV + lo16;
        u32x4 bf[3][5];
#pragma unroll
        for (int n = 0; n < 5; ++n) { bf[0][n] = lds128(b0 + n * 1024); bf[1][n] = lds128(b0 + 5120 + n * 1024); }
        static_for<0, 20>([&](auto S) {
            constexpr int s = S;
            vmw1<(s < 14 ? 5 : 6)>(xa[s]);
            if constexpr (s + 6 < 20) XL(s + 6);
            if constexpr (s >= 13) { if (s < 19 || w < 2) glds16c(wtq + (s - 13) * 8192, lo16, L0 + L_WKV + (100 + w) * 1024 + (s - 13) * 8192); }
            SB0();
#pragma unroll
            for (int n = 0; n < 5; ++n) {
                if (s < 18) bf[(s + 2) % 3][n] = lds128(b0 + (s + 2) * 5120 + n * 1024);
                acc[n] = mfma(bf[s % 3][n], xa[s], acc[n]);
            }
#pragma unroll
            for (int n = 0; n < 5; ++n) { SGB(M_DSR, 1); SGB(M_MFMA, 1); }
            SB0();
        });
        BAR();
        const int row = 256 + 16 * w + i16;
#pragma unroll
        for (int n = 0; n < 5; ++n) {
            const int Q = 4 * n + g;
            const u32x2 v = {pack2(acc[n][0], acc[n][1]), pack2(acc[n][2], acc[n][3])};
            const int wi = Q - 10, hd = wi >= 5 ? 1 : 0;
            const u32 a = Q < 10 ? L_K + row * 80 + 8 * Q : L_V + row * 96 + 48 * hd + 8 * (wi - 5 * hd);
            sts64(L0 + a, v);
        }
        if (g < 2) sts64(L0 + L_V + row * 96 + 48 * g + 40, (u32x2){0x3F80u, 0u});
        if (rb < 2) {
            const int nrow = rb == 0 ? 256 : 128;
            for (int i = tid; i < nrow * 5; i += 512) sts128(L0 + L_K + i * 16, (u32x4){0, 0, 0, 0});
            for (int i = tid; i < nrow * 6; i += 512) sts128(L0 + L_V + i * 16, (u32x4){0, 0, 0, 0});
        }
        vmw<0>();
        BAR();
    }
#undef XL
    {
        u32x4 kvd[3];
#pragma unroll
        for (int e = 0; e < 3; ++e) {
            const int n = min(w + 8 * e, 21);
            kvd[e] = lds128(L0 + (n < 10 ? L_K + 20480 + n * 1024 : L_V + 24576 + (n - 10) * 1024) + lo16);
        }
        const u32 kvo = WS_KV + (rb * 16 + j) * 22528 + w * 1024 + lo16;
        const u16* wroot = wpm ? p.wu : p.wg;
        const char* wsrc = (const char*)wroot + (160 * wpq + 8 * w) * 3456 + wpb * 128;
        const u32 wvo = (l >> 3) * 3456 + (l & 7) * 16;
        f32x4 acc[3];
#pragma unroll
        for (int n = 0; n < 3; ++n) acc[n] = Z4;
        const u32 bq = L0 + L_WQ + lo16, bq2 = L0 + L_WQ + 2048 + g * 128 + (i16 & 7) * 16;
#define QRD(s, n) lds128(((n) < 2 ? bq + (n) * 1024 : bq2) + (s) * 2560)
        u32x4 bf[3][3];
#pragma unroll
        for (int n = 0; n < 3; ++n) { bf[0][n] = QRD(0, n); bf[1][n] = QRD(1, n); }
        Gather G;
        const u32 gg0 = l < 40 ? 480 + 40 * wpq + l : 640 + 40 * wpq + (l - 40), gg1 = 640 + 40 * wpq + 24 + (l & 15);
        static_for<0, 20>([&](auto S) {
            constexpr int s = S;
            if constexpr (s == 6) { if (w == 7 && wpwg) gather_ask(ws, gg0, gg1, G); }
            if constexpr (s == 0) { if (rb < 15) ST16C(kvd[0], kvo); }
            if constexpr (s == 1) { if (rb < 15) ST16C(kvd[1], kvo + 8192); }
            if constexpr (s == 2) { if (rb < 15 && w < 6) ST16C(kvd[2], kvo + 16384); }
            if constexpr (s == 14) {
                vmw<0>();
                u32 old = 0;
                if (l == 0) old = lds_add(L0 + L_CNT + 28, 1u);
                if (__builtin_amdgcn_readfirstlane(old) == 7 && l == 0) st_agent(ws + WS_FKV + (j * 16 + rb) * FS, tag);
                if (w == 7 && wpwg) { f32x4 ra, rc; gather_finish(ws, gg0, gg1, G, tag, ws, 2, ra, rc); put_strip_spread(ra, rc, L0 + L_SB, l); }
            }
            if constexpr (s == 15) { if (wpwg) glds16(wroot, wsrc, wvo, L0 + L_WPRAW + w * 1024); }
            if constexpr (s == 16) { if (wpwg) glds16(wroot, wsrc + 64 * 3456, wvo, L0 + L_WPRAW + (w + 8) * 1024); }
            if constexpr (s == 17) { if (wpwg && w < 4) glds16(wroot, wsrc + 128 * 3456, wvo, L0 + L_WPRAW + (w + 16) * 1024); }
            SB0();
#pragma unroll
            for (int n = 0; n < 3; ++n) {
                if (s < 18) bf[(s + 2) % 3][n] = QRD(s + 2, n);
                acc[n] = mfma(bf[s % 3][n], xa[s], acc[n]);
            }
#pragma unroll
            for (int n = 0; n < 3; ++n) { SGB(M_DSR, 1); SGB(M_MFMA, 1); }
            SB0();
        });
#undef QRD
#pragma unroll
        for (int n = 0; n < 3; ++n) {
            const int Q = 4 * n + g;
            const u32x2 v = {pack2(acc[n][0] * QSCALE, acc[n][1] * QSCALE), pack2(acc[n][2] * QSCALE, acc[n][3] * QSCALE)};
            if (Q < 10) sts64(L0 + L_Q + (16 * w + i16) * 80 + 8 * Q, v);
        }
    }
    if (w == 0 && rb >= 1) poll_flags(ws + WS_FKV + (j * 16 + (rb >= 2 ? rb - 2 : rb - 1)) * FS, rb >= 2 ? 2 : 1, l, tag, ws, 3);
    VMCNT(0);
    BAR();
    if (rb >= 1) {
#pragma unroll
        for (int e = 0; e < 3; ++e) {
            const int n = w + 8 * e;
            if (n < 22) {
                const u32 d = n < 10 ? L_K + n * 1024 : L_V + (n - 10) * 1024, st = n < 10 ? 10240 : 12288;
                glds16c(kvx - 16 * 22528 + n * 1024, lo16, L0 + d + st);
                if (rb >= 2) glds16c(kvx - 32 * 22528 + n * 1024, lo16, L0 + d);
            }
        }
    }
    auto wp_slot = [&](int gam, int& jj, int& slot) {
        jj = gam < 84 ? gam / 7 : 12 + (gam - 84) / 6;
        const int ar = gam - (jj < 12 ? 7 * jj : 84 + 6 * (jj - 12)), a = (jj < 12 || ar < 3) ? ar : ar + 1;
        slot = a < 3 ? 2 * a + wpm : a == 3 ? (wpm ? 13 : 6) : 7 + 2 * (a - 4) + wpm;
    };
    if (wpwg) {
        const u32 one = i16 == 0 ? 0x3F803F80u : 0u;
        const u32x4 ones = {one, one, one, one};
#pragma unroll
        for (int i = 0; i < 3; ++i) {
            if (i < 2 || w < 4) {
                const int f = w + 8 * i, ks = f >> 2, t16 = f & 3;
                int jj, slot; wp_slot(4 * wpb + t16, jj, slot);
                const u32 a = L0 + L_WPRAW + (32 * ks + 8 * g + q4) * 128 + 32 * t16 + 8 * p4;
                u32x4 wr = ldtr2(a, a + 512);
                const u32x4 ga = lds128(L0 + L_G2 + (32 * ks + 8 * g) * 4), gc = lds128(L0 + L_G2 + (32 * ks + 8 * g) * 4 + 16);
                const u32x4 sbz = lds128(L0 + (i16 == 0 ? L_SB + (32 * ks + 8 * g) * 2 : L_Z16));
                const f32x4 vs = mfma(wr, sbz, Z4);
                u32x4 o; float t0, t1, t2, t3, t4, t5, t6, t7;
                asm volatile("v_dot2_f32_bf16 %4, %12, %16, 0\n\tv_dot2_f32_bf16 %5, %12, %17, 0\n\tv_dot2_f32_bf16 %6, %13, %18, 0\n\tv_dot2_f32_bf16 %7, %13, %19, 0\n\t"
                             "v_dot2_f32_bf16 %8, %14, %20, 0\n\tv_dot2_f32_bf16 %9, %14, %21, 0\n\tv_dot2_f32_bf16 %10, %15, %22, 0\n\tv_dot2_f32_bf16 %11, %15, %23, 0\n\t"
                             "v_cvt_pk_bf16_f32 %0, %4, %5\n\tv_cvt_pk_bf16_f32 %1, %6, %7\n\tv_cvt_pk_bf16_f32 %2, %8, %9\n\ts_nop 0\n\tv_cvt_pk_bf16_f32 %3, %10, %11\n\ts_nop 1"
                             : "=&v"(o[0]), "=&v"(o[1]), "=&v"(o[2]), "=&v"(o[3]),
                               "=&v"(t0), "=&v"(t1), "=&v"(t2), "=&v"(t3), "=&v"(t4), "=&v"(t5), "=&v"(t6), "=&v"(t7)
                             : "v"(wr[0]), "v"(wr[1]), "v"(wr[2]), "v"(wr[3]), "v"(ga[0]), "v"(ga[1]), "v"(ga[2]), "v"(ga[3]), "v"(gc[0]), "v"(gc[1]), "v"(gc[2]), "v"(gc[3]));
                const f32x4 us = mfma(o, ones, Z4);
                ST16C(o, WS_WP + ((jj * 20 + 5 * wpq + ks) * 14 + slot) * 1024 + lo16);
                if (i16 == 0) { stsf4(L0 + L_UVL + f * 128 + g * 16, us); stsf4(L0 + L_UVL + f * 128 + 64 + g * 16, vs); }
            }
        }
    }
    if (wpwg) { if (w < 4) vmw<3>(); else vmw<2>(); } else vmw<0>();
    BAR();
    if (wpwg && w == 3) {
#pragma unroll
        for (int e = 0; e < 2; ++e) {
            const int t16 = 2 * e + (l >> 5);
            int jj, slot; wp_slot(4 * wpb + t16, jj, slot);
            float v = 0.f;
#pragma unroll
            for (int ks = 0; ks < 5; ++ks) v += ldsf(L0 + L_UVL + (4 * ks + t16) * 128 + (l & 31) * 4);
            st_agent32(ws + WS_UV + ((jj * 14 + slot) * 4 + wpq) * 128 + (l & 31) * 4, __float_as_uint(v));
        }
    }
    const int hf = j & 1, cb = j >> 1;
    u32 wvoff[5];
#pragma unroll
    for (int i = 0; i < 5; ++i) {
        const u32 sg = 64 * i + l, pos = (sg * 6554) >> 16, c = sg - pos * 10, k = pos ^ (((pos >> 3) & 1) << 2);
        wvoff[i] = k * 1280 + c * 16;
    }
    const char* wosrc = (const char*)p.wo + cb * 160;
#define WO_PIECE(n) glds16(p.wo, wosrc + (4 + w + 8 * ((n) / 5)) * 32 * 1280, wvoff[(n) % 5], L0 + L_WO + (4 + w + 8 * ((n) / 5)) * 5120 + ((n) % 5) * 1024)

    {
        const int hd = w & 1, qi = w >> 1;
        const int kr0 = 32 * qi;
        u32x4 qf[2];
#pragma unroll
        for (int a = 0; a < 2; ++a) {
            const u32 qa = L0 + L_Q + (kr0 + 16 * a + i16) * 80 + hd * 40;
            const u32x2 q0 = lds64(qa + 8 * g);
            u32x2 q1 = lds64(qa + 32);
            if (g != 0) q1 = (u32x2){0, 0};
            qf[a] = (u32x4){q0[0], q0[1], q1[0], q1[1]};
        }
        const u32 kb0 = L0 + L_K + (kr0 + i16) * 80 + hd * 40 + 8 * g, kb1 = L0 + L_K + (kr0 + i16) * 80 + hd * 40 + 32;
        f32x4 mfirst, mlast;
#pragma unroll
        for (int r = 0; r < 4; ++r) { const bool lo = 4 * g + r <= i16; mfirst[r] = lo ? -INFINITY : 0.f; mlast[r] = lo ? 0.f : -INFINITY; }
        f32x4 s0[17], s1[17];
        {
            u32x4 kf[6];
#define KRD(t) do { const u32x2 k0 = lds64(kb0 + (t) * 1280), k1 = lds64(kb1 + (t) * 1280); kf[(t) % 6] = (u32x4){k0[0], k0[1], k1[0], k1[1]}; } while (0)
            KRD(0); KRD(1); KRD(2); KRD(3); KRD(4);
            static_for<0, 18>([&](auto T) {
                constexpr int t = T;
                SB0();
                if constexpr (t < 13) KRD(t + 5);
                if constexpr (t < 17) s0[t] = mfma(kf[t % 6], qf[0], t == 0 ? mfirst : t == 16 ? mlast : Z4);
                if constexpr (t > 0) s1[t - 1] = mfma(kf[t % 6], qf[1], t == 1 ? mfirst : t == 17 ? mlast : Z4);
                SGB(M_DSR, 2); SGB(M_MFMA, 2);
                SB0();
            });
#undef KRD
        }
        WO_PIECE(0);
        if (rb < 2) {
            const int fv = rb == 1 ? 128 : 256;
#pragma unroll
            for (int t = 0; t < 16; ++t) {
                const bool d0 = kr0 + 16 * t < fv, d1 = kr0 + 16 * t + 16 < fv;
#pragma unroll
                for (int r = 0; r < 4; ++r) { s0[t][r] = d0 ? -INFINITY : s0[t][r]; s1[t][r] = d1 ? -INFINITY : s1[t][r]; }
            }
        }
        float m0 = fmaxf(s0[0][0], s0[0][1]), m1 = fmaxf(s1[0][0], s1[0][1]), n0 = fmaxf(s0[0][2], s0[0][3]), n1 = fmaxf(s1[0][2], s1[0][3]);
#pragma unroll
        for (int t = 1; t < 17; ++t) {
            m0 = fmaxf(fmaxf(m0, s0[t][0]), s0[t][1]); m1 = fmaxf(fmaxf(m1, s1[t][0]), s1[t][1]);
            n0 = fmaxf(fmaxf(n0, s0[t][2]), s0[t][3]); n1 = fmaxf(fmaxf(n1, s1[t][2]), s1[t][3]);
        }
        m0 = fmaxf(m0, n0); m1 = fmaxf(m1, n1);
        m0 = xmax4(m0); m1 = xmax4(m1);
        f32x4 o00 = Z4, o01 = Z4, o10 = Z4, o11 = Z4;
        const u32 vb = L0 + L_V + (kr0 + 4 * g + q4) * 96 + hd * 48 + 8 * p4;
        auto pv = [&](auto SUB) {
            constexpr bool sub = SUB;
            u32x4 vf[2][2];
            vf[0][0] = ldtr2(vb, vb + 16 * 96); vf[0][1] = ldtr2(vb + 32, vb + 16 * 96 + 32);
#define EX(v, m) __builtin_amdgcn_exp2f(sub ? (v) - (m) : (v))
            static_for<0, 9>([&](auto U) {
                constexpr int u = U;
                WO_PIECE(u + 1);
                if constexpr (u < 8) { const u32 a = vb + (u + 1) * 32 * 96; vf[(u + 1) & 1][0] = ldtr2(a, a + 16 * 96); vf[(u + 1) & 1][1] = ldtr2(a + 32, a + 16 * 96 + 32); }
                u32x4 pa, pb;
                pa[0] = pack2(EX(s0[2 * u][0], m0), EX(s0[2 * u][1], m0)); pa[1] = pack2(EX(s0[2 * u][2], m0), EX(s0[2 * u][3], m0));
                if constexpr (u < 8) { pa[2] = pack2(EX(s0[2 * u + 1][0], m0), EX(s0[2 * u + 1][1], m0)); pa[3] = pack2(EX(s0[2 * u + 1][2], m0), EX(s0[2 * u + 1][3], m0)); }
                else { pa[2] = 0; pa[3] = 0; }
                if constexpr (u > 0) { pb[0] = pack2(EX(s1[2 * u - 1][0], m1), EX(s1[2 * u - 1][1], m1)); pb[1] = pack2(EX(s1[2 * u - 1][2], m1), EX(s1[2 * u - 1][3], m1)); }
                else { pb[0] = 0; pb[1] = 0; }
                pb[2] = pack2(EX(s1[2 * u][0], m1), EX(s1[2 * u][1], m1)); pb[3] = pack2(EX(s1[2 * u][2], m1), EX(s1[2 * u][3], m1));
                o00 = mfma(vf[u & 1][0], pa, o00); o01 = mfma(vf[u & 1][1], pa, o01);
                o10 = mfma(vf[u & 1][0], pb, o10); o11 = mfma(vf[u & 1][1], pb, o11);
                SB0();
            });
#undef EX
        };
        if (__all(fabsf(m0) <= 64.f && fabsf(m1) <= 64.f)) pv(std::false_type{}); else pv(std::true_type{});
        const float i0 = __builtin_amdgcn_rcpf(__shfl(o01[0], i16 + 16)), i1 = __builtin_amdgcn_rcpf(__shfl(o11[0], i16 + 16));
        const u32 oa = L0 + L_Q + (kr0 + i16) * 80 + hd * 40;
        sts64(oa + 8 * g, (u32x2){pack2(o00[0] * i0, o00[1] * i0), pack2(o00[2] * i0, o00[3] * i0)});
        sts64(oa + 1280 + 8 * g, (u32x2){pack2(o10[0] * i1, o10[1] * i1), pack2(o10[2] * i1, o10[3] * i1)});
        if (g == 0) {
            sts64(oa + 32, (u32x2){pack2(o01[0] * i0, o01[1] * i0), pack2(o01[2] * i0, o01[3] * i0)});
            sts64(oa + 1280 + 32, (u32x2){pack2(o11[0] * i1, o11[1] * i1), pack2(o11[2] * i1, o11[3] * i1)});
        }
    }
    BAR();
    {
        const int tok = 64 * (w & 1) + l;
        const u32 oo = WS_O + (((rb * 8 + (tok >> 4)) * 80 + 5 * j) * 16 + (tok & 15)) * 16;
        ST16C(lds128(L0 + L_Q + tok * 80 + (w >> 1) * 16), oo + (w >> 1) * 256);
        if (w < 2) ST16C(lds128(L0 + L_Q + tok * 80 + 64), oo + 4 * 256);
    }
    VMCNT(0);
    BAR();
    if (tid == 0) {
        st_agent(ws + WS_FO + (rb * 16 + j) * FS, tag);
        if (wpwg) st_agent(ws + WS_FWP + b * FS, tag);
    }

    const int wm = w & 3, wk = w >> 2;
    const int mt3 = rb * 8 + 4 * hf + wm;
    u32x2 x1k[5];
    {
        const int wdu = b + 256 * (w - 6);
        const bool wdw = w >= 6 && wdu < 432;
        if (w >= 1 && w < 5) {
#pragma unroll
            for (int i = 0; i < 5; ++i) glds16(p.wo, wosrc + (w - 1) * 32 * 1280, wvoff[i], L0 + L_WO + (w - 1) * 5120 + i * 1024);
        } else if (w >= 5) {
#pragma unroll
            for (int e = 0; e < 4; ++e) {
                const int pc = w - 5 + 3 * e;
                if (pc < 10) {
                    const u32 sg = 64 * pc + l, r = (sg * 6554) >> 16, c = sg - r * 10;
                    glds16(p.x, (const char*)p.x + (rb * 128 + 64 * hf) * 1280 + cb * 160, r * 1280 + c * 16, L0 + L_XR + pc * 1024);
                }
            }
            if (wdw) {
                const int ucb = wdu / 54, us = wdu - 54 * ucb;
#pragma unroll
                for (int i = 0; i < 5; ++i) glds16(p.wd, (const char*)p.wd + ucb * 160 + us * 32 * 1280, wvoff[i], L0 + L_WDRAW + (w - 6) * 5120 + i * 1024);
            }
        }
        if (w == 1 && l < 20) stsf4(L0 + L_GA + l * 16, gather1(ws, 320 + 20 * cb + l, tag, ws, 4));
        if (w == 0) {
            const int bl = min((Gj >> 2) + ((l >> 2) % 3), (Gj + nj - 1) >> 2);
            poll_at(ws, WS_FWP + (108 * (l >= 12 && l < 24 ? 1 : 0) + 4 * bl + (l & 3)) * FS, l, tag, ws, 6);
            poll_flags(ws + WS_FO + rb * 16 * FS, 16, l, tag, ws, 5);
        }
        if (wdw) vmw<5>(); else vmw<0>();
        BAR();
        u32x4 a[10];
        const char* asrc = ws + WS_O + (mt3 * 80 + 40 * wk) * 256;
#define AL(s) a[s] = gld16c(((s) & 3) * 1024, asrc + ((s) >> 2) * 4096, lo16)
        AL(0); AL(1); AL(2); AL(3);
        f32x4 acc[5];
#pragma unroll
        for (int n = 0; n < 5; ++n) acc[n] = Z4;
        const u32 wb = L0 + L_WO + wk * 51200;
        const u32 b0 = wb + 160 * (8 * g + 4 * (g & 1) + q4) + 8 * p4, b1 = wb + 160 * (8 * g + 4 * ((g & 1) ^ 1) + q4) + 8 * p4;
        u32x4 bf[3][5];
#pragma unroll
        for (int n = 0; n < 5; ++n) { bf[0][n] = ldtr2(b0 + n * 32, b1 + n * 32); bf[1][n] = ldtr2(b0 + 5120 + n * 32, b1 + 5120 + n * 32); }
        static_for<0, 10>([&](auto S) {
            constexpr int s = S;
            vmw1<(s < 7 ? 3 : 9 - s)>(a[s]);
            if constexpr (s + 4 < 10) AL(s + 4);
            SB0();
#pragma unroll
            for (int n = 0; n < 5; ++n) {
                if (s < 8) bf[(s + 2) % 3][n] = ldtr2(b0 + (s + 2) * 5120 + n * 32, b1 + (s + 2) * 5120 + n * 32);
                acc[n] = mfma(bf[s % 3][n], a[s], acc[n]);
            }
#pragma unroll
            for (int n = 0; n < 5; ++n) { SGB(M_DSR, 2); SGB(M_MFMA, 1); }
            SB0();
        });
#undef AL
        if (wk == 1) {
#pragma unroll
            for (int n = 0; n < 5; ++n) stsf4(L0 + L_RED3 + (wm * 5 + n) * 1024 + lo16, acc[n]);
        }
        BAR();
        if (wk == 0) {
            float s1 = 0.f, s2 = 0.f;
#pragma unroll
            for (int n = 0; n < 5; ++n) {
                const f32x4 v = acc[n] + ldsf4(L0 + L_RED3 + (wm * 5 + n) * 1024 + lo16);
                const f32x4 ga = ldsf4(L0 + L_GA + (16 * n + 4 * g) * 4);
                const u32x2 xr = lds64(L0 + L_XR + (16 * wm + i16) * 160 + 32 * n + 8 * g);
                const u32x2 o = {pack2(bflo(xr[0]) + ga[0] * v[0], bfhi(xr[0]) + ga[1] * v[1]), pack2(bflo(xr[1]) + ga[2] * v[2], bfhi(xr[1]) + ga[3] * v[3])};
                x1k[n] = o;
                s1 = dot2(o[0], 0x3F803F80u, s1); s1 = dot2(o[1], 0x3F803F80u, s1);
                s2 = dot2(o[0], o[0], s2); s2 = dot2(o[1], o[1], s2);
            }
            const u32 xo = WS_X1 + ((mt3 * 80 + 10 * cb) * 16 + i16) * 16;
#pragma unroll
            for (int n = 0; n < 4; n += 2) {
                const auto a0 = __builtin_amdgcn_permlane16_swap(x1k[n][0], x1k[n + 1][0], false, false);
                const auto a1 = __builtin_amdgcn_permlane16_swap(x1k[n][1], x1k[n + 1][1], false, false);
                ST16C(((u32x4){a0[0], a1[0], a0[1], a1[1]}), xo + (2 * (n + (g & 1)) + (g >> 1)) * 256);
            }
            ST8C(x1k[4], xo + (8 + (g >> 1)) * 256 + 8 * (g & 1));
            s1 = xsum4(s1); s2 = xsum4(s2);
            if (l < 16) ST8C(((u32x2){__float_as_uint(s1), __float_as_uint(s2)}), WS_STAT + ((rb * 8 + cb) * 128 + 64 * hf + 16 * wm + l) * 8);
            vmw<0>();
        } else {
#pragma unroll
            for (int n = 0; n < 5; ++n) x1k[n] = (u32x2){0, 0};
            if (wdw) {
                const u32 wr = L0 + L_WDRAW + (w - 6) * 5120;
                const u32 r0 = wr + 160 * (8 * g + 4 * (g & 1) + q4) + 8 * p4, r1 = wr + 160 * (8 * g + 4 * ((g & 1) ^ 1) + q4) + 8 * p4;
#pragma unroll
                for (int n = 0; n < 5; ++n) ST16C(ldtr2(r0 + n * 32, r1 + n * 32), WS_WD + (wdu * 5 + n) * 1024 + lo16);
            }
        }
        BAR();
        if (tid == 0) st_agent(ws + WS_FX1 + (rb * 16 + j) * FS, tag);
    }

    const char* wdsrc = ws + WS_WD + (cb * 54 + wk * 27) * 5120;
    const u32 r5w = L0 + wk * 20480 + wm * 5120;
#define P5_B1(t, i) do { if ((i) == 0 || (i) == 4) glds16c(wdsrc + min(4 * (t) + wm, 26) * 5120 + (i) * 1024, lo16, r5w + r5_slot(t) + (i) * 1024); \
        else glds16c_next(((i) & 3) * 1024, wdsrc + min(4 * (t) + wm, 26) * 5120, lo16); } while (0)
    {
        const int wn = w >> 2;
        const int bst = w < 4 ? 4 * w : 16 + 3 * (w - 4);
        const char* wpj = ws + WS_WP + (j * 20 * 14 + bst) * 1024;
        const char* x1b = ws + WS_X1 + (rb * 8 + w) * 80 * 256;
        const u32 r4a = L0 + L_R4 + w * 2048, r4b = L0 + L_R4 + 16384 + bst * 1024;
#define P4_B1(t, i) do { if ((i) == 0) glds16c(wpj + (t) * 28 * 1024, lo16, r4b + ((t) % 3) * R4_STAGE); else glds16c_next((i) * 1024, wpj + (t) * 28 * 1024, lo16); } while (0)
#define P4_A1(t, i) do { if ((i) == 0) glds16c(x1b + 8 * (t) * 256, lo16, r4a + ((t) % 3) * R4_STAGE); else glds16c_next(1024, x1b + 8 * (t) * 256, lo16); } while (0)
#define P4_B(t) do { P4_B1(t, 0); P4_B1(t, 1); P4_B1(t, 2); if (w < 4) P4_B1(t, 3); } while (0)
        P4_B(0); P4_B(1); P4_B(2);
        if (w == 0) poll_flags(ws + WS_FX1 + rb * 16 * FS, 16, l, tag, ws, 7);
        BAR();
        glds16c(ws + WS_STAT + rb * 8192 + w * 1024, lo16, L0 + L_ST4 + w * 1024);
        if (w < 7) glds16c(ws + WS_UV + j * 7168 + w * 1024, lo16, L0 + L_UV + w * 1024);
        P4_A1(0, 0); P4_A1(0, 1); P4_A1(1, 0); P4_A1(1, 1); P4_A1(2, 0); P4_A1(2, 1);
        f32x4 acc[7][2];
#pragma unroll
        for (int n = 0; n < 7; ++n) { acc[n][0] = Z4; acc[n][1] = Z4; }
        const u32 ra = opaque(L0 + L_R4 + 2 * wm * 2048 + lo16), rbb = opaque(L0 + L_R4 + 16384 + 7 * wn * 1024 + lo16);
        u32x4 fa[2][2], fb[2][7];
#define P4_RA(buf, t, ks) do { const u32 so = ((t) % 3) * R4_STAGE + (ks) * 1024; fa[buf][0] = lds128(ra + so); fa[buf][1] = lds128(ra + so + 2048); } while (0)
#define P4_RB(buf, t, ks, n) fb[buf][n] = lds128(rbb + ((t) % 3) * R4_STAGE + (ks) * 14336 + (n) * 1024)
#define P4_MM(buf, n) do { acc[n][0] = mfma(fb[buf][n], fa[buf][0], acc[n][0]); acc[n][1] = mfma(fb[buf][n], fa[buf][1], acc[n][1]); } while (0)
#define P4_HALF(buf, rd, t, ks, V0, V1, V2) do { \
            SB0(); if (rd) { P4_RA((buf) ^ 1, t, ks); P4_RB((buf) ^ 1, t, ks, 0); } P4_MM(buf, 0); SB0(); V0; \
            SB0(); if (rd) P4_RB((buf) ^ 1, t, ks, 1); P4_MM(buf, 1); SB0(); \
            SB0(); if (rd) P4_RB((buf) ^ 1, t, ks, 2); P4_MM(buf, 2); SB0(); V1; \
            SB0(); if (rd) P4_RB((buf) ^ 1, t, ks, 3); P4_MM(buf, 3); SB0(); \
            SB0(); if (rd) P4_RB((buf) ^ 1, t, ks, 4); P4_MM(buf, 4); SB0(); V2; \
            SB0(); if (rd) P4_RB((buf) ^ 1, t, ks, 5); P4_MM(buf, 5); SB0(); \
            SB0(); if (rd) P4_RB((buf) ^ 1, t, ks, 6); P4_MM(buf, 6); SB0(); } while (0)
        VMCNT(4);
        BAR();
        if (tid == 0) st_agent(ws + WS_FW2 + b * FS, tag);
        u32x2 wdf = {0u, 0u};
        const u32 wdo = WS_FW2 + ((54 * cb + (l < 54 ? l : 0)) & 255) * FS;
        P4_RA(0, 0, 0);
#pragma unroll
        for (int n = 0; n < 7; ++n) P4_RB(0, 0, 0, n);
        static_for<0, 10>([&](auto T) {
            constexpr int t = T;
            constexpr bool f = t >= 1 && t + 2 <= 9, sN = t + 3 <= 9;
            if constexpr (t < 9) P4_HALF(0, true, t, 1, if (f && w < 4) P4_B1(t + 2, 3), if (f) P4_A1(t + 2, 0), if (f) P4_A1(t + 2, 1));
            else P4_HALF(0, true, t, 1, P5_B1(0, 3), P5_B1(0, 4), P5_B1(1, 0));
            if constexpr (t == 5) {
                if (tid < 128) {
                    f32x2 st = {0.f, 0.f};
#pragma unroll
                    for (int c = 0; c < 8; ++c) st += *(LAS f32x2*)(size_t)(L0 + L_ST4 + (c * 128 + tid) * 8);
                    const float mu = st[0] * (1.f / 640.f), var = fmaxf(st[1] * (1.f / 640.f) - mu * mu, 0.f), r = __builtin_amdgcn_rsqf(var + 1e-5f);
                    stsf2(L0 + L_MR + tid * 8, (f32x2){r, -mu * r});
                } else {
                    const int e = tid - 128;
                    stsf(L0 + L_UVS + e * 4, (ldsf(L0 + L_UV + (e >> 5) * 512 + (e & 31) * 4) + ldsf(L0 + L_UV + (e >> 5) * 512 + 128 + (e & 31) * 4))
                                           + (ldsf(L0 + L_UV + (e >> 5) * 512 + 256 + (e & 31) * 4) + ldsf(L0 + L_UV + (e >> 5) * 512 + 384 + (e & 31) * 4)));
                    if (e < 64) { const int e2 = e + 384;
                        stsf(L0 + L_UVS + e2 * 4, (ldsf(L0 + L_UV + (e2 >> 5) * 512 + (e2 & 31) * 4) + ldsf(L0 + L_UV + (e2 >> 5) * 512 + 128 + (e2 & 31) * 4))
                                                + (ldsf(L0 + L_UV + (e2 >> 5) * 512 + 256 + (e2 & 31) * 4) + ldsf(L0 + L_UV + (e2 >> 5) * 512 + 384 + (e2 & 31) * 4))); }
                }
            }
            if constexpr (t < 9) {
                if constexpr (t == 0) vmw<2>();
                else if constexpr (t == 8) {
                    asm volatile("s_waitcnt vmcnt(0)" : "+v"(wdf) :: "memory");
                    if (w == 7) { if (!__all(wdf[0] == (u32)tag && wdf[1] == (u32)(tag >> 32))) poll_at(ws, wdo, l, tag, ws, 12); }
                }
                else { if (w < 4) vmw<6>(); else vmw<5>(); }
                BAR();
                if constexpr (t == 7) { if (w == 7) asm volatile("s_nop 4\n\tglobal_load_dwordx2 %0, %1, %2 sc1" : "=v"(wdf) : "v"(wdo), "s"(ws) : "memory"); }
                if constexpr (t < 8) P4_HALF(1, true, t + 1, 0, if (sN) P4_B1(t + 3, 0), if (sN) P4_B1(t + 3, 1), if (sN) P4_B1(t + 3, 2));
                else P4_HALF(1, true, t + 1, 0, P5_B1(0, 0), P5_B1(0, 1), P5_B1(0, 2));
            } else {
                P4_HALF(1, false, 0, 0, P5_B1(1, 1), P5_B1(1, 2), P5_B1(1, 3));
                P5_B1(1, 4);
            }
        });
#undef P4_B
#undef P4_B1
#undef P4_A1
#undef P4_RA
#undef P4_RB
#undef P4_MM
#undef P4_HALF
        f32x2 mr[2];
#pragma unroll
        for (int m = 0; m < 2; ++m) mr[m] = *(LAS f32x2*)(size_t)(L0 + L_MR + (32 * wm + 16 * m + i16) * 8);
#pragma unroll
        for (int n = 0; n < 7; ++n) {
            const f32x4 u = ldsf4(L0 + L_UVS + (7 * wn + n) * 128 + 16 * g), v = ldsf4(L0 + L_UVS + (7 * wn + n) * 128 + 64 + 16 * g);
#pragma unroll
            for (int m = 0; m < 2; ++m) acc[n][m] = acc[n][m] * mr[m][0] + (u * mr[m][1] + v);
        }
        if (nj == 7) stsf4(L0 + L_XCH + w * 1024 + lo16, wn == 0 ? acc[6][1] : acc[6][0]);
        auto silu_mul = [&](const f32x4 gt, const f32x4 up) -> u32x2 {
            const f32x4 e = gt * (-LOG2E), gu = gt * up;
            f32x4 d;
#pragma unroll
            for (int r = 0; r < 4; ++r) d[r] = __builtin_amdgcn_exp2f(e[r]);
            d = d + 1.f;
#pragma unroll
            for (int r = 0; r < 4; ++r) d[r] = __builtin_amdgcn_rcpf(d[r]);
            const f32x4 o = gu * d;
            return (u32x2){pack2(o[0], o[1]), pack2(o[2], o[3])};
        };
#pragma unroll
        for (int a = 0; a < 3; ++a) {
            const int la = a + 4 * wn, gam = Gj + (la <= 3 || nj == 7 ? la : la - 1);
            const u32x2 o0 = silu_mul(acc[2 * a][0], acc[2 * a + 1][0]), o1 = silu_mul(acc[2 * a][1], acc[2 * a + 1][1]);
            const auto s0 = __builtin_amdgcn_permlane16_swap(o0[0], o1[0], false, false);
            const auto s1 = __builtin_amdgcn_permlane16_swap(o0[1], o1[1], false, false);
            const u32x4 v = {s0[0], s1[0], s0[1], s1[1]};
            ST16C(v, WS_ACT + (((rb * 8 + 2 * wm + (g & 1)) * 216 + 2 * gam + (g >> 1)) * 16 + i16) * 16);
        }
        BAR();
        if (nj == 7) {
            const f32x4 other = ldsf4(L0 + L_XCH + (w ^ 4) * 1024 + lo16);
            const u32x2 o = wn == 0 ? silu_mul(acc[6][0], other) : silu_mul(other, acc[6][1]);
            ST8C(o, WS_ACT + (((rb * 8 + 2 * wm + wn) * 216 + 2 * (Gj + 3) + (g >> 1)) * 16 + i16) * 16 + 8 * (g & 1));
        }
        P5_B1(2, 0); P5_B1(2, 1); P5_B1(2, 2); P5_B1(2, 3); P5_B1(2, 4);
        vmw<5>();
        BAR();
        if (tid == 0) st_agent(ws + WS_FACT + (rb * 16 + j) * FS, tag);
    }

    {
        if (w == 1 && l < 20) stsf4(L0 + L_GB + l * 16, gather1(ws, 800 + 20 * cb + l, tag, ws, 8));
        if (w == 0) poll_flags(ws + WS_FACT + rb * 16 * FS, 16, l, tag, ws, 9);
        const bool bump = rb == 15 && j == 0;
        if (bump && w >= 4) poll_at(ws, WS_FO + (tid - 256) * FS, l, tag, ws, 10);
        VMCNT(0);
        BAR();
        if (bump && tid == 0) st_agent(ws + WS_EPOCH, tag);
        const char* asrc = ws + WS_ACT + (mt3 * 216 + wk * 108) * 256;
        u32x4 a[3][4];
#define P5_A1(t, i) a[(t) % 3][i] = gld16c((i) * 1024, asrc + (t) * 4096, lo16)
        P5_A1(0, 0); P5_A1(0, 1); P5_A1(0, 2); P5_A1(0, 3); P5_A1(1, 0); P5_A1(1, 1); P5_A1(1, 2); P5_A1(1, 3); P5_A1(2, 0); P5_A1(2, 1); P5_A1(2, 2); P5_A1(2, 3);
        f32x4 acc[5];
#pragma unroll
        for (int n = 0; n < 5; ++n) acc[n] = Z4;
        const u32 rb5 = opaque(L0 + wk * 20480 + lo16);
        u32x4 fb[3][5];
#define P5_RD(buf, t, kk) do { _Pragma("unroll") for (int n = 0; n < 5; ++n) fb[buf][n] = lds128(rb5 + r5_slot(t) + (kk) * 5120 + n * 1024); } while (0)
#define P5_MM(buf, av) do { _Pragma("unroll") for (int n = 0; n < 5; ++n) acc[n] = mfma(fb[buf][n], (av), acc[n]); } while (0)
#define P5_MIX() do { _Pragma("unroll") for (int n = 0; n < 5; ++n) { SGB(M_DSR, 1); SGB(M_MFMA, 1); } } while (0)
        P5_RD(0, 0, 0); P5_RD(1, 0, 1);
        static_for<0, 7>([&](auto T) {
            constexpr int t = T, q = t % 3, k0 = 4 * t;
            constexpr bool nx = t + 3 < 7;
            constexpr int NA = t == 0 ? 8 : t == 1 ? 13 : t == 2 ? 18 : t == 3 ? 19 : t == 4 ? 18 : t == 5 ? 9 : 3;
            constexpr int NB = t == 2 ? 11 : t == 3 ? 11 : t == 4 ? 8 : t == 5 ? 0 : 63;
            vmw4<NA>(a[q][0], a[q][1], a[q][2], a[q][3]);
            SB0(); P5_RD((k0 + 2) % 3, t, 2); P5_MM(k0 % 3, a[q][0]); P5_MIX(); SB0();
            if constexpr (nx) P5_A1(t + 3, 0);
            if constexpr (t < 6) {
                SB0(); P5_RD((k0 + 3) % 3, t, 3); P5_MM((k0 + 1) % 3, a[q][1]); P5_MIX(); SB0();
                if constexpr (nx) P5_A1(t + 3, 1);
                vmw<NB>(); BAR();
                if constexpr (nx) { P5_B1(t + 3, 0); P5_B1(t + 3, 1); }
                SB0(); P5_RD((k0 + 4) % 3, t + 1, 0); P5_MM((k0 + 2) % 3, a[q][2]); P5_MIX(); SB0();
                if constexpr (nx) { P5_A1(t + 3, 2); P5_B1(t + 3, 2); P5_B1(t + 3, 3); }
                SB0(); P5_RD((k0 + 5) % 3, t + 1, 1); P5_MM((k0 + 3) % 3, a[q][3]); P5_MIX(); SB0();
                if constexpr (nx) { if constexpr (t + 3 < 6) P5_A1(t + 3, 3); P5_B1(t + 3, 4); }
            } else {
                SB0(); P5_MM((k0 + 1) % 3, a[q][1]); SB0(); P5_MM((k0 + 2) % 3, a[q][2]); SB0();
            }
        });
#undef P5_B1
#undef P5_A1
#undef P5_RD
#undef P5_MM
#undef P5_MIX
        if (wk == 1) {
#pragma unroll
            for (int n = 0; n < 5; ++n) stsf4(L0 + L_RED5 + (wm * 5 + n) * 1024 + lo16, acc[n]);
        }
        BAR();
        if (wk == 0) {
#pragma unroll
            for (int n = 0; n < 5; ++n) {
                const f32x4 v = acc[n] + ldsf4(L0 + L_RED5 + (wm * 5 + n) * 1024 + lo16);
                const f32x4 gb = ldsf4(L0 + L_GB + (16 * n + 4 * g) * 4);
                const u32x2 o = {pack2(bflo(x1k[n][0]) + gb[0] * v[0], bfhi(x1k[n][0]) + gb[1] * v[1]), pack2(bflo(x1k[n][1]) + gb[2] * v[2], bfhi(x1k[n][1]) + gb[3] * v[3])};
                sts64(L0 + L_OS + (16 * wm + i16) * 160 + 32 * n + 8 * g, o);
            }
        }
        BAR();
#pragma unroll
        for (int i = 0; i < 2; ++i) {
            const int id = tid + 512 * i;
            if (id < 640) {
                const int r = (id * 6554) >> 16, c = id - 10 * r;
                const u32x4 ov = lds128(L0 + L_OS + id * 16);
                asm volatile("global_store_dwordx4 %0, %1, %2 sc1\n\ts_nop 1" :: "v"((u32)((rb * 128 + 64 * hf + r) * 1280 + cb * 160 + c * 16)), "v"(ov), "s"(p.out) : "memory");
            }
        }
    }
    asm volatile(".Lmk_code_to:");
}

extern "C" void kernel_launch(void* const* d_in, const int* in_sizes, int n_in, void* d_out, int out_size, void* d_ws, size_t ws_size, hipStream_t stream) {
    Params p;
    p.x = (const u16*)d_in[0]; p.wqkv = (const u16*)d_in[1]; p.wo = (const u16*)d_in[2];
    p.wg = (const u16*)d_in[4]; p.wu = (const u16*)d_in[5]; p.wd = (const u16*)d_in[6];
    p.c = (const u16*)d_in[7]; p.wmod = (const u16*)d_in[8];
    p.out = (u16*)d_out; p.ws = (char*)d_ws;
    mega_kernel<<<256, 512, 0, stream>>>(p);
}
```
